```python
import torch

def _softmax(x, axis=-1):
    return torch.softmax(x, dim=axis)
def _gelu(x):
    return torch.nn.functional.gelu(x, approximate="tanh")
def _silu(x):
    return torch.nn.functional.silu(x)
def _sigmoid(x):
    return torch.sigmoid(x)
def _layernorm(x, axis=-1, eps=1e-5):
    m = x.mean(dim=axis, keepdim=True)
    v = x.var(dim=axis, keepdim=True, unbiased=False)
    return (x - m) / torch.sqrt(v + eps)
def _scatter_add(tgt, idx, src, axis=0):
    return tgt.clone().index_add_(axis, idx, src)
def _segment_sum(src, seg, n):
    out = torch.zeros((n,) + src.shape[1:], dtype=src.dtype)
    return out.index_add_(0, seg, src)
def _pad(x, pw):
    flat = tuple(v for pair in reversed(pw) for v in pair)
    return torch.nn.functional.pad(x, flat)

kernel_name = 'standalone__mha_block_cached__near_ridge'

def setup_inputs():

    _w = (lambda a: (lambda t: t.to(torch.bfloat16) if t.is_floating_point() else t)(torch.as_tensor(a)))
    B, Sq, D = 8, 160, 1024
    H, Sk, Dh = 32, 512, 128
    return {
        "x": _w((lambda *s: torch.randn(*s, dtype=torch.bfloat16) * 0.8)(B, Sq, D)),
        "k_cache": _w((lambda *s: torch.randn(*s, dtype=torch.bfloat16) * 0.8)(B, H, Sk, Dh)),
        "v_cache": _w((lambda *s: torch.randn(*s, dtype=torch.bfloat16) * 0.05)(B, H, Sk, Dh)),
        "wq": _w((lambda *s: torch.randn(*s, dtype=torch.bfloat16) * 0.8)(D, H * Dh) * (D ** -0.5)),
        "wo": _w((lambda *s: torch.randn(*s, dtype=torch.bfloat16) * 0.05)(H * Dh, D) * ((H * Dh) ** -0.5)),
    }

def reference(x, k_cache, v_cache, wq, wo):
    B, Sq, D = x.shape
    H, Dh = 32, 128
    q = (lambda x, *d: x.permute(*d))((x @ wq).reshape(B, Sq, H, Dh), 0, 2, 1, 3)
    scores = torch.einsum("bhqd,bhkd->bhqk", q, k_cache) * (Dh ** -0.5)
    p = _softmax(scores, axis=-1)
    o = torch.einsum("bhqk,bhkd->bhqd", p, v_cache)
    return (lambda x, *d: x.permute(*d))(o, 0, 2, 1, 3).reshape(B, Sq, H * Dh) @ wo
```

```cpp
#include <hip/hip_runtime.h>
#include <hip/hip_bf16.h>
typedef short bf16x8 __attribute__((ext_vector_type(8)));
typedef float f32x16 __attribute__((ext_vector_type(16)));
typedef unsigned short u16;
__device__ __forceinline__ u16 f2bf(float x){ unsigned u=__float_as_uint(x); u+=0x7fffu+((u>>16)&1u); return (u16)(u>>16);}
__device__ __forceinline__ float bf2f(u16 x){ return __uint_as_float(((unsigned)x)<<16);}
__device__ __forceinline__ int crow(int r,int h){return (r&3)+8*(r>>2)+4*h;}
__global__ __launch_bounds__(256) void gemm(const u16* A,const u16* B,u16* C,int M,int N,int K){
  __shared__ u16 sB[64][72];
  int tid=threadIdx.x,w=tid>>6,l=tid&63,r=l&31,h=l>>5;
  int m0=blockIdx.y*64+(w>>1)*32,n0=blockIdx.x*64;
  f32x16 acc={};
  for(int k0=0;k0<K;k0+=64){
    __syncthreads();
    for(int i=0;i<2;i++){ int c=tid+256*i; int kr=c>>3, nc=(c&7)*8;
      bf16x8 v=*(const bf16x8*)(B+(size_t)(k0+kr)*N+n0+nc);
      for(int j=0;j<8;j++) sB[nc+j][kr]=(u16)v[j]; }
    __syncthreads();
    for(int s=0;s<4;s++){
      bf16x8 a=*(const bf16x8*)(A+(size_t)(m0+r)*K+k0+s*16+h*8);
      bf16x8 b=*(const bf16x8*)(&sB[(w&1)*32+r][s*16+h*8]);
      acc=__builtin_amdgcn_mfma_f32_32x32x16_bf16(a,b,acc,0,0,0);
    }
  }
  for(int i=0;i<16;i++) C[(size_t)(m0+crow(i,h))*N+n0+(w&1)*32+r]=f2bf(acc[i]);
}
__global__ __launch_bounds__(64) void attn(const u16* Q,const u16* Kc,const u16* Vc,u16* O){
  __shared__ u16 sV[128][40];
  int l=threadIdx.x,r=l&31,h=l>>5;
  int qt=blockIdx.x, hd=blockIdx.y, b=blockIdx.z;
  const u16* q=Q+(size_t)(b*160+qt*32+r)*4096+hd*128;
  const u16* Kp=Kc+(size_t)(b*32+hd)*512*128;
  const u16* Vp=Vc+(size_t)(b*32+hd)*512*128;
  bf16x8 qf[8];
  for(int s=0;s<8;s++) qf[s]=*(const bf16x8*)(q+s*16+h*8);
  f32x16 o[4]={};
  float m=-1e30f,lsum=0.f;
  const float c=0.08838834764831845f*1.4426950408889634f;
  for(int k0=0;k0<512;k0+=32){
    f32x16 s={};
    for(int t=0;t<8;t++){ bf16x8 kf=*(const bf16x8*)(Kp+(size_t)(k0+r)*128+t*16+h*8);
      s=__builtin_amdgcn_mfma_f32_32x32x16_bf16(kf,qf[t],s,0,0,0);}
    float mx=s[0]; for(int i=1;i<16;i++) mx=fmaxf(mx,s[i]);
    mx=fmaxf(mx,__shfl_xor(mx,32));
    float mn=fmaxf(m,mx), al=exp2f((m-mn)*c); m=mn;
    float ps=0; bf16x8 p[2];
    for(int i=0;i<16;i++){ float e=exp2f((s[i]-mn)*c); ps+=e; p[i>>3][i&7]=(short)f2bf(e);}
    ps+=__shfl_xor(ps,32); lsum=lsum*al+ps;
    for(int d=0;d<4;d++) for(int i=0;i<16;i++) o[d][i]*=al;
    __syncthreads();
    for(int i=0;i<8;i++){ int cidx=l+64*i; int kr=cidx>>4, dc=(cidx&15)*8;
      bf16x8 v=*(const bf16x8*)(Vp+(size_t)(k0+kr)*128+dc);
      for(int j=0;j<8;j++) sV[dc+j][kr]=(u16)v[j]; }
    __syncthreads();
    for(int d=0;d<4;d++) for(int st=0;st<2;st++){
      bf16x8 vf; for(int j=0;j<8;j++) vf[j]=(short)sV[d*32+r][16*st+8*(j>>2)+4*h+(j&3)];
      o[d]=__builtin_amdgcn_mfma_f32_32x32x16_bf16(vf,p[st],o[d],0,0,0);
    }
  }
  float inv=1.f/lsum;
  u16* op=O+(size_t)(b*160+qt*32+r)*4096+hd*128;
  for(int d=0;d<4;d++) for(int i=0;i<16;i++) op[d*32+crow(i,h)]=f2bf(o[d][i]*inv);
}
extern "C" void kernel_launch(void* const* d_in, const int* in_sizes, int n_in, void* d_out, int out_size, void* d_ws, size_t ws_size, hipStream_t stream){
  const u16* x=(const u16*)d_in[0]; const u16* kc=(const u16*)d_in[1]; const u16* vc=(const u16*)d_in[2];
  const u16* wq=(const u16*)d_in[3]; const u16* wo=(const u16*)d_in[4];
  u16* q=(u16*)d_ws; u16* o=q+1280*4096;
  gemm<<<dim3(4096/64,1280/64),256,0,stream>>>(x,wq,q,1280,4096,1024);
  attn<<<dim3(5,32,8),64,0,stream>>>(q,kc,vc,o);
  gemm<<<dim3(1024/64,1280/64),256,0,stream>>>(o,wo,(u16*)d_out,1280,1024,4096);
}
```

```cpp
#include <hip/hip_runtime.h>
#include <hip/hip_bf16.h>
typedef short bf16x8 __attribute__((ext_vector_type(8)));
typedef float f32x16 __attribute__((ext_vector_type(16)));
typedef unsigned short u16;
__device__ __forceinline__ u16 f2bf(float x){ unsigned u=__float_as_uint(x); u+=0x7fffu+((u>>16)&1u); return (u16)(u>>16);}
__device__ __forceinline__ float bf2f(u16 x){ return __uint_as_float(((unsigned)x)<<16);}
__device__ __forceinline__ int crow(int r,int h){return (r&3)+8*(r>>2)+4*h;}
__global__ __launch_bounds__(256) void gemm(const u16* A,const u16* B,u16* C,int M,int N,int K){
  __shared__ u16 sB[64][72];
  int tid=threadIdx.x,w=tid>>6,l=tid&63,r=l&31,h=l>>5;
  int m0=blockIdx.y*64+(w>>1)*32,n0=blockIdx.x*64;
  f32x16 acc={};
  for(int k0=0;k0<K;k0+=64){
    __syncthreads();
    for(int i=0;i<2;i++){ int c=tid+256*i; int kr=c>>3, nc=(c&7)*8;
      bf16x8 v=*(const bf16x8*)(B+(size_t)(k0+kr)*N+n0+nc);
      for(int j=0;j<8;j++) sB[nc+j][kr]=(u16)v[j]; }
    __syncthreads();
    for(int s=0;s<4;s++){
      bf16x8 a=*(const bf16x8*)(A+(size_t)(m0+r)*K+k0+s*16+h*8);
      bf16x8 b=*(const bf16x8*)(&sB[(w&1)*32+r][s*16+h*8]);
      acc=__builtin_amdgcn_mfma_f32_32x32x16_bf16(a,b,acc,0,0,0);
    }
  }
  for(int i=0;i<16;i++) C[(size_t)(m0+crow(i,h))*N+n0+(w&1)*32+r]=f2bf(acc[i]);
}
__global__ __launch_bounds__(64) void attn(const u16* Q,const u16* Kc,const u16* Vc,u16* O){
  __shared__ u16 sV[128][40];
  int l=threadIdx.x,r=l&31,h=l>>5;
  int qt=blockIdx.x, hd=blockIdx.y, b=blockIdx.z;
  const u16* q=Q+(size_t)(b*160+qt*32+r)*4096+hd*128;
  const u16* Kp=Kc+(size_t)(b*32+hd)*512*128;
  const u16* Vp=Vc+(size_t)(b*32+hd)*512*128;
  bf16x8 qf[8];
  for(int s=0;s<8;s++) qf[s]=*(const bf16x8*)(q+s*16+h*8);
  f32x16 o[4]={};
  float m=-1e30f,lsum=0.f;
  const float c=0.08838834764831845f*1.4426950408889634f;
  for(int k0=0;k0<512;k0+=32){
    f32x16 s={};
    for(int t=0;t<8;t++){ bf16x8 kf=*(const bf16x8*)(Kp+(size_t)(k0+r)*128+t*16+h*8);
      s=__builtin_amdgcn_mfma_f32_32x32x16_bf16(kf,qf[t],s,0,0,0);}
    float mx=s[0]; for(int i=1;i<16;i++) mx=fmaxf(mx,s[i]);
    mx=fmaxf(mx,__shfl_xor(mx,32));
    float mn=fmaxf(m,mx), al=exp2f((m-mn)*c); m=mn;
    float ps=0; bf16x8 p[2];
    for(int i=0;i<16;i++){ float e=exp2f((s[i]-mn)*c); ps+=e; p[i>>3][i&7]=(short)f2bf(e);}
    ps+=__shfl_xor(ps,32); lsum=lsum*al+ps;
    for(int d=0;d<4;d++) for(int i=0;i<16;i++) o[d][i]*=al;
    __syncthreads();
    for(int i=0;i<8;i++){ int cidx=l+64*i; int kr=cidx>>4, dc=(cidx&15)*8;
      bf16x8 v=*(const bf16x8*)(Vp+(size_t)(k0+kr)*128+dc);
      for(int j=0;j<8;j++) sV[dc+j][kr]=(u16)v[j]; }
    __syncthreads();
    for(int d=0;d<4;d++) for(int st=0;st<2;st++){
      bf16x8 vf; for(int j=0;j<8;j++) vf[j]=(short)sV[d*32+r][16*st+8*(j>>2)+4*h+(j&3)];
      o[d]=__builtin_amdgcn_mfma_f32_32x32x16_bf16(vf,p[st],o[d],0,0,0);
    }
  }
  float inv=1.f/lsum;
  u16* op=O+(size_t)(b*160+qt*32+r)*4096+hd*128;
  for(int d=0;d<4;d++) for(int i=0;i<16;i++) op[d*32+crow(i,h)]=f2bf(o[d][i]*inv);
}

typedef unsigned v4u __attribute__((ext_vector_type(4)));
template<int NT>
__global__ __launch_bounds__(1024) void rdk(const v4u* k,const v4u* v,const v4u* wq,const v4u* x,unsigned* sink,int amp){
  unsigned long long t0=__builtin_amdgcn_s_memrealtime();
  int g=blockIdx.x,t=threadIdx.x; v4u acc={0,0,0,0};
  const v4u* kp=k+(size_t)g*8192+t; const v4u* vp=v+(size_t)g*8192+t;
  v4u r[18];
  #pragma unroll
  for(int i=0;i<8;i++){ if(NT){r[i]=__builtin_nontemporal_load(kp+i*1024); r[8+i]=__builtin_nontemporal_load(vp+i*1024);} else {r[i]=kp[i*1024]; r[8+i]=vp[i*1024];} }
  r[16]=wq[(size_t)g*2048+t]; r[17]=wq[(size_t)g*2048+1024+t];
  #pragma unroll
  for(int i=0;i<18;i++) acc^=r[i];
  if(t<640) acc^=x[(size_t)g*640+t];
  sink[g*1024+t]=acc.x^acc.y^acc.z^acc.w;
  __syncthreads();
  unsigned long long t1=__builtin_amdgcn_s_memrealtime();
  unsigned long long until=t0+(t1-t0)*amp;
  while(__builtin_amdgcn_s_memrealtime()<until) __builtin_amdgcn_s_sleep(8);
}
extern "C" void kernel_launch(void* const* d_in, const int* in_sizes, int n_in, void* d_out, int out_size, void* d_ws, size_t ws_size, hipStream_t stream){
  const u16* x=(const u16*)d_in[0]; const u16* kc=(const u16*)d_in[1]; const u16* vc=(const u16*)d_in[2];
  const u16* wq=(const u16*)d_in[3]; const u16* wo=(const u16*)d_in[4];
  u16* q=(u16*)d_ws; u16* o=q+1280*4096;
  rdk<0><<<256,1024,0,stream>>>((const v4u*)kc,(const v4u*)vc,(const v4u*)wq,(const v4u*)x,(unsigned*)((char*)d_ws+(64<<20)),1);
  gemm<<<dim3(4096/64,1280/64),256,0,stream>>>(x,wq,q,1280,4096,1024);
  attn<<<dim3(5,32,8),64,0,stream>>>(q,kc,vc,o);
  gemm<<<dim3(1024/64,1280/64),256,0,stream>>>(o,wo,(u16*)d_out,1280,1024,4096);
}
```

```cpp
#include <hip/hip_runtime.h>
#include <hip/hip_bf16.h>

__global__ __launch_bounds__(256, 1) void standalone__mha_block_cached__near_ridge_kernel(const void* x, const void* k, const void* v, const void* wq, const void* wo, void* out, void* ws, void* ws2) {
  __shared__ __attribute__((aligned(16))) char smem[163840];
  asm volatile("" :: "v"(smem));
  asm volatile(
"s_mov_b32 s8, 0\n\t"
"LSTART_%=:\n\t"
"s_load_dwordx16 s[16:31], %0, 0x0\n\t"
"s_getpc_b64 s[34:35]\n\t"
"s_mov_b32 s64, %1\n\t"
"v_and_b32 v1, 63, %2\n\t"
"v_lshrrev_b32 v2, 6, %2\n\t"
"s_and_b32 s65, s64, 3\n\t"
"s_lshr_b32 s78, s64, 2\n\t"
"s_and_b32 s78, s78, 1\n\t"
"s_lshl_b32 s78, s78, 2\n\t"
"v_readfirstlane_b32 s69, v2\n\t"
"s_lshr_b32 s66, s64, 6\n\t"
"s_add_u32 s66, s66, s78\n\t"
"s_lshr_b32 s67, s64, 3\n\t"
"s_add_u32 s67, s67, 0\n\t"
"s_and_b32 s67, s67, 7\n\t"
"s_lshl_b32 s68, s65, 3\n\t"
"s_add_u32 s68, s68, s67\n\t"
"s_mul_i32 s72, s69, 40960\n\t"
"s_lshr_b32 s70, s69, 1\n\t"
"s_and_b32 s71, s69, 1\n\t"
"v_lshrrev_b32 v2, 4, v1\n\t"
"v_and_b32 v3, 7, v1\n\t"
"v_xor_b32 v3, v2, v3\n\t"
"v_lshlrev_b32 v3, 4, v3\n\t"
"v_lshrrev_b32 v4, 3, v1\n\t"
"v_lshl_add_u32 v3, v4, 11, v3\n\t"
"s_mul_i32 s78, s66, 327680\n\t"
"s_mul_i32 s79, s69, 81920\n\t"
"s_add_u32 s78, s78, s79\n\t"
"s_lshl_b32 s79, s71, 6\n\t"
"v_xor_b32 v3, s79, v3\n\t"
"v_add_u32 v3, s78, v3\n\t"
"v_xor_b32 v4, 64, v3\n\t"
"v_add_u32 v168, 4096, v3\n\t"
"v_add_u32 v169, 19456, v4\n\t"
"v_add_u32 v170, 34816, v3\n\t"
"v_add_u32 v171, 50176, v4\n\t"
"v_add_u32 v172, 69632, v3\n\t"
"v_and_b32 v3, 15, v1\n\t"
"v_lshlrev_b32 v4, 1, v2\n\t"
"v_xor_b32 v3, v3, v4\n\t"
"v_lshlrev_b32 v3, 4, v3\n\t"
"v_lshl_add_u32 v3, v2, 13, v3\n\t"
"s_lshl_b32 s78, s69, 17\n\t"
"s_xor_b32 s79, s68, 0\n\t"
"s_lshl_b32 s79, s79, 8\n\t"
"s_add_u32 s78, s78, s79\n\t"
"v_add_u32 v3, s78, v3\n\t"
"v_xor_b32 v4, 128, v3\n\t"
"v_add_u32 v173, 3072, v3\n\t"
"v_add_u32 v174, 34816, v3\n\t"
"v_add_u32 v175, 66560, v4\n\t"
"v_add_u32 v176, 102400, v4\n\t"
"s_mul_i32 s78, s67, 1\n\t"
"s_and_b32 s79, s66, 3\n\t"
"s_lshl_b32 s79, s79, 3\n\t"
"s_lshr_b32 s79, 100925952, s79\n\t"
"s_add_u32 s78, s78, s79\n\t"
"s_and_b32 s78, s78, 7\n\t"
"s_lshl_b32 s86, s78, 2\n\t"
"s_mov_b32 s14, 1985221378\n\t"
"s_mov_b32 s15, 4275870602\n\t"
"s_lshr_b32 s78, s14, s86\n\t"
"s_and_b32 s78, s78, 15\n\t"
"s_lshl_b32 s73, s78, 7\n\t"
"s_lshl_b32 s74, s78, 19\n\t"
"s_lshr_b32 s79, s64, 3\n\t"
"s_lshl_b32 s79, s79, 2\n\t"
"s_add_u32 s79, s79, s69\n\t"
"s_mul_i32 s80, s79, 1366\n\t"
"s_lshr_b32 s80, s80, 16\n\t"
"s_mul_i32 s80, s80, 48\n\t"
"s_sub_u32 s79, s79, s80\n\t"
"s_mul_i32 s79, s79, 1024\n\t"
"v_and_b32 v3, 7, v1\n\t"
"v_lshl_add_u32 v3, v3, 7, s79\n\t"
"global_load_dword v231, v3, s[34:35]\n\t"
"s_waitcnt lgkmcnt(0)\n\t"
"s_sub_u32 s36, s16, 4096\n\t"
"s_subb_u32 s37, s17, 0\n\t"
"s_and_b32 s37, s37, 65535\n\t"
"s_mov_b32 s38, 2625536\n\t"
"s_mov_b32 s39, 131072\n\t"
"s_sub_u32 s40, s22, 4096\n\t"
"s_subb_u32 s41, s23, 0\n\t"
"s_and_b32 s41, s41, 65535\n\t"
"s_mov_b32 s42, 8392704\n\t"
"s_mov_b32 s43, 131072\n\t"
"s_add_u32 m0, s72, 0\n\t"
"s_nop 0\n\t"
"buffer_load_dwordx4 v168, s[36:39], s73 offen lds\n\t"
"buffer_load_dwordx4 v169, s[36:39], s73 offen offset:1024 lds\n\t"
"buffer_load_dwordx4 v170, s[36:39], s73 offen offset:2048 lds\n\t"
"buffer_load_dwordx4 v171, s[36:39], s73 offen offset:3072 lds\n\t"
"s_add_u32 m0, s72, 4096\n\t"
"s_nop 0\n\t"
"buffer_load_dwordx4 v172, s[36:39], s73 offen lds\n\t"
"buffer_load_dwordx4 v173, s[40:43], s74 offen offset:1024 lds\n\t"
"buffer_load_dwordx4 v174, s[40:43], s74 offen offset:2048 lds\n\t"
"buffer_load_dwordx4 v175, s[40:43], s74 offen offset:3072 lds\n\t"
"s_add_u32 m0, s72, 8192\n\t"
"s_nop 0\n\t"
"buffer_load_dwordx4 v176, s[40:43], s74 offen lds\n\t"
"s_add_u32 s86, s86, 4\n\t"
"s_and_b32 s86, s86, 31\n\t"
"s_lshr_b32 s87, s14, s86\n\t"
"s_and_b32 s87, s87, 15\n\t"
"s_lshl_b32 s73, s87, 7\n\t"
"s_lshl_b32 s74, s87, 19\n\t"
"s_add_u32 m0, s72, 10240\n\t"
"s_nop 0\n\t"
"buffer_load_dwordx4 v168, s[36:39], s73 offen lds\n\t"
"buffer_load_dwordx4 v169, s[36:39], s73 offen offset:1024 lds\n\t"
"buffer_load_dwordx4 v170, s[36:39], s73 offen offset:2048 lds\n\t"
"buffer_load_dwordx4 v171, s[36:39], s73 offen offset:3072 lds\n\t"
"s_add_u32 m0, s72, 14336\n\t"
"s_nop 0\n\t"
"buffer_load_dwordx4 v172, s[36:39], s73 offen lds\n\t"
"buffer_load_dwordx4 v173, s[40:43], s74 offen offset:1024 lds\n\t"
"buffer_load_dwordx4 v174, s[40:43], s74 offen offset:2048 lds\n\t"
"buffer_load_dwordx4 v175, s[40:43], s74 offen offset:3072 lds\n\t"
"s_add_u32 m0, s72, 18432\n\t"
"s_nop 0\n\t"
"buffer_load_dwordx4 v176, s[40:43], s74 offen lds\n\t"
"s_add_u32 s86, s86, 4\n\t"
"s_and_b32 s86, s86, 31\n\t"
"s_lshr_b32 s87, s14, s86\n\t"
"s_and_b32 s87, s87, 15\n\t"
"s_lshl_b32 s73, s87, 7\n\t"
"s_lshl_b32 s74, s87, 19\n\t"
"s_add_u32 m0, s72, 19456\n\t"
"s_nop 0\n\t"
"buffer_load_dwordx4 v168, s[36:39], s73 offen lds\n\t"
"buffer_load_dwordx4 v169, s[36:39], s73 offen offset:1024 lds\n\t"
"buffer_load_dwordx4 v170, s[36:39], s73 offen offset:2048 lds\n\t"
"buffer_load_dwordx4 v171, s[36:39], s73 offen offset:3072 lds\n\t"
"s_add_u32 m0, s72, 23552\n\t"
"s_nop 0\n\t"
"buffer_load_dwordx4 v172, s[36:39], s73 offen lds\n\t"
"buffer_load_dwordx4 v173, s[40:43], s74 offen offset:1024 lds\n\t"
"buffer_load_dwordx4 v174, s[40:43], s74 offen offset:2048 lds\n\t"
"buffer_load_dwordx4 v175, s[40:43], s74 offen offset:3072 lds\n\t"
"s_add_u32 m0, s72, 27648\n\t"
"s_nop 0\n\t"
"buffer_load_dwordx4 v176, s[40:43], s74 offen lds\n\t"
"s_add_u32 s86, s86, 4\n\t"
"s_and_b32 s86, s86, 31\n\t"
"s_lshr_b32 s87, s14, s86\n\t"
"s_and_b32 s87, s87, 15\n\t"
"s_lshl_b32 s73, s87, 7\n\t"
"s_lshl_b32 s74, s87, 19\n\t"
"s_lshr_b32 s78, s22, 8\n\t"
"s_sub_u32 s78, 1, s78\n\t"
"s_lshr_b32 s79, s24, 8\n\t"
"s_sub_u32 s79, 1, s79\n\t"
"s_xor_b32 s78, s78, s79\n\t"
"s_and_b32 s78, s78, 3\n\t"
"s_cmp_eq_u32 s78, 1\n\t"
"s_cselect_b32 s78, 2, 1\n\t"
"s_xor_b32 s85, s67, s78\n\t"
"s_mov_b32 s82, 1966490264\n\t"
"s_cmp_eq_u32 s65, 1\n\t"
"s_cselect_b32 s82, 391144600, s82\n\t"
"s_cmp_eq_u32 s65, 2\n\t"
"s_cselect_b32 s82, 829556376, s82\n\t"
"s_cmp_eq_u32 s65, 3\n\t"
"s_cselect_b32 s82, 1393827992, s82\n\t"
"s_mul_i32 s83, s65, 34\n\t"
"s_add_u32 s83, s83, 16\n\t"
"s_lshl_b32 s78, s66, 5\n\t"
"s_add_u32 s78, s78, s68\n\t"
"s_xor_b32 s78, s78, 0\n\t"
"s_lshl_b32 s78, s78, 17\n\t"
"s_lshl_b32 s79, s69, 15\n\t"
"s_add_u32 s78, s78, s79\n\t"
"s_add_u32 s44, s18, s78\n\t"
"s_addc_u32 s45, s19, 0\n\t"
"s_and_b32 s45, s45, 65535\n\t"
"s_mov_b32 s46, 32768\n\t"
"s_mov_b32 s47, 131072\n\t"
"s_add_u32 s48, s20, s78\n\t"
"s_addc_u32 s49, s21, 0\n\t"
"s_and_b32 s49, s49, 65535\n\t"
"s_mov_b32 s50, 32768\n\t"
"s_mov_b32 s51, 131072\n\t"
"s_mul_i32 s78, s67, 1\n\t"
"s_mul_i32 s79, s66, 2\n\t"
"s_add_u32 s78, s78, s79\n\t"
"s_mul_i32 s79, s69, 0\n\t"
"s_add_u32 s78, s78, s79\n\t"
"s_and_b32 s78, s78, 7\n\t"
"s_lshl_b32 s75, s78, 12\n\t"
"s_mov_b32 s76, s75\n\t"
"v_lshrrev_b32 v2, 4, v1\n\t"
"v_and_b32 v3, 15, v1\n\t"
"v_xor_b32 v4, v2, v3\n\t"
"v_lshlrev_b32 v4, 4, v4\n\t"
"v_lshl_add_u32 v177, v2, 8, v4\n\t"
"v_xor_b32 v178, 64, v177\n\t"
"v_add_u32 v178, 0, v178\n\t"
"v_xor_b32 v179, 128, v177\n\t"
"v_add_u32 v179, 0, v179\n\t"
"v_xor_b32 v180, 192, v177\n\t"
"v_add_u32 v180, 0, v180\n\t"
"v_lshlrev_b32 v4, 1, v2\n\t"
"v_xor_b32 v4, v4, v3\n\t"
"v_lshlrev_b32 v4, 4, v4\n\t"
"v_lshl_add_u32 v244, v2, 8, v4\n\t"
"v_xor_b32 v245, 128, v244\n\t"
"v_add_u32 v14, 0, v244\n\t"
"v_add_u32 v251, 0, v245\n\t"
"v_add_u32 v245, 0, v245\n\t"
"v_lshl_add_u32 v10, v2, 11, v4\n\t"
"v_xor_b32 v5, 128, v10\n\t"
"v_add_u32 v11, 7168, v5\n\t"
"v_add_u32 v12, 14336, v10\n\t"
"v_add_u32 v13, 21504, v5\n\t"
"v_lshrrev_b32 v4, 1, v3\n\t"
"v_xor_b32 v4, v4, v2\n\t"
"v_lshlrev_b32 v4, 4, v4\n\t"
"v_lshl_add_u32 v4, v3, 7, v4\n\t"
"s_mul_i32 s78, s70, 81920\n\t"
"v_add_u32 v4, s78, v4\n\t"
"v_cmp_lt_u32 vcc, 7, v3\n\t"
"v_mov_b32 v5, 0\n\t"
"v_mov_b32 v254, 35840\n\t"
"v_cndmask_b32 v5, v5, v254, vcc\n\t"
"v_add_u32 v181, 0, v4\n\t"
"v_xor_b32 v182, 64, v181\n\t"
"v_add_u32 v183, 2048, v4\n\t"
"v_xor_b32 v184, 64, v183\n\t"
"v_add_u32 v185, 4096, v4\n\t"
"v_add_u32 v185, v185, v5\n\t"
"v_xor_b32 v186, 64, v185\n\t"
"v_add_u32 v187, 41984, v4\n\t"
"v_xor_b32 v188, 64, v187\n\t"
"v_add_u32 v189, 44032, v4\n\t"
"v_xor_b32 v190, 64, v189\n\t"
"v_bfe_u32 v3, v1, 2, 2\n\t"
"v_and_b32 v4, 1, v2\n\t"
"v_lshl_add_u32 v5, v4, 3, v3\n\t"
"v_lshlrev_b32 v254, 8, v5\n\t"
"v_lshlrev_b32 v4, 2, v4\n\t"
"v_add_u32 v4, v4, v3\n\t"
"v_lshlrev_b32 v4, 5, v4\n\t"
"v_bfe_u32 v5, v1, 1, 1\n\t"
"v_lshl_add_u32 v4, v5, 4, v4\n\t"
"v_and_b32 v5, 1, v1\n\t"
"v_lshl_add_u32 v4, v5, 3, v4\n\t"
"v_add_u32 v254, v254, v4\n\t"
"v_lshrrev_b32 v5, 1, v2\n\t"
"v_mul_u32_u24 v5, 40960, v5\n\t"
"s_lshl_b32 s78, s71, 7\n\t"
"v_xor_b32 v254, s78, v254\n\t"
"v_add_u32 v254, v254, v5\n\t"
"v_xor_b32 v191, 0, v254\n\t"
"v_add_u32 v192, 81920, v191\n\t"
"v_xor_b32 v193, 32, v254\n\t"
"v_add_u32 v194, 81920, v193\n\t"
"v_xor_b32 v195, 64, v254\n\t"
"v_add_u32 v196, 81920, v195\n\t"
"v_xor_b32 v197, 96, v254\n\t"
"v_add_u32 v198, 81920, v197\n\t"
"v_lshl_add_u32 v5, v2, 2, v3\n\t"
"v_lshl_add_u32 v5, v5, 8, v4\n\t"
"s_lshl_b32 s78, s69, 6\n\t"
"v_xor_b32 v5, s78, v5\n\t"
"v_add_u32 v5, s72, v5\n\t"
"v_mov_b32 v236, v5\n\t"
"v_xor_b32 v237, 32, v5\n\t"
"s_xor_b32 s78, s69, 1\n\t"
"s_sub_i32 s78, s78, s69\n\t"
"s_mul_i32 s78, s78, 40960\n\t"
"v_add_u32 v238, s78, v236\n\t"
"v_add_u32 v239, s78, v237\n\t"
"s_xor_b32 s78, s69, 2\n\t"
"s_sub_i32 s78, s78, s69\n\t"
"s_mul_i32 s78, s78, 40960\n\t"
"v_add_u32 v240, s78, v236\n\t"
"v_add_u32 v241, s78, v237\n\t"
"s_xor_b32 s78, s69, 3\n\t"
"s_sub_i32 s78, s78, s69\n\t"
"s_mul_i32 s78, s78, 40960\n\t"
"v_add_u32 v242, s78, v236\n\t"
"v_add_u32 v243, s78, v237\n\t"
"v_and_b32 v3, 15, v1\n\t"
"v_xor_b32 v4, v2, v3\n\t"
"v_lshlrev_b32 v4, 4, v4\n\t"
"v_lshl_add_u32 v4, v3, 8, v4\n\t"
"v_add_u32 v4, s72, v4\n\t"
"v_mov_b32 v255, v4\n\t"
"v_lshlrev_b32 v250, 4, v1\n\t"
"s_mul_i32 s78, s71, 81920\n\t"
"v_lshrrev_b32 v4, 1, v2\n\t"
"v_lshlrev_b32 v4, 8, v4\n\t"
"v_lshl_add_u32 v4, v3, 4, v4\n\t"
"v_and_b32 v5, 1, v2\n\t"
"v_lshl_add_u32 v4, v5, 3, v4\n\t"
"v_add_u32 v4, s78, v4\n\t"
"s_mov_b32 s78, 217752\n\t"
"s_mov_b32 s79, 67398\n\t"
"s_cmp_eq_u32 s65, 1\n\t"
"s_cselect_b32 s78, 170100, s78\n\t"
"s_cselect_b32 s79, 67125, s79\n\t"
"s_cmp_eq_u32 s65, 2\n\t"
"s_cselect_b32 s78, 554083, s78\n\t"
"s_cselect_b32 s79, 66857, s79\n\t"
"s_cmp_eq_u32 s65, 3\n\t"
"s_cselect_b32 s78, 287570, s78\n\t"
"s_cselect_b32 s79, 67975, s79\n\t"
"s_cmp_eq_u32 s70, 0\n\t"
"s_cselect_b32 s78, s78, s79\n\t"
"s_bfe_u32 s79, s78, 262144\n\t"
"s_lshl_b32 s79, s79, 10\n\t"
"v_add_u32 v203, s79, v4\n\t"
"s_bfe_u32 s79, s78, 262148\n\t"
"s_lshl_b32 s79, s79, 10\n\t"
"v_add_u32 v204, s79, v4\n\t"
"s_bfe_u32 s79, s78, 262152\n\t"
"s_lshl_b32 s79, s79, 10\n\t"
"v_add_u32 v205, s79, v4\n\t"
"s_bfe_u32 s79, s78, 262156\n\t"
"s_lshl_b32 s79, s79, 10\n\t"
"v_add_u32 v206, s79, v4\n\t"
"s_bfe_u32 s79, s78, 262160\n\t"
"s_lshl_b32 s79, s79, 10\n\t"
"v_add_u32 v207, s79, v4\n\t"
"v_mov_b32 v246, v250\n\t"
"v_add_u32 v247, 81920, v250\n\t"
"v_lshlrev_b32 v249, 4, v3\n\t"
"v_add_u32 v249, 10240, v249\n\t"
"s_lshl_b32 s78, s69, 2\n\t"
"v_add_u32 v248, s78, v249\n\t"
"s_mov_b32 s84, 1040356334\n\t"
"v_mov_b32 v208, 1040356334\n\t"
"v_mov_b32 v209, 1040356334\n\t"
"s_mov_b32 s52, s24\n\t"
"s_and_b32 s53, s25, 65535\n\t"
"s_mov_b32 s54, 8388608\n\t"
"s_mov_b32 s55, 131072\n\t"
"s_mov_b32 s56, s28\n\t"
"s_and_b32 s57, s29, 65535\n\t"
"s_mov_b32 s58, 44040192\n\t"
"s_mov_b32 s59, 131072\n\t"
"s_mov_b32 s60, s26\n\t"
"s_and_b32 s61, s27, 65535\n\t"
"s_mov_b32 s62, 2621440\n\t"
"s_mov_b32 s63, 131072\n\t"
"s_add_u32 s78, s72, 16384\n\t"
"v_add_u32 v9, s78, v250\n\t"
"s_xor_b32 s78, s69, 1\n\t"
"s_mul_i32 s78, s78, 40960\n\t"
"s_add_u32 s78, s78, 16384\n\t"
"v_add_u32 v6, s78, v250\n\t"
"s_xor_b32 s78, s69, 2\n\t"
"s_mul_i32 s78, s78, 40960\n\t"
"s_add_u32 s78, s78, 16384\n\t"
"v_add_u32 v7, s78, v250\n\t"
"s_xor_b32 s78, s69, 3\n\t"
"s_mul_i32 s78, s78, 40960\n\t"
"s_add_u32 s78, s78, 16384\n\t"
"v_add_u32 v8, s78, v250\n\t"
"s_lshl_b32 s78, s65, 21\n\t"
"s_lshl_b32 s79, s69, 16\n\t"
"s_add_u32 s78, s78, s79\n\t"
"s_lshl_b32 s79, s85, 8\n\t"
"s_add_u32 s78, s78, s79\n\t"
"s_add_u32 s52, s52, s78\n\t"
"s_addc_u32 s53, s53, 0\n\t"
"s_sub_u32 s54, s54, s78\n\t"
"s_mul_i32 s26, s66, 2\n\t"
"s_and_b32 s26, s26, 6\n\t"
"s_lshl_b32 s77, s26, 18\n\t"
"s_lshl_b32 s78, s66, 2\n\t"
"s_add_u32 s78, s78, s65\n\t"
"s_mul_i32 s94, s78, 327680\n\t"
"s_lshl_b32 s79, s69, 13\n\t"
"s_add_u32 s94, s94, s79\n\t"
"s_lshl_b32 s80, s67, 10\n\t"
"s_add_u32 s98, s94, s80\n\t"
"s_lshl_b32 s78, s78, 2\n\t"
"s_add_u32 s78, s78, s69\n\t"
"s_lshl_b32 s78, s78, 10\n\t"
"s_add_u32 s16, s78, 41943040\n\t"
"s_lshl_b32 s80, s67, 7\n\t"
"s_add_u32 s17, s16, s80\n\t"
"s_lshl_b32 s78, s66, 3\n\t"
"s_add_u32 s78, s78, s85\n\t"
"s_mul_i32 s95, s78, 327680\n\t"
"s_add_u32 s95, s95, s79\n\t"
"s_add_u32 s95, s95, 16777216\n\t"
"v_and_b32 v15, 7, v1\n\t"
"v_lshlrev_b32 v15, 7, v15\n\t"
"s_lshl_b32 s99, s66, 8\n\t"
"s_add_u32 s99, s99, 42991616\n\t"
"v_mov_b32 v2, 0\n\t"
"buffer_load_dword v254, v2, s[56:59], s99 offen sc1\n\t"
"v_xor_b32 v2, 0, v255\n\t"
"v_xor_b32 v3, 64, v255\n\t"
"v_xor_b32 v4, 128, v255\n\t"
"v_xor_b32 v5, 192, v255\n\t"
"s_waitcnt vmcnt(19)\n\t"
"s_barrier\n\t"
"ds_read_b64_tr_b16 v[96:97], v191 offset:5120\n\t"
"ds_read_b64_tr_b16 v[98:99], v191 offset:6144\n\t"
"ds_read_b128 v[112:115], v181 offset:0\n\t"
"ds_read_b128 v[116:119], v183 offset:0\n\t"
"ds_read_b128 v[120:123], v185 offset:0\n\t"
"ds_read_b128 v[124:127], v187 offset:0\n\t"
"ds_read_b128 v[128:131], v189 offset:0\n\t"
"ds_read_b64_tr_b16 v[100:101], v193 offset:5120\n\t"
"ds_read_b64_tr_b16 v[102:103], v193 offset:6144\n\t"
"ds_read_b64_tr_b16 v[104:105], v195 offset:5120\n\t"
"ds_read_b64_tr_b16 v[106:107], v195 offset:6144\n\t"
"ds_read_b64_tr_b16 v[108:109], v197 offset:5120\n\t"
"ds_read_b64_tr_b16 v[110:111], v197 offset:6144\n\t"
"s_waitcnt lgkmcnt(10)\n\t"
"v_mfma_f32_16x16x32_bf16 v[16:19], v[96:99], v[112:115], 0\n\t"
"s_waitcnt lgkmcnt(9)\n\t"
"ds_read_b64_tr_b16 v[132:133], v192 offset:5120\n\t"
"v_mfma_f32_16x16x32_bf16 v[20:23], v[96:99], v[116:119], 0\n\t"
"s_waitcnt lgkmcnt(9)\n\t"
"ds_read_b64_tr_b16 v[134:135], v192 offset:6144\n\t"
"v_mfma_f32_16x16x32_bf16 v[24:27], v[96:99], v[120:123], 0\n\t"
"s_waitcnt lgkmcnt(9)\n\t"
"ds_read_b128 v[148:151], v182 offset:0\n\t"
"v_mfma_f32_16x16x32_bf16 v[28:31], v[96:99], v[124:127], 0\n\t"
"s_waitcnt lgkmcnt(9)\n\t"
"ds_read_b128 v[152:155], v184 offset:0\n\t"
"v_mfma_f32_16x16x32_bf16 v[32:35], v[96:99], v[128:131], 0\n\t"
"s_waitcnt lgkmcnt(8)\n\t"
"ds_read_b128 v[156:159], v186 offset:0\n\t"
"v_mfma_f32_16x16x32_bf16 v[52:55], v[100:103], v[128:131], 0\n\t"
"ds_read_b128 v[160:163], v188 offset:0\n\t"
"v_mfma_f32_16x16x32_bf16 v[48:51], v[100:103], v[124:127], 0\n\t"
"ds_read_b128 v[164:167], v190 offset:0\n\t"
"v_mfma_f32_16x16x32_bf16 v[44:47], v[100:103], v[120:123], 0\n\t"
"ds_read_b64_tr_b16 v[136:137], v194 offset:5120\n\t"
"v_mfma_f32_16x16x32_bf16 v[40:43], v[100:103], v[116:119], 0\n\t"
"ds_read_b64_tr_b16 v[138:139], v194 offset:6144\n\t"
"v_mfma_f32_16x16x32_bf16 v[36:39], v[100:103], v[112:115], 0\n\t"
"s_waitcnt lgkmcnt(11)\n\t"
"ds_read_b64_tr_b16 v[140:141], v196 offset:5120\n\t"
"v_mfma_f32_16x16x32_bf16 v[56:59], v[104:107], v[112:115], 0\n\t"
"ds_read_b64_tr_b16 v[142:143], v196 offset:6144\n\t"
"v_mfma_f32_16x16x32_bf16 v[60:63], v[104:107], v[116:119], 0\n\t"
"ds_read_b64_tr_b16 v[144:145], v198 offset:5120\n\t"
"v_mfma_f32_16x16x32_bf16 v[64:67], v[104:107], v[120:123], 0\n\t"
"ds_read_b64_tr_b16 v[146:147], v198 offset:6144\n\t"
"v_mfma_f32_16x16x32_bf16 v[68:71], v[104:107], v[124:127], 0\n\t"
"v_mfma_f32_16x16x32_bf16 v[72:75], v[104:107], v[128:131], 0\n\t"
"s_waitcnt lgkmcnt(13)\n\t"
"v_mfma_f32_16x16x32_bf16 v[92:95], v[108:111], v[128:131], 0\n\t"
"v_mfma_f32_16x16x32_bf16 v[88:91], v[108:111], v[124:127], 0\n\t"
"v_mfma_f32_16x16x32_bf16 v[84:87], v[108:111], v[120:123], 0\n\t"
"v_mfma_f32_16x16x32_bf16 v[80:83], v[108:111], v[116:119], 0\n\t"
"s_add_u32 m0, s72, 0\n\t"
"v_mfma_f32_16x16x32_bf16 v[76:79], v[108:111], v[112:115], 0\n\t"
"s_waitcnt vmcnt(10) lgkmcnt(0)\n\t"
"s_barrier\n\t"
"buffer_load_dwordx4 v168, s[36:39], s73 offen lds\n\t"
"v_mfma_f32_16x16x32_bf16 v[16:19], v[132:135], v[148:151], v[16:19]\n\t"
"ds_read_b64_tr_b16 v[96:97], v191 offset:15360\n\t"
"v_mfma_f32_16x16x32_bf16 v[20:23], v[132:135], v[152:155], v[20:23]\n\t"
"buffer_load_dwordx4 v169, s[36:39], s73 offen offset:1024 lds\n\t"
"v_mfma_f32_16x16x32_bf16 v[24:27], v[132:135], v[156:159], v[24:27]\n\t"
"ds_read_b64_tr_b16 v[98:99], v191 offset:16384\n\t"
"v_mfma_f32_16x16x32_bf16 v[28:31], v[132:135], v[160:163], v[28:31]\n\t"
"buffer_load_dwordx4 v170, s[36:39], s73 offen offset:2048 lds\n\t"
"v_mfma_f32_16x16x32_bf16 v[32:35], v[132:135], v[164:167], v[32:35]\n\t"
"ds_read_b128 v[112:115], v181 offset:10240\n\t"
"v_mfma_f32_16x16x32_bf16 v[52:55], v[136:139], v[164:167], v[52:55]\n\t"
"buffer_load_dwordx4 v171, s[36:39], s73 offen offset:3072 lds\n\t"
"v_mfma_f32_16x16x32_bf16 v[48:51], v[136:139], v[160:163], v[48:51]\n\t"
"s_add_u32 m0, s72, 4096\n\t"
"ds_read_b128 v[116:119], v183 offset:10240\n\t"
"v_mfma_f32_16x16x32_bf16 v[44:47], v[136:139], v[156:159], v[44:47]\n\t"
"buffer_load_dwordx4 v172, s[36:39], s73 offen lds\n\t"
"v_mfma_f32_16x16x32_bf16 v[40:43], v[136:139], v[152:155], v[40:43]\n\t"
"ds_read_b128 v[120:123], v185 offset:10240\n\t"
"v_mfma_f32_16x16x32_bf16 v[36:39], v[136:139], v[148:151], v[36:39]\n\t"
"buffer_load_dwordx4 v173, s[40:43], s74 offen offset:1024 lds\n\t"
"v_mfma_f32_16x16x32_bf16 v[56:59], v[140:143], v[148:151], v[56:59]\n\t"
"ds_read_b128 v[124:127], v187 offset:10240\n\t"
"v_mfma_f32_16x16x32_bf16 v[60:63], v[140:143], v[152:155], v[60:63]\n\t"
"buffer_load_dwordx4 v174, s[40:43], s74 offen offset:2048 lds\n\t"
"v_mfma_f32_16x16x32_bf16 v[64:67], v[140:143], v[156:159], v[64:67]\n\t"
"ds_read_b128 v[128:131], v189 offset:10240\n\t"
"v_mfma_f32_16x16x32_bf16 v[68:71], v[140:143], v[160:163], v[68:71]\n\t"
"buffer_load_dwordx4 v175, s[40:43], s74 offen offset:3072 lds\n\t"
"s_add_u32 m0, s72, 8192\n\t"
"s_nop 0\n\t"
"buffer_load_dwordx4 v176, s[40:43], s74 offen lds\n\t"
"v_mfma_f32_16x16x32_bf16 v[72:75], v[140:143], v[164:167], v[72:75]\n\t"
"ds_read_b64_tr_b16 v[100:101], v193 offset:15360\n\t"
"s_add_u32 s86, s86, 4\n\t"
"s_and_b32 s86, s86, 31\n\t"
"v_mfma_f32_16x16x32_bf16 v[92:95], v[144:147], v[164:167], v[92:95]\n\t"
"ds_read_b64_tr_b16 v[102:103], v193 offset:16384\n\t"
"s_lshr_b32 s87, s14, s86\n\t"
"s_and_b32 s87, s87, 15\n\t"
"v_mfma_f32_16x16x32_bf16 v[88:91], v[144:147], v[160:163], v[88:91]\n\t"
"ds_read_b64_tr_b16 v[104:105], v195 offset:15360\n\t"
"s_lshl_b32 s73, s87, 7\n\t"
"s_lshl_b32 s74, s87, 19\n\t"
"v_mfma_f32_16x16x32_bf16 v[84:87], v[144:147], v[156:159], v[84:87]\n\t"
"ds_read_b64_tr_b16 v[106:107], v195 offset:16384\n\t"
"v_mfma_f32_16x16x32_bf16 v[80:83], v[144:147], v[152:155], v[80:83]\n\t"
"ds_read_b64_tr_b16 v[108:109], v197 offset:15360\n\t"
"v_mfma_f32_16x16x32_bf16 v[76:79], v[144:147], v[148:151], v[76:79]\n\t"
"s_waitcnt lgkmcnt(9)\n\t"
"ds_read_b64_tr_b16 v[110:111], v197 offset:16384\n\t"
"v_mfma_f32_16x16x32_bf16 v[16:19], v[96:99], v[112:115], v[16:19]\n\t"
"s_waitcnt lgkmcnt(9)\n\t"
"ds_read_b64_tr_b16 v[132:133], v192 offset:15360\n\t"
"v_mfma_f32_16x16x32_bf16 v[20:23], v[96:99], v[116:119], v[20:23]\n\t"
"s_waitcnt lgkmcnt(9)\n\t"
"ds_read_b64_tr_b16 v[134:135], v192 offset:16384\n\t"
"v_mfma_f32_16x16x32_bf16 v[24:27], v[96:99], v[120:123], v[24:27]\n\t"
"s_waitcnt lgkmcnt(9)\n\t"
"ds_read_b128 v[148:151], v182 offset:10240\n\t"
"v_mfma_f32_16x16x32_bf16 v[28:31], v[96:99], v[124:127], v[28:31]\n\t"
"s_waitcnt lgkmcnt(9)\n\t"
"ds_read_b128 v[152:155], v184 offset:10240\n\t"
"v_mfma_f32_16x16x32_bf16 v[32:35], v[96:99], v[128:131], v[32:35]\n\t"
"s_waitcnt lgkmcnt(8)\n\t"
"ds_read_b128 v[156:159], v186 offset:10240\n\t"
"v_mfma_f32_16x16x32_bf16 v[52:55], v[100:103], v[128:131], v[52:55]\n\t"
"ds_read_b128 v[160:163], v188 offset:10240\n\t"
"v_mfma_f32_16x16x32_bf16 v[48:51], v[100:103], v[124:127], v[48:51]\n\t"
"ds_read_b128 v[164:167], v190 offset:10240\n\t"
"v_mfma_f32_16x16x32_bf16 v[44:47], v[100:103], v[120:123], v[44:47]\n\t"
"ds_read_b64_tr_b16 v[136:137], v194 offset:15360\n\t"
"v_mfma_f32_16x16x32_bf16 v[40:43], v[100:103], v[116:119], v[40:43]\n\t"
"ds_read_b64_tr_b16 v[138:139], v194 offset:16384\n\t"
"v_mfma_f32_16x16x32_bf16 v[36:39], v[100:103], v[112:115], v[36:39]\n\t"
"s_waitcnt lgkmcnt(11)\n\t"
"ds_read_b64_tr_b16 v[140:141], v196 offset:15360\n\t"
"v_mfma_f32_16x16x32_bf16 v[56:59], v[104:107], v[112:115], v[56:59]\n\t"
"ds_read_b64_tr_b16 v[142:143], v196 offset:16384\n\t"
"v_mfma_f32_16x16x32_bf16 v[60:63], v[104:107], v[116:119], v[60:63]\n\t"
"ds_read_b64_tr_b16 v[144:145], v198 offset:15360\n\t"
"v_mfma_f32_16x16x32_bf16 v[64:67], v[104:107], v[120:123], v[64:67]\n\t"
"ds_read_b64_tr_b16 v[146:147], v198 offset:16384\n\t"
"v_mfma_f32_16x16x32_bf16 v[68:71], v[104:107], v[124:127], v[68:71]\n\t"
"v_mfma_f32_16x16x32_bf16 v[72:75], v[104:107], v[128:131], v[72:75]\n\t"
"s_waitcnt lgkmcnt(13)\n\t"
"v_mfma_f32_16x16x32_bf16 v[92:95], v[108:111], v[128:131], v[92:95]\n\t"
"v_mfma_f32_16x16x32_bf16 v[88:91], v[108:111], v[124:127], v[88:91]\n\t"
"v_mfma_f32_16x16x32_bf16 v[84:87], v[108:111], v[120:123], v[84:87]\n\t"
"v_mfma_f32_16x16x32_bf16 v[80:83], v[108:111], v[116:119], v[80:83]\n\t"
"s_add_u32 m0, s72, 10240\n\t"
"v_mfma_f32_16x16x32_bf16 v[76:79], v[108:111], v[112:115], v[76:79]\n\t"
"s_waitcnt vmcnt(10) lgkmcnt(0)\n\t"
"s_barrier\n\t"
"buffer_load_dwordx4 v168, s[36:39], s73 offen lds\n\t"
"v_mfma_f32_16x16x32_bf16 v[16:19], v[132:135], v[148:151], v[16:19]\n\t"
"ds_read_b64_tr_b16 v[96:97], v191 offset:24576\n\t"
"v_mfma_f32_16x16x32_bf16 v[20:23], v[132:135], v[152:155], v[20:23]\n\t"
"buffer_load_dwordx4 v169, s[36:39], s73 offen offset:1024 lds\n\t"
"v_mfma_f32_16x16x32_bf16 v[24:27], v[132:135], v[156:159], v[24:27]\n\t"
"ds_read_b64_tr_b16 v[98:99], v191 offset:25600\n\t"
"v_mfma_f32_16x16x32_bf16 v[28:31], v[132:135], v[160:163], v[28:31]\n\t"
"buffer_load_dwordx4 v170, s[36:39], s73 offen offset:2048 lds\n\t"
"v_mfma_f32_16x16x32_bf16 v[32:35], v[132:135], v[164:167], v[32:35]\n\t"
"ds_read_b128 v[112:115], v181 offset:19456\n\t"
"v_mfma_f32_16x16x32_bf16 v[52:55], v[136:139], v[164:167], v[52:55]\n\t"
"buffer_load_dwordx4 v171, s[36:39], s73 offen offset:3072 lds\n\t"
"v_mfma_f32_16x16x32_bf16 v[48:51], v[136:139], v[160:163], v[48:51]\n\t"
"s_add_u32 m0, s72, 14336\n\t"
"ds_read_b128 v[116:119], v183 offset:19456\n\t"
"v_mfma_f32_16x16x32_bf16 v[44:47], v[136:139], v[156:159], v[44:47]\n\t"
"buffer_load_dwordx4 v172, s[36:39], s73 offen lds\n\t"
"v_mfma_f32_16x16x32_bf16 v[40:43], v[136:139], v[152:155], v[40:43]\n\t"
"buffer_load_dwordx4 v173, s[40:43], s74 offen offset:1024 lds\n\t"
"buffer_load_dwordx4 v174, s[40:43], s74 offen offset:2048 lds\n\t"
"buffer_load_dwordx4 v175, s[40:43], s74 offen offset:3072 lds\n\t"
"s_add_u32 m0, s72, 18432\n\t"
"s_nop 0\n\t"
"buffer_load_dwordx4 v176, s[40:43], s74 offen lds\n\t"
"s_add_u32 m0, s72, 28672\n\t"
"ds_read_b128 v[120:123], v185 offset:19456\n\t"
"v_mfma_f32_16x16x32_bf16 v[36:39], v[136:139], v[148:151], v[36:39]\n\t"
"buffer_load_dwordx4 v177, s[44:47], s75 offen nt lds\n\t"
"v_mfma_f32_16x16x32_bf16 v[56:59], v[140:143], v[148:151], v[56:59]\n\t"
"ds_read_b128 v[124:127], v187 offset:19456\n\t"
"v_mfma_f32_16x16x32_bf16 v[60:63], v[140:143], v[152:155], v[60:63]\n\t"
"buffer_load_dwordx4 v178, s[44:47], s75 offen offset:1024 nt lds\n\t"
"v_mfma_f32_16x16x32_bf16 v[64:67], v[140:143], v[156:159], v[64:67]\n\t"
"ds_read_b128 v[128:131], v189 offset:19456\n\t"
"v_mfma_f32_16x16x32_bf16 v[68:71], v[140:143], v[160:163], v[68:71]\n\t"
"buffer_load_dwordx4 v179, s[44:47], s75 offen offset:2048 nt lds\n\t"
"v_mfma_f32_16x16x32_bf16 v[72:75], v[140:143], v[164:167], v[72:75]\n\t"
"ds_read_b64_tr_b16 v[100:101], v193 offset:24576\n\t"
"s_add_u32 s86, s86, 4\n\t"
"s_and_b32 s86, s86, 31\n\t"
"v_mfma_f32_16x16x32_bf16 v[92:95], v[144:147], v[164:167], v[92:95]\n\t"
"ds_read_b64_tr_b16 v[102:103], v193 offset:25600\n\t"
"s_lshr_b32 s87, s14, s86\n\t"
"s_and_b32 s87, s87, 15\n\t"
"v_mfma_f32_16x16x32_bf16 v[88:91], v[144:147], v[160:163], v[88:91]\n\t"
"ds_read_b64_tr_b16 v[104:105], v195 offset:24576\n\t"
"s_lshl_b32 s73, s87, 7\n\t"
"s_lshl_b32 s74, s87, 19\n\t"
"v_mfma_f32_16x16x32_bf16 v[84:87], v[144:147], v[156:159], v[84:87]\n\t"
"ds_read_b64_tr_b16 v[106:107], v195 offset:25600\n\t"
"v_mfma_f32_16x16x32_bf16 v[80:83], v[144:147], v[152:155], v[80:83]\n\t"
"ds_read_b64_tr_b16 v[108:109], v197 offset:24576\n\t"
"buffer_load_dwordx4 v180, s[44:47], s75 offen offset:3072 nt lds\n\t"
"v_mfma_f32_16x16x32_bf16 v[76:79], v[144:147], v[148:151], v[76:79]\n\t"
"s_waitcnt lgkmcnt(9)\n\t"
"ds_read_b64_tr_b16 v[110:111], v197 offset:25600\n\t"
"s_add_u32 s75, s75, 4096\n\t"
"s_and_b32 s75, s75, 32767\n\t"
"v_mfma_f32_16x16x32_bf16 v[16:19], v[96:99], v[112:115], v[16:19]\n\t"
"s_waitcnt lgkmcnt(9)\n\t"
"ds_read_b64_tr_b16 v[132:133], v192 offset:24576\n\t"
"v_mfma_f32_16x16x32_bf16 v[20:23], v[96:99], v[116:119], v[20:23]\n\t"
"s_waitcnt lgkmcnt(9)\n\t"
"ds_read_b64_tr_b16 v[134:135], v192 offset:25600\n\t"
"v_mfma_f32_16x16x32_bf16 v[24:27], v[96:99], v[120:123], v[24:27]\n\t"
"s_waitcnt lgkmcnt(9)\n\t"
"ds_read_b128 v[148:151], v182 offset:19456\n\t"
"v_mfma_f32_16x16x32_bf16 v[28:31], v[96:99], v[124:127], v[28:31]\n\t"
"s_waitcnt lgkmcnt(9)\n\t"
"ds_read_b128 v[152:155], v184 offset:19456\n\t"
"v_mfma_f32_16x16x32_bf16 v[32:35], v[96:99], v[128:131], v[32:35]\n\t"
"s_waitcnt lgkmcnt(8)\n\t"
"ds_read_b128 v[156:159], v186 offset:19456\n\t"
"v_mfma_f32_16x16x32_bf16 v[52:55], v[100:103], v[128:131], v[52:55]\n\t"
"ds_read_b128 v[160:163], v188 offset:19456\n\t"
"v_mfma_f32_16x16x32_bf16 v[48:51], v[100:103], v[124:127], v[48:51]\n\t"
"ds_read_b128 v[164:167], v190 offset:19456\n\t"
"v_mfma_f32_16x16x32_bf16 v[44:47], v[100:103], v[120:123], v[44:47]\n\t"
"ds_read_b64_tr_b16 v[136:137], v194 offset:24576\n\t"
"v_mfma_f32_16x16x32_bf16 v[40:43], v[100:103], v[116:119], v[40:43]\n\t"
"ds_read_b64_tr_b16 v[138:139], v194 offset:25600\n\t"
"v_mfma_f32_16x16x32_bf16 v[36:39], v[100:103], v[112:115], v[36:39]\n\t"
"s_waitcnt lgkmcnt(11)\n\t"
"ds_read_b64_tr_b16 v[140:141], v196 offset:24576\n\t"
"v_mfma_f32_16x16x32_bf16 v[56:59], v[104:107], v[112:115], v[56:59]\n\t"
"ds_read_b64_tr_b16 v[142:143], v196 offset:25600\n\t"
"v_mfma_f32_16x16x32_bf16 v[60:63], v[104:107], v[116:119], v[60:63]\n\t"
"ds_read_b64_tr_b16 v[144:145], v198 offset:24576\n\t"
"v_mfma_f32_16x16x32_bf16 v[64:67], v[104:107], v[120:123], v[64:67]\n\t"
"ds_read_b64_tr_b16 v[146:147], v198 offset:25600\n\t"
"v_mfma_f32_16x16x32_bf16 v[68:71], v[104:107], v[124:127], v[68:71]\n\t"
"v_mfma_f32_16x16x32_bf16 v[72:75], v[104:107], v[128:131], v[72:75]\n\t"
"s_waitcnt lgkmcnt(13)\n\t"
"v_mfma_f32_16x16x32_bf16 v[92:95], v[108:111], v[128:131], v[92:95]\n\t"
"s_add_u32 m0, s72, 32768\n\t"
"v_mfma_f32_16x16x32_bf16 v[88:91], v[108:111], v[124:127], v[88:91]\n\t"
"buffer_load_dwordx4 v177, s[44:47], s75 offen nt lds\n\t"
"v_mfma_f32_16x16x32_bf16 v[84:87], v[108:111], v[120:123], v[84:87]\n\t"
"v_mfma_f32_16x16x32_bf16 v[80:83], v[108:111], v[116:119], v[80:83]\n\t"
"buffer_load_dwordx4 v178, s[44:47], s75 offen offset:1024 nt lds\n\t"
"buffer_load_dwordx4 v179, s[44:47], s75 offen offset:2048 nt lds\n\t"
"buffer_load_dwordx4 v180, s[44:47], s75 offen offset:3072 nt lds\n\t"
"s_add_u32 m0, s72, 19456\n\t"
"v_mfma_f32_16x16x32_bf16 v[76:79], v[108:111], v[112:115], v[76:79]\n\t"
"s_waitcnt vmcnt(17) lgkmcnt(0)\n\t"
"s_barrier\n\t"
"buffer_load_dwordx4 v168, s[36:39], s73 offen lds\n\t"
"v_mfma_f32_16x16x32_bf16 v[16:19], v[132:135], v[148:151], v[16:19]\n\t"
"ds_read_b64_tr_b16 v[96:97], v191 offset:5120\n\t"
"v_mfma_f32_16x16x32_bf16 v[20:23], v[132:135], v[152:155], v[20:23]\n\t"
"buffer_load_dwordx4 v169, s[36:39], s73 offen offset:1024 lds\n\t"
"v_mfma_f32_16x16x32_bf16 v[24:27], v[132:135], v[156:159], v[24:27]\n\t"
"ds_read_b64_tr_b16 v[98:99], v191 offset:6144\n\t"
"v_mfma_f32_16x16x32_bf16 v[28:31], v[132:135], v[160:163], v[28:31]\n\t"
"buffer_load_dwordx4 v170, s[36:39], s73 offen offset:2048 lds\n\t"
"v_mfma_f32_16x16x32_bf16 v[32:35], v[132:135], v[164:167], v[32:35]\n\t"
"ds_read_b128 v[112:115], v181 offset:0\n\t"
"v_mfma_f32_16x16x32_bf16 v[52:55], v[136:139], v[164:167], v[52:55]\n\t"
"buffer_load_dwordx4 v171, s[36:39], s73 offen offset:3072 lds\n\t"
"v_mfma_f32_16x16x32_bf16 v[48:51], v[136:139], v[160:163], v[48:51]\n\t"
"s_add_u32 m0, s72, 23552\n\t"
"ds_read_b128 v[116:119], v183 offset:0\n\t"
"s_add_u32 s75, s75, 4096\n\t"
"v_mfma_f32_16x16x32_bf16 v[44:47], v[136:139], v[156:159], v[44:47]\n\t"
"buffer_load_dwordx4 v172, s[36:39], s73 offen lds\n\t"
"v_mfma_f32_16x16x32_bf16 v[40:43], v[136:139], v[152:155], v[40:43]\n\t"
"ds_read_b128 v[120:123], v185 offset:0\n\t"
"s_and_b32 s75, s75, 32767\n\t"
"v_mfma_f32_16x16x32_bf16 v[36:39], v[136:139], v[148:151], v[36:39]\n\t"
"buffer_load_dwordx4 v173, s[40:43], s74 offen offset:1024 lds\n\t"
"v_mfma_f32_16x16x32_bf16 v[56:59], v[140:143], v[148:151], v[56:59]\n\t"
"ds_read_b128 v[124:127], v187 offset:0\n\t"
"v_mfma_f32_16x16x32_bf16 v[60:63], v[140:143], v[152:155], v[60:63]\n\t"
"buffer_load_dwordx4 v174, s[40:43], s74 offen offset:2048 lds\n\t"
"v_mfma_f32_16x16x32_bf16 v[64:67], v[140:143], v[156:159], v[64:67]\n\t"
"ds_read_b128 v[128:131], v189 offset:0\n\t"
"v_mfma_f32_16x16x32_bf16 v[68:71], v[140:143], v[160:163], v[68:71]\n\t"
"buffer_load_dwordx4 v175, s[40:43], s74 offen offset:3072 lds\n\t"
"s_add_u32 m0, s72, 27648\n\t"
"s_nop 0\n\t"
"buffer_load_dwordx4 v176, s[40:43], s74 offen lds\n\t"
"v_mfma_f32_16x16x32_bf16 v[72:75], v[140:143], v[164:167], v[72:75]\n\t"
"ds_read_b64_tr_b16 v[100:101], v193 offset:5120\n\t"
"s_add_u32 s86, s86, 4\n\t"
"s_and_b32 s86, s86, 31\n\t"
"v_mfma_f32_16x16x32_bf16 v[92:95], v[144:147], v[164:167], v[92:95]\n\t"
"ds_read_b64_tr_b16 v[102:103], v193 offset:6144\n\t"
"s_lshr_b32 s87, s14, s86\n\t"
"s_and_b32 s87, s87, 15\n\t"
"v_mfma_f32_16x16x32_bf16 v[88:91], v[144:147], v[160:163], v[88:91]\n\t"
"ds_read_b64_tr_b16 v[104:105], v195 offset:5120\n\t"
"s_lshl_b32 s73, s87, 7\n\t"
"s_lshl_b32 s74, s87, 19\n\t"
"v_mfma_f32_16x16x32_bf16 v[84:87], v[144:147], v[156:159], v[84:87]\n\t"
"ds_read_b64_tr_b16 v[106:107], v195 offset:6144\n\t"
"v_mfma_f32_16x16x32_bf16 v[80:83], v[144:147], v[152:155], v[80:83]\n\t"
"ds_read_b64_tr_b16 v[108:109], v197 offset:5120\n\t"
"v_mfma_f32_16x16x32_bf16 v[76:79], v[144:147], v[148:151], v[76:79]\n\t"
"s_waitcnt lgkmcnt(9)\n\t"
"ds_read_b64_tr_b16 v[110:111], v197 offset:6144\n\t"
"v_mfma_f32_16x16x32_bf16 v[16:19], v[96:99], v[112:115], v[16:19]\n\t"
"s_waitcnt lgkmcnt(9)\n\t"
"ds_read_b64_tr_b16 v[132:133], v192 offset:5120\n\t"
"v_mfma_f32_16x16x32_bf16 v[20:23], v[96:99], v[116:119], v[20:23]\n\t"
"s_waitcnt lgkmcnt(9)\n\t"
"ds_read_b64_tr_b16 v[134:135], v192 offset:6144\n\t"
"v_mfma_f32_16x16x32_bf16 v[24:27], v[96:99], v[120:123], v[24:27]\n\t"
"s_waitcnt lgkmcnt(9)\n\t"
"ds_read_b128 v[148:151], v182 offset:0\n\t"
"v_mfma_f32_16x16x32_bf16 v[28:31], v[96:99], v[124:127], v[28:31]\n\t"
"s_waitcnt lgkmcnt(9)\n\t"
"ds_read_b128 v[152:155], v184 offset:0\n\t"
"v_mfma_f32_16x16x32_bf16 v[32:35], v[96:99], v[128:131], v[32:35]\n\t"
"s_waitcnt lgkmcnt(8)\n\t"
"ds_read_b128 v[156:159], v186 offset:0\n\t"
"v_mfma_f32_16x16x32_bf16 v[52:55], v[100:103], v[128:131], v[52:55]\n\t"
"ds_read_b128 v[160:163], v188 offset:0\n\t"
"v_mfma_f32_16x16x32_bf16 v[48:51], v[100:103], v[124:127], v[48:51]\n\t"
"ds_read_b128 v[164:167], v190 offset:0\n\t"
"v_mfma_f32_16x16x32_bf16 v[44:47], v[100:103], v[120:123], v[44:47]\n\t"
"ds_read_b64_tr_b16 v[136:137], v194 offset:5120\n\t"
"s_add_u32 m0, s72, 36864\n\t"
"v_mfma_f32_16x16x32_bf16 v[40:43], v[100:103], v[116:119], v[40:43]\n\t"
"ds_read_b64_tr_b16 v[138:139], v194 offset:6144\n\t"
"v_mfma_f32_16x16x32_bf16 v[36:39], v[100:103], v[112:115], v[36:39]\n\t"
"s_waitcnt lgkmcnt(11)\n\t"
"ds_read_b64_tr_b16 v[140:141], v196 offset:5120\n\t"
"v_mfma_f32_16x16x32_bf16 v[56:59], v[104:107], v[112:115], v[56:59]\n\t"
"ds_read_b64_tr_b16 v[142:143], v196 offset:6144\n\t"
"v_mfma_f32_16x16x32_bf16 v[60:63], v[104:107], v[116:119], v[60:63]\n\t"
"ds_read_b64_tr_b16 v[144:145], v198 offset:5120\n\t"
"v_mfma_f32_16x16x32_bf16 v[64:67], v[104:107], v[120:123], v[64:67]\n\t"
"ds_read_b64_tr_b16 v[146:147], v198 offset:6144\n\t"
"v_mfma_f32_16x16x32_bf16 v[68:71], v[104:107], v[124:127], v[68:71]\n\t"
"buffer_load_dwordx4 v177, s[44:47], s75 offen nt lds\n\t"
"v_mfma_f32_16x16x32_bf16 v[72:75], v[104:107], v[128:131], v[72:75]\n\t"
"s_waitcnt lgkmcnt(13)\n\t"
"buffer_load_dwordx4 v178, s[44:47], s75 offen offset:1024 nt lds\n\t"
"v_mfma_f32_16x16x32_bf16 v[92:95], v[108:111], v[128:131], v[92:95]\n\t"
"buffer_load_dwordx4 v179, s[44:47], s75 offen offset:2048 nt lds\n\t"
"v_mfma_f32_16x16x32_bf16 v[88:91], v[108:111], v[124:127], v[88:91]\n\t"
"v_mfma_f32_16x16x32_bf16 v[84:87], v[108:111], v[120:123], v[84:87]\n\t"
"buffer_load_dwordx4 v180, s[44:47], s75 offen offset:3072 nt lds\n\t"
"v_mfma_f32_16x16x32_bf16 v[80:83], v[108:111], v[116:119], v[80:83]\n\t"
"s_add_u32 m0, s72, 0\n\t"
"s_add_u32 s75, s75, 4096\n\t"
"s_and_b32 s75, s75, 32767\n\t"
"v_mfma_f32_16x16x32_bf16 v[76:79], v[108:111], v[112:115], v[76:79]\n\t"
"s_waitcnt vmcnt(21) lgkmcnt(0)\n\t"
"s_barrier\n\t"
"buffer_load_dwordx4 v168, s[36:39], s73 offen lds\n\t"
"v_mfma_f32_16x16x32_bf16 v[16:19], v[132:135], v[148:151], v[16:19]\n\t"
"ds_read_b64_tr_b16 v[96:97], v191 offset:15360\n\t"
"v_mfma_f32_16x16x32_bf16 v[20:23], v[132:135], v[152:155], v[20:23]\n\t"
"buffer_load_dwordx4 v169, s[36:39], s73 offen offset:1024 lds\n\t"
"v_mfma_f32_16x16x32_bf16 v[24:27], v[132:135], v[156:159], v[24:27]\n\t"
"ds_read_b64_tr_b16 v[98:99], v191 offset:16384\n\t"
"v_mfma_f32_16x16x32_bf16 v[28:31], v[132:135], v[160:163], v[28:31]\n\t"
"buffer_load_dwordx4 v170, s[36:39], s73 offen offset:2048 lds\n\t"
"v_mfma_f32_16x16x32_bf16 v[32:35], v[132:135], v[164:167], v[32:35]\n\t"
"ds_read_b128 v[112:115], v181 offset:10240\n\t"
"v_mfma_f32_16x16x32_bf16 v[52:55], v[136:139], v[164:167], v[52:55]\n\t"
"buffer_load_dwordx4 v171, s[36:39], s73 offen offset:3072 lds\n\t"
"v_mfma_f32_16x16x32_bf16 v[48:51], v[136:139], v[160:163], v[48:51]\n\t"
"s_add_u32 m0, s72, 4096\n\t"
"ds_read_b128 v[116:119], v183 offset:10240\n\t"
"v_mfma_f32_16x16x32_bf16 v[44:47], v[136:139], v[156:159], v[44:47]\n\t"
"buffer_load_dwordx4 v172, s[36:39], s73 offen lds\n\t"
"v_mfma_f32_16x16x32_bf16 v[40:43], v[136:139], v[152:155], v[40:43]\n\t"
"ds_read_b128 v[120:123], v185 offset:10240\n\t"
"v_mfma_f32_16x16x32_bf16 v[36:39], v[136:139], v[148:151], v[36:39]\n\t"
"buffer_load_dwordx4 v173, s[40:43], s74 offen offset:1024 lds\n\t"
"v_mfma_f32_16x16x32_bf16 v[56:59], v[140:143], v[148:151], v[56:59]\n\t"
"ds_read_b128 v[124:127], v187 offset:10240\n\t"
"v_mfma_f32_16x16x32_bf16 v[60:63], v[140:143], v[152:155], v[60:63]\n\t"
"buffer_load_dwordx4 v174, s[40:43], s74 offen offset:2048 lds\n\t"
"v_mfma_f32_16x16x32_bf16 v[64:67], v[140:143], v[156:159], v[64:67]\n\t"
"ds_read_b128 v[128:131], v189 offset:10240\n\t"
"v_mfma_f32_16x16x32_bf16 v[68:71], v[140:143], v[160:163], v[68:71]\n\t"
"s_waitcnt vmcnt(24)\n\t"
"buffer_load_dwordx4 v175, s[40:43], s74 offen offset:3072 lds\n\t"
"s_add_u32 m0, s72, 8192\n\t"
"s_nop 0\n\t"
"buffer_load_dwordx4 v176, s[40:43], s74 offen lds\n\t"
"ds_read_b128 a[0:3], v2 offset:28672\n\t"
"v_mfma_f32_16x16x32_bf16 v[72:75], v[140:143], v[164:167], v[72:75]\n\t"
"ds_read_b64_tr_b16 v[100:101], v193 offset:15360\n\t"
"s_add_u32 s86, s86, 4\n\t"
"s_and_b32 s86, s86, 31\n\t"
"v_mfma_f32_16x16x32_bf16 v[92:95], v[144:147], v[164:167], v[92:95]\n\t"
"ds_read_b64_tr_b16 v[102:103], v193 offset:16384\n\t"
"ds_read_b128 a[4:7], v3 offset:28672\n\t"
"s_lshr_b32 s87, s14, s86\n\t"
"v_mfma_f32_16x16x32_bf16 v[88:91], v[144:147], v[160:163], v[88:91]\n\t"
"ds_read_b64_tr_b16 v[104:105], v195 offset:15360\n\t"
"s_and_b32 s87, s87, 15\n\t"
"s_lshl_b32 s73, s87, 7\n\t"
"v_mfma_f32_16x16x32_bf16 v[84:87], v[144:147], v[156:159], v[84:87]\n\t"
"ds_read_b64_tr_b16 v[106:107], v195 offset:16384\n\t"
"ds_read_b128 a[8:11], v4 offset:28672\n\t"
"s_lshl_b32 s74, s87, 19\n\t"
"v_mfma_f32_16x16x32_bf16 v[80:83], v[144:147], v[152:155], v[80:83]\n\t"
"ds_read_b64_tr_b16 v[108:109], v197 offset:15360\n\t"
"v_mfma_f32_16x16x32_bf16 v[76:79], v[144:147], v[148:151], v[76:79]\n\t"
"s_waitcnt lgkmcnt(12)\n\t"
"ds_read_b64_tr_b16 v[110:111], v197 offset:16384\n\t"
"ds_read_b128 a[12:15], v5 offset:28672\n\t"
"s_add_u32 m0, s72, 28672\n\t"
"v_mfma_f32_16x16x32_bf16 v[16:19], v[96:99], v[112:115], v[16:19]\n\t"
"s_waitcnt lgkmcnt(13)\n\t"
"ds_read_b64_tr_b16 v[132:133], v192 offset:15360\n\t"
"v_mfma_f32_16x16x32_bf16 v[20:23], v[96:99], v[116:119], v[20:23]\n\t"
"s_waitcnt lgkmcnt(13)\n\t"
"ds_read_b64_tr_b16 v[134:135], v192 offset:16384\n\t"
"v_mfma_f32_16x16x32_bf16 v[24:27], v[96:99], v[120:123], v[24:27]\n\t"
"s_waitcnt lgkmcnt(13)\n\t"
"ds_read_b128 v[148:151], v182 offset:10240\n\t"
"v_mfma_f32_16x16x32_bf16 v[28:31], v[96:99], v[124:127], v[28:31]\n\t"
"s_waitcnt lgkmcnt(13)\n\t"
"ds_read_b128 v[152:155], v184 offset:10240\n\t"
"v_mfma_f32_16x16x32_bf16 v[32:35], v[96:99], v[128:131], v[32:35]\n\t"
"s_waitcnt lgkmcnt(11)\n\t"
"ds_read_b128 v[156:159], v186 offset:10240\n\t"
"v_mfma_f32_16x16x32_bf16 v[52:55], v[100:103], v[128:131], v[52:55]\n\t"
"s_waitcnt lgkmcnt(5)\n\t"
"ds_read_b128 v[160:163], v188 offset:10240\n\t"
"buffer_load_dwordx4 v177, s[44:47], s75 offen nt lds\n\t"
"v_mfma_f32_16x16x32_bf16 v[48:51], v[100:103], v[124:127], v[48:51]\n\t"
"ds_read_b128 v[164:167], v190 offset:10240\n\t"
"v_mfma_f32_16x16x32_bf16 v[44:47], v[100:103], v[120:123], v[44:47]\n\t"
"ds_read_b64_tr_b16 v[136:137], v194 offset:15360\n\t"
"v_mfma_f32_16x16x32_bf16 v[40:43], v[100:103], v[116:119], v[40:43]\n\t"
"ds_read_b64_tr_b16 v[138:139], v194 offset:16384\n\t"
"buffer_load_dwordx4 v178, s[44:47], s75 offen offset:1024 nt lds\n\t"
"v_mfma_f32_16x16x32_bf16 v[36:39], v[100:103], v[112:115], v[36:39]\n\t"
"ds_read_b64_tr_b16 v[140:141], v196 offset:15360\n\t"
"v_mfma_f32_16x16x32_bf16 v[56:59], v[104:107], v[112:115], v[56:59]\n\t"
"ds_read_b64_tr_b16 v[142:143], v196 offset:16384\n\t"
"v_mfma_f32_16x16x32_bf16 v[60:63], v[104:107], v[116:119], v[60:63]\n\t"
"ds_read_b64_tr_b16 v[144:145], v198 offset:15360\n\t"
"buffer_load_dwordx4 v179, s[44:47], s75 offen offset:2048 nt lds\n\t"
"v_mfma_f32_16x16x32_bf16 v[64:67], v[104:107], v[120:123], v[64:67]\n\t"
"ds_read_b64_tr_b16 v[146:147], v198 offset:16384\n\t"
"v_mfma_f32_16x16x32_bf16 v[68:71], v[104:107], v[124:127], v[68:71]\n\t"
"buffer_load_dwordx4 v180, s[44:47], s75 offen offset:3072 nt lds\n\t"
"v_mfma_f32_16x16x32_bf16 v[72:75], v[104:107], v[128:131], v[72:75]\n\t"
"s_add_u32 s75, s75, 4096\n\t"
"s_and_b32 s75, s75, 32767\n\t"
"v_mfma_f32_16x16x32_bf16 v[92:95], v[108:111], v[128:131], v[92:95]\n\t"
"v_mfma_f32_16x16x32_bf16 v[88:91], v[108:111], v[124:127], v[88:91]\n\t"
"v_mfma_f32_16x16x32_bf16 v[84:87], v[108:111], v[120:123], v[84:87]\n\t"
"v_mfma_f32_16x16x32_bf16 v[80:83], v[108:111], v[116:119], v[80:83]\n\t"
"s_add_u32 m0, s72, 10240\n\t"
"v_mfma_f32_16x16x32_bf16 v[76:79], v[108:111], v[112:115], v[76:79]\n\t"
"s_waitcnt vmcnt(17) lgkmcnt(0)\n\t"
"s_barrier\n\t"
"buffer_load_dwordx4 v168, s[36:39], s73 offen lds\n\t"
"v_mfma_f32_16x16x32_bf16 v[16:19], v[132:135], v[148:151], v[16:19]\n\t"
"ds_read_b64_tr_b16 v[96:97], v191 offset:24576\n\t"
"v_mfma_f32_16x16x32_bf16 v[20:23], v[132:135], v[152:155], v[20:23]\n\t"
"buffer_load_dwordx4 v169, s[36:39], s73 offen offset:1024 lds\n\t"
"v_mfma_f32_16x16x32_bf16 v[24:27], v[132:135], v[156:159], v[24:27]\n\t"
"ds_read_b64_tr_b16 v[98:99], v191 offset:25600\n\t"
"v_mfma_f32_16x16x32_bf16 v[28:31], v[132:135], v[160:163], v[28:31]\n\t"
"buffer_load_dwordx4 v170, s[36:39], s73 offen offset:2048 lds\n\t"
"v_mfma_f32_16x16x32_bf16 v[32:35], v[132:135], v[164:167], v[32:35]\n\t"
"ds_read_b128 v[112:115], v181 offset:19456\n\t"
"v_mfma_f32_16x16x32_bf16 v[52:55], v[136:139], v[164:167], v[52:55]\n\t"
"buffer_load_dwordx4 v171, s[36:39], s73 offen offset:3072 lds\n\t"
"ds_read_b128 a[16:19], v2 offset:32768\n\t"
"v_mfma_f32_16x16x32_bf16 v[48:51], v[136:139], v[160:163], v[48:51]\n\t"
"s_add_u32 m0, s72, 14336\n\t"
"ds_read_b128 v[116:119], v183 offset:19456\n\t"
"v_mfma_f32_16x16x32_bf16 v[44:47], v[136:139], v[156:159], v[44:47]\n\t"
"buffer_load_dwordx4 v172, s[36:39], s73 offen lds\n\t"
"ds_read_b128 a[20:23], v3 offset:32768\n\t"
"v_mfma_f32_16x16x32_bf16 v[40:43], v[136:139], v[152:155], v[40:43]\n\t"
"ds_read_b128 v[120:123], v185 offset:19456\n\t"
"v_mfma_f32_16x16x32_bf16 v[36:39], v[136:139], v[148:151], v[36:39]\n\t"
"buffer_load_dwordx4 v173, s[40:43], s74 offen offset:1024 lds\n\t"
"ds_read_b128 a[24:27], v4 offset:32768\n\t"
"v_mfma_f32_16x16x32_bf16 v[56:59], v[140:143], v[148:151], v[56:59]\n\t"
"ds_read_b128 v[124:127], v187 offset:19456\n\t"
"v_mfma_f32_16x16x32_bf16 v[60:63], v[140:143], v[152:155], v[60:63]\n\t"
"buffer_load_dwordx4 v174, s[40:43], s74 offen offset:2048 lds\n\t"
"ds_read_b128 a[28:31], v5 offset:32768\n\t"
"v_mfma_f32_16x16x32_bf16 v[64:67], v[140:143], v[156:159], v[64:67]\n\t"
"ds_read_b128 v[128:131], v189 offset:19456\n\t"
"v_mfma_f32_16x16x32_bf16 v[68:71], v[140:143], v[160:163], v[68:71]\n\t"
"buffer_load_dwordx4 v175, s[40:43], s74 offen offset:3072 lds\n\t"
"s_add_u32 m0, s72, 18432\n\t"
"s_nop 0\n\t"
"buffer_load_dwordx4 v176, s[40:43], s74 offen lds\n\t"
"v_mfma_f32_16x16x32_bf16 v[72:75], v[140:143], v[164:167], v[72:75]\n\t"
"ds_read_b64_tr_b16 v[100:101], v193 offset:24576\n\t"
"s_add_u32 s86, s86, 4\n\t"
"s_and_b32 s86, s86, 31\n\t"
"v_mfma_f32_16x16x32_bf16 v[92:95], v[144:147], v[164:167], v[92:95]\n\t"
"ds_read_b64_tr_b16 v[102:103], v193 offset:25600\n\t"
"s_lshr_b32 s87, s15, s86\n\t"
"s_and_b32 s87, s87, 15\n\t"
"v_mfma_f32_16x16x32_bf16 v[88:91], v[144:147], v[160:163], v[88:91]\n\t"
"ds_read_b64_tr_b16 v[104:105], v195 offset:24576\n\t"
"s_lshl_b32 s73, s87, 7\n\t"
"s_lshl_b32 s74, s87, 19\n\t"
"v_mfma_f32_16x16x32_bf16 v[84:87], v[144:147], v[156:159], v[84:87]\n\t"
"ds_read_b64_tr_b16 v[106:107], v195 offset:25600\n\t"
"v_mfma_f32_16x16x32_bf16 v[80:83], v[144:147], v[152:155], v[80:83]\n\t"
"ds_read_b64_tr_b16 v[108:109], v197 offset:24576\n\t"
"v_mfma_f32_16x16x32_bf16 v[76:79], v[144:147], v[148:151], v[76:79]\n\t"
"s_waitcnt lgkmcnt(13)\n\t"
"ds_read_b64_tr_b16 v[110:111], v197 offset:25600\n\t"
"v_mfma_f32_16x16x32_bf16 v[16:19], v[96:99], v[112:115], v[16:19]\n\t"
"s_waitcnt lgkmcnt(12)\n\t"
"ds_read_b64_tr_b16 v[132:133], v192 offset:24576\n\t"
"v_mfma_f32_16x16x32_bf16 v[20:23], v[96:99], v[116:119], v[20:23]\n\t"
"s_waitcnt lgkmcnt(11)\n\t"
"ds_read_b64_tr_b16 v[134:135], v192 offset:25600\n\t"
"v_mfma_f32_16x16x32_bf16 v[24:27], v[96:99], v[120:123], v[24:27]\n\t"
"s_waitcnt lgkmcnt(10)\n\t"
"ds_read_b128 v[148:151], v182 offset:19456\n\t"
"v_mfma_f32_16x16x32_bf16 v[28:31], v[96:99], v[124:127], v[28:31]\n\t"
"s_waitcnt lgkmcnt(9)\n\t"
"ds_read_b128 v[152:155], v184 offset:19456\n\t"
"v_mfma_f32_16x16x32_bf16 v[32:35], v[96:99], v[128:131], v[32:35]\n\t"
"s_waitcnt lgkmcnt(8)\n\t"
"ds_read_b128 v[156:159], v186 offset:19456\n\t"
"v_mfma_f32_16x16x32_bf16 v[52:55], v[100:103], v[128:131], v[52:55]\n\t"
"ds_read_b128 v[160:163], v188 offset:19456\n\t"
"v_mfma_f32_16x16x32_bf16 v[48:51], v[100:103], v[124:127], v[48:51]\n\t"
"ds_read_b128 v[164:167], v190 offset:19456\n\t"
"v_mfma_f32_16x16x32_bf16 v[44:47], v[100:103], v[120:123], v[44:47]\n\t"
"ds_read_b64_tr_b16 v[136:137], v194 offset:24576\n\t"
"v_mfma_f32_16x16x32_bf16 v[40:43], v[100:103], v[116:119], v[40:43]\n\t"
"ds_read_b64_tr_b16 v[138:139], v194 offset:25600\n\t"
"v_mfma_f32_16x16x32_bf16 v[36:39], v[100:103], v[112:115], v[36:39]\n\t"
"s_waitcnt lgkmcnt(11)\n\t"
"ds_read_b64_tr_b16 v[140:141], v196 offset:24576\n\t"
"v_mfma_f32_16x16x32_bf16 v[56:59], v[104:107], v[112:115], v[56:59]\n\t"
"ds_read_b64_tr_b16 v[142:143], v196 offset:25600\n\t"
"v_mfma_f32_16x16x32_bf16 v[60:63], v[104:107], v[116:119], v[60:63]\n\t"
"ds_read_b64_tr_b16 v[144:145], v198 offset:24576\n\t"
"v_mfma_f32_16x16x32_bf16 v[64:67], v[104:107], v[120:123], v[64:67]\n\t"
"ds_read_b64_tr_b16 v[146:147], v198 offset:25600\n\t"
"v_mfma_f32_16x16x32_bf16 v[68:71], v[104:107], v[124:127], v[68:71]\n\t"
"s_waitcnt vmcnt(22)\n\t"
"ds_read_b128 a[32:35], v2 offset:36864\n\t"
"v_mfma_f32_16x16x32_bf16 v[72:75], v[104:107], v[128:131], v[72:75]\n\t"
"s_waitcnt lgkmcnt(14)\n\t"
"ds_read_b128 a[36:39], v3 offset:36864\n\t"
"v_mfma_f32_16x16x32_bf16 v[92:95], v[108:111], v[128:131], v[92:95]\n\t"
"v_mfma_f32_16x16x32_bf16 v[88:91], v[108:111], v[124:127], v[88:91]\n\t"
"ds_read_b128 a[40:43], v4 offset:36864\n\t"
"v_mfma_f32_16x16x32_bf16 v[84:87], v[108:111], v[120:123], v[84:87]\n\t"
"v_mfma_f32_16x16x32_bf16 v[80:83], v[108:111], v[116:119], v[80:83]\n\t"
"s_add_u32 m0, s72, 19456\n\t"
"ds_read_b128 a[44:47], v5 offset:36864\n\t"
"v_mfma_f32_16x16x32_bf16 v[76:79], v[108:111], v[112:115], v[76:79]\n\t"
"s_waitcnt vmcnt(13) lgkmcnt(4)\n\t"
"s_barrier\n\t"
"buffer_load_dwordx4 v168, s[36:39], s73 offen lds\n\t"
"v_mfma_f32_16x16x32_bf16 v[16:19], v[132:135], v[148:151], v[16:19]\n\t"
"ds_read_b64_tr_b16 v[96:97], v191 offset:5120\n\t"
"v_mfma_f32_16x16x32_bf16 v[20:23], v[132:135], v[152:155], v[20:23]\n\t"
"buffer_load_dwordx4 v169, s[36:39], s73 offen offset:1024 lds\n\t"
"v_mfma_f32_16x16x32_bf16 v[24:27], v[132:135], v[156:159], v[24:27]\n\t"
"ds_read_b64_tr_b16 v[98:99], v191 offset:6144\n\t"
"v_mfma_f32_16x16x32_bf16 v[28:31], v[132:135], v[160:163], v[28:31]\n\t"
"buffer_load_dwordx4 v170, s[36:39], s73 offen offset:2048 lds\n\t"
"v_mfma_f32_16x16x32_bf16 v[32:35], v[132:135], v[164:167], v[32:35]\n\t"
"ds_read_b128 v[112:115], v181 offset:0\n\t"
"v_mfma_f32_16x16x32_bf16 v[52:55], v[136:139], v[164:167], v[52:55]\n\t"
"buffer_load_dwordx4 v171, s[36:39], s73 offen offset:3072 lds\n\t"
"v_mfma_f32_16x16x32_bf16 v[48:51], v[136:139], v[160:163], v[48:51]\n\t"
"s_add_u32 m0, s72, 23552\n\t"
"ds_read_b128 v[116:119], v183 offset:0\n\t"
"v_mfma_f32_16x16x32_bf16 v[44:47], v[136:139], v[156:159], v[44:47]\n\t"
"buffer_load_dwordx4 v172, s[36:39], s73 offen lds\n\t"
"v_mfma_f32_16x16x32_bf16 v[40:43], v[136:139], v[152:155], v[40:43]\n\t"
"ds_read_b128 v[120:123], v185 offset:0\n\t"
"v_mfma_f32_16x16x32_bf16 v[36:39], v[136:139], v[148:151], v[36:39]\n\t"
"buffer_load_dwordx4 v173, s[40:43], s74 offen offset:1024 lds\n\t"
"v_mfma_f32_16x16x32_bf16 v[56:59], v[140:143], v[148:151], v[56:59]\n\t"
"ds_read_b128 v[124:127], v187 offset:0\n\t"
"v_mfma_f32_16x16x32_bf16 v[60:63], v[140:143], v[152:155], v[60:63]\n\t"
"buffer_load_dwordx4 v174, s[40:43], s74 offen offset:2048 lds\n\t"
"v_mfma_f32_16x16x32_bf16 v[64:67], v[140:143], v[156:159], v[64:67]\n\t"
"ds_read_b128 v[128:131], v189 offset:0\n\t"
"v_mfma_f32_16x16x32_bf16 v[68:71], v[140:143], v[160:163], v[68:71]\n\t"
"buffer_load_dwordx4 v175, s[40:43], s74 offen offset:3072 lds\n\t"
"s_add_u32 m0, s72, 27648\n\t"
"s_nop 0\n\t"
"buffer_load_dwordx4 v176, s[40:43], s74 offen lds\n\t"
"v_mfma_f32_16x16x32_bf16 v[72:75], v[140:143], v[164:167], v[72:75]\n\t"
"ds_read_b64_tr_b16 v[100:101], v193 offset:5120\n\t"
"s_add_u32 s86, s86, 4\n\t"
"s_and_b32 s86, s86, 31\n\t"
"v_mfma_f32_16x16x32_bf16 v[92:95], v[144:147], v[164:167], v[92:95]\n\t"
"ds_read_b64_tr_b16 v[102:103], v193 offset:6144\n\t"
"s_lshr_b32 s87, s15, s86\n\t"
"s_and_b32 s87, s87, 15\n\t"
"v_mfma_f32_16x16x32_bf16 v[88:91], v[144:147], v[160:163], v[88:91]\n\t"
"ds_read_b64_tr_b16 v[104:105], v195 offset:5120\n\t"
"s_lshl_b32 s73, s87, 7\n\t"
"s_lshl_b32 s74, s87, 19\n\t"
"v_mfma_f32_16x16x32_bf16 v[84:87], v[144:147], v[156:159], v[84:87]\n\t"
"ds_read_b64_tr_b16 v[106:107], v195 offset:6144\n\t"
"v_mfma_f32_16x16x32_bf16 v[80:83], v[144:147], v[152:155], v[80:83]\n\t"
"ds_read_b64_tr_b16 v[108:109], v197 offset:5120\n\t"
"v_mfma_f32_16x16x32_bf16 v[76:79], v[144:147], v[148:151], v[76:79]\n\t"
"s_waitcnt lgkmcnt(9)\n\t"
"ds_read_b64_tr_b16 v[110:111], v197 offset:6144\n\t"
"v_mfma_f32_16x16x32_bf16 v[16:19], v[96:99], v[112:115], v[16:19]\n\t"
"s_waitcnt lgkmcnt(9)\n\t"
"ds_read_b64_tr_b16 v[132:133], v192 offset:5120\n\t"
"v_mfma_f32_16x16x32_bf16 v[20:23], v[96:99], v[116:119], v[20:23]\n\t"
"s_waitcnt lgkmcnt(9)\n\t"
"ds_read_b64_tr_b16 v[134:135], v192 offset:6144\n\t"
"v_mfma_f32_16x16x32_bf16 v[24:27], v[96:99], v[120:123], v[24:27]\n\t"
"s_waitcnt lgkmcnt(9)\n\t"
"ds_read_b128 v[148:151], v182 offset:0\n\t"
"v_mfma_f32_16x16x32_bf16 v[28:31], v[96:99], v[124:127], v[28:31]\n\t"
"s_waitcnt lgkmcnt(9)\n\t"
"ds_read_b128 v[152:155], v184 offset:0\n\t"
"v_mfma_f32_16x16x32_bf16 v[32:35], v[96:99], v[128:131], v[32:35]\n\t"
"s_waitcnt lgkmcnt(8)\n\t"
"ds_read_b128 v[156:159], v186 offset:0\n\t"
"v_mfma_f32_16x16x32_bf16 v[52:55], v[100:103], v[128:131], v[52:55]\n\t"
"ds_read_b128 v[160:163], v188 offset:0\n\t"
"v_mfma_f32_16x16x32_bf16 v[48:51], v[100:103], v[124:127], v[48:51]\n\t"
"ds_read_b128 v[164:167], v190 offset:0\n\t"
"v_mfma_f32_16x16x32_bf16 v[44:47], v[100:103], v[120:123], v[44:47]\n\t"
"ds_read_b64_tr_b16 v[136:137], v194 offset:5120\n\t"
"v_mfma_f32_16x16x32_bf16 v[40:43], v[100:103], v[116:119], v[40:43]\n\t"
"ds_read_b64_tr_b16 v[138:139], v194 offset:6144\n\t"
"v_mfma_f32_16x16x32_bf16 v[36:39], v[100:103], v[112:115], v[36:39]\n\t"
"s_waitcnt lgkmcnt(11)\n\t"
"ds_read_b64_tr_b16 v[140:141], v196 offset:5120\n\t"
"v_mfma_f32_16x16x32_bf16 v[56:59], v[104:107], v[112:115], v[56:59]\n\t"
"ds_read_b64_tr_b16 v[142:143], v196 offset:6144\n\t"
"v_mfma_f32_16x16x32_bf16 v[60:63], v[104:107], v[116:119], v[60:63]\n\t"
"ds_read_b64_tr_b16 v[144:145], v198 offset:5120\n\t"
"v_mfma_f32_16x16x32_bf16 v[64:67], v[104:107], v[120:123], v[64:67]\n\t"
"ds_read_b64_tr_b16 v[146:147], v198 offset:6144\n\t"
"v_mfma_f32_16x16x32_bf16 v[68:71], v[104:107], v[124:127], v[68:71]\n\t"
"v_mfma_f32_16x16x32_bf16 v[72:75], v[104:107], v[128:131], v[72:75]\n\t"
"s_waitcnt lgkmcnt(13)\n\t"
"v_mfma_f32_16x16x32_bf16 v[92:95], v[108:111], v[128:131], v[92:95]\n\t"
"v_mfma_f32_16x16x32_bf16 v[88:91], v[108:111], v[124:127], v[88:91]\n\t"
"v_mfma_f32_16x16x32_bf16 v[84:87], v[108:111], v[120:123], v[84:87]\n\t"
"v_mfma_f32_16x16x32_bf16 v[80:83], v[108:111], v[116:119], v[80:83]\n\t"
"s_add_u32 m0, s72, 0\n\t"
"v_mfma_f32_16x16x32_bf16 v[76:79], v[108:111], v[112:115], v[76:79]\n\t"
"s_waitcnt vmcnt(9) lgkmcnt(0)\n\t"
"s_barrier\n\t"
"buffer_load_dwordx4 v168, s[36:39], s73 offen lds\n\t"
"v_mfma_f32_16x16x32_bf16 v[16:19], v[132:135], v[148:151], v[16:19]\n\t"
"ds_read_b64_tr_b16 v[96:97], v191 offset:15360\n\t"
"v_mfma_f32_16x16x32_bf16 v[20:23], v[132:135], v[152:155], v[20:23]\n\t"
"buffer_load_dwordx4 v169, s[36:39], s73 offen offset:1024 lds\n\t"
"v_mfma_f32_16x16x32_bf16 v[24:27], v[132:135], v[156:159], v[24:27]\n\t"
"ds_read_b64_tr_b16 v[98:99], v191 offset:16384\n\t"
"v_mfma_f32_16x16x32_bf16 v[28:31], v[132:135], v[160:163], v[28:31]\n\t"
"buffer_load_dwordx4 v170, s[36:39], s73 offen offset:2048 lds\n\t"
"v_mfma_f32_16x16x32_bf16 v[32:35], v[132:135], v[164:167], v[32:35]\n\t"
"ds_read_b128 v[112:115], v181 offset:10240\n\t"
"v_mfma_f32_16x16x32_bf16 v[52:55], v[136:139], v[164:167], v[52:55]\n\t"
"buffer_load_dwordx4 v171, s[36:39], s73 offen offset:3072 lds\n\t"
"v_mfma_f32_16x16x32_bf16 v[48:51], v[136:139], v[160:163], v[48:51]\n\t"
"s_add_u32 m0, s72, 4096\n\t"
"ds_read_b128 v[116:119], v183 offset:10240\n\t"
"v_mfma_f32_16x16x32_bf16 v[44:47], v[136:139], v[156:159], v[44:47]\n\t"
"buffer_load_dwordx4 v172, s[36:39], s73 offen lds\n\t"
"v_mfma_f32_16x16x32_bf16 v[40:43], v[136:139], v[152:155], v[40:43]\n\t"
"ds_read_b128 v[120:123], v185 offset:10240\n\t"
"v_mfma_f32_16x16x32_bf16 v[36:39], v[136:139], v[148:151], v[36:39]\n\t"
"buffer_load_dwordx4 v173, s[40:43], s74 offen offset:1024 lds\n\t"
"v_mfma_f32_16x16x32_bf16 v[56:59], v[140:143], v[148:151], v[56:59]\n\t"
"ds_read_b128 v[124:127], v187 offset:10240\n\t"
"v_mfma_f32_16x16x32_bf16 v[60:63], v[140:143], v[152:155], v[60:63]\n\t"
"buffer_load_dwordx4 v174, s[40:43], s74 offen offset:2048 lds\n\t"
"v_mfma_f32_16x16x32_bf16 v[64:67], v[140:143], v[156:159], v[64:67]\n\t"
"ds_read_b128 v[128:131], v189 offset:10240\n\t"
"v_mfma_f32_16x16x32_bf16 v[68:71], v[140:143], v[160:163], v[68:71]\n\t"
"buffer_load_dwordx4 v175, s[40:43], s74 offen offset:3072 lds\n\t"
"s_add_u32 m0, s72, 8192\n\t"
"s_nop 0\n\t"
"buffer_load_dwordx4 v176, s[40:43], s74 offen lds\n\t"
"v_mfma_f32_16x16x32_bf16 v[72:75], v[140:143], v[164:167], v[72:75]\n\t"
"ds_read_b64_tr_b16 v[100:101], v193 offset:15360\n\t"
"s_add_u32 s86, s86, 4\n\t"
"s_and_b32 s86, s86, 31\n\t"
"v_mfma_f32_16x16x32_bf16 v[92:95], v[144:147], v[164:167], v[92:95]\n\t"
"ds_read_b64_tr_b16 v[102:103], v193 offset:16384\n\t"
"s_lshr_b32 s87, s15, s86\n\t"
"s_and_b32 s87, s87, 15\n\t"
"v_mfma_f32_16x16x32_bf16 v[88:91], v[144:147], v[160:163], v[88:91]\n\t"
"ds_read_b64_tr_b16 v[104:105], v195 offset:15360\n\t"
"s_lshl_b32 s73, s87, 7\n\t"
"s_lshl_b32 s74, s87, 19\n\t"
"v_mfma_f32_16x16x32_bf16 v[84:87], v[144:147], v[156:159], v[84:87]\n\t"
"ds_read_b64_tr_b16 v[106:107], v195 offset:16384\n\t"
"v_mfma_f32_16x16x32_bf16 v[80:83], v[144:147], v[152:155], v[80:83]\n\t"
"ds_read_b64_tr_b16 v[108:109], v197 offset:15360\n\t"
"v_mfma_f32_16x16x32_bf16 v[76:79], v[144:147], v[148:151], v[76:79]\n\t"
"s_waitcnt lgkmcnt(9)\n\t"
"ds_read_b64_tr_b16 v[110:111], v197 offset:16384\n\t"
"v_mfma_f32_16x16x32_bf16 v[16:19], v[96:99], v[112:115], v[16:19]\n\t"
"s_waitcnt lgkmcnt(9)\n\t"
"ds_read_b64_tr_b16 v[132:133], v192 offset:15360\n\t"
"v_mfma_f32_16x16x32_bf16 v[20:23], v[96:99], v[116:119], v[20:23]\n\t"
"s_waitcnt lgkmcnt(9)\n\t"
"ds_read_b128 a[48:51], v2 offset:28672\n\t"
"ds_read_b64_tr_b16 v[134:135], v192 offset:16384\n\t"
"v_mfma_f32_16x16x32_bf16 v[24:27], v[96:99], v[120:123], v[24:27]\n\t"
"s_waitcnt lgkmcnt(10)\n\t"
"ds_read_b128 v[148:151], v182 offset:10240\n\t"
"v_mfma_f32_16x16x32_bf16 v[28:31], v[96:99], v[124:127], v[28:31]\n\t"
"s_waitcnt lgkmcnt(10)\n\t"
"ds_read_b128 a[52:55], v3 offset:28672\n\t"
"ds_read_b128 v[152:155], v184 offset:10240\n\t"
"v_mfma_f32_16x16x32_bf16 v[32:35], v[96:99], v[128:131], v[32:35]\n\t"
"s_waitcnt lgkmcnt(10)\n\t"
"ds_read_b128 v[156:159], v186 offset:10240\n\t"
"v_mfma_f32_16x16x32_bf16 v[52:55], v[100:103], v[128:131], v[52:55]\n\t"
"ds_read_b128 a[56:59], v4 offset:28672\n\t"
"ds_read_b128 v[160:163], v188 offset:10240\n\t"
"v_mfma_f32_16x16x32_bf16 v[48:51], v[100:103], v[124:127], v[48:51]\n\t"
"ds_read_b128 v[164:167], v190 offset:10240\n\t"
"v_mfma_f32_16x16x32_bf16 v[44:47], v[100:103], v[120:123], v[44:47]\n\t"
"ds_read_b128 a[60:63], v5 offset:28672\n\t"
"ds_read_b64_tr_b16 v[136:137], v194 offset:15360\n\t"
"v_mfma_f32_16x16x32_bf16 v[40:43], v[100:103], v[116:119], v[40:43]\n\t"
"ds_read_b64_tr_b16 v[138:139], v194 offset:16384\n\t"
"v_mfma_f32_16x16x32_bf16 v[36:39], v[100:103], v[112:115], v[36:39]\n\t"
"s_waitcnt lgkmcnt(15)\n\t"
"ds_read_b64_tr_b16 v[140:141], v196 offset:15360\n\t"
"v_mfma_f32_16x16x32_bf16 v[56:59], v[104:107], v[112:115], v[56:59]\n\t"
"ds_read_b64_tr_b16 v[142:143], v196 offset:16384\n\t"
"v_mfma_f32_16x16x32_bf16 v[60:63], v[104:107], v[116:119], v[60:63]\n\t"
"ds_read_b64_tr_b16 v[144:145], v198 offset:15360\n\t"
"v_mfma_f32_16x16x32_bf16 v[64:67], v[104:107], v[120:123], v[64:67]\n\t"
"ds_read_b64_tr_b16 v[146:147], v198 offset:16384\n\t"
"v_mfma_f32_16x16x32_bf16 v[68:71], v[104:107], v[124:127], v[68:71]\n\t"
"v_mfma_f32_16x16x32_bf16 v[72:75], v[104:107], v[128:131], v[72:75]\n\t"
"s_waitcnt lgkmcnt(15)\n\t"
"v_mfma_f32_16x16x32_bf16 v[92:95], v[108:111], v[128:131], v[92:95]\n\t"
"v_mfma_f32_16x16x32_bf16 v[88:91], v[108:111], v[124:127], v[88:91]\n\t"
"v_mfma_f32_16x16x32_bf16 v[84:87], v[108:111], v[120:123], v[84:87]\n\t"
"v_mfma_f32_16x16x32_bf16 v[80:83], v[108:111], v[116:119], v[80:83]\n\t"
"s_add_u32 m0, s72, 10240\n\t"
"v_mfma_f32_16x16x32_bf16 v[76:79], v[108:111], v[112:115], v[76:79]\n\t"
"s_waitcnt vmcnt(9) lgkmcnt(0)\n\t"
"s_barrier\n\t"
"buffer_load_dwordx4 v168, s[36:39], s73 offen lds\n\t"
"v_mfma_f32_16x16x32_bf16 v[16:19], v[132:135], v[148:151], v[16:19]\n\t"
"ds_read_b64_tr_b16 v[96:97], v191 offset:24576\n\t"
"v_mfma_f32_16x16x32_bf16 v[20:23], v[132:135], v[152:155], v[20:23]\n\t"
"buffer_load_dwordx4 v169, s[36:39], s73 offen offset:1024 lds\n\t"
"v_mfma_f32_16x16x32_bf16 v[24:27], v[132:135], v[156:159], v[24:27]\n\t"
"ds_read_b64_tr_b16 v[98:99], v191 offset:25600\n\t"
"v_mfma_f32_16x16x32_bf16 v[28:31], v[132:135], v[160:163], v[28:31]\n\t"
"buffer_load_dwordx4 v170, s[36:39], s73 offen offset:2048 lds\n\t"
"v_mfma_f32_16x16x32_bf16 v[32:35], v[132:135], v[164:167], v[32:35]\n\t"
"ds_read_b128 v[112:115], v181 offset:19456\n\t"
"v_mfma_f32_16x16x32_bf16 v[52:55], v[136:139], v[164:167], v[52:55]\n\t"
"buffer_load_dwordx4 v171, s[36:39], s73 offen offset:3072 lds\n\t"
"v_mfma_f32_16x16x32_bf16 v[48:51], v[136:139], v[160:163], v[48:51]\n\t"
"s_add_u32 m0, s72, 14336\n\t"
"ds_read_b128 v[116:119], v183 offset:19456\n\t"
"v_mfma_f32_16x16x32_bf16 v[44:47], v[136:139], v[156:159], v[44:47]\n\t"
"buffer_load_dwordx4 v172, s[36:39], s73 offen lds\n\t"
"v_mfma_f32_16x16x32_bf16 v[40:43], v[136:139], v[152:155], v[40:43]\n\t"
"ds_read_b128 v[120:123], v185 offset:19456\n\t"
"v_mfma_f32_16x16x32_bf16 v[36:39], v[136:139], v[148:151], v[36:39]\n\t"
"buffer_load_dwordx4 v173, s[40:43], s74 offen offset:1024 lds\n\t"
"v_mfma_f32_16x16x32_bf16 v[56:59], v[140:143], v[148:151], v[56:59]\n\t"
"ds_read_b128 v[124:127], v187 offset:19456\n\t"
"v_mfma_f32_16x16x32_bf16 v[60:63], v[140:143], v[152:155], v[60:63]\n\t"
"buffer_load_dwordx4 v174, s[40:43], s74 offen offset:2048 lds\n\t"
"v_mfma_f32_16x16x32_bf16 v[64:67], v[140:143], v[156:159], v[64:67]\n\t"
"buffer_load_dwordx4 v175, s[40:43], s74 offen offset:3072 lds\n\t"
"s_add_u32 m0, s72, 18432\n\t"
"s_nop 0\n\t"
"buffer_load_dwordx4 v176, s[40:43], s74 offen lds\n\t"
"s_add_u32 m0, s72, 32768\n\t"
"ds_read_b128 v[128:131], v189 offset:19456\n\t"
"v_mfma_f32_16x16x32_bf16 v[68:71], v[140:143], v[160:163], v[68:71]\n\t"
"buffer_load_dwordx4 v177, s[44:47], s75 offen nt lds\n\t"
"v_mfma_f32_16x16x32_bf16 v[72:75], v[140:143], v[164:167], v[72:75]\n\t"
"ds_read_b64_tr_b16 v[100:101], v193 offset:24576\n\t"
"s_add_u32 s86, s86, 4\n\t"
"s_and_b32 s86, s86, 31\n\t"
"v_mfma_f32_16x16x32_bf16 v[92:95], v[144:147], v[164:167], v[92:95]\n\t"
"ds_read_b64_tr_b16 v[102:103], v193 offset:25600\n\t"
"s_lshr_b32 s87, s15, s86\n\t"
"s_and_b32 s87, s87, 15\n\t"
"v_mfma_f32_16x16x32_bf16 v[88:91], v[144:147], v[160:163], v[88:91]\n\t"
"ds_read_b64_tr_b16 v[104:105], v195 offset:24576\n\t"
"buffer_load_dwordx4 v178, s[44:47], s75 offen offset:1024 nt lds\n\t"
"v_mfma_f32_16x16x32_bf16 v[84:87], v[144:147], v[156:159], v[84:87]\n\t"
"ds_read_b64_tr_b16 v[106:107], v195 offset:25600\n\t"
"s_lshl_b32 s73, s87, 7\n\t"
"s_lshl_b32 s74, s87, 19\n\t"
"v_mfma_f32_16x16x32_bf16 v[80:83], v[144:147], v[152:155], v[80:83]\n\t"
"ds_read_b64_tr_b16 v[108:109], v197 offset:24576\n\t"
"v_mfma_f32_16x16x32_bf16 v[76:79], v[144:147], v[148:151], v[76:79]\n\t"
"s_waitcnt lgkmcnt(9)\n\t"
"ds_read_b64_tr_b16 v[110:111], v197 offset:25600\n\t"
"buffer_load_dwordx4 v179, s[44:47], s75 offen offset:2048 nt lds\n\t"
"v_mfma_f32_16x16x32_bf16 v[16:19], v[96:99], v[112:115], v[16:19]\n\t"
"s_waitcnt lgkmcnt(9)\n\t"
"ds_read_b64_tr_b16 v[132:133], v192 offset:24576\n\t"
"v_mfma_f32_16x16x32_bf16 v[20:23], v[96:99], v[116:119], v[20:23]\n\t"
"s_waitcnt lgkmcnt(9)\n\t"
"ds_read_b64_tr_b16 v[134:135], v192 offset:25600\n\t"
"v_mfma_f32_16x16x32_bf16 v[24:27], v[96:99], v[120:123], v[24:27]\n\t"
"s_waitcnt lgkmcnt(9)\n\t"
"buffer_load_dwordx4 v180, s[44:47], s75 offen offset:3072 nt lds\n\t"
"ds_read_b128 v[148:151], v182 offset:19456\n\t"
"v_mfma_f32_16x16x32_bf16 v[28:31], v[96:99], v[124:127], v[28:31]\n\t"
"s_waitcnt lgkmcnt(9)\n\t"
"ds_read_b128 v[152:155], v184 offset:19456\n\t"
"s_add_u32 s75, s75, 4096\n\t"
"s_and_b32 s75, s75, 32767\n\t"
"v_mfma_f32_16x16x32_bf16 v[32:35], v[96:99], v[128:131], v[32:35]\n\t"
"s_waitcnt lgkmcnt(8)\n\t"
"ds_read_b128 v[156:159], v186 offset:19456\n\t"
"v_mfma_f32_16x16x32_bf16 v[52:55], v[100:103], v[128:131], v[52:55]\n\t"
"ds_read_b128 v[160:163], v188 offset:19456\n\t"
"v_mfma_f32_16x16x32_bf16 v[48:51], v[100:103], v[124:127], v[48:51]\n\t"
"ds_read_b128 v[164:167], v190 offset:19456\n\t"
"v_mfma_f32_16x16x32_bf16 v[44:47], v[100:103], v[120:123], v[44:47]\n\t"
"ds_read_b64_tr_b16 v[136:137], v194 offset:24576\n\t"
"v_mfma_f32_16x16x32_bf16 v[40:43], v[100:103], v[116:119], v[40:43]\n\t"
"ds_read_b64_tr_b16 v[138:139], v194 offset:25600\n\t"
"v_mfma_f32_16x16x32_bf16 v[36:39], v[100:103], v[112:115], v[36:39]\n\t"
"s_waitcnt lgkmcnt(11)\n\t"
"ds_read_b64_tr_b16 v[140:141], v196 offset:24576\n\t"
"v_mfma_f32_16x16x32_bf16 v[56:59], v[104:107], v[112:115], v[56:59]\n\t"
"ds_read_b64_tr_b16 v[142:143], v196 offset:25600\n\t"
"v_mfma_f32_16x16x32_bf16 v[60:63], v[104:107], v[116:119], v[60:63]\n\t"
"ds_read_b64_tr_b16 v[144:145], v198 offset:24576\n\t"
"v_mfma_f32_16x16x32_bf16 v[64:67], v[104:107], v[120:123], v[64:67]\n\t"
"ds_read_b64_tr_b16 v[146:147], v198 offset:25600\n\t"
"v_mfma_f32_16x16x32_bf16 v[68:71], v[104:107], v[124:127], v[68:71]\n\t"
"v_mfma_f32_16x16x32_bf16 v[72:75], v[104:107], v[128:131], v[72:75]\n\t"
"s_waitcnt lgkmcnt(13)\n\t"
"v_mfma_f32_16x16x32_bf16 v[92:95], v[108:111], v[128:131], v[92:95]\n\t"
"v_mfma_f32_16x16x32_bf16 v[88:91], v[108:111], v[124:127], v[88:91]\n\t"
"v_mfma_f32_16x16x32_bf16 v[84:87], v[108:111], v[120:123], v[84:87]\n\t"
"v_mfma_f32_16x16x32_bf16 v[80:83], v[108:111], v[116:119], v[80:83]\n\t"
"s_add_u32 m0, s72, 19456\n\t"
"v_mfma_f32_16x16x32_bf16 v[76:79], v[108:111], v[112:115], v[76:79]\n\t"
"s_waitcnt vmcnt(13) lgkmcnt(0)\n\t"
"s_barrier\n\t"
"buffer_load_dwordx4 v168, s[36:39], s73 offen lds\n\t"
"v_mfma_f32_16x16x32_bf16 v[16:19], v[132:135], v[148:151], v[16:19]\n\t"
"ds_read_b64_tr_b16 v[96:97], v191 offset:5120\n\t"
"v_mfma_f32_16x16x32_bf16 v[20:23], v[132:135], v[152:155], v[20:23]\n\t"
"buffer_load_dwordx4 v169, s[36:39], s73 offen offset:1024 lds\n\t"
"v_mfma_f32_16x16x32_bf16 v[24:27], v[132:135], v[156:159], v[24:27]\n\t"
"ds_read_b64_tr_b16 v[98:99], v191 offset:6144\n\t"
"v_mfma_f32_16x16x32_bf16 v[28:31], v[132:135], v[160:163], v[28:31]\n\t"
"buffer_load_dwordx4 v170, s[36:39], s73 offen offset:2048 lds\n\t"
"v_mfma_f32_16x16x32_bf16 v[32:35], v[132:135], v[164:167], v[32:35]\n\t"
"buffer_load_dwordx4 v171, s[36:39], s73 offen offset:3072 lds\n\t"
"s_add_u32 m0, s72, 23552\n\t"
"s_nop 0\n\t"
"buffer_load_dwordx4 v172, s[36:39], s73 offen lds\n\t"
"buffer_load_dwordx4 v173, s[40:43], s74 offen offset:1024 lds\n\t"
"buffer_load_dwordx4 v174, s[40:43], s74 offen offset:2048 lds\n\t"
"buffer_load_dwordx4 v175, s[40:43], s74 offen offset:3072 lds\n\t"
"s_add_u32 m0, s72, 27648\n\t"
"s_nop 0\n\t"
"buffer_load_dwordx4 v176, s[40:43], s74 offen lds\n\t"
"s_add_u32 m0, s72, 36864\n\t"
"ds_read_b128 v[112:115], v181 offset:0\n\t"
"v_mfma_f32_16x16x32_bf16 v[52:55], v[136:139], v[164:167], v[52:55]\n\t"
"buffer_load_dwordx4 v177, s[44:47], s75 offen nt lds\n\t"
"v_mfma_f32_16x16x32_bf16 v[48:51], v[136:139], v[160:163], v[48:51]\n\t"
"ds_read_b128 v[116:119], v183 offset:0\n\t"
"v_mfma_f32_16x16x32_bf16 v[44:47], v[136:139], v[156:159], v[44:47]\n\t"
"buffer_load_dwordx4 v178, s[44:47], s75 offen offset:1024 nt lds\n\t"
"v_mfma_f32_16x16x32_bf16 v[40:43], v[136:139], v[152:155], v[40:43]\n\t"
"ds_read_b128 v[120:123], v185 offset:0\n\t"
"v_mfma_f32_16x16x32_bf16 v[36:39], v[136:139], v[148:151], v[36:39]\n\t"
"buffer_load_dwordx4 v179, s[44:47], s75 offen offset:2048 nt lds\n\t"
"v_mfma_f32_16x16x32_bf16 v[56:59], v[140:143], v[148:151], v[56:59]\n\t"
"ds_read_b128 v[124:127], v187 offset:0\n\t"
"v_mfma_f32_16x16x32_bf16 v[60:63], v[140:143], v[152:155], v[60:63]\n\t"
"buffer_load_dwordx4 v180, s[44:47], s75 offen offset:3072 nt lds\n\t"
"v_mfma_f32_16x16x32_bf16 v[64:67], v[140:143], v[156:159], v[64:67]\n\t"
"ds_read_b128 v[128:131], v189 offset:0\n\t"
"s_add_u32 s75, s75, 4096\n\t"
"s_and_b32 s75, s75, 32767\n\t"
"v_mfma_f32_16x16x32_bf16 v[68:71], v[140:143], v[160:163], v[68:71]\n\t"
"v_mfma_f32_16x16x32_bf16 v[72:75], v[140:143], v[164:167], v[72:75]\n\t"
"ds_read_b64_tr_b16 v[100:101], v193 offset:5120\n\t"
"s_add_u32 s86, s86, 4\n\t"
"s_and_b32 s86, s86, 31\n\t"
"v_mfma_f32_16x16x32_bf16 v[92:95], v[144:147], v[164:167], v[92:95]\n\t"
"ds_read_b64_tr_b16 v[102:103], v193 offset:6144\n\t"
"s_lshr_b32 s87, s15, s86\n\t"
"s_and_b32 s87, s87, 15\n\t"
"v_mfma_f32_16x16x32_bf16 v[88:91], v[144:147], v[160:163], v[88:91]\n\t"
"ds_read_b64_tr_b16 v[104:105], v195 offset:5120\n\t"
"s_lshl_b32 s73, s87, 7\n\t"
"s_lshl_b32 s74, s87, 19\n\t"
"v_mfma_f32_16x16x32_bf16 v[84:87], v[144:147], v[156:159], v[84:87]\n\t"
"ds_read_b64_tr_b16 v[106:107], v195 offset:6144\n\t"
"v_mfma_f32_16x16x32_bf16 v[80:83], v[144:147], v[152:155], v[80:83]\n\t"
"ds_read_b64_tr_b16 v[108:109], v197 offset:5120\n\t"
"v_mfma_f32_16x16x32_bf16 v[76:79], v[144:147], v[148:151], v[76:79]\n\t"
"s_waitcnt lgkmcnt(9)\n\t"
"ds_read_b64_tr_b16 v[110:111], v197 offset:6144\n\t"
"v_mfma_f32_16x16x32_bf16 v[16:19], v[96:99], v[112:115], v[16:19]\n\t"
"s_waitcnt lgkmcnt(9)\n\t"
"ds_read_b64_tr_b16 v[132:133], v192 offset:5120\n\t"
"v_mfma_f32_16x16x32_bf16 v[20:23], v[96:99], v[116:119], v[20:23]\n\t"
"s_waitcnt lgkmcnt(9)\n\t"
"ds_read_b64_tr_b16 v[134:135], v192 offset:6144\n\t"
"v_mfma_f32_16x16x32_bf16 v[24:27], v[96:99], v[120:123], v[24:27]\n\t"
"s_waitcnt lgkmcnt(9)\n\t"
"ds_read_b128 v[148:151], v182 offset:0\n\t"
"v_mfma_f32_16x16x32_bf16 v[28:31], v[96:99], v[124:127], v[28:31]\n\t"
"s_waitcnt lgkmcnt(9)\n\t"
"ds_read_b128 v[152:155], v184 offset:0\n\t"
"v_mfma_f32_16x16x32_bf16 v[32:35], v[96:99], v[128:131], v[32:35]\n\t"
"s_waitcnt lgkmcnt(8)\n\t"
"ds_read_b128 v[156:159], v186 offset:0\n\t"
"v_mfma_f32_16x16x32_bf16 v[52:55], v[100:103], v[128:131], v[52:55]\n\t"
"ds_read_b128 v[160:163], v188 offset:0\n\t"
"v_mfma_f32_16x16x32_bf16 v[48:51], v[100:103], v[124:127], v[48:51]\n\t"
"ds_read_b128 v[164:167], v190 offset:0\n\t"
"v_mfma_f32_16x16x32_bf16 v[44:47], v[100:103], v[120:123], v[44:47]\n\t"
"ds_read_b64_tr_b16 v[136:137], v194 offset:5120\n\t"
"v_mfma_f32_16x16x32_bf16 v[40:43], v[100:103], v[116:119], v[40:43]\n\t"
"ds_read_b64_tr_b16 v[138:139], v194 offset:6144\n\t"
"v_mfma_f32_16x16x32_bf16 v[36:39], v[100:103], v[112:115], v[36:39]\n\t"
"s_waitcnt lgkmcnt(11)\n\t"
"ds_read_b64_tr_b16 v[140:141], v196 offset:5120\n\t"
"v_mfma_f32_16x16x32_bf16 v[56:59], v[104:107], v[112:115], v[56:59]\n\t"
"ds_read_b64_tr_b16 v[142:143], v196 offset:6144\n\t"
"v_mfma_f32_16x16x32_bf16 v[60:63], v[104:107], v[116:119], v[60:63]\n\t"
"ds_read_b64_tr_b16 v[144:145], v198 offset:5120\n\t"
"s_add_u32 m0, s72, 28672\n\t"
"v_mfma_f32_16x16x32_bf16 v[64:67], v[104:107], v[120:123], v[64:67]\n\t"
"ds_read_b64_tr_b16 v[146:147], v198 offset:6144\n\t"
"v_mfma_f32_16x16x32_bf16 v[68:71], v[104:107], v[124:127], v[68:71]\n\t"
"buffer_load_dwordx4 v177, s[44:47], s75 offen nt lds\n\t"
"v_mfma_f32_16x16x32_bf16 v[72:75], v[104:107], v[128:131], v[72:75]\n\t"
"s_waitcnt lgkmcnt(13)\n\t"
"v_mfma_f32_16x16x32_bf16 v[92:95], v[108:111], v[128:131], v[92:95]\n\t"
"buffer_load_dwordx4 v178, s[44:47], s75 offen offset:1024 nt lds\n\t"
"v_mfma_f32_16x16x32_bf16 v[88:91], v[108:111], v[124:127], v[88:91]\n\t"
"v_mfma_f32_16x16x32_bf16 v[84:87], v[108:111], v[120:123], v[84:87]\n\t"
"v_mfma_f32_16x16x32_bf16 v[80:83], v[108:111], v[116:119], v[80:83]\n\t"
"buffer_load_dwordx4 v179, s[44:47], s75 offen offset:2048 nt lds\n\t"
"buffer_load_dwordx4 v180, s[44:47], s75 offen offset:3072 nt lds\n\t"
"s_add_u32 m0, s72, 0\n\t"
"v_mfma_f32_16x16x32_bf16 v[76:79], v[108:111], v[112:115], v[76:79]\n\t"
"s_waitcnt vmcnt(21) lgkmcnt(0)\n\t"
"s_barrier\n\t"
"buffer_load_dwordx4 v168, s[36:39], s73 offen lds\n\t"
"v_mfma_f32_16x16x32_bf16 v[16:19], v[132:135], v[148:151], v[16:19]\n\t"
"ds_read_b64_tr_b16 v[96:97], v191 offset:15360\n\t"
"v_mfma_f32_16x16x32_bf16 v[20:23], v[132:135], v[152:155], v[20:23]\n\t"
"buffer_load_dwordx4 v169, s[36:39], s73 offen offset:1024 lds\n\t"
"v_mfma_f32_16x16x32_bf16 v[24:27], v[132:135], v[156:159], v[24:27]\n\t"
"ds_read_b64_tr_b16 v[98:99], v191 offset:16384\n\t"
"s_add_u32 s75, s75, 4096\n\t"
"s_and_b32 s75, s75, 32767\n\t"
"v_mfma_f32_16x16x32_bf16 v[28:31], v[132:135], v[160:163], v[28:31]\n\t"
"buffer_load_dwordx4 v170, s[36:39], s73 offen offset:2048 lds\n\t"
"v_mfma_f32_16x16x32_bf16 v[32:35], v[132:135], v[164:167], v[32:35]\n\t"
"ds_read_b128 v[112:115], v181 offset:10240\n\t"
"v_mfma_f32_16x16x32_bf16 v[52:55], v[136:139], v[164:167], v[52:55]\n\t"
"buffer_load_dwordx4 v171, s[36:39], s73 offen offset:3072 lds\n\t"
"v_mfma_f32_16x16x32_bf16 v[48:51], v[136:139], v[160:163], v[48:51]\n\t"
"s_add_u32 m0, s72, 4096\n\t"
"ds_read_b128 v[116:119], v183 offset:10240\n\t"
"v_mfma_f32_16x16x32_bf16 v[44:47], v[136:139], v[156:159], v[44:47]\n\t"
"buffer_load_dwordx4 v172, s[36:39], s73 offen lds\n\t"
"v_mfma_f32_16x16x32_bf16 v[40:43], v[136:139], v[152:155], v[40:43]\n\t"
"ds_read_b128 v[120:123], v185 offset:10240\n\t"
"v_mfma_f32_16x16x32_bf16 v[36:39], v[136:139], v[148:151], v[36:39]\n\t"
"buffer_load_dwordx4 v173, s[40:43], s74 offen offset:1024 lds\n\t"
"v_mfma_f32_16x16x32_bf16 v[56:59], v[140:143], v[148:151], v[56:59]\n\t"
"ds_read_b128 v[124:127], v187 offset:10240\n\t"
"v_mfma_f32_16x16x32_bf16 v[60:63], v[140:143], v[152:155], v[60:63]\n\t"
"buffer_load_dwordx4 v174, s[40:43], s74 offen offset:2048 lds\n\t"
"v_mfma_f32_16x16x32_bf16 v[64:67], v[140:143], v[156:159], v[64:67]\n\t"
"ds_read_b128 v[128:131], v189 offset:10240\n\t"
"v_mfma_f32_16x16x32_bf16 v[68:71], v[140:143], v[160:163], v[68:71]\n\t"
"s_waitcnt vmcnt(24)\n\t"
"buffer_load_dwordx4 v175, s[40:43], s74 offen offset:3072 lds\n\t"
"s_add_u32 m0, s72, 8192\n\t"
"s_nop 0\n\t"
"buffer_load_dwordx4 v176, s[40:43], s74 offen lds\n\t"
"ds_read_b128 a[64:67], v2 offset:32768\n\t"
"v_mfma_f32_16x16x32_bf16 v[72:75], v[140:143], v[164:167], v[72:75]\n\t"
"ds_read_b64_tr_b16 v[100:101], v193 offset:15360\n\t"
"s_add_u32 s86, s86, 4\n\t"
"s_and_b32 s86, s86, 31\n\t"
"v_mfma_f32_16x16x32_bf16 v[92:95], v[144:147], v[164:167], v[92:95]\n\t"
"ds_read_b64_tr_b16 v[102:103], v193 offset:16384\n\t"
"ds_read_b128 a[68:71], v3 offset:32768\n\t"
"s_lshr_b32 s87, s15, s86\n\t"
"v_mfma_f32_16x16x32_bf16 v[88:91], v[144:147], v[160:163], v[88:91]\n\t"
"ds_read_b64_tr_b16 v[104:105], v195 offset:15360\n\t"
"s_and_b32 s87, s87, 15\n\t"
"s_lshl_b32 s73, s87, 7\n\t"
"v_mfma_f32_16x16x32_bf16 v[84:87], v[144:147], v[156:159], v[84:87]\n\t"
"ds_read_b64_tr_b16 v[106:107], v195 offset:16384\n\t"
"ds_read_b128 a[72:75], v4 offset:32768\n\t"
"s_lshl_b32 s74, s87, 19\n\t"
"v_mfma_f32_16x16x32_bf16 v[80:83], v[144:147], v[152:155], v[80:83]\n\t"
"ds_read_b64_tr_b16 v[108:109], v197 offset:15360\n\t"
"v_mfma_f32_16x16x32_bf16 v[76:79], v[144:147], v[148:151], v[76:79]\n\t"
"s_waitcnt lgkmcnt(12)\n\t"
"ds_read_b64_tr_b16 v[110:111], v197 offset:16384\n\t"
"ds_read_b128 a[76:79], v5 offset:32768\n\t"
"v_mfma_f32_16x16x32_bf16 v[16:19], v[96:99], v[112:115], v[16:19]\n\t"
"s_waitcnt lgkmcnt(13)\n\t"
"ds_read_b64_tr_b16 v[132:133], v192 offset:15360\n\t"
"v_mfma_f32_16x16x32_bf16 v[20:23], v[96:99], v[116:119], v[20:23]\n\t"
"s_waitcnt lgkmcnt(13)\n\t"
"ds_read_b64_tr_b16 v[134:135], v192 offset:16384\n\t"
"v_mfma_f32_16x16x32_bf16 v[24:27], v[96:99], v[120:123], v[24:27]\n\t"
"s_waitcnt lgkmcnt(13)\n\t"
"ds_read_b128 v[148:151], v182 offset:10240\n\t"
"v_mfma_f32_16x16x32_bf16 v[28:31], v[96:99], v[124:127], v[28:31]\n\t"
"s_waitcnt lgkmcnt(13)\n\t"
"ds_read_b128 v[152:155], v184 offset:10240\n\t"
"s_add_u32 m0, s72, 32768\n\t"
"v_mfma_f32_16x16x32_bf16 v[32:35], v[96:99], v[128:131], v[32:35]\n\t"
"s_waitcnt lgkmcnt(11)\n\t"
"ds_read_b128 v[156:159], v186 offset:10240\n\t"
"v_mfma_f32_16x16x32_bf16 v[52:55], v[100:103], v[128:131], v[52:55]\n\t"
"ds_read_b128 v[160:163], v188 offset:10240\n\t"
"v_mfma_f32_16x16x32_bf16 v[48:51], v[100:103], v[124:127], v[48:51]\n\t"
"ds_read_b128 v[164:167], v190 offset:10240\n\t"
"v_mfma_f32_16x16x32_bf16 v[44:47], v[100:103], v[120:123], v[44:47]\n\t"
"ds_read_b64_tr_b16 v[136:137], v194 offset:15360\n\t"
"v_mfma_f32_16x16x32_bf16 v[40:43], v[100:103], v[116:119], v[40:43]\n\t"
"ds_read_b64_tr_b16 v[138:139], v194 offset:16384\n\t"
"v_mfma_f32_16x16x32_bf16 v[36:39], v[100:103], v[112:115], v[36:39]\n\t"
"s_waitcnt lgkmcnt(9)\n\t"
"ds_read_b64_tr_b16 v[140:141], v196 offset:15360\n\t"
"buffer_load_dwordx4 v177, s[44:47], s75 offen nt lds\n\t"
"v_mfma_f32_16x16x32_bf16 v[56:59], v[104:107], v[112:115], v[56:59]\n\t"
"ds_read_b64_tr_b16 v[142:143], v196 offset:16384\n\t"
"v_mfma_f32_16x16x32_bf16 v[60:63], v[104:107], v[116:119], v[60:63]\n\t"
"ds_read_b64_tr_b16 v[144:145], v198 offset:15360\n\t"
"v_mfma_f32_16x16x32_bf16 v[64:67], v[104:107], v[120:123], v[64:67]\n\t"
"ds_read_b64_tr_b16 v[146:147], v198 offset:16384\n\t"
"buffer_load_dwordx4 v178, s[44:47], s75 offen offset:1024 nt lds\n\t"
"v_mfma_f32_16x16x32_bf16 v[68:71], v[104:107], v[124:127], v[68:71]\n\t"
"buffer_load_dwordx4 v179, s[44:47], s75 offen offset:2048 nt lds\n\t"
"v_mfma_f32_16x16x32_bf16 v[72:75], v[104:107], v[128:131], v[72:75]\n\t"
"buffer_load_dwordx4 v180, s[44:47], s75 offen offset:3072 nt lds\n\t"
"v_mfma_f32_16x16x32_bf16 v[92:95], v[108:111], v[128:131], v[92:95]\n\t"
"s_add_u32 s75, s75, 4096\n\t"
"s_and_b32 s75, s75, 32767\n\t"
"v_mfma_f32_16x16x32_bf16 v[88:91], v[108:111], v[124:127], v[88:91]\n\t"
"v_mfma_f32_16x16x32_bf16 v[84:87], v[108:111], v[120:123], v[84:87]\n\t"
"v_mfma_f32_16x16x32_bf16 v[80:83], v[108:111], v[116:119], v[80:83]\n\t"
"s_add_u32 m0, s72, 10240\n\t"
"v_mfma_f32_16x16x32_bf16 v[76:79], v[108:111], v[112:115], v[76:79]\n\t"
"s_waitcnt vmcnt(21) lgkmcnt(0)\n\t"
"s_barrier\n\t"
"buffer_load_dwordx4 v168, s[36:39], s73 offen lds\n\t"
"v_mfma_f32_16x16x32_bf16 v[16:19], v[132:135], v[148:151], v[16:19]\n\t"
"ds_read_b64_tr_b16 v[96:97], v191 offset:24576\n\t"
"v_mfma_f32_16x16x32_bf16 v[20:23], v[132:135], v[152:155], v[20:23]\n\t"
"buffer_load_dwordx4 v169, s[36:39], s73 offen offset:1024 lds\n\t"
"v_mfma_f32_16x16x32_bf16 v[24:27], v[132:135], v[156:159], v[24:27]\n\t"
"ds_read_b64_tr_b16 v[98:99], v191 offset:25600\n\t"
"v_mfma_f32_16x16x32_bf16 v[28:31], v[132:135], v[160:163], v[28:31]\n\t"
"buffer_load_dwordx4 v170, s[36:39], s73 offen offset:2048 lds\n\t"
"v_mfma_f32_16x16x32_bf16 v[32:35], v[132:135], v[164:167], v[32:35]\n\t"
"ds_read_b128 v[112:115], v181 offset:19456\n\t"
"v_mfma_f32_16x16x32_bf16 v[52:55], v[136:139], v[164:167], v[52:55]\n\t"
"s_waitcnt vmcnt(20)\n\t"
"buffer_load_dwordx4 v171, s[36:39], s73 offen offset:3072 lds\n\t"
"ds_read_b128 a[80:83], v2 offset:36864\n\t"
"v_mfma_f32_16x16x32_bf16 v[48:51], v[136:139], v[160:163], v[48:51]\n\t"
"s_add_u32 m0, s72, 14336\n\t"
"ds_read_b128 v[116:119], v183 offset:19456\n\t"
"v_mfma_f32_16x16x32_bf16 v[44:47], v[136:139], v[156:159], v[44:47]\n\t"
"buffer_load_dwordx4 v172, s[36:39], s73 offen lds\n\t"
"ds_read_b128 a[84:87], v3 offset:36864\n\t"
"v_mfma_f32_16x16x32_bf16 v[40:43], v[136:139], v[152:155], v[40:43]\n\t"
"ds_read_b128 v[120:123], v185 offset:19456\n\t"
"v_mfma_f32_16x16x32_bf16 v[36:39], v[136:139], v[148:151], v[36:39]\n\t"
"buffer_load_dwordx4 v173, s[40:43], s74 offen offset:1024 lds\n\t"
"ds_read_b128 a[88:91], v4 offset:36864\n\t"
"v_mfma_f32_16x16x32_bf16 v[56:59], v[140:143], v[148:151], v[56:59]\n\t"
"ds_read_b128 v[124:127], v187 offset:19456\n\t"
"v_mfma_f32_16x16x32_bf16 v[60:63], v[140:143], v[152:155], v[60:63]\n\t"
"buffer_load_dwordx4 v174, s[40:43], s74 offen offset:2048 lds\n\t"
"ds_read_b128 a[92:95], v5 offset:36864\n\t"
"v_mfma_f32_16x16x32_bf16 v[64:67], v[140:143], v[156:159], v[64:67]\n\t"
"ds_read_b128 v[128:131], v189 offset:19456\n\t"
"v_mfma_f32_16x16x32_bf16 v[68:71], v[140:143], v[160:163], v[68:71]\n\t"
"buffer_load_dwordx4 v175, s[40:43], s74 offen offset:3072 lds\n\t"
"s_add_u32 m0, s72, 18432\n\t"
"s_nop 0\n\t"
"buffer_load_dwordx4 v176, s[40:43], s74 offen lds\n\t"
"v_mfma_f32_16x16x32_bf16 v[72:75], v[140:143], v[164:167], v[72:75]\n\t"
"ds_read_b64_tr_b16 v[100:101], v193 offset:24576\n\t"
"s_add_u32 s86, s86, 4\n\t"
"s_and_b32 s86, s86, 31\n\t"
"v_mfma_f32_16x16x32_bf16 v[92:95], v[144:147], v[164:167], v[92:95]\n\t"
"ds_read_b64_tr_b16 v[102:103], v193 offset:25600\n\t"
"s_add_u32 m0, s72, 36864\n\t"
"s_lshr_b32 s87, s15, s86\n\t"
"v_mfma_f32_16x16x32_bf16 v[88:91], v[144:147], v[160:163], v[88:91]\n\t"
"ds_read_b64_tr_b16 v[104:105], v195 offset:24576\n\t"
"s_and_b32 s87, s87, 15\n\t"
"s_lshl_b32 s73, s87, 7\n\t"
"v_mfma_f32_16x16x32_bf16 v[84:87], v[144:147], v[156:159], v[84:87]\n\t"
"ds_read_b64_tr_b16 v[106:107], v195 offset:25600\n\t"
"s_lshl_b32 s74, s87, 19\n\t"
"v_mfma_f32_16x16x32_bf16 v[80:83], v[144:147], v[152:155], v[80:83]\n\t"
"ds_read_b64_tr_b16 v[108:109], v197 offset:24576\n\t"
"v_mfma_f32_16x16x32_bf16 v[76:79], v[144:147], v[148:151], v[76:79]\n\t"
"s_waitcnt lgkmcnt(13)\n\t"
"ds_read_b64_tr_b16 v[110:111], v197 offset:25600\n\t"
"v_mfma_f32_16x16x32_bf16 v[16:19], v[96:99], v[112:115], v[16:19]\n\t"
"s_waitcnt lgkmcnt(12)\n\t"
"ds_read_b64_tr_b16 v[132:133], v192 offset:24576\n\t"
"v_mfma_f32_16x16x32_bf16 v[20:23], v[96:99], v[116:119], v[20:23]\n\t"
"s_waitcnt lgkmcnt(8)\n\t"
"buffer_load_dwordx4 v244, s[48:51], s76 offen nt lds\n\t"
"ds_read_b64_tr_b16 v[134:135], v192 offset:25600\n\t"
"v_mfma_f32_16x16x32_bf16 v[24:27], v[96:99], v[120:123], v[24:27]\n\t"
"ds_read_b128 v[148:151], v182 offset:19456\n\t"
"v_mfma_f32_16x16x32_bf16 v[28:31], v[96:99], v[124:127], v[28:31]\n\t"
"s_waitcnt lgkmcnt(9)\n\t"
"ds_read_b128 v[152:155], v184 offset:19456\n\t"
"v_mfma_f32_16x16x32_bf16 v[32:35], v[96:99], v[128:131], v[32:35]\n\t"
"s_waitcnt lgkmcnt(8)\n\t"
"buffer_load_dwordx4 v245, s[48:51], s76 offen offset:1024 nt lds\n\t"
"ds_read_b128 v[156:159], v186 offset:19456\n\t"
"v_mfma_f32_16x16x32_bf16 v[52:55], v[100:103], v[128:131], v[52:55]\n\t"
"ds_read_b128 v[160:163], v188 offset:19456\n\t"
"v_mfma_f32_16x16x32_bf16 v[48:51], v[100:103], v[124:127], v[48:51]\n\t"
"ds_read_b128 v[164:167], v190 offset:19456\n\t"
"v_mfma_f32_16x16x32_bf16 v[44:47], v[100:103], v[120:123], v[44:47]\n\t"
"buffer_load_dwordx4 v14, s[48:51], s76 offen offset:2048 nt lds\n\t"
"ds_read_b64_tr_b16 v[136:137], v194 offset:24576\n\t"
"v_mfma_f32_16x16x32_bf16 v[40:43], v[100:103], v[116:119], v[40:43]\n\t"
"ds_read_b64_tr_b16 v[138:139], v194 offset:25600\n\t"
"v_mfma_f32_16x16x32_bf16 v[36:39], v[100:103], v[112:115], v[36:39]\n\t"
"s_waitcnt lgkmcnt(11)\n\t"
"ds_read_b64_tr_b16 v[140:141], v196 offset:24576\n\t"
"v_mfma_f32_16x16x32_bf16 v[56:59], v[104:107], v[112:115], v[56:59]\n\t"
"buffer_load_dwordx4 v251, s[48:51], s76 offen offset:3072 nt lds\n\t"
"ds_read_b64_tr_b16 v[142:143], v196 offset:25600\n\t"
"v_mfma_f32_16x16x32_bf16 v[60:63], v[104:107], v[116:119], v[60:63]\n\t"
"ds_read_b64_tr_b16 v[144:145], v198 offset:24576\n\t"
"s_add_u32 s76, s76, 4096\n\t"
"s_and_b32 s76, s76, 32767\n\t"
"v_mfma_f32_16x16x32_bf16 v[64:67], v[104:107], v[120:123], v[64:67]\n\t"
"ds_read_b64_tr_b16 v[146:147], v198 offset:25600\n\t"
"v_mfma_f32_16x16x32_bf16 v[68:71], v[104:107], v[124:127], v[68:71]\n\t"
"s_waitcnt vmcnt(26)\n\t"
"ds_read_b128 a[96:99], v2 offset:28672\n\t"
"v_mfma_f32_16x16x32_bf16 v[72:75], v[104:107], v[128:131], v[72:75]\n\t"
"s_waitcnt lgkmcnt(14)\n\t"
"ds_read_b128 a[100:103], v3 offset:28672\n\t"
"v_mfma_f32_16x16x32_bf16 v[92:95], v[108:111], v[128:131], v[92:95]\n\t"
"v_mfma_f32_16x16x32_bf16 v[88:91], v[108:111], v[124:127], v[88:91]\n\t"
"ds_read_b128 a[104:107], v4 offset:28672\n\t"
"v_mfma_f32_16x16x32_bf16 v[84:87], v[108:111], v[120:123], v[84:87]\n\t"
"v_mfma_f32_16x16x32_bf16 v[80:83], v[108:111], v[116:119], v[80:83]\n\t"
"s_add_u32 m0, s72, 19456\n\t"
"ds_read_b128 a[108:111], v5 offset:28672\n\t"
"v_mfma_f32_16x16x32_bf16 v[76:79], v[108:111], v[112:115], v[76:79]\n\t"
"s_waitcnt vmcnt(17) lgkmcnt(4)\n\t"
"s_barrier\n\t"
"buffer_load_dwordx4 v168, s[36:39], s73 offen lds\n\t"
"v_mfma_f32_16x16x32_bf16 v[16:19], v[132:135], v[148:151], v[16:19]\n\t"
"ds_read_b64_tr_b16 v[96:97], v191 offset:5120\n\t"
"v_mfma_f32_16x16x32_bf16 v[20:23], v[132:135], v[152:155], v[20:23]\n\t"
"buffer_load_dwordx4 v169, s[36:39], s73 offen offset:1024 lds\n\t"
"v_mfma_f32_16x16x32_bf16 v[24:27], v[132:135], v[156:159], v[24:27]\n\t"
"ds_read_b64_tr_b16 v[98:99], v191 offset:6144\n\t"
"v_mfma_f32_16x16x32_bf16 v[28:31], v[132:135], v[160:163], v[28:31]\n\t"
"buffer_load_dwordx4 v170, s[36:39], s73 offen offset:2048 lds\n\t"
"v_mfma_f32_16x16x32_bf16 v[32:35], v[132:135], v[164:167], v[32:35]\n\t"
"ds_read_b128 v[112:115], v181 offset:0\n\t"
"v_mfma_f32_16x16x32_bf16 v[52:55], v[136:139], v[164:167], v[52:55]\n\t"
"buffer_load_dwordx4 v171, s[36:39], s73 offen offset:3072 lds\n\t"
"v_mfma_f32_16x16x32_bf16 v[48:51], v[136:139], v[160:163], v[48:51]\n\t"
"s_add_u32 m0, s72, 23552\n\t"
"ds_read_b128 v[116:119], v183 offset:0\n\t"
"v_mfma_f32_16x16x32_bf16 v[44:47], v[136:139], v[156:159], v[44:47]\n\t"
"buffer_load_dwordx4 v172, s[36:39], s73 offen lds\n\t"
"v_mfma_f32_16x16x32_bf16 v[40:43], v[136:139], v[152:155], v[40:43]\n\t"
"ds_read_b128 v[120:123], v185 offset:0\n\t"
"v_mfma_f32_16x16x32_bf16 v[36:39], v[136:139], v[148:151], v[36:39]\n\t"
"buffer_load_dwordx4 v173, s[40:43], s74 offen offset:1024 lds\n\t"
"v_mfma_f32_16x16x32_bf16 v[56:59], v[140:143], v[148:151], v[56:59]\n\t"
"ds_read_b128 v[124:127], v187 offset:0\n\t"
"v_mfma_f32_16x16x32_bf16 v[60:63], v[140:143], v[152:155], v[60:63]\n\t"
"buffer_load_dwordx4 v174, s[40:43], s74 offen offset:2048 lds\n\t"
"v_mfma_f32_16x16x32_bf16 v[64:67], v[140:143], v[156:159], v[64:67]\n\t"
"ds_read_b128 v[128:131], v189 offset:0\n\t"
"v_mfma_f32_16x16x32_bf16 v[68:71], v[140:143], v[160:163], v[68:71]\n\t"
"buffer_load_dwordx4 v175, s[40:43], s74 offen offset:3072 lds\n\t"
"s_add_u32 m0, s72, 27648\n\t"
"s_nop 0\n\t"
"buffer_load_dwordx4 v176, s[40:43], s74 offen lds\n\t"
"v_mfma_f32_16x16x32_bf16 v[72:75], v[140:143], v[164:167], v[72:75]\n\t"
"ds_read_b64_tr_b16 v[100:101], v193 offset:5120\n\t"
"s_add_u32 m0, s72, 28672\n\t"
"s_add_u32 s86, s86, 4\n\t"
"v_mfma_f32_16x16x32_bf16 v[92:95], v[144:147], v[164:167], v[92:95]\n\t"
"ds_read_b64_tr_b16 v[102:103], v193 offset:6144\n\t"
"s_and_b32 s86, s86, 31\n\t"
"s_lshr_b32 s87, s15, s86\n\t"
"v_mfma_f32_16x16x32_bf16 v[88:91], v[144:147], v[160:163], v[88:91]\n\t"
"ds_read_b64_tr_b16 v[104:105], v195 offset:5120\n\t"
"s_and_b32 s87, s87, 15\n\t"
"s_lshl_b32 s73, s87, 7\n\t"
"v_mfma_f32_16x16x32_bf16 v[84:87], v[144:147], v[156:159], v[84:87]\n\t"
"s_waitcnt lgkmcnt(10)\n\t"
"ds_read_b64_tr_b16 v[106:107], v195 offset:6144\n\t"
"buffer_load_dwordx4 v244, s[48:51], s76 offen nt lds\n\t"
"v_mfma_f32_16x16x32_bf16 v[80:83], v[144:147], v[152:155], v[80:83]\n\t"
"ds_read_b64_tr_b16 v[108:109], v197 offset:5120\n\t"
"s_lshl_b32 s74, s87, 19\n\t"
"v_mfma_f32_16x16x32_bf16 v[76:79], v[144:147], v[148:151], v[76:79]\n\t"
"s_waitcnt lgkmcnt(9)\n\t"
"ds_read_b64_tr_b16 v[110:111], v197 offset:6144\n\t"
"v_mfma_f32_16x16x32_bf16 v[16:19], v[96:99], v[112:115], v[16:19]\n\t"
"s_waitcnt lgkmcnt(9)\n\t"
"buffer_load_dwordx4 v245, s[48:51], s76 offen offset:1024 nt lds\n\t"
"ds_read_b64_tr_b16 v[132:133], v192 offset:5120\n\t"
"v_mfma_f32_16x16x32_bf16 v[20:23], v[96:99], v[116:119], v[20:23]\n\t"
"s_waitcnt lgkmcnt(9)\n\t"
"ds_read_b64_tr_b16 v[134:135], v192 offset:6144\n\t"
"v_mfma_f32_16x16x32_bf16 v[24:27], v[96:99], v[120:123], v[24:27]\n\t"
"s_waitcnt lgkmcnt(9)\n\t"
"ds_read_b128 v[148:151], v182 offset:0\n\t"
"v_mfma_f32_16x16x32_bf16 v[28:31], v[96:99], v[124:127], v[28:31]\n\t"
"s_waitcnt lgkmcnt(9)\n\t"
"buffer_load_dwordx4 v14, s[48:51], s76 offen offset:2048 nt lds\n\t"
"ds_read_b128 v[152:155], v184 offset:0\n\t"
"v_mfma_f32_16x16x32_bf16 v[32:35], v[96:99], v[128:131], v[32:35]\n\t"
"s_waitcnt lgkmcnt(8)\n\t"
"ds_read_b128 v[156:159], v186 offset:0\n\t"
"v_mfma_f32_16x16x32_bf16 v[52:55], v[100:103], v[128:131], v[52:55]\n\t"
"ds_read_b128 v[160:163], v188 offset:0\n\t"
"v_mfma_f32_16x16x32_bf16 v[48:51], v[100:103], v[124:127], v[48:51]\n\t"
"buffer_load_dwordx4 v251, s[48:51], s76 offen offset:3072 nt lds\n\t"
"ds_read_b128 v[164:167], v190 offset:0\n\t"
"v_mfma_f32_16x16x32_bf16 v[44:47], v[100:103], v[120:123], v[44:47]\n\t"
"ds_read_b64_tr_b16 v[136:137], v194 offset:5120\n\t"
"s_add_u32 s76, s76, 4096\n\t"
"s_and_b32 s76, s76, 32767\n\t"
"v_mfma_f32_16x16x32_bf16 v[40:43], v[100:103], v[116:119], v[40:43]\n\t"
"ds_read_b64_tr_b16 v[138:139], v194 offset:6144\n\t"
"v_mfma_f32_16x16x32_bf16 v[36:39], v[100:103], v[112:115], v[36:39]\n\t"
"s_waitcnt lgkmcnt(11)\n\t"
"s_waitcnt vmcnt(26)\n\t"
"ds_read_b64_tr_b16 v[140:141], v196 offset:5120\n\t"
"ds_read_b128 a[112:115], v2 offset:32768\n\t"
"v_mfma_f32_16x16x32_bf16 v[56:59], v[104:107], v[112:115], v[56:59]\n\t"
"ds_read_b64_tr_b16 v[142:143], v196 offset:6144\n\t"
"v_mfma_f32_16x16x32_bf16 v[60:63], v[104:107], v[116:119], v[60:63]\n\t"
"ds_read_b64_tr_b16 v[144:145], v198 offset:5120\n\t"
"ds_read_b128 a[116:119], v3 offset:32768\n\t"
"v_mfma_f32_16x16x32_bf16 v[64:67], v[104:107], v[120:123], v[64:67]\n\t"
"ds_read_b64_tr_b16 v[146:147], v198 offset:6144\n\t"
"v_mfma_f32_16x16x32_bf16 v[68:71], v[104:107], v[124:127], v[68:71]\n\t"
"ds_read_b128 a[120:123], v4 offset:32768\n\t"
"v_mfma_f32_16x16x32_bf16 v[72:75], v[104:107], v[128:131], v[72:75]\n\t"
"s_waitcnt lgkmcnt(15)\n\t"
"ds_read_b128 a[124:127], v5 offset:32768\n\t"
"v_mfma_f32_16x16x32_bf16 v[92:95], v[108:111], v[128:131], v[92:95]\n\t"
"v_mfma_f32_16x16x32_bf16 v[88:91], v[108:111], v[124:127], v[88:91]\n\t"
"v_mfma_f32_16x16x32_bf16 v[84:87], v[108:111], v[120:123], v[84:87]\n\t"
"v_mfma_f32_16x16x32_bf16 v[80:83], v[108:111], v[116:119], v[80:83]\n\t"
"s_add_u32 m0, s72, 0\n\t"
"v_mfma_f32_16x16x32_bf16 v[76:79], v[108:111], v[112:115], v[76:79]\n\t"
"s_waitcnt vmcnt(17) lgkmcnt(2)\n\t"
"s_barrier\n\t"
"buffer_load_dwordx4 v168, s[36:39], s73 offen lds\n\t"
"v_mfma_f32_16x16x32_bf16 v[16:19], v[132:135], v[148:151], v[16:19]\n\t"
"ds_read_b64_tr_b16 v[96:97], v191 offset:15360\n\t"
"v_mfma_f32_16x16x32_bf16 v[20:23], v[132:135], v[152:155], v[20:23]\n\t"
"buffer_load_dwordx4 v169, s[36:39], s73 offen offset:1024 lds\n\t"
"v_mfma_f32_16x16x32_bf16 v[24:27], v[132:135], v[156:159], v[24:27]\n\t"
"ds_read_b64_tr_b16 v[98:99], v191 offset:16384\n\t"
"v_mfma_f32_16x16x32_bf16 v[28:31], v[132:135], v[160:163], v[28:31]\n\t"
"buffer_load_dwordx4 v170, s[36:39], s73 offen offset:2048 lds\n\t"
"v_mfma_f32_16x16x32_bf16 v[32:35], v[132:135], v[164:167], v[32:35]\n\t"
"ds_read_b128 v[112:115], v181 offset:10240\n\t"
"v_mfma_f32_16x16x32_bf16 v[52:55], v[136:139], v[164:167], v[52:55]\n\t"
"buffer_load_dwordx4 v171, s[36:39], s73 offen offset:3072 lds\n\t"
"v_mfma_f32_16x16x32_bf16 v[48:51], v[136:139], v[160:163], v[48:51]\n\t"
"s_add_u32 m0, s72, 4096\n\t"
"ds_read_b128 v[116:119], v183 offset:10240\n\t"
"v_mfma_f32_16x16x32_bf16 v[44:47], v[136:139], v[156:159], v[44:47]\n\t"
"buffer_load_dwordx4 v172, s[36:39], s73 offen lds\n\t"
"v_mfma_f32_16x16x32_bf16 v[40:43], v[136:139], v[152:155], v[40:43]\n\t"
"ds_read_b128 v[120:123], v185 offset:10240\n\t"
"v_mfma_f32_16x16x32_bf16 v[36:39], v[136:139], v[148:151], v[36:39]\n\t"
"buffer_load_dwordx4 v173, s[40:43], s74 offen offset:1024 lds\n\t"
"v_mfma_f32_16x16x32_bf16 v[56:59], v[140:143], v[148:151], v[56:59]\n\t"
"ds_read_b128 v[124:127], v187 offset:10240\n\t"
"v_mfma_f32_16x16x32_bf16 v[60:63], v[140:143], v[152:155], v[60:63]\n\t"
"buffer_load_dwordx4 v174, s[40:43], s74 offen offset:2048 lds\n\t"
"v_mfma_f32_16x16x32_bf16 v[64:67], v[140:143], v[156:159], v[64:67]\n\t"
"ds_read_b128 v[128:131], v189 offset:10240\n\t"
"v_mfma_f32_16x16x32_bf16 v[68:71], v[140:143], v[160:163], v[68:71]\n\t"
"buffer_load_dwordx4 v175, s[40:43], s74 offen offset:3072 lds\n\t"
"s_add_u32 m0, s72, 8192\n\t"
"s_nop 0\n\t"
"buffer_load_dwordx4 v176, s[40:43], s74 offen lds\n\t"
"v_mfma_f32_16x16x32_bf16 v[72:75], v[140:143], v[164:167], v[72:75]\n\t"
"ds_read_b64_tr_b16 v[100:101], v193 offset:15360\n\t"
"s_add_u32 s86, s86, 4\n\t"
"s_and_b32 s86, s86, 31\n\t"
"v_mfma_f32_16x16x32_bf16 v[92:95], v[144:147], v[164:167], v[92:95]\n\t"
"ds_read_b64_tr_b16 v[102:103], v193 offset:16384\n\t"
"s_lshr_b32 s87, s15, s86\n\t"
"s_and_b32 s87, s87, 15\n\t"
"v_mfma_f32_16x16x32_bf16 v[88:91], v[144:147], v[160:163], v[88:91]\n\t"
"ds_read_b64_tr_b16 v[104:105], v195 offset:15360\n\t"
"s_lshl_b32 s73, s87, 7\n\t"
"s_lshl_b32 s74, s87, 19\n\t"
"v_mfma_f32_16x16x32_bf16 v[84:87], v[144:147], v[156:159], v[84:87]\n\t"
"ds_read_b64_tr_b16 v[106:107], v195 offset:16384\n\t"
"s_add_u32 m0, s72, 32768\n\t"
"v_mfma_f32_16x16x32_bf16 v[80:83], v[144:147], v[152:155], v[80:83]\n\t"
"ds_read_b64_tr_b16 v[108:109], v197 offset:15360\n\t"
"v_mfma_f32_16x16x32_bf16 v[76:79], v[144:147], v[148:151], v[76:79]\n\t"
"s_waitcnt lgkmcnt(9)\n\t"
"ds_read_b64_tr_b16 v[110:111], v197 offset:16384\n\t"
"v_mfma_f32_16x16x32_bf16 v[16:19], v[96:99], v[112:115], v[16:19]\n\t"
"s_waitcnt lgkmcnt(9)\n\t"
"ds_read_b64_tr_b16 v[132:133], v192 offset:15360\n\t"
"v_mfma_f32_16x16x32_bf16 v[20:23], v[96:99], v[116:119], v[20:23]\n\t"
"s_waitcnt lgkmcnt(9)\n\t"
"ds_read_b64_tr_b16 v[134:135], v192 offset:16384\n\t"
"v_mfma_f32_16x16x32_bf16 v[24:27], v[96:99], v[120:123], v[24:27]\n\t"
"s_waitcnt lgkmcnt(9)\n\t"
"ds_read_b128 v[148:151], v182 offset:10240\n\t"
"v_mfma_f32_16x16x32_bf16 v[28:31], v[96:99], v[124:127], v[28:31]\n\t"
"s_waitcnt lgkmcnt(9)\n\t"
"buffer_load_dwordx4 v244, s[48:51], s76 offen nt lds\n\t"
"ds_read_b128 v[152:155], v184 offset:10240\n\t"
"v_mfma_f32_16x16x32_bf16 v[32:35], v[96:99], v[128:131], v[32:35]\n\t"
"s_waitcnt lgkmcnt(8)\n\t"
"ds_read_b128 v[156:159], v186 offset:10240\n\t"
"v_mfma_f32_16x16x32_bf16 v[52:55], v[100:103], v[128:131], v[52:55]\n\t"
"ds_read_b128 v[160:163], v188 offset:10240\n\t"
"v_mfma_f32_16x16x32_bf16 v[48:51], v[100:103], v[124:127], v[48:51]\n\t"
"ds_read_b128 v[164:167], v190 offset:10240\n\t"
"v_mfma_f32_16x16x32_bf16 v[44:47], v[100:103], v[120:123], v[44:47]\n\t"
"ds_read_b64_tr_b16 v[136:137], v194 offset:15360\n\t"
"v_mfma_f32_16x16x32_bf16 v[40:43], v[100:103], v[116:119], v[40:43]\n\t"
"ds_read_b64_tr_b16 v[138:139], v194 offset:16384\n\t"
"v_mfma_f32_16x16x32_bf16 v[36:39], v[100:103], v[112:115], v[36:39]\n\t"
"s_waitcnt lgkmcnt(11)\n\t"
"ds_read_b64_tr_b16 v[140:141], v196 offset:15360\n\t"
"v_mfma_f32_16x16x32_bf16 v[56:59], v[104:107], v[112:115], v[56:59]\n\t"
"ds_read_b64_tr_b16 v[142:143], v196 offset:16384\n\t"
"v_mfma_f32_16x16x32_bf16 v[60:63], v[104:107], v[116:119], v[60:63]\n\t"
"buffer_load_dwordx4 v245, s[48:51], s76 offen offset:1024 nt lds\n\t"
"ds_read_b64_tr_b16 v[144:145], v198 offset:15360\n\t"
"v_mfma_f32_16x16x32_bf16 v[64:67], v[104:107], v[120:123], v[64:67]\n\t"
"ds_read_b64_tr_b16 v[146:147], v198 offset:16384\n\t"
"v_mfma_f32_16x16x32_bf16 v[68:71], v[104:107], v[124:127], v[68:71]\n\t"
"v_mfma_f32_16x16x32_bf16 v[72:75], v[104:107], v[128:131], v[72:75]\n\t"
"s_waitcnt lgkmcnt(13)\n\t"
"buffer_load_dwordx4 v14, s[48:51], s76 offen offset:2048 nt lds\n\t"
"v_mfma_f32_16x16x32_bf16 v[92:95], v[108:111], v[128:131], v[92:95]\n\t"
"v_mfma_f32_16x16x32_bf16 v[88:91], v[108:111], v[124:127], v[88:91]\n\t"
"v_mfma_f32_16x16x32_bf16 v[84:87], v[108:111], v[120:123], v[84:87]\n\t"
"v_mfma_f32_16x16x32_bf16 v[80:83], v[108:111], v[116:119], v[80:83]\n\t"
"v_mfma_f32_16x16x32_bf16 v[76:79], v[108:111], v[112:115], v[76:79]\n\t"
"s_waitcnt vmcnt(16) lgkmcnt(0)\n\t"
"s_barrier\n\t"
"v_mfma_f32_16x16x32_bf16 v[16:19], v[132:135], v[148:151], v[16:19]\n\t"
"ds_read_b64_tr_b16 v[96:97], v191 offset:24576\n\t"
"v_mfma_f32_16x16x32_bf16 v[20:23], v[132:135], v[152:155], v[20:23]\n\t"
"v_mfma_f32_16x16x32_bf16 v[24:27], v[132:135], v[156:159], v[24:27]\n\t"
"ds_read_b64_tr_b16 v[98:99], v191 offset:25600\n\t"
"v_mfma_f32_16x16x32_bf16 v[28:31], v[132:135], v[160:163], v[28:31]\n\t"
"buffer_load_dwordx4 v251, s[48:51], s76 offen offset:3072 nt lds\n\t"
"v_mfma_f32_16x16x32_bf16 v[32:35], v[132:135], v[164:167], v[32:35]\n\t"
"ds_read_b128 v[112:115], v181 offset:19456\n\t"
"s_add_u32 s76, s76, 4096\n\t"
"s_and_b32 s76, s76, 32767\n\t"
"v_mfma_f32_16x16x32_bf16 v[52:55], v[136:139], v[164:167], v[52:55]\n\t"
"v_mfma_f32_16x16x32_bf16 v[48:51], v[136:139], v[160:163], v[48:51]\n\t"
"ds_read_b128 v[116:119], v183 offset:19456\n\t"
"v_mfma_f32_16x16x32_bf16 v[44:47], v[136:139], v[156:159], v[44:47]\n\t"
"v_mfma_f32_16x16x32_bf16 v[40:43], v[136:139], v[152:155], v[40:43]\n\t"
"ds_read_b128 v[120:123], v185 offset:19456\n\t"
"v_mfma_f32_16x16x32_bf16 v[36:39], v[136:139], v[148:151], v[36:39]\n\t"
"v_mfma_f32_16x16x32_bf16 v[56:59], v[140:143], v[148:151], v[56:59]\n\t"
"ds_read_b128 v[124:127], v187 offset:19456\n\t"
"v_mfma_f32_16x16x32_bf16 v[60:63], v[140:143], v[152:155], v[60:63]\n\t"
"v_mfma_f32_16x16x32_bf16 v[64:67], v[140:143], v[156:159], v[64:67]\n\t"
"ds_read_b128 v[128:131], v189 offset:19456\n\t"
"v_mfma_f32_16x16x32_bf16 v[68:71], v[140:143], v[160:163], v[68:71]\n\t"
"v_mfma_f32_16x16x32_bf16 v[72:75], v[140:143], v[164:167], v[72:75]\n\t"
"ds_read_b64_tr_b16 v[100:101], v193 offset:24576\n\t"
"v_mfma_f32_16x16x32_bf16 v[92:95], v[144:147], v[164:167], v[92:95]\n\t"
"ds_read_b64_tr_b16 v[102:103], v193 offset:25600\n\t"
"s_add_u32 m0, s72, 12288\n\t"
"v_mfma_f32_16x16x32_bf16 v[88:91], v[144:147], v[160:163], v[88:91]\n\t"
"ds_read_b64_tr_b16 v[104:105], v195 offset:24576\n\t"
"v_mfma_f32_16x16x32_bf16 v[84:87], v[144:147], v[156:159], v[84:87]\n\t"
"ds_read_b64_tr_b16 v[106:107], v195 offset:25600\n\t"
"v_mfma_f32_16x16x32_bf16 v[80:83], v[144:147], v[152:155], v[80:83]\n\t"
"ds_read_b64_tr_b16 v[108:109], v197 offset:24576\n\t"
"v_mfma_f32_16x16x32_bf16 v[76:79], v[144:147], v[148:151], v[76:79]\n\t"
"s_waitcnt lgkmcnt(9)\n\t"
"ds_read_b64_tr_b16 v[110:111], v197 offset:25600\n\t"
"v_mfma_f32_16x16x32_bf16 v[16:19], v[96:99], v[112:115], v[16:19]\n\t"
"s_waitcnt lgkmcnt(9)\n\t"
"ds_read_b64_tr_b16 v[132:133], v192 offset:24576\n\t"
"v_mfma_f32_16x16x32_bf16 v[20:23], v[96:99], v[116:119], v[20:23]\n\t"
"s_waitcnt lgkmcnt(9)\n\t"
"buffer_load_dwordx4 v244, s[48:51], s76 offen nt lds\n\t"
"ds_read_b64_tr_b16 v[134:135], v192 offset:25600\n\t"
"v_mfma_f32_16x16x32_bf16 v[24:27], v[96:99], v[120:123], v[24:27]\n\t"
"s_waitcnt lgkmcnt(9)\n\t"
"ds_read_b128 v[148:151], v182 offset:19456\n\t"
"v_mfma_f32_16x16x32_bf16 v[28:31], v[96:99], v[124:127], v[28:31]\n\t"
"s_waitcnt lgkmcnt(9)\n\t"
"ds_read_b128 v[152:155], v184 offset:19456\n\t"
"v_mfma_f32_16x16x32_bf16 v[32:35], v[96:99], v[128:131], v[32:35]\n\t"
"s_waitcnt lgkmcnt(8)\n\t"
"ds_read_b128 v[156:159], v186 offset:19456\n\t"
"v_mfma_f32_16x16x32_bf16 v[52:55], v[100:103], v[128:131], v[52:55]\n\t"
"ds_read_b128 v[160:163], v188 offset:19456\n\t"
"v_mfma_f32_16x16x32_bf16 v[48:51], v[100:103], v[124:127], v[48:51]\n\t"
"ds_read_b128 v[164:167], v190 offset:19456\n\t"
"v_mfma_f32_16x16x32_bf16 v[44:47], v[100:103], v[120:123], v[44:47]\n\t"
"ds_read_b64_tr_b16 v[136:137], v194 offset:24576\n\t"
"v_mfma_f32_16x16x32_bf16 v[40:43], v[100:103], v[116:119], v[40:43]\n\t"
"ds_read_b64_tr_b16 v[138:139], v194 offset:25600\n\t"
"v_mfma_f32_16x16x32_bf16 v[36:39], v[100:103], v[112:115], v[36:39]\n\t"
"s_waitcnt lgkmcnt(11)\n\t"
"buffer_load_dwordx4 v245, s[48:51], s76 offen offset:1024 nt lds\n\t"
"ds_read_b64_tr_b16 v[140:141], v196 offset:24576\n\t"
"v_mfma_f32_16x16x32_bf16 v[56:59], v[104:107], v[112:115], v[56:59]\n\t"
"ds_read_b64_tr_b16 v[142:143], v196 offset:25600\n\t"
"v_mfma_f32_16x16x32_bf16 v[60:63], v[104:107], v[116:119], v[60:63]\n\t"
"ds_read_b64_tr_b16 v[144:145], v198 offset:24576\n\t"
"v_mfma_f32_16x16x32_bf16 v[64:67], v[104:107], v[120:123], v[64:67]\n\t"
"ds_read_b64_tr_b16 v[146:147], v198 offset:25600\n\t"
"v_mfma_f32_16x16x32_bf16 v[68:71], v[104:107], v[124:127], v[68:71]\n\t"
"buffer_load_dwordx4 v14, s[48:51], s76 offen offset:2048 nt lds\n\t"
"v_mfma_f32_16x16x32_bf16 v[72:75], v[104:107], v[128:131], v[72:75]\n\t"
"s_waitcnt lgkmcnt(13)\n\t"
"v_mfma_f32_16x16x32_bf16 v[92:95], v[108:111], v[128:131], v[92:95]\n\t"
"v_mfma_f32_16x16x32_bf16 v[88:91], v[108:111], v[124:127], v[88:91]\n\t"
"v_mfma_f32_16x16x32_bf16 v[84:87], v[108:111], v[120:123], v[84:87]\n\t"
"v_mfma_f32_16x16x32_bf16 v[80:83], v[108:111], v[116:119], v[80:83]\n\t"
"v_mfma_f32_16x16x32_bf16 v[76:79], v[108:111], v[112:115], v[76:79]\n\t"
"s_waitcnt vmcnt(7) lgkmcnt(0)\n\t"
"s_barrier\n\t"
"v_mfma_f32_16x16x32_bf16 v[16:19], v[132:135], v[148:151], v[16:19]\n\t"
"ds_read_b64_tr_b16 v[96:97], v191 offset:5120\n\t"
"v_mfma_f32_16x16x32_bf16 v[20:23], v[132:135], v[152:155], v[20:23]\n\t"
"buffer_load_dwordx4 v251, s[48:51], s76 offen offset:3072 nt lds\n\t"
"v_mfma_f32_16x16x32_bf16 v[24:27], v[132:135], v[156:159], v[24:27]\n\t"
"ds_read_b64_tr_b16 v[98:99], v191 offset:6144\n\t"
"s_add_u32 s76, s76, 4096\n\t"
"s_and_b32 s76, s76, 32767\n\t"
"v_mfma_f32_16x16x32_bf16 v[28:31], v[132:135], v[160:163], v[28:31]\n\t"
"v_mfma_f32_16x16x32_bf16 v[32:35], v[132:135], v[164:167], v[32:35]\n\t"
"ds_read_b128 v[112:115], v181 offset:0\n\t"
"v_mfma_f32_16x16x32_bf16 v[52:55], v[136:139], v[164:167], v[52:55]\n\t"
"v_mfma_f32_16x16x32_bf16 v[48:51], v[136:139], v[160:163], v[48:51]\n\t"
"ds_read_b128 v[116:119], v183 offset:0\n\t"
"v_mfma_f32_16x16x32_bf16 v[44:47], v[136:139], v[156:159], v[44:47]\n\t"
"v_mfma_f32_16x16x32_bf16 v[40:43], v[136:139], v[152:155], v[40:43]\n\t"
"ds_read_b128 v[120:123], v185 offset:0\n\t"
"v_mfma_f32_16x16x32_bf16 v[36:39], v[136:139], v[148:151], v[36:39]\n\t"
"v_mfma_f32_16x16x32_bf16 v[56:59], v[140:143], v[148:151], v[56:59]\n\t"
"ds_read_b128 v[124:127], v187 offset:0\n\t"
"v_mfma_f32_16x16x32_bf16 v[60:63], v[140:143], v[152:155], v[60:63]\n\t"
"s_add_u32 m0, s72, 16384\n\t"
"v_mfma_f32_16x16x32_bf16 v[64:67], v[140:143], v[156:159], v[64:67]\n\t"
"ds_read_b128 v[128:131], v189 offset:0\n\t"
"v_mfma_f32_16x16x32_bf16 v[68:71], v[140:143], v[160:163], v[68:71]\n\t"
"buffer_load_dwordx4 v244, s[48:51], s76 offen nt lds\n\t"
"v_mfma_f32_16x16x32_bf16 v[72:75], v[140:143], v[164:167], v[72:75]\n\t"
"ds_read_b64_tr_b16 v[100:101], v193 offset:5120\n\t"
"v_mfma_f32_16x16x32_bf16 v[92:95], v[144:147], v[164:167], v[92:95]\n\t"
"ds_read_b64_tr_b16 v[102:103], v193 offset:6144\n\t"
"v_mfma_f32_16x16x32_bf16 v[88:91], v[144:147], v[160:163], v[88:91]\n\t"
"ds_read_b64_tr_b16 v[104:105], v195 offset:5120\n\t"
"v_mfma_f32_16x16x32_bf16 v[84:87], v[144:147], v[156:159], v[84:87]\n\t"
"ds_read_b64_tr_b16 v[106:107], v195 offset:6144\n\t"
"v_mfma_f32_16x16x32_bf16 v[80:83], v[144:147], v[152:155], v[80:83]\n\t"
"ds_read_b64_tr_b16 v[108:109], v197 offset:5120\n\t"
"v_mfma_f32_16x16x32_bf16 v[76:79], v[144:147], v[148:151], v[76:79]\n\t"
"s_waitcnt lgkmcnt(9)\n\t"
"ds_read_b64_tr_b16 v[110:111], v197 offset:6144\n\t"
"v_mfma_f32_16x16x32_bf16 v[16:19], v[96:99], v[112:115], v[16:19]\n\t"
"s_waitcnt lgkmcnt(9)\n\t"
"ds_read_b64_tr_b16 v[132:133], v192 offset:5120\n\t"
"v_mfma_f32_16x16x32_bf16 v[20:23], v[96:99], v[116:119], v[20:23]\n\t"
"s_waitcnt lgkmcnt(9)\n\t"
"ds_read_b64_tr_b16 v[134:135], v192 offset:6144\n\t"
"v_mfma_f32_16x16x32_bf16 v[24:27], v[96:99], v[120:123], v[24:27]\n\t"
"s_waitcnt lgkmcnt(9)\n\t"
"ds_read_b128 v[148:151], v182 offset:0\n\t"
"v_mfma_f32_16x16x32_bf16 v[28:31], v[96:99], v[124:127], v[28:31]\n\t"
"s_waitcnt lgkmcnt(9)\n\t"
"ds_read_b128 v[152:155], v184 offset:0\n\t"
"v_mfma_f32_16x16x32_bf16 v[32:35], v[96:99], v[128:131], v[32:35]\n\t"
"s_waitcnt lgkmcnt(8)\n\t"
"ds_read_b128 v[156:159], v186 offset:0\n\t"
"v_mfma_f32_16x16x32_bf16 v[52:55], v[100:103], v[128:131], v[52:55]\n\t"
"buffer_load_dwordx4 v245, s[48:51], s76 offen offset:1024 nt lds\n\t"
"ds_read_b128 v[160:163], v188 offset:0\n\t"
"v_mfma_f32_16x16x32_bf16 v[48:51], v[100:103], v[124:127], v[48:51]\n\t"
"ds_read_b128 v[164:167], v190 offset:0\n\t"
"v_mfma_f32_16x16x32_bf16 v[44:47], v[100:103], v[120:123], v[44:47]\n\t"
"ds_read_b64_tr_b16 v[136:137], v194 offset:5120\n\t"
"v_mfma_f32_16x16x32_bf16 v[40:43], v[100:103], v[116:119], v[40:43]\n\t"
"ds_read_b64_tr_b16 v[138:139], v194 offset:6144\n\t"
"v_mfma_f32_16x16x32_bf16 v[36:39], v[100:103], v[112:115], v[36:39]\n\t"
"s_waitcnt lgkmcnt(11)\n\t"
"ds_read_b64_tr_b16 v[140:141], v196 offset:5120\n\t"
"v_mfma_f32_16x16x32_bf16 v[56:59], v[104:107], v[112:115], v[56:59]\n\t"
"ds_read_b64_tr_b16 v[142:143], v196 offset:6144\n\t"
"v_mfma_f32_16x16x32_bf16 v[60:63], v[104:107], v[116:119], v[60:63]\n\t"
"ds_read_b64_tr_b16 v[144:145], v198 offset:5120\n\t"
"v_mfma_f32_16x16x32_bf16 v[64:67], v[104:107], v[120:123], v[64:67]\n\t"
"ds_read_b64_tr_b16 v[146:147], v198 offset:6144\n\t"
"v_mfma_f32_16x16x32_bf16 v[68:71], v[104:107], v[124:127], v[68:71]\n\t"
"buffer_load_dwordx4 v14, s[48:51], s76 offen offset:2048 nt lds\n\t"
"v_mfma_f32_16x16x32_bf16 v[72:75], v[104:107], v[128:131], v[72:75]\n\t"
"s_waitcnt lgkmcnt(13)\n\t"
"v_mfma_f32_16x16x32_bf16 v[92:95], v[108:111], v[128:131], v[92:95]\n\t"
"v_mfma_f32_16x16x32_bf16 v[88:91], v[108:111], v[124:127], v[88:91]\n\t"
"buffer_load_dwordx4 v251, s[48:51], s76 offen offset:3072 nt lds\n\t"
"v_mfma_f32_16x16x32_bf16 v[84:87], v[108:111], v[120:123], v[84:87]\n\t"
"s_add_u32 s76, s76, 4096\n\t"
"s_and_b32 s76, s76, 32767\n\t"
"v_mfma_f32_16x16x32_bf16 v[80:83], v[108:111], v[116:119], v[80:83]\n\t"
"v_mfma_f32_16x16x32_bf16 v[76:79], v[108:111], v[112:115], v[76:79]\n\t"
"s_waitcnt lgkmcnt(0)\n\t"
"s_barrier\n\t"
"v_mfma_f32_16x16x32_bf16 v[16:19], v[132:135], v[148:151], v[16:19]\n\t"
"v_mfma_f32_16x16x32_bf16 v[36:39], v[136:139], v[148:151], v[36:39]\n\t"
"v_mfma_f32_16x16x32_bf16 v[56:59], v[140:143], v[148:151], v[56:59]\n\t"
"v_mfma_f32_16x16x32_bf16 v[76:79], v[144:147], v[148:151], v[76:79]\n\t"
"v_mfma_f32_16x16x32_bf16 v[20:23], v[132:135], v[152:155], v[20:23]\n\t"
"v_mfma_f32_16x16x32_bf16 v[40:43], v[136:139], v[152:155], v[40:43]\n\t"
"v_mfma_f32_16x16x32_bf16 v[60:63], v[140:143], v[152:155], v[60:63]\n\t"
"v_mfma_f32_16x16x32_bf16 v[80:83], v[144:147], v[152:155], v[80:83]\n\t"
"v_mfma_f32_16x16x32_bf16 v[24:27], v[132:135], v[156:159], v[24:27]\n\t"
"v_mul_f32 v16, s84, v16\n\t"
"v_mul_f32 v17, s84, v17\n\t"
"v_mfma_f32_16x16x32_bf16 v[44:47], v[136:139], v[156:159], v[44:47]\n\t"
"v_mul_f32 v18, s84, v18\n\t"
"v_mul_f32 v19, s84, v19\n\t"
"v_mfma_f32_16x16x32_bf16 v[64:67], v[140:143], v[156:159], v[64:67]\n\t"
"v_cvt_pk_bf16_f32 v96, v16, v17\n\t"
"v_cvt_pk_bf16_f32 v97, v18, v19\n\t"
"v_mfma_f32_16x16x32_bf16 v[84:87], v[144:147], v[156:159], v[84:87]\n\t"
"s_add_u32 m0, s72, 24576\n\t"
"ds_write_b64 v203, v[96:97] offset:0\n\t"
"v_mfma_f32_16x16x32_bf16 v[28:31], v[132:135], v[160:163], v[28:31]\n\t"
"buffer_load_dwordx4 v244, s[48:51], s76 offen nt lds\n\t"
"v_mfma_f32_16x16x32_bf16 v[48:51], v[136:139], v[160:163], v[48:51]\n\t"
"v_mul_f32 v36, s84, v36\n\t"
"v_mul_f32 v37, s84, v37\n\t"
"v_mfma_f32_16x16x32_bf16 v[68:71], v[140:143], v[160:163], v[68:71]\n\t"
"v_mul_f32 v38, s84, v38\n\t"
"v_mul_f32 v39, s84, v39\n\t"
"v_mfma_f32_16x16x32_bf16 v[88:91], v[144:147], v[160:163], v[88:91]\n\t"
"v_cvt_pk_bf16_f32 v98, v36, v37\n\t"
"v_cvt_pk_bf16_f32 v99, v38, v39\n\t"
"v_mfma_f32_16x16x32_bf16 v[32:35], v[132:135], v[164:167], v[32:35]\n\t"
"buffer_load_dwordx4 v245, s[48:51], s76 offen offset:1024 nt lds\n\t"
"v_mfma_f32_16x16x32_bf16 v[52:55], v[136:139], v[164:167], v[52:55]\n\t"
"buffer_load_dwordx4 v14, s[48:51], s76 offen offset:2048 nt lds\n\t"
"v_mfma_f32_16x16x32_bf16 v[72:75], v[140:143], v[164:167], v[72:75]\n\t"
"buffer_load_dwordx4 v251, s[48:51], s76 offen offset:3072 nt lds\n\t"
"v_mfma_f32_16x16x32_bf16 v[92:95], v[144:147], v[164:167], v[92:95]\n\t"
"s_add_u32 s76, s76, 4096\n\t"
"ds_write_b64 v203, v[98:99] offset:512\n\t"
"v_mul_f32 v56, s84, v56\n\t"
"v_mul_f32 v57, s84, v57\n\t"
"v_mul_f32 v58, s84, v58\n\t"
"v_mul_f32 v59, s84, v59\n\t"
"v_cvt_pk_bf16_f32 v100, v56, v57\n\t"
"v_cvt_pk_bf16_f32 v101, v58, v59\n\t"
"ds_write_b64 v203, v[100:101] offset:40960\n\t"
"v_mul_f32 v76, s84, v76\n\t"
"v_mul_f32 v77, s84, v77\n\t"
"v_mul_f32 v78, s84, v78\n\t"
"v_mul_f32 v79, s84, v79\n\t"
"v_cvt_pk_bf16_f32 v102, v76, v77\n\t"
"v_cvt_pk_bf16_f32 v103, v78, v79\n\t"
"ds_write_b64 v203, v[102:103] offset:41472\n\t"
"v_pk_mul_f32 v[20:21], v[20:21], v[208:209]\n\t"
"v_pk_mul_f32 v[22:23], v[22:23], v[208:209]\n\t"
"v_cvt_pk_bf16_f32 v104, v20, v21\n\t"
"v_cvt_pk_bf16_f32 v105, v22, v23\n\t"
"ds_write_b64 v204, v[104:105] offset:0\n\t"
"v_pk_mul_f32 v[40:41], v[40:41], v[208:209]\n\t"
"v_pk_mul_f32 v[42:43], v[42:43], v[208:209]\n\t"
"v_cvt_pk_bf16_f32 v106, v40, v41\n\t"
"v_cvt_pk_bf16_f32 v107, v42, v43\n\t"
"ds_write_b64 v204, v[106:107] offset:512\n\t"
"v_pk_mul_f32 v[60:61], v[60:61], v[208:209]\n\t"
"v_pk_mul_f32 v[62:63], v[62:63], v[208:209]\n\t"
"v_cvt_pk_bf16_f32 v108, v60, v61\n\t"
"v_cvt_pk_bf16_f32 v109, v62, v63\n\t"
"ds_write_b64 v204, v[108:109] offset:40960\n\t"
"v_pk_mul_f32 v[80:81], v[80:81], v[208:209]\n\t"
"v_pk_mul_f32 v[82:83], v[82:83], v[208:209]\n\t"
"v_cvt_pk_bf16_f32 v110, v80, v81\n\t"
"v_cvt_pk_bf16_f32 v111, v82, v83\n\t"
"ds_write_b64 v204, v[110:111] offset:41472\n\t"
"v_pk_mul_f32 v[24:25], v[24:25], v[208:209]\n\t"
"v_pk_mul_f32 v[26:27], v[26:27], v[208:209]\n\t"
"v_cvt_pk_bf16_f32 v112, v24, v25\n\t"
"v_cvt_pk_bf16_f32 v113, v26, v27\n\t"
"ds_write_b64 v205, v[112:113] offset:0\n\t"
"v_pk_mul_f32 v[44:45], v[44:45], v[208:209]\n\t"
"v_pk_mul_f32 v[46:47], v[46:47], v[208:209]\n\t"
"v_cvt_pk_bf16_f32 v114, v44, v45\n\t"
"v_cvt_pk_bf16_f32 v115, v46, v47\n\t"
"ds_write_b64 v205, v[114:115] offset:512\n\t"
"v_pk_mul_f32 v[64:65], v[64:65], v[208:209]\n\t"
"v_pk_mul_f32 v[66:67], v[66:67], v[208:209]\n\t"
"v_cvt_pk_bf16_f32 v116, v64, v65\n\t"
"v_cvt_pk_bf16_f32 v117, v66, v67\n\t"
"ds_write_b64 v205, v[116:117] offset:40960\n\t"
"v_pk_mul_f32 v[84:85], v[84:85], v[208:209]\n\t"
"v_pk_mul_f32 v[86:87], v[86:87], v[208:209]\n\t"
"v_cvt_pk_bf16_f32 v118, v84, v85\n\t"
"v_cvt_pk_bf16_f32 v119, v86, v87\n\t"
"ds_write_b64 v205, v[118:119] offset:41472\n\t"
"v_pk_mul_f32 v[28:29], v[28:29], v[208:209]\n\t"
"v_pk_mul_f32 v[30:31], v[30:31], v[208:209]\n\t"
"v_cvt_pk_bf16_f32 v120, v28, v29\n\t"
"v_cvt_pk_bf16_f32 v121, v30, v31\n\t"
"ds_write_b64 v206, v[120:121] offset:0\n\t"
"v_pk_mul_f32 v[48:49], v[48:49], v[208:209]\n\t"
"v_pk_mul_f32 v[50:51], v[50:51], v[208:209]\n\t"
"v_cvt_pk_bf16_f32 v122, v48, v49\n\t"
"v_cvt_pk_bf16_f32 v123, v50, v51\n\t"
"ds_write_b64 v206, v[122:123] offset:512\n\t"
"v_pk_mul_f32 v[68:69], v[68:69], v[208:209]\n\t"
"v_pk_mul_f32 v[70:71], v[70:71], v[208:209]\n\t"
"v_cvt_pk_bf16_f32 v124, v68, v69\n\t"
"v_cvt_pk_bf16_f32 v125, v70, v71\n\t"
"ds_write_b64 v206, v[124:125] offset:40960\n\t"
"v_pk_mul_f32 v[88:89], v[88:89], v[208:209]\n\t"
"v_pk_mul_f32 v[90:91], v[90:91], v[208:209]\n\t"
"v_cvt_pk_bf16_f32 v126, v88, v89\n\t"
"v_cvt_pk_bf16_f32 v127, v90, v91\n\t"
"ds_write_b64 v206, v[126:127] offset:41472\n\t"
"v_pk_mul_f32 v[32:33], v[32:33], v[208:209]\n\t"
"v_pk_mul_f32 v[34:35], v[34:35], v[208:209]\n\t"
"v_cvt_pk_bf16_f32 v96, v32, v33\n\t"
"v_cvt_pk_bf16_f32 v97, v34, v35\n\t"
"ds_write_b64 v207, v[96:97] offset:0\n\t"
"v_pk_mul_f32 v[52:53], v[52:53], v[208:209]\n\t"
"v_pk_mul_f32 v[54:55], v[54:55], v[208:209]\n\t"
"v_cvt_pk_bf16_f32 v98, v52, v53\n\t"
"v_cvt_pk_bf16_f32 v99, v54, v55\n\t"
"ds_write_b64 v207, v[98:99] offset:512\n\t"
"v_pk_mul_f32 v[72:73], v[72:73], v[208:209]\n\t"
"v_pk_mul_f32 v[74:75], v[74:75], v[208:209]\n\t"
"v_cvt_pk_bf16_f32 v100, v72, v73\n\t"
"v_cvt_pk_bf16_f32 v101, v74, v75\n\t"
"ds_write_b64 v207, v[100:101] offset:40960\n\t"
"v_pk_mul_f32 v[92:93], v[92:93], v[208:209]\n\t"
"v_pk_mul_f32 v[94:95], v[94:95], v[208:209]\n\t"
"v_cvt_pk_bf16_f32 v102, v92, v93\n\t"
"v_cvt_pk_bf16_f32 v103, v94, v95\n\t"
"ds_write_b64 v207, v[102:103] offset:41472\n\t"
"s_and_b32 s76, s76, 32767\n\t"
"v_readfirstlane_b32 s78, v254\n\t"
"s_nop 0\n\t"
"s_cmp_eq_u32 s8, 0\n\t"
"s_cselect_b32 s97, s78, s97\n\t"
"v_and_b32 v254, 3, v1\n\t"
"v_cmp_eq_u32 vcc, 0, v254\n\t"
"v_mov_b32 v254, 1065369472\n\t"
"v_mov_b32 v255, 0\n\t"
"s_mul_i32 s86, s97, 2654435761\n\t"
"s_add_u32 s86, s86, 2135587861\n\t"
"s_lshl_b32 s86, s86, 4\n\t"
"s_xor_b32 s87, s97, 1540483477\n\t"
"s_mul_i32 s87, s87, 2246822507\n\t"
"s_add_u32 s87, s87, 668265263\n\t"
"s_or_b32 s88, s86, 15\n\t"
"s_mov_b32 s89, s87\n\t"
"s_mov_b32 s91, s87\n\t"
"s_mov_b32 s24, 729808896\n\t"
"s_mov_b32 s25, 671088640\n\t"
"v_cndmask_b32 v216, v255, v254, vcc\n\t"
"v_cndmask_b32 v217, v255, v254, vcc\n\t"
"v_cndmask_b32 v218, v255, v254, vcc\n\t"
"v_cndmask_b32 v219, v255, v254, vcc\n\t"
"s_waitcnt lgkmcnt(0)\n\t"
"s_barrier\n\t"
"s_cmp_lg_u32 s8, 0\n\t"
"s_cbranch_scc1 LSAFE_%=\n\t"
"ds_read_b128 v[168:171], v246 offset:0\n\t"
"ds_read_b128 v[172:175], v246 offset:40960\n\t"
"ds_read_b128 v[176:179], v247 offset:0\n\t"
"ds_read_b128 v[180:183], v247 offset:40960\n\t"
"s_waitcnt lgkmcnt(3)\n\t"
"v_mfma_f32_16x16x32_bf16 v[200:203], a[0:3], v[168:171], 0\n\t"
"s_waitcnt lgkmcnt(2)\n\t"
"v_mfma_f32_16x16x32_bf16 v[200:203], a[4:7], v[172:175], v[200:203]\n\t"
"s_waitcnt lgkmcnt(1)\n\t"
"v_mfma_f32_16x16x32_bf16 v[200:203], a[8:11], v[176:179], v[200:203]\n\t"
"s_waitcnt lgkmcnt(0)\n\t"
"ds_read_b64_tr_b16 a[128:129], v236 offset:36864\n\t"
"v_mfma_f32_16x16x32_bf16 v[200:203], a[12:15], v[180:183], v[200:203]\n\t"
"v_mfma_f32_16x16x32_bf16 v[204:207], a[16:19], v[168:171], 0\n\t"
"ds_read_b64_tr_b16 a[192:193], v237 offset:36864\n\t"
"v_mfma_f32_16x16x32_bf16 v[204:207], a[20:23], v[172:175], v[204:207]\n\t"
"v_mfma_f32_16x16x32_bf16 v[204:207], a[24:27], v[176:179], v[204:207]\n\t"
"ds_read_b64_tr_b16 a[144:145], v238 offset:36864\n\t"
"v_mfma_f32_16x16x32_bf16 v[204:207], a[28:31], v[180:183], v[204:207]\n\t"
"v_mfma_f32_16x16x32_bf16 v[208:211], a[32:35], v[168:171], 0\n\t"
"ds_read_b64_tr_b16 a[208:209], v239 offset:36864\n\t"
"v_exp_f32 v200, v200\n\t"
"v_mfma_f32_16x16x32_bf16 v[208:211], a[36:39], v[172:175], v[208:211]\n\t"
"v_exp_f32 v201, v201\n\t"
"v_mfma_f32_16x16x32_bf16 v[208:211], a[40:43], v[176:179], v[208:211]\n\t"
"ds_read_b64_tr_b16 a[160:161], v240 offset:36864\n\t"
"v_exp_f32 v202, v202\n\t"
"v_mfma_f32_16x16x32_bf16 v[208:211], a[44:47], v[180:183], v[208:211]\n\t"
"v_exp_f32 v203, v203\n\t"
"v_cvt_pk_bf16_f32 v16, v200, v201\n\t"
"v_cvt_pk_bf16_f32 v17, v202, v203\n\t"
"v_mfma_f32_16x16x32_bf16 v[200:203], a[48:51], v[168:171], 0\n\t"
"ds_read_b128 v[184:187], v246 offset:1024\n\t"
"v_exp_f32 v204, v204\n\t"
"v_mfma_f32_16x16x32_bf16 v[200:203], a[52:55], v[172:175], v[200:203]\n\t"
"ds_read_b64_tr_b16 a[224:225], v241 offset:36864\n\t"
"v_exp_f32 v205, v205\n\t"
"v_mfma_f32_16x16x32_bf16 v[200:203], a[56:59], v[176:179], v[200:203]\n\t"
"ds_read_b64_tr_b16 a[176:177], v242 offset:36864\n\t"
"v_exp_f32 v206, v206\n\t"
"v_mfma_f32_16x16x32_bf16 v[200:203], a[60:63], v[180:183], v[200:203]\n\t"
"v_exp_f32 v207, v207\n\t"
"v_cvt_pk_bf16_f32 v18, v204, v205\n\t"
"v_cvt_pk_bf16_f32 v19, v206, v207\n\t"
"v_mfma_f32_16x16x32_bf16 v[204:207], a[64:67], v[168:171], 0\n\t"
"ds_read_b128 v[188:191], v246 offset:41984\n\t"
"v_exp_f32 v208, v208\n\t"
"v_mfma_f32_16x16x32_bf16 v[204:207], a[68:71], v[172:175], v[204:207]\n\t"
"ds_read_b64_tr_b16 a[240:241], v243 offset:36864\n\t"
"v_exp_f32 v209, v209\n\t"
"v_mfma_f32_16x16x32_bf16 v[204:207], a[72:75], v[176:179], v[204:207]\n\t"
"ds_read_b64_tr_b16 a[130:131], v236 offset:28672\n\t"
"v_exp_f32 v210, v210\n\t"
"v_mfma_f32_16x16x32_bf16 v[204:207], a[76:79], v[180:183], v[204:207]\n\t"
"v_exp_f32 v211, v211\n\t"
"v_mfma_f32_16x16x32_bf16 v[212:215], v[216:219], v[16:19], 0\n\t"
"v_cvt_pk_bf16_f32 v20, v208, v209\n\t"
"v_cvt_pk_bf16_f32 v21, v210, v211\n\t"
"v_mfma_f32_16x16x32_bf16 v[208:211], a[80:83], v[168:171], 0\n\t"
"ds_read_b128 v[192:195], v247 offset:1024\n\t"
"v_exp_f32 v200, v200\n\t"
"v_mfma_f32_16x16x32_bf16 v[208:211], a[84:87], v[172:175], v[208:211]\n\t"
"ds_read_b64_tr_b16 a[194:195], v237 offset:28672\n\t"
"v_exp_f32 v201, v201\n\t"
"v_mfma_f32_16x16x32_bf16 v[208:211], a[88:91], v[176:179], v[208:211]\n\t"
"ds_read_b64_tr_b16 a[146:147], v238 offset:28672\n\t"
"v_exp_f32 v202, v202\n\t"
"v_mfma_f32_16x16x32_bf16 v[208:211], a[92:95], v[180:183], v[208:211]\n\t"
"v_exp_f32 v203, v203\n\t"
"v_cvt_pk_bf16_f32 v22, v200, v201\n\t"
"v_cvt_pk_bf16_f32 v23, v202, v203\n\t"
"v_mfma_f32_16x16x32_bf16 v[200:203], a[96:99], v[168:171], 0\n\t"
"ds_read_b128 v[196:199], v247 offset:41984\n\t"
"v_exp_f32 v204, v204\n\t"
"v_mfma_f32_16x16x32_bf16 v[200:203], a[100:103], v[172:175], v[200:203]\n\t"
"ds_read_b64_tr_b16 a[210:211], v239 offset:28672\n\t"
"v_exp_f32 v205, v205\n\t"
"v_mfma_f32_16x16x32_bf16 v[200:203], a[104:107], v[176:179], v[200:203]\n\t"
"ds_read_b64_tr_b16 a[162:163], v240 offset:28672\n\t"
"v_exp_f32 v206, v206\n\t"
"v_mfma_f32_16x16x32_bf16 v[200:203], a[108:111], v[180:183], v[200:203]\n\t"
"v_exp_f32 v207, v207\n\t"
"v_cvt_pk_bf16_f32 v24, v204, v205\n\t"
"v_cvt_pk_bf16_f32 v25, v206, v207\n\t"
"v_mfma_f32_16x16x32_bf16 v[212:215], v[216:219], v[20:23], v[212:215]\n\t"
"s_add_u32 m0, s72, 20480\n\t"
"ds_read_b64_tr_b16 a[226:227], v241 offset:28672\n\t"
"v_exp_f32 v208, v208\n\t"
"v_mfma_f32_16x16x32_bf16 v[204:207], a[112:115], v[168:171], 0\n\t"
"ds_read_b64_tr_b16 a[178:179], v242 offset:28672\n\t"
"buffer_load_dwordx4 v244, s[48:51], s76 offen nt lds\n\t"
"v_mfma_f32_16x16x32_bf16 v[204:207], a[116:119], v[172:175], v[204:207]\n\t"
"v_exp_f32 v209, v209\n\t"
"v_mfma_f32_16x16x32_bf16 v[204:207], a[120:123], v[176:179], v[204:207]\n\t"
"ds_read_b64_tr_b16 a[242:243], v243 offset:28672\n\t"
"v_exp_f32 v210, v210\n\t"
"v_mfma_f32_16x16x32_bf16 v[204:207], a[124:127], v[180:183], v[204:207]\n\t"
"s_waitcnt lgkmcnt(0)\n\t"
"s_barrier\n\t"
"v_exp_f32 v211, v211\n\t"
"v_cvt_pk_bf16_f32 v26, v208, v209\n\t"
"v_cvt_pk_bf16_f32 v27, v210, v211\n\t"
"v_mfma_f32_16x16x32_bf16 v[208:211], a[0:3], v[184:187], 0\n\t"
"v_exp_f32 v200, v200\n\t"
"v_mfma_f32_16x16x32_bf16 v[208:211], a[4:7], v[188:191], v[208:211]\n\t"
"v_exp_f32 v201, v201\n\t"
"v_mfma_f32_16x16x32_bf16 v[208:211], a[8:11], v[192:195], v[208:211]\n\t"
"v_exp_f32 v202, v202\n\t"
"v_mfma_f32_16x16x32_bf16 v[208:211], a[12:15], v[196:199], v[208:211]\n\t"
"v_exp_f32 v203, v203\n\t"
"v_cvt_pk_bf16_f32 v28, v200, v201\n\t"
"v_cvt_pk_bf16_f32 v29, v202, v203\n\t"
"v_mfma_f32_16x16x32_bf16 v[212:215], v[216:219], v[24:27], v[212:215]\n\t"
"buffer_load_dwordx4 v245, s[48:51], s76 offen offset:1024 nt lds\n\t"
"v_mfma_f32_16x16x32_bf16 v[200:203], a[16:19], v[184:187], 0\n\t"
"v_exp_f32 v204, v204\n\t"
"v_mfma_f32_16x16x32_bf16 v[200:203], a[20:23], v[188:191], v[200:203]\n\t"
"v_exp_f32 v205, v205\n\t"
"v_mfma_f32_16x16x32_bf16 v[200:203], a[24:27], v[192:195], v[200:203]\n\t"
"v_exp_f32 v206, v206\n\t"
"v_mov_b32 v4, s86\n\t"
"v_mfma_f32_16x16x32_bf16 v[200:203], a[28:31], v[196:199], v[200:203]\n\t"
"v_exp_f32 v207, v207\n\t"
"v_cvt_pk_bf16_f32 v30, v204, v205\n\t"
"v_cvt_pk_bf16_f32 v31, v206, v207\n\t"
"v_mfma_f32_16x16x32_bf16 v[204:207], a[32:35], v[184:187], 0\n\t"
"v_exp_f32 v208, v208\n\t"
"v_mfma_f32_16x16x32_bf16 v[204:207], a[36:39], v[188:191], v[204:207]\n\t"
"v_exp_f32 v209, v209\n\t"
"v_mfma_f32_16x16x32_bf16 v[204:207], a[40:43], v[192:195], v[204:207]\n\t"
"v_exp_f32 v210, v210\n\t"
"v_mfma_f32_16x16x32_bf16 v[204:207], a[44:47], v[196:199], v[204:207]\n\t"
"v_exp_f32 v211, v211\n\t"
"v_cvt_pk_bf16_f32 v32, v208, v209\n\t"
"v_cvt_pk_bf16_f32 v33, v210, v211\n\t"
"v_mfma_f32_16x16x32_bf16 v[212:215], v[216:219], v[28:31], v[212:215]\n\t"
"buffer_load_dwordx4 v14, s[48:51], s76 offen offset:2048 nt lds\n\t"
"v_mfma_f32_16x16x32_bf16 v[208:211], a[48:51], v[184:187], 0\n\t"
"ds_read_b128 v[168:171], v246 offset:2048\n\t"
"v_exp_f32 v200, v200\n\t"
"v_mfma_f32_16x16x32_bf16 v[208:211], a[52:55], v[188:191], v[208:211]\n\t"
"v_exp_f32 v201, v201\n\t"
"v_mfma_f32_16x16x32_bf16 v[208:211], a[56:59], v[192:195], v[208:211]\n\t"
"v_exp_f32 v202, v202\n\t"
"v_mfma_f32_16x16x32_bf16 v[208:211], a[60:63], v[196:199], v[208:211]\n\t"
"v_exp_f32 v203, v203\n\t"
"v_cvt_pk_bf16_f32 v34, v200, v201\n\t"
"v_cvt_pk_bf16_f32 v35, v202, v203\n\t"
"v_mfma_f32_16x16x32_bf16 v[200:203], a[64:67], v[184:187], 0\n\t"
"ds_read_b128 v[172:175], v246 offset:43008\n\t"
"v_exp_f32 v204, v204\n\t"
"v_mfma_f32_16x16x32_bf16 v[200:203], a[68:71], v[188:191], v[200:203]\n\t"
"ds_write_b32 v248, v212 offset:0\n\t"
"v_mfma_f32_16x16x32_bf16 v[200:203], a[72:75], v[192:195], v[200:203]\n\t"
"buffer_load_dwordx4 v251, s[48:51], s76 offen offset:3072 nt lds\n\t"
"v_mfma_f32_16x16x32_bf16 v[200:203], a[76:79], v[196:199], v[200:203]\n\t"
"v_exp_f32 v205, v205\n\t"
"v_exp_f32 v206, v206\n\t"
"v_exp_f32 v207, v207\n\t"
"v_cvt_pk_bf16_f32 v36, v204, v205\n\t"
"v_cvt_pk_bf16_f32 v37, v206, v207\n\t"
"v_mfma_f32_16x16x32_bf16 v[212:215], v[216:219], v[32:35], 0\n\t"
"s_add_u32 s76, s76, 4096\n\t"
"ds_read_b128 v[176:179], v247 offset:2048\n\t"
"v_exp_f32 v208, v208\n\t"
"v_mfma_f32_16x16x32_bf16 v[204:207], a[80:83], v[184:187], 0\n\t"
"s_and_b32 s76, s76, 32767\n\t"
"v_exp_f32 v209, v209\n\t"
"v_mfma_f32_16x16x32_bf16 v[204:207], a[84:87], v[188:191], v[204:207]\n\t"
"v_exp_f32 v210, v210\n\t"
"v_mfma_f32_16x16x32_bf16 v[204:207], a[88:91], v[192:195], v[204:207]\n\t"
"ds_read_b128 v[180:183], v247 offset:43008\n\t"
"v_exp_f32 v211, v211\n\t"
"v_mfma_f32_16x16x32_bf16 v[204:207], a[92:95], v[196:199], v[204:207]\n\t"
"v_cvt_pk_bf16_f32 v38, v208, v209\n\t"
"v_cvt_pk_bf16_f32 v39, v210, v211\n\t"
"v_mfma_f32_16x16x32_bf16 v[208:211], a[96:99], v[184:187], 0\n\t"
"s_add_u32 m0, s72, 36864\n\t"
"v_exp_f32 v200, v200\n\t"
"v_mfma_f32_16x16x32_bf16 v[208:211], a[100:103], v[188:191], v[208:211]\n\t"
"buffer_load_dwordx4 v244, s[48:51], s76 offen nt lds\n\t"
"v_mov_b32 v229, s87\n\t"
"v_mfma_f32_16x16x32_bf16 v[208:211], a[104:107], v[192:195], v[208:211]\n\t"
"v_exp_f32 v201, v201\n\t"
"v_mfma_f32_16x16x32_bf16 v[208:211], a[108:111], v[196:199], v[208:211]\n\t"
"v_exp_f32 v202, v202\n\t"
"v_exp_f32 v203, v203\n\t"
"v_cvt_pk_bf16_f32 v40, v200, v201\n\t"
"v_cvt_pk_bf16_f32 v41, v202, v203\n\t"
"v_mfma_f32_16x16x32_bf16 v[212:215], v[216:219], v[36:39], v[212:215]\n\t"
"v_exp_f32 v204, v204\n\t"
"v_mfma_f32_16x16x32_bf16 v[200:203], a[112:115], v[184:187], 0\n\t"
"v_exp_f32 v205, v205\n\t"
"v_mfma_f32_16x16x32_bf16 v[200:203], a[116:119], v[188:191], v[200:203]\n\t"
"v_exp_f32 v206, v206\n\t"
"v_mfma_f32_16x16x32_bf16 v[200:203], a[120:123], v[192:195], v[200:203]\n\t"
"v_exp_f32 v207, v207\n\t"
"v_cvt_pk_bf16_f32 v42, v204, v205\n\t"
"v_mfma_f32_16x16x32_bf16 v[200:203], a[124:127], v[196:199], v[200:203]\n\t"
"s_waitcnt lgkmcnt(2)\n\t"
"s_barrier\n\t"
"v_cvt_pk_bf16_f32 v43, v206, v207\n\t"
"v_exp_f32 v208, v208\n\t"
"v_mfma_f32_16x16x32_bf16 v[204:207], a[0:3], v[168:171], 0\n\t"
"buffer_load_dwordx4 v245, s[48:51], s76 offen offset:1024 nt lds\n\t"
"v_mfma_f32_16x16x32_bf16 v[204:207], a[4:7], v[172:175], v[204:207]\n\t"
"s_waitcnt lgkmcnt(1)\n\t"
"v_exp_f32 v209, v209\n\t"
"v_mfma_f32_16x16x32_bf16 v[204:207], a[8:11], v[176:179], v[204:207]\n\t"
"s_waitcnt lgkmcnt(0)\n\t"
"v_exp_f32 v210, v210\n\t"
"v_mov_b32 v5, 1073741824\n\t"
"v_mfma_f32_16x16x32_bf16 v[204:207], a[12:15], v[180:183], v[204:207]\n\t"
"v_exp_f32 v211, v211\n\t"
"v_cvt_pk_bf16_f32 v44, v208, v209\n\t"
"v_cvt_pk_bf16_f32 v45, v210, v211\n\t"
"v_mfma_f32_16x16x32_bf16 v[212:215], v[216:219], v[40:43], v[212:215]\n\t"
"v_exp_f32 v200, v200\n\t"
"v_mfma_f32_16x16x32_bf16 v[208:211], a[16:19], v[168:171], 0\n\t"
"v_exp_f32 v201, v201\n\t"
"v_mfma_f32_16x16x32_bf16 v[208:211], a[20:23], v[172:175], v[208:211]\n\t"
"v_exp_f32 v202, v202\n\t"
"v_mfma_f32_16x16x32_bf16 v[208:211], a[24:27], v[176:179], v[208:211]\n\t"
"v_exp_f32 v203, v203\n\t"
"v_cvt_pk_bf16_f32 v46, v200, v201\n\t"
"v_mfma_f32_16x16x32_bf16 v[208:211], a[28:31], v[180:183], v[208:211]\n\t"
"v_cvt_pk_bf16_f32 v47, v202, v203\n\t"
"buffer_load_dwordx4 v14, s[48:51], s76 offen offset:2048 nt lds\n\t"
"v_mfma_f32_16x16x32_bf16 v[200:203], a[32:35], v[168:171], 0\n\t"
"v_exp_f32 v204, v204\n\t"
"v_mfma_f32_16x16x32_bf16 v[200:203], a[36:39], v[172:175], v[200:203]\n\t"
"v_exp_f32 v205, v205\n\t"
"v_mfma_f32_16x16x32_bf16 v[200:203], a[40:43], v[176:179], v[200:203]\n\t"
"v_exp_f32 v206, v206\n\t"
"v_writelane_b32 v5, 0, 0\n\t"
"v_mfma_f32_16x16x32_bf16 v[200:203], a[44:47], v[180:183], v[200:203]\n\t"
"v_exp_f32 v207, v207\n\t"
"v_cvt_pk_bf16_f32 v48, v204, v205\n\t"
"v_cvt_pk_bf16_f32 v49, v206, v207\n\t"
"v_mfma_f32_16x16x32_bf16 v[212:215], v[216:219], v[44:47], v[212:215]\n\t"
"s_add_u32 s81, s26, 0\n\t"
"s_and_b32 s81, s81, 7\n\t"
"ds_read_b128 v[184:187], v246 offset:3072\n\t"
"v_exp_f32 v208, v208\n\t"
"v_mfma_f32_16x16x32_bf16 v[204:207], a[48:51], v[168:171], 0\n\t"
"s_lshl_b32 s81, s81, 10\n\t"
"v_exp_f32 v209, v209\n\t"
"v_mfma_f32_16x16x32_bf16 v[204:207], a[52:55], v[172:175], v[204:207]\n\t"
"ds_write_b32 v248, v212 offset:256\n\t"
"v_mfma_f32_16x16x32_bf16 v[204:207], a[56:59], v[176:179], v[204:207]\n\t"
"buffer_load_dwordx4 v251, s[48:51], s76 offen offset:3072 nt lds\n\t"
"v_mfma_f32_16x16x32_bf16 v[204:207], a[60:63], v[180:183], v[204:207]\n\t"
"v_exp_f32 v210, v210\n\t"
"v_exp_f32 v211, v211\n\t"
"v_cvt_pk_bf16_f32 v50, v208, v209\n\t"
"v_cvt_pk_bf16_f32 v51, v210, v211\n\t"
"v_mfma_f32_16x16x32_bf16 v[208:211], a[64:67], v[168:171], 0\n\t"
"s_add_u32 s76, s76, 4096\n\t"
"ds_read_b128 v[188:191], v246 offset:44032\n\t"
"v_exp_f32 v200, v200\n\t"
"v_mfma_f32_16x16x32_bf16 v[208:211], a[68:71], v[172:175], v[208:211]\n\t"
"s_and_b32 s76, s76, 32767\n\t"
"v_exp_f32 v201, v201\n\t"
"v_mfma_f32_16x16x32_bf16 v[208:211], a[72:75], v[176:179], v[208:211]\n\t"
"v_exp_f32 v202, v202\n\t"
"v_mfma_f32_16x16x32_bf16 v[208:211], a[76:79], v[180:183], v[208:211]\n\t"
"v_exp_f32 v203, v203\n\t"
"v_cvt_pk_bf16_f32 v52, v200, v201\n\t"
"v_cvt_pk_bf16_f32 v53, v202, v203\n\t"
"v_mfma_f32_16x16x32_bf16 v[212:215], v[216:219], v[48:51], 0\n\t"
"ds_read_b128 v[192:195], v247 offset:3072\n\t"
"v_exp_f32 v204, v204\n\t"
"v_mfma_f32_16x16x32_bf16 v[200:203], a[80:83], v[168:171], 0\n\t"
"v_exp_f32 v205, v205\n\t"
"v_mfma_f32_16x16x32_bf16 v[200:203], a[84:87], v[172:175], v[200:203]\n\t"
"v_exp_f32 v206, v206\n\t"
"v_mfma_f32_16x16x32_bf16 v[200:203], a[88:91], v[176:179], v[200:203]\n\t"
"ds_read_b128 v[196:199], v247 offset:44032\n\t"
"v_exp_f32 v207, v207\n\t"
"v_mfma_f32_16x16x32_bf16 v[200:203], a[92:95], v[180:183], v[200:203]\n\t"
"v_cvt_pk_bf16_f32 v54, v204, v205\n\t"
"v_cvt_pk_bf16_f32 v55, v206, v207\n\t"
"v_mfma_f32_16x16x32_bf16 v[204:207], a[96:99], v[168:171], 0\n\t"
"v_exp_f32 v208, v208\n\t"
"v_mfma_f32_16x16x32_bf16 v[204:207], a[100:103], v[172:175], v[204:207]\n\t"
"v_exp_f32 v209, v209\n\t"
"v_add_u32 v232, s81, v250\n\t"
"v_mfma_f32_16x16x32_bf16 v[204:207], a[104:107], v[176:179], v[204:207]\n\t"
"s_add_u32 s81, s26, 2\n\t"
"s_and_b32 s81, s81, 7\n\t"
"v_exp_f32 v210, v210\n\t"
"v_mfma_f32_16x16x32_bf16 v[204:207], a[108:111], v[180:183], v[204:207]\n\t"
"v_exp_f32 v211, v211\n\t"
"v_cvt_pk_bf16_f32 v56, v208, v209\n\t"
"v_cvt_pk_bf16_f32 v57, v210, v211\n\t"
"v_mfma_f32_16x16x32_bf16 v[212:215], v[216:219], v[52:55], v[212:215]\n\t"
"s_lshl_b32 s81, s81, 10\n\t"
"v_exp_f32 v200, v200\n\t"
"v_mfma_f32_16x16x32_bf16 v[208:211], a[112:115], v[168:171], 0\n\t"
"v_exp_f32 v201, v201\n\t"
"v_mfma_f32_16x16x32_bf16 v[208:211], a[116:119], v[172:175], v[208:211]\n\t"
"v_exp_f32 v202, v202\n\t"
"v_mfma_f32_16x16x32_bf16 v[208:211], a[120:123], v[176:179], v[208:211]\n\t"
"v_exp_f32 v203, v203\n\t"
"v_cvt_pk_bf16_f32 v58, v200, v201\n\t"
"v_mfma_f32_16x16x32_bf16 v[208:211], a[124:127], v[180:183], v[208:211]\n\t"
"s_waitcnt vmcnt(4) lgkmcnt(0)\n\t"
"s_barrier\n\t"
"v_cvt_pk_bf16_f32 v59, v202, v203\n\t"
"v_exp_f32 v204, v204\n\t"
"v_mfma_f32_16x16x32_bf16 v[200:203], a[0:3], v[184:187], 0\n\t"
"v_exp_f32 v205, v205\n\t"
"v_mfma_f32_16x16x32_bf16 v[200:203], a[4:7], v[188:191], v[200:203]\n\t"
"ds_read_b64_tr_b16 a[132:133], v236 offset:32768\n\t"
"v_exp_f32 v206, v206\n\t"
"v_mfma_f32_16x16x32_bf16 v[200:203], a[8:11], v[192:195], v[200:203]\n\t"
"v_exp_f32 v207, v207\n\t"
"v_mfma_f32_16x16x32_bf16 v[200:203], a[12:15], v[196:199], v[200:203]\n\t"
"v_cvt_pk_bf16_f32 v60, v204, v205\n\t"
"v_cvt_pk_bf16_f32 v61, v206, v207\n\t"
"v_mfma_f32_16x16x32_bf16 v[212:215], v[216:219], v[56:59], v[212:215]\n\t"
"ds_read_b64_tr_b16 a[196:197], v237 offset:32768\n\t"
"v_exp_f32 v208, v208\n\t"
"v_mfma_f32_16x16x32_bf16 v[204:207], a[16:19], v[184:187], 0\n\t"
"ds_read_b64_tr_b16 a[148:149], v238 offset:32768\n\t"
"v_exp_f32 v209, v209\n\t"
"v_mfma_f32_16x16x32_bf16 v[204:207], a[20:23], v[188:191], v[204:207]\n\t"
"v_exp_f32 v210, v210\n\t"
"v_mfma_f32_16x16x32_bf16 v[204:207], a[24:27], v[192:195], v[204:207]\n\t"
"ds_read_b64_tr_b16 a[212:213], v239 offset:32768\n\t"
"v_exp_f32 v211, v211\n\t"
"v_mfma_f32_16x16x32_bf16 v[204:207], a[28:31], v[196:199], v[204:207]\n\t"
"v_cvt_pk_bf16_f32 v62, v208, v209\n\t"
"v_cvt_pk_bf16_f32 v63, v210, v211\n\t"
"v_mfma_f32_16x16x32_bf16 v[208:211], a[32:35], v[184:187], 0\n\t"
"ds_read_b64_tr_b16 a[164:165], v240 offset:32768\n\t"
"v_exp_f32 v200, v200\n\t"
"v_mfma_f32_16x16x32_bf16 v[208:211], a[36:39], v[188:191], v[208:211]\n\t"
"v_exp_f32 v201, v201\n\t"
"v_mfma_f32_16x16x32_bf16 v[208:211], a[40:43], v[192:195], v[208:211]\n\t"
"ds_read_b64_tr_b16 a[228:229], v241 offset:32768\n\t"
"v_exp_f32 v202, v202\n\t"
"v_mfma_f32_16x16x32_bf16 v[208:211], a[44:47], v[196:199], v[208:211]\n\t"
"v_exp_f32 v203, v203\n\t"
"v_cvt_pk_bf16_f32 v64, v200, v201\n\t"
"v_cvt_pk_bf16_f32 v65, v202, v203\n\t"
"v_mfma_f32_16x16x32_bf16 v[212:215], v[216:219], v[60:63], v[212:215]\n\t"
"ds_read_b128 v[168:171], v246 offset:4096\n\t"
"v_exp_f32 v204, v204\n\t"
"v_mfma_f32_16x16x32_bf16 v[200:203], a[48:51], v[184:187], 0\n\t"
"ds_read_b64_tr_b16 a[180:181], v242 offset:32768\n\t"
"v_exp_f32 v205, v205\n\t"
"v_mfma_f32_16x16x32_bf16 v[200:203], a[52:55], v[188:191], v[200:203]\n\t"
"ds_read_b64_tr_b16 a[244:245], v243 offset:32768\n\t"
"v_exp_f32 v206, v206\n\t"
"v_mfma_f32_16x16x32_bf16 v[200:203], a[56:59], v[192:195], v[200:203]\n\t"
"ds_write_b32 v248, v212 offset:512\n\t"
"v_mfma_f32_16x16x32_bf16 v[200:203], a[60:63], v[196:199], v[200:203]\n\t"
"v_exp_f32 v207, v207\n\t"
"v_cvt_pk_bf16_f32 v66, v204, v205\n\t"
"v_cvt_pk_bf16_f32 v67, v206, v207\n\t"
"v_mfma_f32_16x16x32_bf16 v[204:207], a[64:67], v[184:187], 0\n\t"
"v_add_u32 v233, s81, v250\n\t"
"s_add_u32 s81, s26, 4\n\t"
"s_and_b32 s81, s81, 7\n\t"
"s_lshl_b32 s81, s81, 10\n\t"
"v_add_u32 v234, s81, v250\n\t"
"s_add_u32 s81, s26, 6\n\t"
"s_and_b32 s81, s81, 7\n\t"
"s_lshl_b32 s81, s81, 10\n\t"
"v_add_u32 v235, s81, v250\n\t"
"v_mfma_f32_16x16x32_bf16 v[204:207], a[68:71], v[188:191], v[204:207]\n\t"
"ds_read_b128 v[172:175], v246 offset:45056\n\t"
"v_exp_f32 v208, v208\n\t"
"v_mfma_f32_16x16x32_bf16 v[204:207], a[72:75], v[192:195], v[204:207]\n\t"
"ds_read_b64_tr_b16 a[134:135], v236 offset:12288\n\t"
"v_exp_f32 v209, v209\n\t"
"v_mfma_f32_16x16x32_bf16 v[204:207], a[76:79], v[196:199], v[204:207]\n\t"
"v_exp_f32 v210, v210\n\t"
"v_exp_f32 v211, v211\n\t"
"v_cvt_pk_bf16_f32 v68, v208, v209\n\t"
"v_cvt_pk_bf16_f32 v69, v210, v211\n\t"
"v_mfma_f32_16x16x32_bf16 v[212:215], v[216:219], v[64:67], 0\n\t"
"ds_read_b64_tr_b16 a[198:199], v237 offset:12288\n\t"
"v_exp_f32 v200, v200\n\t"
"v_mfma_f32_16x16x32_bf16 v[208:211], a[80:83], v[184:187], 0\n\t"
"ds_read_b128 v[176:179], v247 offset:4096\n\t"
"v_exp_f32 v201, v201\n\t"
"v_mfma_f32_16x16x32_bf16 v[208:211], a[84:87], v[188:191], v[208:211]\n\t"
"ds_read_b64_tr_b16 a[150:151], v238 offset:12288\n\t"
"v_exp_f32 v202, v202\n\t"
"v_mfma_f32_16x16x32_bf16 v[208:211], a[88:91], v[192:195], v[208:211]\n\t"
"ds_read_b64_tr_b16 a[214:215], v239 offset:12288\n\t"
"v_exp_f32 v203, v203\n\t"
"v_mfma_f32_16x16x32_bf16 v[208:211], a[92:95], v[196:199], v[208:211]\n\t"
"v_cvt_pk_bf16_f32 v70, v200, v201\n\t"
"v_cvt_pk_bf16_f32 v71, v202, v203\n\t"
"v_mfma_f32_16x16x32_bf16 v[200:203], a[96:99], v[184:187], 0\n\t"
"ds_read_b128 v[180:183], v247 offset:45056\n\t"
"v_exp_f32 v204, v204\n\t"
"v_mfma_f32_16x16x32_bf16 v[200:203], a[100:103], v[188:191], v[200:203]\n\t"
"ds_read_b64_tr_b16 a[166:167], v240 offset:12288\n\t"
"v_exp_f32 v205, v205\n\t"
"v_mfma_f32_16x16x32_bf16 v[200:203], a[104:107], v[192:195], v[200:203]\n\t"
"ds_read_b64_tr_b16 a[230:231], v241 offset:12288\n\t"
"v_exp_f32 v206, v206\n\t"
"v_mfma_f32_16x16x32_bf16 v[200:203], a[108:111], v[196:199], v[200:203]\n\t"
"v_exp_f32 v207, v207\n\t"
"v_cvt_pk_bf16_f32 v72, v204, v205\n\t"
"v_cvt_pk_bf16_f32 v73, v206, v207\n\t"
"v_mfma_f32_16x16x32_bf16 v[212:215], v[216:219], v[68:71], v[212:215]\n\t"
"ds_read_b64_tr_b16 a[182:183], v242 offset:12288\n\t"
"v_exp_f32 v208, v208\n\t"
"v_mfma_f32_16x16x32_bf16 v[204:207], a[112:115], v[184:187], 0\n\t"
"ds_read_b64_tr_b16 a[246:247], v243 offset:12288\n\t"
"v_exp_f32 v209, v209\n\t"
"v_mfma_f32_16x16x32_bf16 v[204:207], a[116:119], v[188:191], v[204:207]\n\t"
"ds_read_b64_tr_b16 a[136:137], v236 offset:16384\n\t"
"v_exp_f32 v210, v210\n\t"
"v_mfma_f32_16x16x32_bf16 v[204:207], a[120:123], v[192:195], v[204:207]\n\t"
"v_exp_f32 v211, v211\n\t"
"v_mfma_f32_16x16x32_bf16 v[204:207], a[124:127], v[196:199], v[204:207]\n\t"
"ds_read_b64_tr_b16 a[200:201], v237 offset:16384\n\t"
"ds_read_b64_tr_b16 a[152:153], v238 offset:16384\n\t"
"ds_read_b64_tr_b16 a[216:217], v239 offset:16384\n\t"
"ds_read_b64_tr_b16 a[168:169], v240 offset:16384\n\t"
"ds_read_b64_tr_b16 a[232:233], v241 offset:16384\n\t"
"ds_read_b64_tr_b16 a[184:185], v242 offset:16384\n\t"
"ds_read_b64_tr_b16 a[248:249], v243 offset:16384\n\t"
"ds_read_b64_tr_b16 a[138:139], v236 offset:24576\n\t"
"ds_read_b64_tr_b16 a[202:203], v237 offset:24576\n\t"
"ds_read_b64_tr_b16 a[154:155], v238 offset:24576\n\t"
"ds_read_b64_tr_b16 a[218:219], v239 offset:24576\n\t"
"ds_read_b64_tr_b16 a[170:171], v240 offset:24576\n\t"
"ds_read_b64_tr_b16 a[234:235], v241 offset:24576\n\t"
"ds_read_b64_tr_b16 a[186:187], v242 offset:24576\n\t"
"ds_read_b64_tr_b16 a[250:251], v243 offset:24576\n\t"
"ds_read_b64_tr_b16 a[140:141], v236 offset:20480\n\t"
"ds_read_b64_tr_b16 a[204:205], v237 offset:20480\n\t"
"ds_read_b64_tr_b16 a[156:157], v238 offset:20480\n\t"
"ds_read_b64_tr_b16 a[220:221], v239 offset:20480\n\t"
"ds_read_b64_tr_b16 a[172:173], v240 offset:20480\n\t"
"ds_read_b64_tr_b16 a[236:237], v241 offset:20480\n\t"
"ds_read_b64_tr_b16 a[188:189], v242 offset:20480\n\t"
"ds_read_b64_tr_b16 a[252:253], v243 offset:20480\n\t"
"s_waitcnt vmcnt(0) lgkmcnt(0)\n\t"
"s_barrier\n\t"
"v_cvt_pk_bf16_f32 v74, v208, v209\n\t"
"v_cvt_pk_bf16_f32 v75, v210, v211\n\t"
"v_mfma_f32_16x16x32_bf16 v[208:211], a[0:3], v[168:171], 0\n\t"
"v_exp_f32 v200, v200\n\t"
"v_mfma_f32_16x16x32_bf16 v[208:211], a[4:7], v[172:175], v[208:211]\n\t"
"ds_read_b64_tr_b16 a[142:143], v236 offset:36864\n\t"
"v_exp_f32 v201, v201\n\t"
"v_mfma_f32_16x16x32_bf16 v[208:211], a[8:11], v[176:179], v[208:211]\n\t"
"v_exp_f32 v202, v202\n\t"
"v_mfma_f32_16x16x32_bf16 v[208:211], a[12:15], v[180:183], v[208:211]\n\t"
"v_exp_f32 v203, v203\n\t"
"v_cvt_pk_bf16_f32 v76, v200, v201\n\t"
"v_cvt_pk_bf16_f32 v77, v202, v203\n\t"
"v_mfma_f32_16x16x32_bf16 v[212:215], v[216:219], v[72:75], v[212:215]\n\t"
"ds_read_b64_tr_b16 a[206:207], v237 offset:36864\n\t"
"v_exp_f32 v204, v204\n\t"
"v_mfma_f32_16x16x32_bf16 v[200:203], a[16:19], v[168:171], 0\n\t"
"ds_read_b64_tr_b16 a[158:159], v238 offset:36864\n\t"
"v_exp_f32 v205, v205\n\t"
"v_mfma_f32_16x16x32_bf16 v[200:203], a[20:23], v[172:175], v[200:203]\n\t"
"v_exp_f32 v206, v206\n\t"
"v_mfma_f32_16x16x32_bf16 v[200:203], a[24:27], v[176:179], v[200:203]\n\t"
"ds_read_b64_tr_b16 a[222:223], v239 offset:36864\n\t"
"v_exp_f32 v207, v207\n\t"
"v_mfma_f32_16x16x32_bf16 v[200:203], a[28:31], v[180:183], v[200:203]\n\t"
"v_cvt_pk_bf16_f32 v78, v204, v205\n\t"
"v_cvt_pk_bf16_f32 v79, v206, v207\n\t"
"v_mfma_f32_16x16x32_bf16 v[204:207], a[32:35], v[168:171], 0\n\t"
"s_add_u32 m0, s72, 0\n\t"
"ds_read_b64_tr_b16 a[174:175], v240 offset:36864\n\t"
"v_exp_f32 v208, v208\n\t"
"v_mfma_f32_16x16x32_bf16 v[204:207], a[36:39], v[172:175], v[204:207]\n\t"
"buffer_load_dwordx4 v10, s[52:55], s77 offen lds\n\t"
"v_mfma_f32_16x16x32_bf16 v[204:207], a[40:43], v[176:179], v[204:207]\n\t"
"ds_read_b64_tr_b16 a[238:239], v241 offset:36864\n\t"
"v_exp_f32 v209, v209\n\t"
"v_mfma_f32_16x16x32_bf16 v[204:207], a[44:47], v[180:183], v[204:207]\n\t"
"v_exp_f32 v210, v210\n\t"
"v_exp_f32 v211, v211\n\t"
"v_cvt_pk_bf16_f32 v80, v208, v209\n\t"
"v_cvt_pk_bf16_f32 v81, v210, v211\n\t"
"v_mfma_f32_16x16x32_bf16 v[212:215], v[216:219], v[76:79], v[212:215]\n\t"
"ds_read_b128 v[184:187], v246 offset:5120\n\t"
"v_exp_f32 v200, v200\n\t"
"v_mfma_f32_16x16x32_bf16 v[208:211], a[48:51], v[168:171], 0\n\t"
"ds_read_b64_tr_b16 a[190:191], v242 offset:36864\n\t"
"v_exp_f32 v201, v201\n\t"
"v_mfma_f32_16x16x32_bf16 v[208:211], a[52:55], v[172:175], v[208:211]\n\t"
"ds_read_b64_tr_b16 a[254:255], v243 offset:36864\n\t"
"v_exp_f32 v202, v202\n\t"
"v_mfma_f32_16x16x32_bf16 v[208:211], a[56:59], v[176:179], v[208:211]\n\t"
"buffer_load_dwordx4 v11, s[52:55], s77 offen offset:1024 lds\n\t"
"v_mfma_f32_16x16x32_bf16 v[208:211], a[60:63], v[180:183], v[208:211]\n\t"
"v_exp_f32 v203, v203\n\t"
"v_cvt_pk_bf16_f32 v82, v200, v201\n\t"
"v_cvt_pk_bf16_f32 v83, v202, v203\n\t"
"v_mfma_f32_16x16x32_bf16 v[200:203], a[64:67], v[168:171], 0\n\t"
"ds_write_b128 v9, v[16:19] offset:0\n\t"
"v_mfma_f32_16x16x32_bf16 v[200:203], a[68:71], v[172:175], v[200:203]\n\t"
"v_exp_f32 v204, v204\n\t"
"v_mfma_f32_16x16x32_bf16 v[200:203], a[72:75], v[176:179], v[200:203]\n\t"
"v_exp_f32 v205, v205\n\t"
"v_mfma_f32_16x16x32_bf16 v[200:203], a[76:79], v[180:183], v[200:203]\n\t"
"v_exp_f32 v206, v206\n\t"
"v_exp_f32 v207, v207\n\t"
"v_cvt_pk_bf16_f32 v84, v204, v205\n\t"
"v_cvt_pk_bf16_f32 v85, v206, v207\n\t"
"ds_write_b32 v248, v212 offset:768\n\t"
"v_mfma_f32_16x16x32_bf16 v[212:215], v[216:219], v[80:83], 0\n\t"
"ds_read_b128 v[188:191], v246 offset:46080\n\t"
"buffer_load_dwordx4 v12, s[52:55], s77 offen offset:2048 lds\n\t"
"v_mfma_f32_16x16x32_bf16 v[204:207], a[80:83], v[168:171], 0\n\t"
"ds_write_b128 v9, v[20:23] offset:1024\n\t"
"v_mfma_f32_16x16x32_bf16 v[204:207], a[84:87], v[172:175], v[204:207]\n\t"
"v_exp_f32 v208, v208\n\t"
"v_mfma_f32_16x16x32_bf16 v[204:207], a[88:91], v[176:179], v[204:207]\n\t"
"v_exp_f32 v209, v209\n\t"
"v_mfma_f32_16x16x32_bf16 v[204:207], a[92:95], v[180:183], v[204:207]\n\t"
"ds_read_b128 v[192:195], v247 offset:5120\n\t"
"ds_write_b128 v9, v[24:27] offset:2048\n\t"
"v_exp_f32 v210, v210\n\t"
"v_exp_f32 v211, v211\n\t"
"v_cvt_pk_bf16_f32 v86, v208, v209\n\t"
"v_cvt_pk_bf16_f32 v87, v210, v211\n\t"
"v_mfma_f32_16x16x32_bf16 v[208:211], a[96:99], v[168:171], 0\n\t"
"v_exp_f32 v200, v200\n\t"
"v_mfma_f32_16x16x32_bf16 v[208:211], a[100:103], v[172:175], v[208:211]\n\t"
"buffer_load_dwordx4 v13, s[52:55], s77 offen offset:3072 lds\n\t"
"v_mfma_f32_16x16x32_bf16 v[208:211], a[104:107], v[176:179], v[208:211]\n\t"
"ds_write_b128 v9, v[28:31] offset:3072\n\t"
"v_mfma_f32_16x16x32_bf16 v[208:211], a[108:111], v[180:183], v[208:211]\n\t"
"ds_read_b128 v[196:199], v247 offset:46080\n\t"
"v_exp_f32 v201, v201\n\t"
"v_exp_f32 v202, v202\n\t"
"v_exp_f32 v203, v203\n\t"
"v_cvt_pk_bf16_f32 v88, v200, v201\n\t"
"v_cvt_pk_bf16_f32 v89, v202, v203\n\t"
"v_mfma_f32_16x16x32_bf16 v[212:215], v[216:219], v[84:87], v[212:215]\n\t"
"s_add_u32 m0, s72, 28672\n\t"
"v_exp_f32 v204, v204\n\t"
"v_mfma_f32_16x16x32_bf16 v[200:203], a[112:115], v[168:171], 0\n\t"
"s_add_u32 s77, s77, 32768\n\t"
"buffer_load_dwordx4 v10, s[52:55], s77 offen lds\n\t"
"v_mfma_f32_16x16x32_bf16 v[200:203], a[116:119], v[172:175], v[200:203]\n\t"
"v_exp_f32 v205, v205\n\t"
"v_mfma_f32_16x16x32_bf16 v[200:203], a[120:123], v[176:179], v[200:203]\n\t"
"v_exp_f32 v206, v206\n\t"
"v_mfma_f32_16x16x32_bf16 v[200:203], a[124:127], v[180:183], v[200:203]\n\t"
"s_waitcnt lgkmcnt(1)\n\t"
"s_barrier\n\t"
"v_exp_f32 v207, v207\n\t"
"v_cvt_pk_bf16_f32 v90, v204, v205\n\t"
"v_cvt_pk_bf16_f32 v91, v206, v207\n\t"
"v_mfma_f32_16x16x32_bf16 v[204:207], a[0:3], v[184:187], 0\n\t"
"ds_read_b128 v[128:131], v6 offset:0\n\t"
"v_exp_f32 v208, v208\n\t"
"v_mfma_f32_16x16x32_bf16 v[204:207], a[4:7], v[188:191], v[204:207]\n\t"
"ds_read_b128 v[132:135], v6 offset:1024\n\t"
"v_exp_f32 v209, v209\n\t"
"v_mfma_f32_16x16x32_bf16 v[204:207], a[8:11], v[192:195], v[204:207]\n\t"
"s_waitcnt lgkmcnt(2)\n\t"
"ds_read_b128 v[136:139], v6 offset:2048\n\t"
"buffer_load_dwordx4 v11, s[52:55], s77 offen offset:1024 lds\n\t"
"v_mfma_f32_16x16x32_bf16 v[204:207], a[12:15], v[196:199], v[204:207]\n\t"
"v_exp_f32 v210, v210\n\t"
"v_exp_f32 v211, v211\n\t"
"v_cvt_pk_bf16_f32 v92, v208, v209\n\t"
"v_cvt_pk_bf16_f32 v93, v210, v211\n\t"
"v_mfma_f32_16x16x32_bf16 v[212:215], v[216:219], v[88:91], v[212:215]\n\t"
"ds_read_b128 v[140:143], v6 offset:3072\n\t"
"v_exp_f32 v200, v200\n\t"
"v_mfma_f32_16x16x32_bf16 v[152:155], a[128:131], v[16:19], 0\n\t"
"ds_read_b128 v[144:147], v7 offset:0\n\t"
"v_exp_f32 v201, v201\n\t"
"v_mfma_f32_16x16x32_bf16 v[152:155], a[132:135], v[20:23], v[152:155]\n\t"
"ds_read_b128 v[148:151], v7 offset:1024\n\t"
"v_exp_f32 v202, v202\n\t"
"v_mfma_f32_16x16x32_bf16 v[152:155], a[136:139], v[24:27], v[152:155]\n\t"
"v_exp_f32 v203, v203\n\t"
"v_mfma_f32_16x16x32_bf16 v[152:155], a[140:143], v[28:31], v[152:155]\n\t"
"v_cvt_pk_bf16_f32 v94, v200, v201\n\t"
"v_cvt_pk_bf16_f32 v95, v202, v203\n\t"
"v_mfma_f32_16x16x32_bf16 v[208:211], a[16:19], v[184:187], 0\n\t"
"buffer_load_dwordx4 v12, s[52:55], s77 offen offset:2048 lds\n\t"
"v_mfma_f32_16x16x32_bf16 v[208:211], a[20:23], v[188:191], v[208:211]\n\t"
"v_exp_f32 v204, v204\n\t"
"v_mfma_f32_16x16x32_bf16 v[208:211], a[24:27], v[192:195], v[208:211]\n\t"
"v_exp_f32 v205, v205\n\t"
"v_mfma_f32_16x16x32_bf16 v[208:211], a[28:31], v[196:199], v[208:211]\n\t"
"v_exp_f32 v206, v206\n\t"
"v_mfma_f32_16x16x32_bf16 v[156:159], a[192:195], v[16:19], 0\n\t"
"v_exp_f32 v207, v207\n\t"
"v_mfma_f32_16x16x32_bf16 v[156:159], a[196:199], v[20:23], v[156:159]\n\t"
"ds_write_b128 v9, v[32:35] offset:4096\n\t"
"v_mfma_f32_16x16x32_bf16 v[156:159], a[200:203], v[24:27], v[156:159]\n\t"
"buffer_load_dwordx4 v13, s[52:55], s77 offen offset:3072 lds\n\t"
"v_mfma_f32_16x16x32_bf16 v[156:159], a[204:207], v[28:31], v[156:159]\n\t"
"s_add_u32 s77, s77, 229376\n\t"
"v_cvt_pk_bf16_f32 v96, v204, v205\n\t"
"v_cvt_pk_bf16_f32 v97, v206, v207\n\t"
"v_mfma_f32_16x16x32_bf16 v[200:203], a[32:35], v[184:187], 0\n\t"
"s_and_b32 s77, s77, 2097151\n\t"
"v_exp_f32 v208, v208\n\t"
"v_mfma_f32_16x16x32_bf16 v[200:203], a[36:39], v[188:191], v[200:203]\n\t"
"ds_write_b128 v9, v[36:39] offset:5120\n\t"
"v_mfma_f32_16x16x32_bf16 v[200:203], a[40:43], v[192:195], v[200:203]\n\t"
"v_exp_f32 v209, v209\n\t"
"v_mfma_f32_16x16x32_bf16 v[200:203], a[44:47], v[196:199], v[200:203]\n\t"
"v_exp_f32 v210, v210\n\t"
"v_mfma_f32_16x16x32_bf16 v[212:215], v[216:219], v[92:95], v[212:215]\n\t"
"s_waitcnt lgkmcnt(7)\n\t"
"v_exp_f32 v211, v211\n\t"
"v_mfma_f32_16x16x32_bf16 v[152:155], a[144:147], v[128:131], v[152:155]\n\t"
"s_waitcnt lgkmcnt(6)\n\t"
"ds_write_b128 v9, v[40:43] offset:6144\n\t"
"v_mfma_f32_16x16x32_bf16 v[152:155], a[148:151], v[132:135], v[152:155]\n\t"
"s_waitcnt lgkmcnt(6)\n\t"
"v_cvt_pk_bf16_f32 v98, v208, v209\n\t"
"v_cvt_pk_bf16_f32 v99, v210, v211\n\t"
"v_mfma_f32_16x16x32_bf16 v[152:155], a[152:155], v[136:139], v[152:155]\n\t"
"s_waitcnt lgkmcnt(5)\n\t"
"v_xor_b32 v238, 64, v236\n\t"
"v_xor_b32 v239, 96, v236\n\t"
"v_mfma_f32_16x16x32_bf16 v[152:155], a[156:159], v[140:143], v[152:155]\n\t"
"v_xor_b32 v240, 128, v236\n\t"
"v_xor_b32 v241, 160, v236\n\t"
"v_mfma_f32_16x16x32_bf16 v[204:207], a[48:51], v[184:187], 0\n\t"
"s_add_u32 m0, s72, 32768\n\t"
"ds_write_b128 v9, v[44:47] offset:7168\n\t"
"v_mfma_f32_16x16x32_bf16 v[204:207], a[52:55], v[188:191], v[204:207]\n\t"
"buffer_load_dwordx4 v10, s[52:55], s77 offen lds\n\t"
"v_mfma_f32_16x16x32_bf16 v[204:207], a[56:59], v[192:195], v[204:207]\n\t"
"v_xor_b32 v242, 192, v236\n\t"
"v_xor_b32 v243, 224, v236\n\t"
"v_mfma_f32_16x16x32_bf16 v[204:207], a[60:63], v[196:199], v[204:207]\n\t"
"v_exp_f32 v200, v200\n\t"
"v_mfma_f32_16x16x32_bf16 v[156:159], a[208:211], v[128:131], v[156:159]\n\t"
"ds_write_b32 v248, v212 offset:1024\n\t"
"v_mfma_f32_16x16x32_bf16 v[156:159], a[212:215], v[132:135], v[156:159]\n\t"
"ds_read_b128 v[128:131], v7 offset:2048\n\t"
"v_exp_f32 v201, v201\n\t"
"v_mfma_f32_16x16x32_bf16 v[156:159], a[216:219], v[136:139], v[156:159]\n\t"
"ds_read_b128 v[168:171], v246 offset:6144\n\t"
"v_exp_f32 v202, v202\n\t"
"v_mfma_f32_16x16x32_bf16 v[156:159], a[220:223], v[140:143], v[156:159]\n\t"
"ds_read_b128 v[132:135], v7 offset:3072\n\t"
"buffer_load_dwordx4 v11, s[52:55], s77 offen offset:1024 lds\n\t"
"v_mfma_f32_16x16x32_bf16 v[208:211], a[64:67], v[184:187], 0\n\t"
"ds_read_b128 v[136:139], v8 offset:0\n\t"
"v_exp_f32 v203, v203\n\t"
"v_mfma_f32_16x16x32_bf16 v[208:211], a[68:71], v[188:191], v[208:211]\n\t"
"ds_read_b128 v[140:143], v8 offset:1024\n\t"
"v_cvt_pk_bf16_f32 v100, v200, v201\n\t"
"v_mfma_f32_16x16x32_bf16 v[208:211], a[72:75], v[192:195], v[208:211]\n\t"
"ds_read_b128 v[172:175], v246 offset:47104\n\t"
"v_cvt_pk_bf16_f32 v101, v202, v203\n\t"
"v_mfma_f32_16x16x32_bf16 v[208:211], a[76:79], v[196:199], v[208:211]\n\t"
"ds_read_b128 v[176:179], v247 offset:6144\n\t"
"v_exp_f32 v204, v204\n\t"
"v_mfma_f32_16x16x32_bf16 v[212:215], v[216:219], v[96:99], 0\n\t"
"s_waitcnt lgkmcnt(13)\n\t"
"v_exp_f32 v205, v205\n\t"
"v_mfma_f32_16x16x32_bf16 v[152:155], a[160:163], v[144:147], v[152:155]\n\t"
"s_waitcnt lgkmcnt(12)\n\t"
"buffer_load_dwordx4 v12, s[52:55], s77 offen offset:2048 lds\n\t"
"v_mfma_f32_16x16x32_bf16 v[152:155], a[164:167], v[148:151], v[152:155]\n\t"
"s_waitcnt lgkmcnt(6)\n\t"
"v_exp_f32 v206, v206\n\t"
"v_mfma_f32_16x16x32_bf16 v[152:155], a[168:171], v[128:131], v[152:155]\n\t"
"s_waitcnt lgkmcnt(4)\n\t"
"v_exp_f32 v207, v207\n\t"
"v_mfma_f32_16x16x32_bf16 v[152:155], a[172:175], v[132:135], v[152:155]\n\t"
"v_cvt_pk_bf16_f32 v102, v204, v205\n\t"
"v_cvt_pk_bf16_f32 v103, v206, v207\n\t"
"v_mfma_f32_16x16x32_bf16 v[200:203], a[80:83], v[184:187], 0\n\t"
"v_exp_f32 v208, v208\n\t"
"v_mfma_f32_16x16x32_bf16 v[200:203], a[84:87], v[188:191], v[200:203]\n\t"
"v_exp_f32 v209, v209\n\t"
"v_mfma_f32_16x16x32_bf16 v[200:203], a[88:91], v[192:195], v[200:203]\n\t"
"buffer_load_dwordx4 v13, s[52:55], s77 offen offset:3072 lds\n\t"
"v_mfma_f32_16x16x32_bf16 v[200:203], a[92:95], v[196:199], v[200:203]\n\t"
"s_add_u32 s77, s77, 32768\n\t"
"ds_read_b128 v[180:183], v247 offset:47104\n\t"
"v_exp_f32 v210, v210\n\t"
"v_mfma_f32_16x16x32_bf16 v[156:159], a[224:227], v[144:147], v[156:159]\n\t"
"ds_read_b128 v[144:147], v8 offset:2048\n\t"
"v_exp_f32 v211, v211\n\t"
"v_mfma_f32_16x16x32_bf16 v[156:159], a[228:231], v[148:151], v[156:159]\n\t"
"ds_read_b128 v[148:151], v8 offset:3072\n\t"
"v_cvt_pk_bf16_f32 v104, v208, v209\n\t"
"v_mfma_f32_16x16x32_bf16 v[156:159], a[232:235], v[128:131], v[156:159]\n\t"
"v_cvt_pk_bf16_f32 v105, v210, v211\n\t"
"v_exp_f32 v200, v200\n\t"
"v_mfma_f32_16x16x32_bf16 v[156:159], a[236:239], v[132:135], v[156:159]\n\t"
"v_exp_f32 v201, v201\n\t"
"v_mfma_f32_16x16x32_bf16 v[204:207], a[96:99], v[184:187], 0\n\t"
"ds_read_b128 v[220:223], v249 offset:0\n\t"
"v_exp_f32 v202, v202\n\t"
"v_mfma_f32_16x16x32_bf16 v[204:207], a[100:103], v[188:191], v[204:207]\n\t"
"v_exp_f32 v203, v203\n\t"
"v_mfma_f32_16x16x32_bf16 v[204:207], a[104:107], v[192:195], v[204:207]\n\t"
"v_cvt_pk_bf16_f32 v106, v200, v201\n\t"
"v_cvt_pk_bf16_f32 v107, v202, v203\n\t"
"v_mfma_f32_16x16x32_bf16 v[204:207], a[108:111], v[196:199], v[204:207]\n\t"
"v_mfma_f32_16x16x32_bf16 v[212:215], v[216:219], v[100:103], v[212:215]\n\t"
"s_waitcnt lgkmcnt(7)\n\t"
"v_mfma_f32_16x16x32_bf16 v[152:155], a[176:179], v[136:139], v[152:155]\n\t"
"s_waitcnt lgkmcnt(0)\n\t"
"v_add_f32 v220, v220, v221\n\t"
"v_add_f32 v222, v222, v223\n\t"
"v_add_f32 v220, v220, v222\n\t"
"v_mfma_f32_16x16x32_bf16 v[152:155], a[180:183], v[140:143], v[152:155]\n\t"
"v_rcp_f32 v230, v220\n\t"
"v_mfma_f32_16x16x32_bf16 v[152:155], a[184:187], v[144:147], v[152:155]\n\t"
"v_subrev_u32 v221, s24, v220\n\t"
"v_cmp_lt_u32 vcc, s25, v221\n\t"
"s_nop 0\n\t"
"s_cmp_eq_u64 vcc, 0\n\t"
"s_cbranch_scc0 LCK0_%=\n\t"
"v_mfma_f32_16x16x32_bf16 v[152:155], a[188:191], v[148:151], v[152:155]\n\t"
"v_exp_f32 v204, v204\n\t"
"v_mfma_f32_16x16x32_bf16 v[208:211], a[112:115], v[184:187], 0\n\t"
"s_add_u32 m0, s72, 12288\n\t"
"v_exp_f32 v205, v205\n\t"
"v_mfma_f32_16x16x32_bf16 v[208:211], a[116:119], v[188:191], v[208:211]\n\t"
"buffer_load_dwordx4 v10, s[52:55], s77 offen lds\n\t"
"v_mfma_f32_16x16x32_bf16 v[208:211], a[120:123], v[192:195], v[208:211]\n\t"
"v_exp_f32 v206, v206\n\t"
"v_mfma_f32_16x16x32_bf16 v[208:211], a[124:127], v[196:199], v[208:211]\n\t"
"v_exp_f32 v207, v207\n\t"
"v_cvt_pk_bf16_f32 v108, v204, v205\n\t"
"v_mfma_f32_16x16x32_bf16 v[156:159], a[240:243], v[136:139], v[156:159]\n\t"
"v_cvt_pk_bf16_f32 v109, v206, v207\n\t"
"v_mfma_f32_16x16x32_bf16 v[156:159], a[244:247], v[140:143], v[156:159]\n\t"
"v_mfma_f32_16x16x32_bf16 v[156:159], a[248:251], v[144:147], v[156:159]\n\t"
"v_mfma_f32_16x16x32_bf16 v[156:159], a[252:255], v[148:151], v[156:159]\n\t"
"s_barrier\n\t"
"ds_read_b128 v[128:131], v6 offset:4096\n\t"
"buffer_load_dwordx4 v11, s[52:55], s77 offen offset:1024 lds\n\t"
"v_mfma_f32_16x16x32_bf16 v[200:203], a[0:3], v[168:171], 0\n\t"
"ds_read_b128 v[132:135], v6 offset:5120\n\t"
"v_exp_f32 v208, v208\n\t"
"v_mfma_f32_16x16x32_bf16 v[200:203], a[4:7], v[172:175], v[200:203]\n\t"
"ds_read_b128 v[136:139], v6 offset:6144\n\t"
"v_exp_f32 v209, v209\n\t"
"v_mfma_f32_16x16x32_bf16 v[200:203], a[8:11], v[176:179], v[200:203]\n\t"
"ds_read_b128 v[140:143], v6 offset:7168\n\t"
"v_exp_f32 v210, v210\n\t"
"v_mfma_f32_16x16x32_bf16 v[200:203], a[12:15], v[180:183], v[200:203]\n\t"
"ds_read_b128 v[144:147], v7 offset:4096\n\t"
"v_exp_f32 v211, v211\n\t"
"v_mfma_f32_16x16x32_bf16 v[212:215], v[216:219], v[104:107], v[212:215]\n\t"
"ds_read_b128 v[148:151], v7 offset:5120\n\t"
"v_cvt_pk_bf16_f32 v110, v208, v209\n\t"
"v_mfma_f32_16x16x32_bf16 v[160:163], a[128:131], v[32:35], 0\n\t"
"buffer_load_dwordx4 v12, s[52:55], s77 offen offset:2048 lds\n\t"
"v_mfma_f32_16x16x32_bf16 v[160:163], a[132:135], v[36:39], v[160:163]\n\t"
"v_cvt_pk_bf16_f32 v111, v210, v211\n\t"
"v_exp_f32 v200, v200\n\t"
"v_mfma_f32_16x16x32_bf16 v[160:163], a[136:139], v[40:43], v[160:163]\n\t"
"v_exp_f32 v201, v201\n\t"
"v_mfma_f32_16x16x32_bf16 v[160:163], a[140:143], v[44:47], v[160:163]\n\t"
"v_exp_f32 v202, v202\n\t"
"v_mfma_f32_16x16x32_bf16 v[204:207], a[16:19], v[168:171], 0\n\t"
"v_exp_f32 v203, v203\n\t"
"v_mfma_f32_16x16x32_bf16 v[204:207], a[20:23], v[172:175], v[204:207]\n\t"
"v_cvt_pk_bf16_f32 v112, v200, v201\n\t"
"v_cvt_pk_bf16_f32 v113, v202, v203\n\t"
"v_mfma_f32_16x16x32_bf16 v[204:207], a[24:27], v[176:179], v[204:207]\n\t"
"buffer_load_dwordx4 v13, s[52:55], s77 offen offset:3072 lds\n\t"
"v_mfma_f32_16x16x32_bf16 v[204:207], a[28:31], v[180:183], v[204:207]\n\t"
"s_add_u32 s77, s77, 229376\n\t"
"v_mul_f32 v152, v152, v230\n\t"
"v_mul_f32 v153, v153, v230\n\t"
"v_mfma_f32_16x16x32_bf16 v[164:167], a[192:195], v[32:35], 0\n\t"
"s_and_b32 s77, s77, 2097151\n\t"
"v_mul_f32 v154, v154, v230\n\t"
"v_mul_f32 v155, v155, v230\n\t"
"v_mfma_f32_16x16x32_bf16 v[164:167], a[196:199], v[36:39], v[164:167]\n\t"
"ds_write_b128 v9, v[48:51] offset:0\n\t"
"v_mfma_f32_16x16x32_bf16 v[164:167], a[200:203], v[40:43], v[164:167]\n\t"
"v_mul_f32 v156, v156, v230\n\t"
"v_mul_f32 v157, v157, v230\n\t"
"v_mfma_f32_16x16x32_bf16 v[164:167], a[204:207], v[44:47], v[164:167]\n\t"
"v_mul_f32 v158, v158, v230\n\t"
"v_mul_f32 v159, v159, v230\n\t"
"v_mfma_f32_16x16x32_bf16 v[208:211], a[32:35], v[168:171], 0\n\t"
"v_cvt_pk_bf16_f32 v224, v152, v153\n\t"
"v_cvt_pk_bf16_f32 v225, v154, v155\n\t"
"v_mfma_f32_16x16x32_bf16 v[208:211], a[36:39], v[172:175], v[208:211]\n\t"
"ds_write_b128 v9, v[52:55] offset:1024\n\t"
"v_mfma_f32_16x16x32_bf16 v[208:211], a[40:43], v[176:179], v[208:211]\n\t"
"v_cvt_pk_bf16_f32 v226, v156, v157\n\t"
"v_cvt_pk_bf16_f32 v227, v158, v159\n\t"
"v_mfma_f32_16x16x32_bf16 v[208:211], a[44:47], v[180:183], v[208:211]\n\t"
"buffer_store_dwordx4 v[224:227], v250, s[56:59], s98 offen sc1\n\t"
"v_mfma_f32_16x16x32_bf16 v[212:215], v[216:219], v[108:111], v[212:215]\n\t"
"s_waitcnt lgkmcnt(7)\n\t"
"s_add_u32 s98, s98, 32768\n\t"
"v_exp_f32 v204, v204\n\t"
"v_mfma_f32_16x16x32_bf16 v[160:163], a[144:147], v[128:131], v[160:163]\n\t"
"s_waitcnt lgkmcnt(6)\n\t"
"ds_write_b128 v9, v[56:59] offset:2048\n\t"
"v_mfma_f32_16x16x32_bf16 v[160:163], a[148:151], v[132:135], v[160:163]\n\t"
"s_waitcnt lgkmcnt(6)\n\t"
"v_exp_f32 v205, v205\n\t"
"v_mfma_f32_16x16x32_bf16 v[160:163], a[152:155], v[136:139], v[160:163]\n\t"
"s_waitcnt lgkmcnt(5)\n\t"
"v_exp_f32 v206, v206\n\t"
"v_mfma_f32_16x16x32_bf16 v[160:163], a[156:159], v[140:143], v[160:163]\n\t"
"v_exp_f32 v207, v207\n\t"
"v_mfma_f32_16x16x32_bf16 v[200:203], a[48:51], v[168:171], 0\n\t"
"s_add_u32 m0, s72, 24576\n\t"
"ds_write_b128 v9, v[60:63] offset:3072\n\t"
"v_mfma_f32_16x16x32_bf16 v[200:203], a[52:55], v[172:175], v[200:203]\n\t"
"buffer_load_dwordx4 v10, s[52:55], s77 offen lds\n\t"
"v_mfma_f32_16x16x32_bf16 v[200:203], a[56:59], v[176:179], v[200:203]\n\t"
"v_cvt_pk_bf16_f32 v114, v204, v205\n\t"
"v_cvt_pk_bf16_f32 v115, v206, v207\n\t"
"v_mfma_f32_16x16x32_bf16 v[200:203], a[60:63], v[180:183], v[200:203]\n\t"
"v_exp_f32 v208, v208\n\t"
"v_mfma_f32_16x16x32_bf16 v[164:167], a[208:211], v[128:131], v[164:167]\n\t"
"ds_write_b32 v248, v212 offset:40960\n\t"
"v_mfma_f32_16x16x32_bf16 v[164:167], a[212:215], v[132:135], v[164:167]\n\t"
"ds_read_b128 v[128:131], v7 offset:6144\n\t"
"v_exp_f32 v209, v209\n\t"
"v_mfma_f32_16x16x32_bf16 v[164:167], a[216:219], v[136:139], v[164:167]\n\t"
"ds_read_b128 v[184:187], v246 offset:7168\n\t"
"v_exp_f32 v210, v210\n\t"
"v_mfma_f32_16x16x32_bf16 v[164:167], a[220:223], v[140:143], v[164:167]\n\t"
"ds_read_b128 v[132:135], v7 offset:7168\n\t"
"buffer_load_dwordx4 v11, s[52:55], s77 offen offset:1024 lds\n\t"
"v_mfma_f32_16x16x32_bf16 v[204:207], a[64:67], v[168:171], 0\n\t"
"ds_read_b128 v[136:139], v8 offset:4096\n\t"
"v_exp_f32 v211, v211\n\t"
"v_mfma_f32_16x16x32_bf16 v[204:207], a[68:71], v[172:175], v[204:207]\n\t"
"ds_read_b128 v[140:143], v8 offset:5120\n\t"
"v_cvt_pk_bf16_f32 v116, v208, v209\n\t"
"v_mfma_f32_16x16x32_bf16 v[204:207], a[72:75], v[176:179], v[204:207]\n\t"
"ds_read_b128 v[188:191], v246 offset:48128\n\t"
"v_cvt_pk_bf16_f32 v117, v210, v211\n\t"
"v_mfma_f32_16x16x32_bf16 v[204:207], a[76:79], v[180:183], v[204:207]\n\t"
"ds_read_b128 v[192:195], v247 offset:7168\n\t"
"v_exp_f32 v200, v200\n\t"
"v_mfma_f32_16x16x32_bf16 v[212:215], v[216:219], v[112:115], 0\n\t"
"s_waitcnt lgkmcnt(13)\n\t"
"v_exp_f32 v201, v201\n\t"
"v_mfma_f32_16x16x32_bf16 v[160:163], a[160:163], v[144:147], v[160:163]\n\t"
"s_waitcnt lgkmcnt(12)\n\t"
"buffer_load_dwordx4 v12, s[52:55], s77 offen offset:2048 lds\n\t"
"v_mfma_f32_16x16x32_bf16 v[160:163], a[164:167], v[148:151], v[160:163]\n\t"
"s_waitcnt lgkmcnt(6)\n\t"
"v_exp_f32 v202, v202\n\t"
"v_mfma_f32_16x16x32_bf16 v[160:163], a[168:171], v[128:131], v[160:163]\n\t"
"s_waitcnt lgkmcnt(4)\n\t"
"v_exp_f32 v203, v203\n\t"
"v_mfma_f32_16x16x32_bf16 v[160:163], a[172:175], v[132:135], v[160:163]\n\t"
"v_cvt_pk_bf16_f32 v118, v200, v201\n\t"
"v_cvt_pk_bf16_f32 v119, v202, v203\n\t"
"v_mfma_f32_16x16x32_bf16 v[208:211], a[80:83], v[168:171], 0\n\t"
"v_exp_f32 v204, v204\n\t"
"v_mfma_f32_16x16x32_bf16 v[208:211], a[84:87], v[172:175], v[208:211]\n\t"
"v_exp_f32 v205, v205\n\t"
"v_mfma_f32_16x16x32_bf16 v[208:211], a[88:91], v[176:179], v[208:211]\n\t"
"buffer_load_dwordx4 v13, s[52:55], s77 offen offset:3072 lds\n\t"
"v_mfma_f32_16x16x32_bf16 v[208:211], a[92:95], v[180:183], v[208:211]\n\t"
"s_add_u32 s77, s77, 32768\n\t"
"ds_read_b128 v[196:199], v247 offset:48128\n\t"
"v_exp_f32 v206, v206\n\t"
"v_mfma_f32_16x16x32_bf16 v[164:167], a[224:227], v[144:147], v[164:167]\n\t"
"ds_read_b128 v[144:147], v8 offset:6144\n\t"
"v_exp_f32 v207, v207\n\t"
"v_mfma_f32_16x16x32_bf16 v[164:167], a[228:231], v[148:151], v[164:167]\n\t"
"ds_read_b128 v[148:151], v8 offset:7168\n\t"
"v_cvt_pk_bf16_f32 v120, v204, v205\n\t"
"v_mfma_f32_16x16x32_bf16 v[164:167], a[232:235], v[128:131], v[164:167]\n\t"
"v_cvt_pk_bf16_f32 v121, v206, v207\n\t"
"v_exp_f32 v208, v208\n\t"
"v_mfma_f32_16x16x32_bf16 v[164:167], a[236:239], v[132:135], v[164:167]\n\t"
"v_exp_f32 v209, v209\n\t"
"v_mfma_f32_16x16x32_bf16 v[200:203], a[96:99], v[168:171], 0\n\t"
"ds_read_b128 v[220:223], v249 offset:256\n\t"
"v_exp_f32 v210, v210\n\t"
"v_mfma_f32_16x16x32_bf16 v[200:203], a[100:103], v[172:175], v[200:203]\n\t"
"v_exp_f32 v211, v211\n\t"
"v_mfma_f32_16x16x32_bf16 v[200:203], a[104:107], v[176:179], v[200:203]\n\t"
"v_cvt_pk_bf16_f32 v122, v208, v209\n\t"
"v_cvt_pk_bf16_f32 v123, v210, v211\n\t"
"v_mfma_f32_16x16x32_bf16 v[200:203], a[108:111], v[180:183], v[200:203]\n\t"
"v_mfma_f32_16x16x32_bf16 v[212:215], v[216:219], v[116:119], v[212:215]\n\t"
"s_waitcnt lgkmcnt(7)\n\t"
"v_mfma_f32_16x16x32_bf16 v[160:163], a[176:179], v[136:139], v[160:163]\n\t"
"s_waitcnt lgkmcnt(0)\n\t"
"v_add_f32 v220, v220, v221\n\t"
"v_add_f32 v222, v222, v223\n\t"
"v_add_f32 v220, v220, v222\n\t"
"v_mfma_f32_16x16x32_bf16 v[160:163], a[180:183], v[140:143], v[160:163]\n\t"
"v_rcp_f32 v230, v220\n\t"
"v_mfma_f32_16x16x32_bf16 v[160:163], a[184:187], v[144:147], v[160:163]\n\t"
"v_subrev_u32 v221, s24, v220\n\t"
"v_cmp_lt_u32 vcc, s25, v221\n\t"
"s_nop 0\n\t"
"s_cmp_eq_u64 vcc, 0\n\t"
"s_cbranch_scc0 LCK1_%=\n\t"
"v_mfma_f32_16x16x32_bf16 v[160:163], a[188:191], v[148:151], v[160:163]\n\t"
"v_exp_f32 v200, v200\n\t"
"v_mfma_f32_16x16x32_bf16 v[204:207], a[112:115], v[168:171], 0\n\t"
"s_add_u32 m0, s72, 36864\n\t"
"v_exp_f32 v201, v201\n\t"
"v_mfma_f32_16x16x32_bf16 v[204:207], a[116:119], v[172:175], v[204:207]\n\t"
"buffer_load_dwordx4 v10, s[52:55], s77 offen lds\n\t"
"v_mfma_f32_16x16x32_bf16 v[204:207], a[120:123], v[176:179], v[204:207]\n\t"
"v_exp_f32 v202, v202\n\t"
"v_mfma_f32_16x16x32_bf16 v[204:207], a[124:127], v[180:183], v[204:207]\n\t"
"v_exp_f32 v203, v203\n\t"
"v_cvt_pk_bf16_f32 v124, v200, v201\n\t"
"v_mfma_f32_16x16x32_bf16 v[164:167], a[240:243], v[136:139], v[164:167]\n\t"
"v_cvt_pk_bf16_f32 v125, v202, v203\n\t"
"v_mfma_f32_16x16x32_bf16 v[164:167], a[244:247], v[140:143], v[164:167]\n\t"
"v_mfma_f32_16x16x32_bf16 v[164:167], a[248:251], v[144:147], v[164:167]\n\t"
"v_mfma_f32_16x16x32_bf16 v[164:167], a[252:255], v[148:151], v[164:167]\n\t"
"s_barrier\n\t"
"ds_read_b128 v[128:131], v6 offset:0\n\t"
"buffer_load_dwordx4 v11, s[52:55], s77 offen offset:1024 lds\n\t"
"v_mfma_f32_16x16x32_bf16 v[208:211], a[0:3], v[184:187], 0\n\t"
"ds_read_b128 v[132:135], v6 offset:1024\n\t"
"v_exp_f32 v204, v204\n\t"
"v_mfma_f32_16x16x32_bf16 v[208:211], a[4:7], v[188:191], v[208:211]\n\t"
"ds_read_b128 v[136:139], v6 offset:2048\n\t"
"v_exp_f32 v205, v205\n\t"
"v_mfma_f32_16x16x32_bf16 v[208:211], a[8:11], v[192:195], v[208:211]\n\t"
"ds_read_b128 v[140:143], v6 offset:3072\n\t"
"v_exp_f32 v206, v206\n\t"
"v_mfma_f32_16x16x32_bf16 v[208:211], a[12:15], v[196:199], v[208:211]\n\t"
"ds_read_b128 v[144:147], v7 offset:0\n\t"
"v_exp_f32 v207, v207\n\t"
"v_mfma_f32_16x16x32_bf16 v[212:215], v[216:219], v[120:123], v[212:215]\n\t"
"ds_read_b128 v[148:151], v7 offset:1024\n\t"
"v_cvt_pk_bf16_f32 v126, v204, v205\n\t"
"v_mfma_f32_16x16x32_bf16 v[152:155], a[128:131], v[48:51], 0\n\t"
"buffer_load_dwordx4 v12, s[52:55], s77 offen offset:2048 lds\n\t"
"v_mfma_f32_16x16x32_bf16 v[152:155], a[132:135], v[52:55], v[152:155]\n\t"
"v_cvt_pk_bf16_f32 v127, v206, v207\n\t"
"v_exp_f32 v208, v208\n\t"
"v_mfma_f32_16x16x32_bf16 v[152:155], a[136:139], v[56:59], v[152:155]\n\t"
"v_exp_f32 v209, v209\n\t"
"v_mfma_f32_16x16x32_bf16 v[152:155], a[140:143], v[60:63], v[152:155]\n\t"
"v_exp_f32 v210, v210\n\t"
"v_mfma_f32_16x16x32_bf16 v[200:203], a[16:19], v[184:187], 0\n\t"
"v_exp_f32 v211, v211\n\t"
"v_mfma_f32_16x16x32_bf16 v[200:203], a[20:23], v[188:191], v[200:203]\n\t"
"v_cvt_pk_bf16_f32 v16, v208, v209\n\t"
"v_cvt_pk_bf16_f32 v17, v210, v211\n\t"
"v_mfma_f32_16x16x32_bf16 v[200:203], a[24:27], v[192:195], v[200:203]\n\t"
"buffer_load_dwordx4 v13, s[52:55], s77 offen offset:3072 lds\n\t"
"v_mfma_f32_16x16x32_bf16 v[200:203], a[28:31], v[196:199], v[200:203]\n\t"
"s_add_u32 s77, s77, 229376\n\t"
"v_mul_f32 v160, v160, v230\n\t"
"v_mul_f32 v161, v161, v230\n\t"
"v_mfma_f32_16x16x32_bf16 v[156:159], a[192:195], v[48:51], 0\n\t"
"s_and_b32 s77, s77, 2097151\n\t"
"v_mul_f32 v162, v162, v230\n\t"
"v_mul_f32 v163, v163, v230\n\t"
"v_mfma_f32_16x16x32_bf16 v[156:159], a[196:199], v[52:55], v[156:159]\n\t"
"ds_write_b128 v9, v[64:67] offset:4096\n\t"
"v_mfma_f32_16x16x32_bf16 v[156:159], a[200:203], v[56:59], v[156:159]\n\t"
"v_mul_f32 v164, v164, v230\n\t"
"v_mul_f32 v165, v165, v230\n\t"
"v_mfma_f32_16x16x32_bf16 v[156:159], a[204:207], v[60:63], v[156:159]\n\t"
"v_mul_f32 v166, v166, v230\n\t"
"v_mul_f32 v167, v167, v230\n\t"
"v_mfma_f32_16x16x32_bf16 v[204:207], a[32:35], v[184:187], 0\n\t"
"v_cvt_pk_bf16_f32 v224, v160, v161\n\t"
"v_cvt_pk_bf16_f32 v225, v162, v163\n\t"
"v_mfma_f32_16x16x32_bf16 v[204:207], a[36:39], v[188:191], v[204:207]\n\t"
"ds_write_b128 v9, v[68:71] offset:5120\n\t"
"v_mfma_f32_16x16x32_bf16 v[204:207], a[40:43], v[192:195], v[204:207]\n\t"
"v_cvt_pk_bf16_f32 v226, v164, v165\n\t"
"v_cvt_pk_bf16_f32 v227, v166, v167\n\t"
"v_mfma_f32_16x16x32_bf16 v[204:207], a[44:47], v[196:199], v[204:207]\n\t"
"buffer_store_dwordx4 v[224:227], v250, s[56:59], s98 offen sc1\n\t"
"v_mfma_f32_16x16x32_bf16 v[212:215], v[216:219], v[124:127], v[212:215]\n\t"
"s_waitcnt lgkmcnt(7)\n\t"
"s_add_u32 s98, s98, 32768\n\t"
"v_exp_f32 v200, v200\n\t"
"v_mfma_f32_16x16x32_bf16 v[152:155], a[144:147], v[128:131], v[152:155]\n\t"
"s_waitcnt lgkmcnt(6)\n\t"
"ds_write_b128 v9, v[72:75] offset:6144\n\t"
"v_mfma_f32_16x16x32_bf16 v[152:155], a[148:151], v[132:135], v[152:155]\n\t"
"s_waitcnt lgkmcnt(6)\n\t"
"v_exp_f32 v201, v201\n\t"
"v_mfma_f32_16x16x32_bf16 v[152:155], a[152:155], v[136:139], v[152:155]\n\t"
"s_waitcnt lgkmcnt(5)\n\t"
"v_exp_f32 v202, v202\n\t"
"v_mfma_f32_16x16x32_bf16 v[152:155], a[156:159], v[140:143], v[152:155]\n\t"
"v_exp_f32 v203, v203\n\t"
"v_mfma_f32_16x16x32_bf16 v[208:211], a[48:51], v[184:187], 0\n\t"
"s_add_u32 m0, s72, 4096\n\t"
"ds_write_b128 v9, v[76:79] offset:7168\n\t"
"v_mfma_f32_16x16x32_bf16 v[208:211], a[52:55], v[188:191], v[208:211]\n\t"
"buffer_load_dwordx4 v10, s[52:55], s77 offen lds\n\t"
"v_mfma_f32_16x16x32_bf16 v[208:211], a[56:59], v[192:195], v[208:211]\n\t"
"v_cvt_pk_bf16_f32 v18, v200, v201\n\t"
"v_cvt_pk_bf16_f32 v19, v202, v203\n\t"
"v_mfma_f32_16x16x32_bf16 v[208:211], a[60:63], v[196:199], v[208:211]\n\t"
"v_exp_f32 v204, v204\n\t"
"v_mfma_f32_16x16x32_bf16 v[156:159], a[208:211], v[128:131], v[156:159]\n\t"
"ds_write_b32 v248, v212 offset:41216\n\t"
"v_mfma_f32_16x16x32_bf16 v[156:159], a[212:215], v[132:135], v[156:159]\n\t"
"ds_read_b128 v[128:131], v7 offset:2048\n\t"
"v_exp_f32 v205, v205\n\t"
"v_mfma_f32_16x16x32_bf16 v[156:159], a[216:219], v[136:139], v[156:159]\n\t"
"ds_read_b128 v[168:171], v246 offset:8192\n\t"
"v_exp_f32 v206, v206\n\t"
"v_mfma_f32_16x16x32_bf16 v[156:159], a[220:223], v[140:143], v[156:159]\n\t"
"ds_read_b128 v[132:135], v7 offset:3072\n\t"
"buffer_load_dwordx4 v11, s[52:55], s77 offen offset:1024 lds\n\t"
"v_mfma_f32_16x16x32_bf16 v[200:203], a[64:67], v[184:187], 0\n\t"
"ds_read_b128 v[136:139], v8 offset:0\n\t"
"v_exp_f32 v207, v207\n\t"
"v_mfma_f32_16x16x32_bf16 v[200:203], a[68:71], v[188:191], v[200:203]\n\t"
"ds_read_b128 v[140:143], v8 offset:1024\n\t"
"v_cvt_pk_bf16_f32 v20, v204, v205\n\t"
"v_mfma_f32_16x16x32_bf16 v[200:203], a[72:75], v[192:195], v[200:203]\n\t"
"ds_read_b128 v[172:175], v246 offset:49152\n\t"
"v_cvt_pk_bf16_f32 v21, v206, v207\n\t"
"v_mfma_f32_16x16x32_bf16 v[200:203], a[76:79], v[196:199], v[200:203]\n\t"
"ds_read_b128 v[176:179], v247 offset:8192\n\t"
"v_exp_f32 v208, v208\n\t"
"v_mfma_f32_16x16x32_bf16 v[212:215], v[216:219], v[16:19], 0\n\t"
"s_waitcnt lgkmcnt(13)\n\t"
"v_exp_f32 v209, v209\n\t"
"v_mfma_f32_16x16x32_bf16 v[152:155], a[160:163], v[144:147], v[152:155]\n\t"
"s_waitcnt lgkmcnt(12)\n\t"
"buffer_load_dwordx4 v12, s[52:55], s77 offen offset:2048 lds\n\t"
"v_mfma_f32_16x16x32_bf16 v[152:155], a[164:167], v[148:151], v[152:155]\n\t"
"s_waitcnt lgkmcnt(6)\n\t"
"v_exp_f32 v210, v210\n\t"
"v_mfma_f32_16x16x32_bf16 v[152:155], a[168:171], v[128:131], v[152:155]\n\t"
"s_waitcnt lgkmcnt(4)\n\t"
"v_exp_f32 v211, v211\n\t"
"v_mfma_f32_16x16x32_bf16 v[152:155], a[172:175], v[132:135], v[152:155]\n\t"
"v_cvt_pk_bf16_f32 v22, v208, v209\n\t"
"v_cvt_pk_bf16_f32 v23, v210, v211\n\t"
"v_mfma_f32_16x16x32_bf16 v[204:207], a[80:83], v[184:187], 0\n\t"
"v_exp_f32 v200, v200\n\t"
"v_mfma_f32_16x16x32_bf16 v[204:207], a[84:87], v[188:191], v[204:207]\n\t"
"v_exp_f32 v201, v201\n\t"
"v_mfma_f32_16x16x32_bf16 v[204:207], a[88:91], v[192:195], v[204:207]\n\t"
"buffer_load_dwordx4 v13, s[52:55], s77 offen offset:3072 lds\n\t"
"v_mfma_f32_16x16x32_bf16 v[204:207], a[92:95], v[196:199], v[204:207]\n\t"
"s_add_u32 s77, s77, 32768\n\t"
"ds_read_b128 v[180:183], v247 offset:49152\n\t"
"v_exp_f32 v202, v202\n\t"
"v_mfma_f32_16x16x32_bf16 v[156:159], a[224:227], v[144:147], v[156:159]\n\t"
"ds_read_b128 v[144:147], v8 offset:2048\n\t"
"v_exp_f32 v203, v203\n\t"
"v_mfma_f32_16x16x32_bf16 v[156:159], a[228:231], v[148:151], v[156:159]\n\t"
"ds_read_b128 v[148:151], v8 offset:3072\n\t"
"v_cvt_pk_bf16_f32 v24, v200, v201\n\t"
"v_mfma_f32_16x16x32_bf16 v[156:159], a[232:235], v[128:131], v[156:159]\n\t"
"v_cvt_pk_bf16_f32 v25, v202, v203\n\t"
"v_exp_f32 v204, v204\n\t"
"v_mfma_f32_16x16x32_bf16 v[156:159], a[236:239], v[132:135], v[156:159]\n\t"
"v_exp_f32 v205, v205\n\t"
"v_mfma_f32_16x16x32_bf16 v[208:211], a[96:99], v[184:187], 0\n\t"
"ds_read_b128 v[220:223], v249 offset:512\n\t"
"v_exp_f32 v206, v206\n\t"
"v_mfma_f32_16x16x32_bf16 v[208:211], a[100:103], v[188:191], v[208:211]\n\t"
"v_exp_f32 v207, v207\n\t"
"v_mfma_f32_16x16x32_bf16 v[208:211], a[104:107], v[192:195], v[208:211]\n\t"
"v_cvt_pk_bf16_f32 v26, v204, v205\n\t"
"v_cvt_pk_bf16_f32 v27, v206, v207\n\t"
"v_mfma_f32_16x16x32_bf16 v[208:211], a[108:111], v[196:199], v[208:211]\n\t"
"v_mfma_f32_16x16x32_bf16 v[212:215], v[216:219], v[20:23], v[212:215]\n\t"
"s_waitcnt lgkmcnt(7)\n\t"
"v_mfma_f32_16x16x32_bf16 v[152:155], a[176:179], v[136:139], v[152:155]\n\t"
"s_waitcnt lgkmcnt(0)\n\t"
"v_add_f32 v220, v220, v221\n\t"
"v_add_f32 v222, v222, v223\n\t"
"v_add_f32 v220, v220, v222\n\t"
"v_mfma_f32_16x16x32_bf16 v[152:155], a[180:183], v[140:143], v[152:155]\n\t"
"v_rcp_f32 v230, v220\n\t"
"v_mfma_f32_16x16x32_bf16 v[152:155], a[184:187], v[144:147], v[152:155]\n\t"
"v_subrev_u32 v221, s24, v220\n\t"
"v_cmp_lt_u32 vcc, s25, v221\n\t"
"s_nop 0\n\t"
"s_cmp_eq_u64 vcc, 0\n\t"
"s_cbranch_scc0 LCK2_%=\n\t"
"v_mfma_f32_16x16x32_bf16 v[152:155], a[188:191], v[148:151], v[152:155]\n\t"
"v_exp_f32 v208, v208\n\t"
"v_mfma_f32_16x16x32_bf16 v[200:203], a[112:115], v[184:187], 0\n\t"
"v_exp_f32 v209, v209\n\t"
"v_mfma_f32_16x16x32_bf16 v[200:203], a[116:119], v[188:191], v[200:203]\n\t"
"v_exp_f32 v210, v210\n\t"
"v_mfma_f32_16x16x32_bf16 v[200:203], a[120:123], v[192:195], v[200:203]\n\t"
"v_exp_f32 v211, v211\n\t"
"v_cvt_pk_bf16_f32 v28, v208, v209\n\t"
"v_mfma_f32_16x16x32_bf16 v[200:203], a[124:127], v[196:199], v[200:203]\n\t"
"ds_read_b128 v[184:187], v246 offset:9216\n\t"
"v_cvt_pk_bf16_f32 v29, v210, v211\n\t"
"v_mfma_f32_16x16x32_bf16 v[156:159], a[240:243], v[136:139], v[156:159]\n\t"
"ds_read_b128 v[188:191], v246 offset:50176\n\t"
"v_mfma_f32_16x16x32_bf16 v[156:159], a[244:247], v[140:143], v[156:159]\n\t"
"ds_read_b128 v[192:195], v247 offset:9216\n\t"
"v_mfma_f32_16x16x32_bf16 v[156:159], a[248:251], v[144:147], v[156:159]\n\t"
"ds_read_b128 v[196:199], v247 offset:50176\n\t"
"v_exp_f32 v200, v200\n\t"
"v_mfma_f32_16x16x32_bf16 v[156:159], a[252:255], v[148:151], v[156:159]\n\t"
"s_barrier\n\t"
"ds_read_b128 v[128:131], v6 offset:4096\n\t"
"v_exp_f32 v201, v201\n\t"
"v_mfma_f32_16x16x32_bf16 v[204:207], a[0:3], v[168:171], 0\n\t"
"ds_read_b128 v[132:135], v6 offset:5120\n\t"
"v_exp_f32 v202, v202\n\t"
"v_mfma_f32_16x16x32_bf16 v[204:207], a[4:7], v[172:175], v[204:207]\n\t"
"ds_read_b128 v[136:139], v6 offset:6144\n\t"
"v_exp_f32 v203, v203\n\t"
"v_mfma_f32_16x16x32_bf16 v[204:207], a[8:11], v[176:179], v[204:207]\n\t"
"ds_read_b128 v[140:143], v6 offset:7168\n\t"
"v_cvt_pk_bf16_f32 v30, v200, v201\n\t"
"v_mfma_f32_16x16x32_bf16 v[204:207], a[12:15], v[180:183], v[204:207]\n\t"
"ds_read_b128 v[144:147], v7 offset:4096\n\t"
"v_cvt_pk_bf16_f32 v31, v202, v203\n\t"
"v_mfma_f32_16x16x32_bf16 v[212:215], v[216:219], v[24:27], v[212:215]\n\t"
"ds_read_b128 v[148:151], v7 offset:5120\n\t"
"v_mul_f32 v152, v152, v230\n\t"
"v_mfma_f32_16x16x32_bf16 v[160:163], a[128:131], v[64:67], 0\n\t"
"v_mul_f32 v153, v153, v230\n\t"
"v_mul_f32 v154, v154, v230\n\t"
"v_mfma_f32_16x16x32_bf16 v[160:163], a[132:135], v[68:71], v[160:163]\n\t"
"v_exp_f32 v204, v204\n\t"
"v_mfma_f32_16x16x32_bf16 v[160:163], a[136:139], v[72:75], v[160:163]\n\t"
"v_exp_f32 v205, v205\n\t"
"v_mfma_f32_16x16x32_bf16 v[160:163], a[140:143], v[76:79], v[160:163]\n\t"
"s_waitcnt lgkmcnt(9)\n\t"
"v_exp_f32 v206, v206\n\t"
"v_mfma_f32_16x16x32_bf16 v[208:211], a[0:3], v[184:187], 0\n\t"
"s_waitcnt lgkmcnt(8)\n\t"
"v_exp_f32 v207, v207\n\t"
"v_mfma_f32_16x16x32_bf16 v[208:211], a[4:7], v[188:191], v[208:211]\n\t"
"s_waitcnt lgkmcnt(7)\n\t"
"v_cvt_pk_bf16_f32 v32, v204, v205\n\t"
"v_cvt_pk_bf16_f32 v33, v206, v207\n\t"
"v_mfma_f32_16x16x32_bf16 v[208:211], a[8:11], v[192:195], v[208:211]\n\t"
"s_waitcnt lgkmcnt(6)\n\t"
"v_mul_f32 v155, v155, v230\n\t"
"v_mul_f32 v156, v156, v230\n\t"
"v_mul_f32 v157, v157, v230\n\t"
"v_mfma_f32_16x16x32_bf16 v[208:211], a[12:15], v[196:199], v[208:211]\n\t"
"v_mul_f32 v158, v158, v230\n\t"
"v_mul_f32 v159, v159, v230\n\t"
"v_mfma_f32_16x16x32_bf16 v[164:167], a[192:195], v[64:67], 0\n\t"
"v_cvt_pk_bf16_f32 v224, v152, v153\n\t"
"v_cvt_pk_bf16_f32 v225, v154, v155\n\t"
"v_mfma_f32_16x16x32_bf16 v[164:167], a[196:199], v[68:71], v[164:167]\n\t"
"ds_write_b128 v9, v[80:83] offset:0\n\t"
"v_mfma_f32_16x16x32_bf16 v[164:167], a[200:203], v[72:75], v[164:167]\n\t"
"v_cvt_pk_bf16_f32 v226, v156, v157\n\t"
"v_cvt_pk_bf16_f32 v227, v158, v159\n\t"
"v_mfma_f32_16x16x32_bf16 v[164:167], a[204:207], v[76:79], v[164:167]\n\t"
"buffer_store_dwordx4 v[224:227], v250, s[56:59], s98 offen sc1\n\t"
"v_mfma_f32_16x16x32_bf16 v[200:203], a[16:19], v[168:171], 0\n\t"
"s_add_u32 s98, s98, 32768\n\t"
"v_exp_f32 v208, v208\n\t"
"v_mfma_f32_16x16x32_bf16 v[200:203], a[20:23], v[172:175], v[200:203]\n\t"
"ds_write_b128 v9, v[84:87] offset:1024\n\t"
"v_mfma_f32_16x16x32_bf16 v[200:203], a[24:27], v[176:179], v[200:203]\n\t"
"v_exp_f32 v209, v209\n\t"
"v_mfma_f32_16x16x32_bf16 v[200:203], a[28:31], v[180:183], v[200:203]\n\t"
"v_exp_f32 v210, v210\n\t"
"v_mfma_f32_16x16x32_bf16 v[212:215], v[216:219], v[28:31], v[212:215]\n\t"
"s_waitcnt lgkmcnt(7)\n\t"
"v_exp_f32 v211, v211\n\t"
"v_mfma_f32_16x16x32_bf16 v[160:163], a[144:147], v[128:131], v[160:163]\n\t"
"s_waitcnt lgkmcnt(6)\n\t"
"ds_write_b128 v9, v[88:91] offset:2048\n\t"
"v_mfma_f32_16x16x32_bf16 v[160:163], a[148:151], v[132:135], v[160:163]\n\t"
"s_waitcnt lgkmcnt(6)\n\t"
"v_cvt_pk_bf16_f32 v48, v208, v209\n\t"
"v_cvt_pk_bf16_f32 v49, v210, v211\n\t"
"v_mfma_f32_16x16x32_bf16 v[160:163], a[152:155], v[136:139], v[160:163]\n\t"
"s_waitcnt lgkmcnt(5)\n\t"
"v_exp_f32 v200, v200\n\t"
"v_mfma_f32_16x16x32_bf16 v[160:163], a[156:159], v[140:143], v[160:163]\n\t"
"v_exp_f32 v201, v201\n\t"
"v_mfma_f32_16x16x32_bf16 v[204:207], a[16:19], v[184:187], 0\n\t"
"ds_write_b128 v9, v[92:95] offset:3072\n\t"
"v_mfma_f32_16x16x32_bf16 v[204:207], a[20:23], v[188:191], v[204:207]\n\t"
"v_exp_f32 v202, v202\n\t"
"v_mfma_f32_16x16x32_bf16 v[204:207], a[24:27], v[192:195], v[204:207]\n\t"
"v_exp_f32 v203, v203\n\t"
"v_mfma_f32_16x16x32_bf16 v[204:207], a[28:31], v[196:199], v[204:207]\n\t"
"v_cvt_pk_bf16_f32 v34, v200, v201\n\t"
"v_cvt_pk_bf16_f32 v35, v202, v203\n\t"
"v_mfma_f32_16x16x32_bf16 v[164:167], a[208:211], v[128:131], v[164:167]\n\t"
"ds_write_b32 v248, v212 offset:41472\n\t"
"v_mfma_f32_16x16x32_bf16 v[164:167], a[212:215], v[132:135], v[164:167]\n\t"
"ds_read_b128 v[128:131], v7 offset:6144\n\t"
"v_mfma_f32_16x16x32_bf16 v[164:167], a[216:219], v[136:139], v[164:167]\n\t"
"ds_read_b128 v[132:135], v7 offset:7168\n\t"
"v_exp_f32 v204, v204\n\t"
"v_mfma_f32_16x16x32_bf16 v[164:167], a[220:223], v[140:143], v[164:167]\n\t"
"ds_read_b128 v[136:139], v8 offset:4096\n\t"
"v_exp_f32 v205, v205\n\t"
"v_mfma_f32_16x16x32_bf16 v[208:211], a[32:35], v[168:171], 0\n\t"
"ds_read_b128 v[140:143], v8 offset:5120\n\t"
"v_exp_f32 v206, v206\n\t"
"v_mfma_f32_16x16x32_bf16 v[208:211], a[36:39], v[172:175], v[208:211]\n\t"
"v_exp_f32 v207, v207\n\t"
"v_mfma_f32_16x16x32_bf16 v[208:211], a[40:43], v[176:179], v[208:211]\n\t"
"s_waitcnt vmcnt(27)\n\t"
"ds_read_b64_tr_b16 a[0:1], v236 offset:0\n\t"
"v_cvt_pk_bf16_f32 v50, v204, v205\n\t"
"v_mfma_f32_16x16x32_bf16 v[208:211], a[44:47], v[180:183], v[208:211]\n\t"
"s_waitcnt lgkmcnt(11)\n\t"
"v_cvt_pk_bf16_f32 v51, v206, v207\n\t"
"v_mfma_f32_16x16x32_bf16 v[160:163], a[160:163], v[144:147], v[160:163]\n\t"
"s_waitcnt lgkmcnt(10)\n\t"
"v_mfma_f32_16x16x32_bf16 v[160:163], a[164:167], v[148:151], v[160:163]\n\t"
"s_waitcnt lgkmcnt(4)\n\t"
"ds_read_b64_tr_b16 a[4:5], v237 offset:0\n\t"
"v_mfma_f32_16x16x32_bf16 v[160:163], a[168:171], v[128:131], v[160:163]\n\t"
"s_waitcnt lgkmcnt(4)\n\t"
"v_mfma_f32_16x16x32_bf16 v[160:163], a[172:175], v[132:135], v[160:163]\n\t"
"v_mfma_f32_16x16x32_bf16 v[200:203], a[32:35], v[184:187], 0\n\t"
"ds_read_b64_tr_b16 a[8:9], v238 offset:0\n\t"
"v_exp_f32 v208, v208\n\t"
"v_mfma_f32_16x16x32_bf16 v[200:203], a[36:39], v[188:191], v[200:203]\n\t"
"v_exp_f32 v209, v209\n\t"
"v_mfma_f32_16x16x32_bf16 v[200:203], a[40:43], v[192:195], v[200:203]\n\t"
"v_exp_f32 v210, v210\n\t"
"v_mfma_f32_16x16x32_bf16 v[200:203], a[44:47], v[196:199], v[200:203]\n\t"
"ds_read_b64_tr_b16 a[12:13], v239 offset:0\n\t"
"v_exp_f32 v211, v211\n\t"
"v_mfma_f32_16x16x32_bf16 v[212:215], v[216:219], v[32:35], 0\n\t"
"v_cvt_pk_bf16_f32 v36, v208, v209\n\t"
"v_cvt_pk_bf16_f32 v37, v210, v211\n\t"
"v_mfma_f32_16x16x32_bf16 v[164:167], a[224:227], v[144:147], v[164:167]\n\t"
"ds_read_b128 v[144:147], v8 offset:6144\n\t"
"v_mfma_f32_16x16x32_bf16 v[164:167], a[228:231], v[148:151], v[164:167]\n\t"
"ds_read_b128 v[148:151], v8 offset:7168\n\t"
"v_exp_f32 v200, v200\n\t"
"v_mfma_f32_16x16x32_bf16 v[164:167], a[232:235], v[128:131], v[164:167]\n\t"
"ds_read_b64_tr_b16 a[16:17], v240 offset:0\n\t"
"v_exp_f32 v201, v201\n\t"
"v_mfma_f32_16x16x32_bf16 v[164:167], a[236:239], v[132:135], v[164:167]\n\t"
"v_exp_f32 v202, v202\n\t"
"v_mfma_f32_16x16x32_bf16 v[204:207], a[48:51], v[168:171], 0\n\t"
"ds_read_b128 v[220:223], v249 offset:768\n\t"
"v_exp_f32 v203, v203\n\t"
"v_mfma_f32_16x16x32_bf16 v[204:207], a[52:55], v[172:175], v[204:207]\n\t"
"ds_read_b64_tr_b16 a[20:21], v241 offset:0\n\t"
"v_cvt_pk_bf16_f32 v52, v200, v201\n\t"
"v_mfma_f32_16x16x32_bf16 v[204:207], a[56:59], v[176:179], v[204:207]\n\t"
"v_cvt_pk_bf16_f32 v53, v202, v203\n\t"
"v_mfma_f32_16x16x32_bf16 v[204:207], a[60:63], v[180:183], v[204:207]\n\t"
"s_waitcnt lgkmcnt(10)\n\t"
"ds_read_b64_tr_b16 a[24:25], v242 offset:0\n\t"
"v_mfma_f32_16x16x32_bf16 v[160:163], a[176:179], v[136:139], v[160:163]\n\t"
"s_waitcnt lgkmcnt(10)\n\t"
"s_add_u32 m0, s72, 0\n\t"
"v_mfma_f32_16x16x32_bf16 v[160:163], a[180:183], v[140:143], v[160:163]\n\t"
"s_waitcnt lgkmcnt(2)\n\t"
"v_add_f32 v220, v220, v221\n\t"
"v_add_f32 v222, v222, v223\n\t"
"v_add_f32 v220, v220, v222\n\t"
"v_mfma_f32_16x16x32_bf16 v[160:163], a[184:187], v[144:147], v[160:163]\n\t"
"ds_read_b64_tr_b16 a[28:29], v243 offset:0\n\t"
"s_waitcnt lgkmcnt(0)\n\t"
"buffer_load_dwordx4 v10, s[52:55], s77 offen lds\n\t"
"v_mfma_f32_16x16x32_bf16 v[160:163], a[188:191], v[148:151], v[160:163]\n\t"
"s_waitcnt vmcnt(24)\n\t"
"ds_read_b64_tr_b16 a[2:3], v236 offset:28672\n\t"
"v_rcp_f32 v230, v220\n\t"
"v_mfma_f32_16x16x32_bf16 v[208:211], a[48:51], v[184:187], 0\n\t"
"ds_read_b64_tr_b16 a[6:7], v237 offset:28672\n\t"
"v_subrev_u32 v221, s24, v220\n\t"
"v_cmp_lt_u32 vcc, s25, v221\n\t"
"s_nop 0\n\t"
"s_cmp_eq_u64 vcc, 0\n\t"
"s_cbranch_scc0 LCK3_%=\n\t"
"v_mfma_f32_16x16x32_bf16 v[208:211], a[52:55], v[188:191], v[208:211]\n\t"
"ds_read_b64_tr_b16 a[10:11], v238 offset:28672\n\t"
"v_exp_f32 v204, v204\n\t"
"v_mfma_f32_16x16x32_bf16 v[208:211], a[56:59], v[192:195], v[208:211]\n\t"
"v_exp_f32 v205, v205\n\t"
"v_mfma_f32_16x16x32_bf16 v[208:211], a[60:63], v[196:199], v[208:211]\n\t"
"buffer_load_dwordx4 v11, s[52:55], s77 offen offset:1024 lds\n\t"
"v_mfma_f32_16x16x32_bf16 v[164:167], a[240:243], v[136:139], v[164:167]\n\t"
"ds_read_b64_tr_b16 a[14:15], v239 offset:28672\n\t"
"v_exp_f32 v206, v206\n\t"
"v_mfma_f32_16x16x32_bf16 v[164:167], a[244:247], v[140:143], v[164:167]\n\t"
"v_exp_f32 v207, v207\n\t"
"v_mfma_f32_16x16x32_bf16 v[164:167], a[248:251], v[144:147], v[164:167]\n\t"
"ds_read_b64_tr_b16 a[18:19], v240 offset:28672\n\t"
"v_cvt_pk_bf16_f32 v38, v204, v205\n\t"
"v_mfma_f32_16x16x32_bf16 v[164:167], a[252:255], v[148:151], v[164:167]\n\t"
"s_barrier\n\t"
"ds_read_b128 v[128:131], v6 offset:0\n\t"
"v_cvt_pk_bf16_f32 v39, v206, v207\n\t"
"v_mfma_f32_16x16x32_bf16 v[200:203], a[64:67], v[168:171], 0\n\t"
"ds_read_b128 v[132:135], v6 offset:1024\n\t"
"v_or_b32 v228, 2, v4\n\t"
"v_mfma_f32_16x16x32_bf16 v[200:203], a[68:71], v[172:175], v[200:203]\n\t"
"ds_read_b128 v[136:139], v6 offset:2048\n\t"
"buffer_load_dwordx4 v12, s[52:55], s77 offen offset:2048 lds\n\t"
"v_mfma_f32_16x16x32_bf16 v[200:203], a[72:75], v[176:179], v[200:203]\n\t"
"ds_read_b128 v[140:143], v6 offset:3072\n\t"
"v_exp_f32 v208, v208\n\t"
"v_mfma_f32_16x16x32_bf16 v[200:203], a[76:79], v[180:183], v[200:203]\n\t"
"ds_read_b128 v[144:147], v7 offset:0\n\t"
"v_exp_f32 v209, v209\n\t"
"v_mfma_f32_16x16x32_bf16 v[152:155], a[128:131], v[80:83], 0\n\t"
"ds_read_b128 v[148:151], v7 offset:1024\n\t"
"v_exp_f32 v210, v210\n\t"
"v_mfma_f32_16x16x32_bf16 v[152:155], a[132:135], v[84:87], v[152:155]\n\t"
"ds_read_b64_tr_b16 a[22:23], v241 offset:28672\n\t"
"v_exp_f32 v211, v211\n\t"
"v_mfma_f32_16x16x32_bf16 v[152:155], a[136:139], v[88:91], v[152:155]\n\t"
"v_mul_f32 v160, v160, v230\n\t"
"v_mul_f32 v161, v161, v230\n\t"
"v_mul_f32 v162, v162, v230\n\t"
"v_mul_f32 v163, v163, v230\n\t"
"v_mul_f32 v164, v164, v230\n\t"
"v_mul_f32 v165, v165, v230\n\t"
"v_mul_f32 v166, v166, v230\n\t"
"v_mul_f32 v167, v167, v230\n\t"
"v_cvt_pk_bf16_f32 v224, v160, v161\n\t"
"v_cvt_pk_bf16_f32 v225, v162, v163\n\t"
"v_cvt_pk_bf16_f32 v226, v164, v165\n\t"
"v_cvt_pk_bf16_f32 v227, v166, v167\n\t"
"buffer_store_dwordx4 v[224:227], v250, s[56:59], s98 offen sc1\n\t"
"s_waitcnt vmcnt(9)\n\t"
"buffer_store_dwordx2 v[228:229], v5, s[56:59], s17 offen sc1\n\t"
"v_mfma_f32_16x16x32_bf16 v[152:155], a[140:143], v[92:95], v[152:155]\n\t"
"s_add_u32 s98, s98, 32768\n\t"
"buffer_load_dwordx4 v13, s[52:55], s77 offen offset:3072 lds\n\t"
"v_mfma_f32_16x16x32_bf16 v[204:207], a[64:67], v[184:187], 0\n\t"
"s_add_u32 m0, s72, 28672\n\t"
"ds_read_b64_tr_b16 a[26:27], v242 offset:28672\n\t"
"v_cvt_pk_bf16_f32 v54, v208, v209\n\t"
"v_mfma_f32_16x16x32_bf16 v[204:207], a[68:71], v[188:191], v[204:207]\n\t"
"s_add_u32 s77, s77, 229376\n\t"
"ds_read_b64_tr_b16 a[30:31], v243 offset:28672\n\t"
"v_cvt_pk_bf16_f32 v55, v210, v211\n\t"
"v_mfma_f32_16x16x32_bf16 v[204:207], a[72:75], v[192:195], v[204:207]\n\t"
"s_waitcnt lgkmcnt(0)\n\t"
"s_and_b32 s77, s77, 2097151\n\t"
"buffer_load_dwordx4 v10, s[52:55], s77 offen lds\n\t"
"v_mfma_f32_16x16x32_bf16 v[204:207], a[76:79], v[196:199], v[204:207]\n\t"
"ds_read_b64_tr_b16 a[32:33], v236 offset:32768\n\t"
"v_exp_f32 v200, v200\n\t"
"v_mfma_f32_16x16x32_bf16 v[212:215], v[216:219], v[36:39], v[212:215]\n\t"
"buffer_load_dwordx4 v11, s[52:55], s77 offen offset:1024 lds\n\t"
"v_mfma_f32_16x16x32_bf16 v[156:159], a[192:195], v[80:83], 0\n\t"
"ds_read_b64_tr_b16 a[36:37], v237 offset:32768\n\t"
"v_exp_f32 v201, v201\n\t"
"v_mfma_f32_16x16x32_bf16 v[156:159], a[196:199], v[84:87], v[156:159]\n\t"
"ds_write_b128 v9, v[96:99] offset:4096\n\t"
"v_mfma_f32_16x16x32_bf16 v[156:159], a[200:203], v[88:91], v[156:159]\n\t"
"v_exp_f32 v202, v202\n\t"
"v_mfma_f32_16x16x32_bf16 v[156:159], a[204:207], v[92:95], v[156:159]\n\t"
"v_exp_f32 v203, v203\n\t"
"v_mfma_f32_16x16x32_bf16 v[208:211], a[80:83], v[168:171], 0\n\t"
"v_cvt_pk_bf16_f32 v40, v200, v201\n\t"
"v_cvt_pk_bf16_f32 v41, v202, v203\n\t"
"v_mfma_f32_16x16x32_bf16 v[208:211], a[84:87], v[172:175], v[208:211]\n\t"
"ds_write_b128 v9, v[100:103] offset:5120\n\t"
"v_mfma_f32_16x16x32_bf16 v[208:211], a[88:91], v[176:179], v[208:211]\n\t"
"buffer_load_dwordx4 v12, s[52:55], s77 offen offset:2048 lds\n\t"
"v_mfma_f32_16x16x32_bf16 v[208:211], a[92:95], v[180:183], v[208:211]\n\t"
"v_exp_f32 v204, v204\n\t"
"v_mfma_f32_16x16x32_bf16 v[152:155], a[144:147], v[128:131], v[152:155]\n\t"
"v_exp_f32 v205, v205\n\t"
"v_mfma_f32_16x16x32_bf16 v[152:155], a[148:151], v[132:135], v[152:155]\n\t"
"ds_read_b64_tr_b16 a[40:41], v238 offset:32768\n\t"
"v_exp_f32 v206, v206\n\t"
"v_mfma_f32_16x16x32_bf16 v[152:155], a[152:155], v[136:139], v[152:155]\n\t"
"ds_write_b128 v9, v[104:107] offset:6144\n\t"
"v_mfma_f32_16x16x32_bf16 v[152:155], a[156:159], v[140:143], v[152:155]\n\t"
"buffer_load_dwordx4 v13, s[52:55], s77 offen offset:3072 lds\n\t"
"v_mfma_f32_16x16x32_bf16 v[200:203], a[80:83], v[184:187], 0\n\t"
"s_add_u32 s77, s77, 32768\n\t"
"v_exp_f32 v207, v207\n\t"
"v_mfma_f32_16x16x32_bf16 v[200:203], a[84:87], v[188:191], v[200:203]\n\t"
"ds_read_b64_tr_b16 a[44:45], v239 offset:32768\n\t"
"v_cvt_pk_bf16_f32 v56, v204, v205\n\t"
"v_mfma_f32_16x16x32_bf16 v[200:203], a[88:91], v[192:195], v[200:203]\n\t"
"ds_write_b128 v9, v[108:111] offset:7168\n\t"
"v_mfma_f32_16x16x32_bf16 v[200:203], a[92:95], v[196:199], v[200:203]\n\t"
"v_cvt_pk_bf16_f32 v57, v206, v207\n\t"
"v_exp_f32 v208, v208\n\t"
"v_mfma_f32_16x16x32_bf16 v[156:159], a[208:211], v[128:131], v[156:159]\n\t"
"ds_read_b64_tr_b16 a[48:49], v240 offset:32768\n\t"
"v_exp_f32 v209, v209\n\t"
"v_mfma_f32_16x16x32_bf16 v[156:159], a[212:215], v[132:135], v[156:159]\n\t"
"v_exp_f32 v210, v210\n\t"
"v_mfma_f32_16x16x32_bf16 v[156:159], a[216:219], v[136:139], v[156:159]\n\t"
"ds_read_b128 v[128:131], v7 offset:2048\n\t"
"v_exp_f32 v211, v211\n\t"
"v_mfma_f32_16x16x32_bf16 v[156:159], a[220:223], v[140:143], v[156:159]\n\t"
"ds_read_b64_tr_b16 a[52:53], v241 offset:32768\n\t"
"ds_read_b128 v[132:135], v7 offset:3072\n\t"
"v_mfma_f32_16x16x32_bf16 v[204:207], a[96:99], v[168:171], 0\n\t"
"ds_read_b64_tr_b16 a[56:57], v242 offset:32768\n\t"
"ds_read_b64_tr_b16 a[60:61], v243 offset:32768\n\t"
"ds_read_b64_tr_b16 a[34:35], v236 offset:12288\n\t"
"ds_read_b128 v[136:139], v8 offset:0\n\t"
"v_mfma_f32_16x16x32_bf16 v[204:207], a[100:103], v[172:175], v[204:207]\n\t"
"ds_read_b128 v[140:143], v8 offset:1024\n\t"
"v_cvt_pk_bf16_f32 v42, v208, v209\n\t"
"v_mfma_f32_16x16x32_bf16 v[204:207], a[104:107], v[176:179], v[204:207]\n\t"
"ds_read_b64_tr_b16 a[38:39], v237 offset:12288\n\t"
"v_cvt_pk_bf16_f32 v43, v210, v211\n\t"
"v_mfma_f32_16x16x32_bf16 v[204:207], a[108:111], v[180:183], v[204:207]\n\t"
"ds_read_b64_tr_b16 a[42:43], v238 offset:12288\n\t"
"v_exp_f32 v200, v200\n\t"
"v_mfma_f32_16x16x32_bf16 v[152:155], a[160:163], v[144:147], v[152:155]\n\t"
"ds_read_b64_tr_b16 a[46:47], v239 offset:12288\n\t"
"v_exp_f32 v201, v201\n\t"
"v_mfma_f32_16x16x32_bf16 v[152:155], a[164:167], v[148:151], v[152:155]\n\t"
"s_waitcnt lgkmcnt(10)\n\t"
"s_add_u32 m0, s72, 12288\n\t"
"ds_read_b64_tr_b16 a[50:51], v240 offset:12288\n\t"
"v_exp_f32 v202, v202\n\t"
"v_mfma_f32_16x16x32_bf16 v[152:155], a[168:171], v[128:131], v[152:155]\n\t"
"s_waitcnt lgkmcnt(9)\n\t"
"ds_read_b64_tr_b16 a[54:55], v241 offset:12288\n\t"
"v_exp_f32 v203, v203\n\t"
"v_mfma_f32_16x16x32_bf16 v[152:155], a[172:175], v[132:135], v[152:155]\n\t"
"ds_read_b64_tr_b16 a[58:59], v242 offset:12288\n\t"
"v_cvt_pk_bf16_f32 v58, v200, v201\n\t"
"v_mfma_f32_16x16x32_bf16 v[208:211], a[96:99], v[184:187], 0\n\t"
"ds_read_b64_tr_b16 a[62:63], v243 offset:12288\n\t"
"s_waitcnt lgkmcnt(0)\n\t"
"buffer_load_dwordx4 v10, s[52:55], s77 offen lds\n\t"
"v_mfma_f32_16x16x32_bf16 v[208:211], a[100:103], v[188:191], v[208:211]\n\t"
"ds_read_b64_tr_b16 a[64:65], v236 offset:24576\n\t"
"v_cvt_pk_bf16_f32 v59, v202, v203\n\t"
"v_mfma_f32_16x16x32_bf16 v[208:211], a[104:107], v[192:195], v[208:211]\n\t"
"ds_read_b64_tr_b16 a[68:69], v237 offset:24576\n\t"
"v_exp_f32 v204, v204\n\t"
"v_mfma_f32_16x16x32_bf16 v[208:211], a[108:111], v[196:199], v[208:211]\n\t"
"ds_read_b64_tr_b16 a[72:73], v238 offset:24576\n\t"
"v_exp_f32 v205, v205\n\t"
"v_mfma_f32_16x16x32_bf16 v[212:215], v[216:219], v[40:43], v[212:215]\n\t"
"buffer_load_dwordx4 v11, s[52:55], s77 offen offset:1024 lds\n\t"
"v_mfma_f32_16x16x32_bf16 v[156:159], a[224:227], v[144:147], v[156:159]\n\t"
"ds_read_b128 v[144:147], v8 offset:2048\n\t"
"v_exp_f32 v206, v206\n\t"
"v_mfma_f32_16x16x32_bf16 v[156:159], a[228:231], v[148:151], v[156:159]\n\t"
"ds_read_b128 v[148:151], v8 offset:3072\n\t"
"v_exp_f32 v207, v207\n\t"
"v_mfma_f32_16x16x32_bf16 v[156:159], a[232:235], v[128:131], v[156:159]\n\t"
"ds_read_b64_tr_b16 a[76:77], v239 offset:24576\n\t"
"v_cvt_pk_bf16_f32 v44, v204, v205\n\t"
"v_mfma_f32_16x16x32_bf16 v[156:159], a[236:239], v[132:135], v[156:159]\n\t"
"ds_read_b64_tr_b16 a[80:81], v240 offset:24576\n\t"
"v_cvt_pk_bf16_f32 v45, v206, v207\n\t"
"v_mfma_f32_16x16x32_bf16 v[200:203], a[112:115], v[168:171], 0\n\t"
"ds_read_b128 v[220:223], v249 offset:1024\n\t"
"v_exp_f32 v208, v208\n\t"
"v_mfma_f32_16x16x32_bf16 v[200:203], a[116:119], v[172:175], v[200:203]\n\t"
"buffer_load_dwordx4 v12, s[52:55], s77 offen offset:2048 lds\n\t"
"v_mfma_f32_16x16x32_bf16 v[200:203], a[120:123], v[176:179], v[200:203]\n\t"
"ds_read_b64_tr_b16 a[84:85], v241 offset:24576\n\t"
"v_exp_f32 v209, v209\n\t"
"v_mfma_f32_16x16x32_bf16 v[200:203], a[124:127], v[180:183], v[200:203]\n\t"
"v_exp_f32 v210, v210\n\t"
"v_mfma_f32_16x16x32_bf16 v[152:155], a[176:179], v[136:139], v[152:155]\n\t"
"ds_read_b64_tr_b16 a[88:89], v242 offset:24576\n\t"
"v_exp_f32 v211, v211\n\t"
"v_mfma_f32_16x16x32_bf16 v[152:155], a[180:183], v[140:143], v[152:155]\n\t"
"s_waitcnt lgkmcnt(6)\n\t"
"v_cvt_pk_bf16_f32 v60, v208, v209\n\t"
"v_cvt_pk_bf16_f32 v61, v210, v211\n\t"
"v_mfma_f32_16x16x32_bf16 v[152:155], a[184:187], v[144:147], v[152:155]\n\t"
"s_waitcnt lgkmcnt(2)\n\t"
"v_add_f32 v220, v220, v221\n\t"
"v_add_f32 v222, v222, v223\n\t"
"v_mfma_f32_16x16x32_bf16 v[152:155], a[188:191], v[148:151], v[152:155]\n\t"
"buffer_load_dwordx4 v13, s[52:55], s77 offen offset:3072 lds\n\t"
"s_add_u32 m0, s72, 32768\n\t"
"s_add_u32 s77, s77, 229376\n\t"
"s_and_b32 s77, s77, 2097151\n\t"
"buffer_load_dwordx4 v10, s[52:55], s77 offen lds\n\t"
"buffer_load_dwordx4 v11, s[52:55], s77 offen offset:1024 lds\n\t"
"v_mfma_f32_16x16x32_bf16 v[204:207], a[112:115], v[184:187], 0\n\t"
"buffer_load_dwordx4 v12, s[52:55], s77 offen offset:2048 lds\n\t"
"v_mfma_f32_16x16x32_bf16 v[204:207], a[116:119], v[188:191], v[204:207]\n\t"
"buffer_load_dwordx4 v13, s[52:55], s77 offen offset:3072 lds\n\t"
"v_mfma_f32_16x16x32_bf16 v[204:207], a[120:123], v[192:195], v[204:207]\n\t"
"s_add_u32 m0, s72, 24576\n\t"
"ds_read_b64_tr_b16 a[92:93], v243 offset:24576\n\t"
"v_add_f32 v220, v220, v222\n\t"
"v_mfma_f32_16x16x32_bf16 v[204:207], a[124:127], v[196:199], v[204:207]\n\t"
"s_waitcnt lgkmcnt(0)\n\t"
"s_add_u32 s77, s77, 32768\n\t"
"buffer_load_dwordx4 v10, s[52:55], s77 offen lds\n\t"
"v_mfma_f32_16x16x32_bf16 v[156:159], a[240:243], v[136:139], v[156:159]\n\t"
"ds_read_b64_tr_b16 a[66:67], v236 offset:36864\n\t"
"buffer_load_dwordx4 v11, s[52:55], s77 offen offset:1024 lds\n\t"
"v_mfma_f32_16x16x32_bf16 v[156:159], a[244:247], v[140:143], v[156:159]\n\t"
"ds_read_b64_tr_b16 a[70:71], v237 offset:36864\n\t"
"v_rcp_f32 v230, v220\n\t"
"v_mfma_f32_16x16x32_bf16 v[156:159], a[248:251], v[144:147], v[156:159]\n\t"
"ds_read_b64_tr_b16 a[74:75], v238 offset:36864\n\t"
"v_subrev_u32 v221, s24, v220\n\t"
"v_cmp_lt_u32 vcc, s25, v221\n\t"
"s_nop 0\n\t"
"s_cmp_eq_u64 vcc, 0\n\t"
"s_cbranch_scc0 LCK4_%=\n\t"
"v_mfma_f32_16x16x32_bf16 v[156:159], a[252:255], v[148:151], v[156:159]\n\t"
"s_barrier\n\t"
"ds_read_b128 v[128:131], v6 offset:4096\n\t"
"v_exp_f32 v200, v200\n\t"
"v_mfma_f32_16x16x32_bf16 v[160:163], a[128:131], v[96:99], 0\n\t"
"ds_read_b128 v[132:135], v6 offset:5120\n\t"
"v_exp_f32 v201, v201\n\t"
"v_mfma_f32_16x16x32_bf16 v[160:163], a[132:135], v[100:103], v[160:163]\n\t"
"ds_read_b128 v[136:139], v6 offset:6144\n\t"
"buffer_load_dwordx2 v[2:3], v15, s[56:59], s16 offen sc1\n\t"
"v_mfma_f32_16x16x32_bf16 v[160:163], a[136:139], v[104:107], v[160:163]\n\t"
"ds_read_b128 v[140:143], v6 offset:7168\n\t"
"buffer_load_dwordx4 v12, s[52:55], s77 offen offset:2048 lds\n\t"
"v_mfma_f32_16x16x32_bf16 v[160:163], a[140:143], v[108:111], v[160:163]\n\t"
"ds_read_b64_tr_b16 a[78:79], v239 offset:36864\n\t"
"v_exp_f32 v202, v202\n\t"
"v_exp_f32 v203, v203\n\t"
"v_cvt_pk_bf16_f32 v46, v200, v201\n\t"
"v_cvt_pk_bf16_f32 v47, v202, v203\n\t"
"s_nop 1\n\t"
"v_mfma_f32_16x16x32_bf16 v[212:215], v[216:219], v[44:47], v[212:215]\n\t"
"ds_read_b128 v[144:147], v7 offset:4096\n\t"
"v_exp_f32 v204, v204\n\t"
"v_mfma_f32_16x16x32_bf16 v[164:167], a[192:195], v[96:99], 0\n\t"
"ds_read_b128 v[148:151], v7 offset:5120\n\t"
"v_exp_f32 v205, v205\n\t"
"v_mfma_f32_16x16x32_bf16 v[164:167], a[196:199], v[100:103], v[164:167]\n\t"
"ds_read_b128 v[200:203], v7 offset:6144\n\t"
"v_exp_f32 v206, v206\n\t"
"v_mfma_f32_16x16x32_bf16 v[164:167], a[200:203], v[104:107], v[164:167]\n\t"
"ds_read_b64_tr_b16 a[82:83], v240 offset:36864\n\t"
"v_exp_f32 v207, v207\n\t"
"v_mfma_f32_16x16x32_bf16 v[164:167], a[204:207], v[108:111], v[164:167]\n\t"
"s_waitcnt lgkmcnt(8)\n\t"
"buffer_load_dwordx4 v13, s[52:55], s77 offen offset:3072 lds\n\t"
"s_add_u32 m0, s72, 36864\n\t"
"v_mfma_f32_16x16x32_bf16 v[160:163], a[144:147], v[128:131], v[160:163]\n\t"
"ds_read_b64_tr_b16 a[86:87], v241 offset:36864\n\t"
"ds_read_b64_tr_b16 a[90:91], v242 offset:36864\n\t"
"ds_read_b64_tr_b16 a[94:95], v243 offset:36864\n\t"
"s_add_u32 s77, s77, 229376\n\t"
"s_and_b32 s77, s77, 2097151\n\t"
"s_waitcnt lgkmcnt(0)\n\t"
"buffer_load_dwordx4 v10, s[52:55], s77 offen lds\n\t"
"v_mfma_f32_16x16x32_bf16 v[160:163], a[148:151], v[132:135], v[160:163]\n\t"
"ds_write_b128 v9, v[112:115] offset:0\n\t"
"v_mfma_f32_16x16x32_bf16 v[160:163], a[152:155], v[136:139], v[160:163]\n\t"
"buffer_load_dwordx4 v11, s[52:55], s77 offen offset:1024 lds\n\t"
"v_mfma_f32_16x16x32_bf16 v[160:163], a[156:159], v[140:143], v[160:163]\n\t"
"v_cvt_pk_bf16_f32 v62, v204, v205\n\t"
"v_cvt_pk_bf16_f32 v63, v206, v207\n\t"
"ds_read_b128 v[204:207], v7 offset:7168\n\t"
"ds_write_b32 v248, v212 offset:41728\n\t"
"v_mfma_f32_16x16x32_bf16 v[212:215], v[216:219], v[48:51], 0\n\t"
"buffer_load_dwordx4 v12, s[52:55], s77 offen offset:2048 lds\n\t"
"v_mfma_f32_16x16x32_bf16 v[164:167], a[208:211], v[128:131], v[164:167]\n\t"
"v_mul_f32 v152, v152, v230\n\t"
"v_mul_f32 v153, v153, v230\n\t"
"v_mul_f32 v154, v154, v230\n\t"
"v_mul_f32 v155, v155, v230\n\t"
"v_mul_f32 v156, v156, v230\n\t"
"v_mul_f32 v157, v157, v230\n\t"
"v_mul_f32 v158, v158, v230\n\t"
"v_mul_f32 v159, v159, v230\n\t"
"v_cvt_pk_bf16_f32 v224, v152, v153\n\t"
"v_cvt_pk_bf16_f32 v225, v154, v155\n\t"
"v_cvt_pk_bf16_f32 v226, v156, v157\n\t"
"v_cvt_pk_bf16_f32 v227, v158, v159\n\t"
"v_mfma_f32_16x16x32_bf16 v[164:167], a[212:215], v[132:135], v[164:167]\n\t"
"s_waitcnt vmcnt(27)\n\t"
"ds_read_b64_tr_b16 a[96:97], v236 offset:4096\n\t"
"buffer_load_dwordx4 v13, s[52:55], s77 offen offset:3072 lds\n\t"
"ds_write_b128 v9, v[116:119] offset:1024\n\t"
"v_mfma_f32_16x16x32_bf16 v[164:167], a[216:219], v[136:139], v[164:167]\n\t"
"buffer_store_dwordx4 v[224:227], v250, s[56:59], s98 offen sc1\n\t"
"ds_read_b64_tr_b16 a[100:101], v237 offset:4096\n\t"
"ds_read_b128 v[128:131], v8 offset:4096\n\t"
"v_mfma_f32_16x16x32_bf16 v[164:167], a[220:223], v[140:143], v[164:167]\n\t"
"s_add_u32 m0, s72, 4096\n\t"
"ds_read_b64_tr_b16 a[104:105], v238 offset:4096\n\t"
"ds_read_b128 v[132:135], v8 offset:5120\n\t"
"v_mfma_f32_16x16x32_bf16 v[212:215], v[216:219], v[52:55], v[212:215]\n\t"
"ds_read_b64_tr_b16 a[108:109], v239 offset:4096\n\t"
"ds_read_b64_tr_b16 a[112:113], v240 offset:4096\n\t"
"ds_read_b64_tr_b16 a[116:117], v241 offset:4096\n\t"
"ds_read_b64_tr_b16 a[120:121], v242 offset:4096\n\t"
"ds_read_b64_tr_b16 a[124:125], v243 offset:4096\n\t"
"s_add_u32 s77, s77, 32768\n\t"
"s_waitcnt lgkmcnt(0)\n\t"
"buffer_load_dwordx4 v10, s[52:55], s77 offen lds\n\t"
"ds_read_b128 v[136:139], v8 offset:6144\n\t"
"v_mfma_f32_16x16x32_bf16 v[160:163], a[160:163], v[144:147], v[160:163]\n\t"
"ds_read_b128 v[140:143], v8 offset:7168\n\t"
"s_add_u32 s98, s98, 32768\n\t"
"buffer_load_dwordx4 v11, s[52:55], s77 offen offset:1024 lds\n\t"
"v_mfma_f32_16x16x32_bf16 v[160:163], a[164:167], v[148:151], v[160:163]\n\t"
"ds_write_b128 v9, v[120:123] offset:2048\n\t"
"v_mfma_f32_16x16x32_bf16 v[160:163], a[168:171], v[200:203], v[160:163]\n\t"
"buffer_load_dwordx4 v12, s[52:55], s77 offen offset:2048 lds\n\t"
"v_mfma_f32_16x16x32_bf16 v[160:163], a[172:175], v[204:207], v[160:163]\n\t"
"buffer_load_dwordx4 v13, s[52:55], s77 offen offset:3072 lds\n\t"
"v_mfma_f32_16x16x32_bf16 v[212:215], v[216:219], v[56:59], v[212:215]\n\t"
"s_add_u32 s77, s77, 229376\n\t"
"s_and_b32 s77, s77, 2097151\n\t"
"v_mfma_f32_16x16x32_bf16 v[164:167], a[224:227], v[144:147], v[164:167]\n\t"
"ds_write_b128 v9, v[124:127] offset:3072\n\t"
"v_mfma_f32_16x16x32_bf16 v[164:167], a[228:231], v[148:151], v[164:167]\n\t"
"v_mfma_f32_16x16x32_bf16 v[164:167], a[232:235], v[200:203], v[164:167]\n\t"
"v_mfma_f32_16x16x32_bf16 v[164:167], a[236:239], v[204:207], v[164:167]\n\t"
"v_mfma_f32_16x16x32_bf16 v[212:215], v[216:219], v[60:63], v[212:215]\n\t"
"ds_read_b128 v[220:223], v249 offset:40960\n\t"
"v_mfma_f32_16x16x32_bf16 v[160:163], a[176:179], v[128:131], v[160:163]\n\t"
"s_waitcnt vmcnt(26)\n\t"
"ds_read_b64_tr_b16 a[98:99], v236 offset:0\n\t"
"v_mfma_f32_16x16x32_bf16 v[160:163], a[180:183], v[132:135], v[160:163]\n\t"
"s_waitcnt lgkmcnt(5)\n\t"
"ds_read_b64_tr_b16 a[102:103], v237 offset:0\n\t"
"v_mfma_f32_16x16x32_bf16 v[160:163], a[184:187], v[136:139], v[160:163]\n\t"
"s_waitcnt lgkmcnt(5)\n\t"
"ds_read_b64_tr_b16 a[106:107], v238 offset:0\n\t"
"v_mfma_f32_16x16x32_bf16 v[160:163], a[188:191], v[140:143], v[160:163]\n\t"
"s_add_u32 m0, s72, 0\n\t"
"ds_read_b64_tr_b16 a[110:111], v239 offset:0\n\t"
"v_mfma_f32_16x16x32_bf16 v[164:167], a[240:243], v[128:131], v[164:167]\n\t"
"s_waitcnt lgkmcnt(4)\n\t"
"ds_read_b64_tr_b16 a[114:115], v240 offset:0\n\t"
"v_add_f32 v220, v220, v221\n\t"
"v_add_f32 v222, v222, v223\n\t"
"v_mfma_f32_16x16x32_bf16 v[164:167], a[244:247], v[132:135], v[164:167]\n\t"
"ds_read_b64_tr_b16 a[118:119], v241 offset:0\n\t"
"v_add_f32 v220, v220, v222\n\t"
"v_mfma_f32_16x16x32_bf16 v[164:167], a[248:251], v[136:139], v[164:167]\n\t"
"ds_read_b64_tr_b16 a[122:123], v242 offset:0\n\t"
"v_rcp_f32 v230, v220\n\t"
"v_mfma_f32_16x16x32_bf16 v[164:167], a[252:255], v[140:143], v[164:167]\n\t"
"v_subrev_u32 v221, s24, v220\n\t"
"v_cmp_lt_u32 vcc, s25, v221\n\t"
"s_nop 0\n\t"
"s_cmp_eq_u64 vcc, 0\n\t"
"s_cbranch_scc0 LCK5_%=\n\t"
"ds_write_b32 v248, v212 offset:41984\n\t"
"s_waitcnt lgkmcnt(0)\n\t"
"s_barrier\n\t"
"v_mfma_f32_16x16x32_bf16 v[152:155], a[128:131], v[112:115], 0\n\t"
"ds_read_b128 v[128:131], v6 offset:0\n\t"
"v_mul_f32 v160, v160, v230\n\t"
"v_mfma_f32_16x16x32_bf16 v[152:155], a[132:135], v[116:119], v[152:155]\n\t"
"ds_read_b128 v[132:135], v6 offset:1024\n\t"
"v_mul_f32 v161, v161, v230\n\t"
"v_mfma_f32_16x16x32_bf16 v[152:155], a[136:139], v[120:123], v[152:155]\n\t"
"ds_read_b64_tr_b16 a[126:127], v243 offset:0\n\t"
"s_waitcnt lgkmcnt(0)\n\t"
"buffer_load_dwordx4 v10, s[52:55], s77 offen lds\n\t"
"v_mfma_f32_16x16x32_bf16 v[152:155], a[140:143], v[124:127], v[152:155]\n\t"
"ds_read_b128 v[136:139], v6 offset:2048\n\t"
"buffer_load_dwordx4 v11, s[52:55], s77 offen offset:1024 lds\n\t"
"v_mfma_f32_16x16x32_bf16 v[156:159], a[192:195], v[112:115], 0\n\t"
"ds_read_b128 v[140:143], v6 offset:3072\n\t"
"buffer_load_dwordx4 v12, s[52:55], s77 offen offset:2048 lds\n\t"
"v_mfma_f32_16x16x32_bf16 v[156:159], a[196:199], v[116:119], v[156:159]\n\t"
"buffer_load_dwordx4 v13, s[52:55], s77 offen offset:3072 lds\n\t"
"v_mfma_f32_16x16x32_bf16 v[156:159], a[200:203], v[120:123], v[156:159]\n\t"
"s_add_u32 s77, s77, 32768\n\t"
"ds_read_b128 v[144:147], v7 offset:0\n\t"
"v_mul_f32 v162, v162, v230\n\t"
"v_mfma_f32_16x16x32_bf16 v[156:159], a[204:207], v[124:127], v[156:159]\n\t"
"ds_read_b128 v[148:151], v7 offset:1024\n\t"
"v_mul_f32 v163, v163, v230\n\t"
"v_mfma_f32_16x16x32_bf16 v[152:155], a[144:147], v[128:131], v[152:155]\n\t"
"ds_read_b128 v[200:203], v7 offset:2048\n\t"
"v_mul_f32 v164, v164, v230\n\t"
"v_mfma_f32_16x16x32_bf16 v[152:155], a[148:151], v[132:135], v[152:155]\n\t"
"s_waitcnt lgkmcnt(4)\n\t"
"ds_write_b128 v9, v[16:19] offset:4096\n\t"
"v_mfma_f32_16x16x32_bf16 v[152:155], a[152:155], v[136:139], v[152:155]\n\t"
"s_waitcnt lgkmcnt(4)\n\t"
"v_mul_f32 v165, v165, v230\n\t"
"v_mul_f32 v166, v166, v230\n\t"
"v_mfma_f32_16x16x32_bf16 v[152:155], a[156:159], v[140:143], v[152:155]\n\t"
"v_mul_f32 v167, v167, v230\n\t"
"v_cvt_pk_bf16_f32 v224, v160, v161\n\t"
"v_mfma_f32_16x16x32_bf16 v[156:159], a[208:211], v[128:131], v[156:159]\n\t"
"ds_read_b128 v[204:207], v7 offset:3072\n\t"
"v_cvt_pk_bf16_f32 v225, v162, v163\n\t"
"v_mfma_f32_16x16x32_bf16 v[156:159], a[212:215], v[132:135], v[156:159]\n\t"
"ds_read_b128 v[128:131], v8 offset:0\n\t"
"v_cvt_pk_bf16_f32 v226, v164, v165\n\t"
"v_mfma_f32_16x16x32_bf16 v[156:159], a[216:219], v[136:139], v[156:159]\n\t"
"v_cvt_pk_bf16_f32 v227, v166, v167\n\t"
"v_mfma_f32_16x16x32_bf16 v[156:159], a[220:223], v[140:143], v[156:159]\n\t"
"s_waitcnt lgkmcnt(5)\n\t"
"ds_write_b128 v9, v[20:23] offset:5120\n\t"
"ds_read_b128 v[132:135], v8 offset:1024\n\t"
"v_mfma_f32_16x16x32_bf16 v[152:155], a[160:163], v[144:147], v[152:155]\n\t"
"s_waitcnt lgkmcnt(6)\n\t"
"buffer_store_dwordx4 v[224:227], v250, s[56:59], s98 offen sc1\n\t"
"ds_read_b128 v[136:139], v8 offset:2048\n\t"
"v_mfma_f32_16x16x32_bf16 v[152:155], a[164:167], v[148:151], v[152:155]\n\t"
"s_waitcnt lgkmcnt(6)\n\t"
"ds_read_b128 v[140:143], v8 offset:3072\n\t"
"s_add_u32 m0, s72, 28672\n\t"
"v_or_b32 v228, 4, v4\n\t"
"v_mfma_f32_16x16x32_bf16 v[152:155], a[168:171], v[200:203], v[152:155]\n\t"
"s_waitcnt lgkmcnt(5)\n\t"
"s_add_u32 s98, s98, 32768\n\t"
"buffer_store_dwordx2 v[228:229], v5, s[56:59], s17 offen sc1\n\t"
"v_mfma_f32_16x16x32_bf16 v[152:155], a[172:175], v[204:207], v[152:155]\n\t"
"ds_write_b128 v9, v[24:27] offset:6144\n\t"
"v_mfma_f32_16x16x32_bf16 v[156:159], a[224:227], v[144:147], v[156:159]\n\t"
"s_waitcnt vmcnt(28)\n\t"
"ds_read_b64_tr_b16 v[64:65], v236 offset:28672\n\t"
"v_mfma_f32_16x16x32_bf16 v[156:159], a[228:231], v[148:151], v[156:159]\n\t"
"ds_read_b64_tr_b16 v[68:69], v237 offset:28672\n\t"
"v_mfma_f32_16x16x32_bf16 v[156:159], a[232:235], v[200:203], v[156:159]\n\t"
"ds_read_b64_tr_b16 v[72:73], v238 offset:28672\n\t"
"ds_read_b64_tr_b16 v[76:77], v239 offset:28672\n\t"
"ds_read_b64_tr_b16 v[80:81], v240 offset:28672\n\t"
"ds_read_b64_tr_b16 v[84:85], v241 offset:28672\n\t"
"ds_read_b64_tr_b16 v[88:89], v242 offset:28672\n\t"
"ds_read_b64_tr_b16 v[92:93], v243 offset:28672\n\t"
"s_waitcnt lgkmcnt(0)\n\t"
"buffer_load_dwordx4 v10, s[52:55], s77 offen lds\n\t"
"v_mfma_f32_16x16x32_bf16 v[156:159], a[236:239], v[204:207], v[156:159]\n\t"
"buffer_load_dwordx4 v11, s[52:55], s77 offen offset:1024 lds\n\t"
"v_mfma_f32_16x16x32_bf16 v[152:155], a[176:179], v[128:131], v[152:155]\n\t"
"ds_write_b128 v9, v[28:31] offset:7168\n\t"
"v_mfma_f32_16x16x32_bf16 v[152:155], a[180:183], v[132:135], v[152:155]\n\t"
"buffer_load_dwordx4 v12, s[52:55], s77 offen offset:2048 lds\n\t"
"v_mfma_f32_16x16x32_bf16 v[152:155], a[184:187], v[136:139], v[152:155]\n\t"
"buffer_load_dwordx4 v13, s[52:55], s77 offen offset:3072 lds\n\t"
"v_mfma_f32_16x16x32_bf16 v[152:155], a[188:191], v[140:143], v[152:155]\n\t"
"ds_read_b128 v[220:223], v249 offset:41216\n\t"
"s_add_u32 s77, s77, 229376\n\t"
"s_and_b32 s77, s77, 2097151\n\t"
"v_mfma_f32_16x16x32_bf16 v[156:159], a[240:243], v[128:131], v[156:159]\n\t"
"s_waitcnt vmcnt(28)\n\t"
"ds_read_b64_tr_b16 v[66:67], v236 offset:12288\n\t"
"v_mfma_f32_16x16x32_bf16 v[156:159], a[244:247], v[132:135], v[156:159]\n\t"
"s_waitcnt lgkmcnt(1)\n\t"
"ds_read_b64_tr_b16 v[70:71], v237 offset:12288\n\t"
"v_add_f32 v220, v220, v221\n\t"
"v_mfma_f32_16x16x32_bf16 v[156:159], a[248:251], v[136:139], v[156:159]\n\t"
"ds_read_b64_tr_b16 v[74:75], v238 offset:12288\n\t"
"v_add_f32 v222, v222, v223\n\t"
"v_mfma_f32_16x16x32_bf16 v[156:159], a[252:255], v[140:143], v[156:159]\n\t"
"s_barrier\n\t"
"ds_read_b128 v[128:131], v6 offset:4096\n\t"
"v_add_f32 v220, v220, v222\n\t"
"v_mfma_f32_16x16x32_bf16 v[160:163], a[128:131], v[16:19], 0\n\t"
"ds_read_b128 v[132:135], v6 offset:5120\n\t"
"v_rcp_f32 v230, v220\n\t"
"v_mfma_f32_16x16x32_bf16 v[160:163], a[132:135], v[20:23], v[160:163]\n\t"
"ds_read_b128 v[136:139], v6 offset:6144\n\t"
"v_subrev_u32 v221, s24, v220\n\t"
"v_cmp_lt_u32 vcc, s25, v221\n\t"
"s_nop 0\n\t"
"s_cmp_eq_u64 vcc, 0\n\t"
"s_cbranch_scc0 LCK6_%=\n\t"
"v_mfma_f32_16x16x32_bf16 v[160:163], a[136:139], v[24:27], v[160:163]\n\t"
"ds_read_b64_tr_b16 v[78:79], v239 offset:12288\n\t"
"v_mul_f32 v152, v152, v230\n\t"
"v_mfma_f32_16x16x32_bf16 v[160:163], a[140:143], v[28:31], v[160:163]\n\t"
"ds_read_b128 v[140:143], v6 offset:7168\n\t"
"v_mul_f32 v153, v153, v230\n\t"
"v_mfma_f32_16x16x32_bf16 v[164:167], a[192:195], v[16:19], 0\n\t"
"ds_read_b64_tr_b16 v[82:83], v240 offset:12288\n\t"
"v_mul_f32 v154, v154, v230\n\t"
"v_mfma_f32_16x16x32_bf16 v[164:167], a[196:199], v[20:23], v[164:167]\n\t"
"ds_read_b64_tr_b16 v[86:87], v241 offset:12288\n\t"
"v_mul_f32 v155, v155, v230\n\t"
"v_mfma_f32_16x16x32_bf16 v[164:167], a[200:203], v[24:27], v[164:167]\n\t"
"ds_read_b64_tr_b16 v[90:91], v242 offset:12288\n\t"
"v_mul_f32 v156, v156, v230\n\t"
"v_mfma_f32_16x16x32_bf16 v[164:167], a[204:207], v[28:31], v[164:167]\n\t"
"s_waitcnt lgkmcnt(7)\n\t"
"ds_read_b64_tr_b16 v[94:95], v243 offset:12288\n\t"
"v_mul_f32 v157, v157, v230\n\t"
"v_mfma_f32_16x16x32_bf16 v[160:163], a[144:147], v[128:131], v[160:163]\n\t"
"s_waitcnt lgkmcnt(7)\n\t"
"ds_read_b128 v[144:147], v7 offset:4096\n\t"
"v_mul_f32 v158, v158, v230\n\t"
"v_mfma_f32_16x16x32_bf16 v[160:163], a[148:151], v[132:135], v[160:163]\n\t"
"s_waitcnt lgkmcnt(7)\n\t"
"ds_read_b128 v[148:151], v7 offset:5120\n\t"
"v_mul_f32 v159, v159, v230\n\t"
"v_mfma_f32_16x16x32_bf16 v[160:163], a[152:155], v[136:139], v[160:163]\n\t"
"s_waitcnt lgkmcnt(6)\n\t"
"ds_write_b128 v9, v[32:35] offset:0\n\t"
"v_mfma_f32_16x16x32_bf16 v[160:163], a[156:159], v[140:143], v[160:163]\n\t"
"ds_read_b128 v[200:203], v7 offset:6144\n\t"
"v_cvt_pk_bf16_f32 v224, v152, v153\n\t"
"v_mfma_f32_16x16x32_bf16 v[164:167], a[208:211], v[128:131], v[164:167]\n\t"
"ds_read_b128 v[204:207], v7 offset:7168\n\t"
"v_cvt_pk_bf16_f32 v225, v154, v155\n\t"
"v_mfma_f32_16x16x32_bf16 v[164:167], a[212:215], v[132:135], v[164:167]\n\t"
"v_cvt_pk_bf16_f32 v226, v156, v157\n\t"
"v_cvt_pk_bf16_f32 v227, v158, v159\n\t"
"v_mfma_f32_16x16x32_bf16 v[164:167], a[216:219], v[136:139], v[164:167]\n\t"
"ds_read_b128 v[128:131], v8 offset:4096\n\t"
"v_mfma_f32_16x16x32_bf16 v[164:167], a[220:223], v[140:143], v[164:167]\n\t"
"s_waitcnt lgkmcnt(5)\n\t"
"ds_write_b128 v9, v[36:39] offset:1024\n\t"
"ds_read_b128 v[132:135], v8 offset:5120\n\t"
"v_mfma_f32_16x16x32_bf16 v[160:163], a[160:163], v[144:147], v[160:163]\n\t"
"s_waitcnt lgkmcnt(6)\n\t"
"buffer_store_dwordx4 v[224:227], v250, s[56:59], s98 offen sc1\n\t"
"ds_read_b128 v[136:139], v8 offset:6144\n\t"
"v_mfma_f32_16x16x32_bf16 v[160:163], a[164:167], v[148:151], v[160:163]\n\t"
"s_waitcnt lgkmcnt(5)\n\t"
"ds_read_b128 v[140:143], v8 offset:7168\n\t"
"s_add_u32 s98, s98, 32768\n\t"
"v_mfma_f32_16x16x32_bf16 v[160:163], a[168:171], v[200:203], v[160:163]\n\t"
"s_waitcnt lgkmcnt(5)\n\t"
"v_mfma_f32_16x16x32_bf16 v[160:163], a[172:175], v[204:207], v[160:163]\n\t"
"ds_write_b128 v9, v[40:43] offset:2048\n\t"
"v_mfma_f32_16x16x32_bf16 v[164:167], a[224:227], v[144:147], v[164:167]\n\t"
"v_mfma_f32_16x16x32_bf16 v[164:167], a[228:231], v[148:151], v[164:167]\n\t"
"v_mfma_f32_16x16x32_bf16 v[164:167], a[232:235], v[200:203], v[164:167]\n\t"
"v_mfma_f32_16x16x32_bf16 v[164:167], a[236:239], v[204:207], v[164:167]\n\t"
"s_waitcnt lgkmcnt(5)\n\t"
"v_mfma_f32_16x16x32_bf16 v[160:163], a[176:179], v[128:131], v[160:163]\n\t"
"s_waitcnt lgkmcnt(3)\n\t"
"ds_write_b128 v9, v[44:47] offset:3072\n\t"
"v_mfma_f32_16x16x32_bf16 v[160:163], a[180:183], v[132:135], v[160:163]\n\t"
"s_waitcnt lgkmcnt(3)\n\t"
"v_mfma_f32_16x16x32_bf16 v[160:163], a[184:187], v[136:139], v[160:163]\n\t"
"s_waitcnt lgkmcnt(2)\n\t"
"v_mfma_f32_16x16x32_bf16 v[160:163], a[188:191], v[140:143], v[160:163]\n\t"
"ds_read_b128 v[220:223], v249 offset:41472\n\t"
"v_mfma_f32_16x16x32_bf16 v[164:167], a[240:243], v[128:131], v[164:167]\n\t"
"v_mfma_f32_16x16x32_bf16 v[164:167], a[244:247], v[132:135], v[164:167]\n\t"
"s_waitcnt lgkmcnt(0)\n\t"
"v_add_f32 v220, v220, v221\n\t"
"v_add_f32 v222, v222, v223\n\t"
"v_add_f32 v220, v220, v222\n\t"
"v_mfma_f32_16x16x32_bf16 v[164:167], a[248:251], v[136:139], v[164:167]\n\t"
"v_rcp_f32 v230, v220\n\t"
"v_mfma_f32_16x16x32_bf16 v[164:167], a[252:255], v[140:143], v[164:167]\n\t"
"s_barrier\n\t"
"ds_read_b128 v[128:131], v6 offset:0\n\t"
"v_subrev_u32 v221, s24, v220\n\t"
"v_cmp_lt_u32 vcc, s25, v221\n\t"
"s_nop 0\n\t"
"s_cmp_eq_u64 vcc, 0\n\t"
"s_cbranch_scc0 LCK7_%=\n\t"
"v_mfma_f32_16x16x32_bf16 v[152:155], a[128:131], v[32:35], 0\n\t"
"s_waitcnt vmcnt(22)\n\t"
"s_or_b32 s90, s86, 2\n\t"
"v_cmp_le_u64 vcc, s[90:91], v[2:3]\n\t"
"v_cmp_ge_u64 s[92:93], s[88:89], v[2:3]\n\t"
"s_nop 0\n\t"
"s_and_b64 vcc, vcc, s[92:93]\n\t"
"s_cmp_eq_u64 vcc, -1\n\t"
"s_cbranch_scc0 LPS8_%=\n\t"
"LPR8_%=:\n\t"
"v_mfma_f32_16x16x32_bf16 v[152:155], a[132:135], v[36:39], v[152:155]\n\t"
"ds_read_b128 v[132:135], v6 offset:1024\n\t"
"buffer_load_dwordx2 v[2:3], v15, s[56:59], s16 offen sc1\n\t"
"v_mfma_f32_16x16x32_bf16 v[152:155], a[136:139], v[40:43], v[152:155]\n\t"
"ds_read_b128 v[136:139], v6 offset:2048\n\t"
"v_mul_f32 v160, v160, v230\n\t"
"v_mfma_f32_16x16x32_bf16 v[152:155], a[140:143], v[44:47], v[152:155]\n\t"
"ds_read_b128 v[140:143], v6 offset:3072\n\t"
"v_mul_f32 v161, v161, v230\n\t"
"v_mfma_f32_16x16x32_bf16 v[156:159], a[192:195], v[32:35], 0\n\t"
"ds_read_b128 v[144:147], v7 offset:0\n\t"
"v_mul_f32 v162, v162, v230\n\t"
"v_mfma_f32_16x16x32_bf16 v[156:159], a[196:199], v[36:39], v[156:159]\n\t"
"s_add_u32 s22, s94, 0\n\t"
"ds_read_b128 v[148:151], v7 offset:1024\n\t"
"v_mul_f32 v163, v163, v230\n\t"
"v_mfma_f32_16x16x32_bf16 v[156:159], a[200:203], v[40:43], v[156:159]\n\t"
"ds_read_b128 v[200:203], v7 offset:2048\n\t"
"buffer_load_dwordx4 v[168:171], v232, s[56:59], s22 offen sc1\n\t"
"v_mfma_f32_16x16x32_bf16 v[156:159], a[204:207], v[44:47], v[156:159]\n\t"
"s_waitcnt lgkmcnt(6)\n\t"
"ds_read_b128 v[204:207], v7 offset:3072\n\t"
"v_mul_f32 v164, v164, v230\n\t"
"v_mfma_f32_16x16x32_bf16 v[152:155], a[144:147], v[128:131], v[152:155]\n\t"
"s_waitcnt lgkmcnt(6)\n\t"
"v_mul_f32 v165, v165, v230\n\t"
"v_mul_f32 v166, v166, v230\n\t"
"v_mfma_f32_16x16x32_bf16 v[152:155], a[148:151], v[132:135], v[152:155]\n\t"
"s_waitcnt lgkmcnt(5)\n\t"
"ds_write_b128 v9, v[48:51] offset:4096\n\t"
"v_mfma_f32_16x16x32_bf16 v[152:155], a[152:155], v[136:139], v[152:155]\n\t"
"s_waitcnt lgkmcnt(5)\n\t"
"buffer_load_dwordx4 v[172:175], v232, s[56:59], s22 offen offset:1024 sc1\n\t"
"v_mfma_f32_16x16x32_bf16 v[152:155], a[156:159], v[140:143], v[152:155]\n\t"
"v_mul_f32 v167, v167, v230\n\t"
"v_cvt_pk_bf16_f32 v224, v160, v161\n\t"
"v_mfma_f32_16x16x32_bf16 v[156:159], a[208:211], v[128:131], v[156:159]\n\t"
"buffer_load_dwordx4 v[176:179], v233, s[56:59], s22 offen sc1\n\t"
"v_mfma_f32_16x16x32_bf16 v[156:159], a[212:215], v[132:135], v[156:159]\n\t"
"ds_read_b128 v[128:131], v8 offset:0\n\t"
"v_cvt_pk_bf16_f32 v225, v162, v163\n\t"
"v_mfma_f32_16x16x32_bf16 v[156:159], a[216:219], v[136:139], v[156:159]\n\t"
"v_cvt_pk_bf16_f32 v226, v164, v165\n\t"
"v_cvt_pk_bf16_f32 v227, v166, v167\n\t"
"v_mfma_f32_16x16x32_bf16 v[156:159], a[220:223], v[140:143], v[156:159]\n\t"
"s_waitcnt lgkmcnt(5)\n\t"
"ds_write_b128 v9, v[52:55] offset:5120\n\t"
"ds_read_b128 v[132:135], v8 offset:1024\n\t"
"v_mfma_f32_16x16x32_bf16 v[152:155], a[160:163], v[144:147], v[152:155]\n\t"
"s_waitcnt lgkmcnt(6)\n\t"
"buffer_store_dwordx4 v[224:227], v250, s[56:59], s98 offen sc1\n\t"
"ds_read_b128 v[136:139], v8 offset:2048\n\t"
"v_mfma_f32_16x16x32_bf16 v[152:155], a[164:167], v[148:151], v[152:155]\n\t"
"s_waitcnt lgkmcnt(6)\n\t"
"ds_read_b128 v[140:143], v8 offset:3072\n\t"
"s_add_u32 s98, s98, 32768\n\t"
"buffer_load_dwordx4 v[180:183], v233, s[56:59], s22 offen offset:1024 sc1\n\t"
"v_mfma_f32_16x16x32_bf16 v[152:155], a[168:171], v[200:203], v[152:155]\n\t"
"s_waitcnt lgkmcnt(6)\n\t"
"buffer_load_dwordx4 v[184:187], v234, s[56:59], s22 offen sc1\n\t"
"v_mfma_f32_16x16x32_bf16 v[152:155], a[172:175], v[204:207], v[152:155]\n\t"
"ds_write_b128 v9, v[56:59] offset:6144\n\t"
"v_mfma_f32_16x16x32_bf16 v[156:159], a[224:227], v[144:147], v[156:159]\n\t"
"v_mfma_f32_16x16x32_bf16 v[156:159], a[228:231], v[148:151], v[156:159]\n\t"
"buffer_load_dwordx4 v[188:191], v234, s[56:59], s22 offen offset:1024 sc1\n\t"
"v_mfma_f32_16x16x32_bf16 v[156:159], a[232:235], v[200:203], v[156:159]\n\t"
"v_mfma_f32_16x16x32_bf16 v[156:159], a[236:239], v[204:207], v[156:159]\n\t"
"s_waitcnt lgkmcnt(5)\n\t"
"v_mfma_f32_16x16x32_bf16 v[152:155], a[176:179], v[128:131], v[152:155]\n\t"
"s_waitcnt lgkmcnt(3)\n\t"
"ds_write_b128 v9, v[60:63] offset:7168\n\t"
"v_mfma_f32_16x16x32_bf16 v[152:155], a[180:183], v[132:135], v[152:155]\n\t"
"s_waitcnt lgkmcnt(3)\n\t"
"buffer_load_dwordx4 v[192:195], v235, s[56:59], s22 offen sc1\n\t"
"v_mfma_f32_16x16x32_bf16 v[152:155], a[184:187], v[136:139], v[152:155]\n\t"
"s_waitcnt lgkmcnt(2)\n\t"
"v_mfma_f32_16x16x32_bf16 v[152:155], a[188:191], v[140:143], v[152:155]\n\t"
"ds_read_b128 v[220:223], v249 offset:41728\n\t"
"buffer_load_dwordx4 v[196:199], v235, s[56:59], s22 offen offset:1024 sc1\n\t"
"v_mfma_f32_16x16x32_bf16 v[156:159], a[240:243], v[128:131], v[156:159]\n\t"
"v_mfma_f32_16x16x32_bf16 v[156:159], a[244:247], v[132:135], v[156:159]\n\t"
"s_waitcnt lgkmcnt(0)\n\t"
"v_add_f32 v220, v220, v221\n\t"
"v_add_f32 v222, v222, v223\n\t"
"v_add_f32 v220, v220, v222\n\t"
"v_mfma_f32_16x16x32_bf16 v[156:159], a[248:251], v[136:139], v[156:159]\n\t"
"v_rcp_f32 v230, v220\n\t"
"v_mfma_f32_16x16x32_bf16 v[156:159], a[252:255], v[140:143], v[156:159]\n\t"
"s_barrier\n\t"
"ds_read_b128 v[128:131], v6 offset:4096\n\t"
"v_subrev_u32 v221, s24, v220\n\t"
"v_cmp_lt_u32 vcc, s25, v221\n\t"
"s_nop 0\n\t"
"s_cmp_eq_u64 vcc, 0\n\t"
"s_cbranch_scc0 LCK8_%=\n\t"
"v_mfma_f32_16x16x32_bf16 v[160:163], a[128:131], v[48:51], 0\n\t"
"s_add_u32 s22, s94, 32768\n\t"
"ds_read_b128 v[132:135], v6 offset:5120\n\t"
"v_mul_f32 v152, v152, v230\n\t"
"v_mfma_f32_16x16x32_bf16 v[160:163], a[132:135], v[52:55], v[160:163]\n\t"
"ds_read_b128 v[136:139], v6 offset:6144\n\t"
"buffer_load_dwordx4 v[96:99], v232, s[56:59], s22 offen sc1\n\t"
"v_mfma_f32_16x16x32_bf16 v[160:163], a[136:139], v[56:59], v[160:163]\n\t"
"ds_read_b128 v[140:143], v6 offset:7168\n\t"
"v_mul_f32 v153, v153, v230\n\t"
"v_mfma_f32_16x16x32_bf16 v[160:163], a[140:143], v[60:63], v[160:163]\n\t"
"ds_read_b128 v[144:147], v7 offset:4096\n\t"
"v_mul_f32 v154, v154, v230\n\t"
"v_mfma_f32_16x16x32_bf16 v[164:167], a[192:195], v[48:51], 0\n\t"
"ds_read_b128 v[148:151], v7 offset:5120\n\t"
"buffer_load_dwordx4 v[100:103], v232, s[56:59], s22 offen offset:1024 sc1\n\t"
"v_mfma_f32_16x16x32_bf16 v[164:167], a[196:199], v[52:55], v[164:167]\n\t"
"ds_read_b128 v[200:203], v7 offset:6144\n\t"
"v_mul_f32 v155, v155, v230\n\t"
"v_mfma_f32_16x16x32_bf16 v[164:167], a[200:203], v[56:59], v[164:167]\n\t"
"ds_read_b128 v[204:207], v7 offset:7168\n\t"
"v_mul_f32 v156, v156, v230\n\t"
"v_mfma_f32_16x16x32_bf16 v[164:167], a[204:207], v[60:63], v[164:167]\n\t"
"s_waitcnt lgkmcnt(7)\n\t"
"buffer_load_dwordx4 v[104:107], v233, s[56:59], s22 offen sc1\n\t"
"v_mfma_f32_16x16x32_bf16 v[160:163], a[144:147], v[128:131], v[160:163]\n\t"
"s_waitcnt lgkmcnt(6)\n\t"
"v_mul_f32 v157, v157, v230\n\t"
"v_mul_f32 v158, v158, v230\n\t"
"v_mfma_f32_16x16x32_bf16 v[160:163], a[148:151], v[132:135], v[160:163]\n\t"
"s_waitcnt lgkmcnt(5)\n\t"
"v_mul_f32 v159, v159, v230\n\t"
"v_cvt_pk_bf16_f32 v224, v152, v153\n\t"
"v_mfma_f32_16x16x32_bf16 v[160:163], a[152:155], v[136:139], v[160:163]\n\t"
"s_waitcnt lgkmcnt(4)\n\t"
"buffer_load_dwordx4 v[108:111], v233, s[56:59], s22 offen offset:1024 sc1\n\t"
"v_mfma_f32_16x16x32_bf16 v[160:163], a[156:159], v[140:143], v[160:163]\n\t"
"s_add_u32 s81, s65, 0\n\t"
"s_and_b32 s81, s81, 3\n\t"
"v_cvt_pk_bf16_f32 v225, v154, v155\n\t"
"v_cvt_pk_bf16_f32 v226, v156, v157\n\t"
"v_mfma_f32_16x16x32_bf16 v[164:167], a[208:211], v[128:131], v[164:167]\n\t"
"s_lshl_b32 s81, s81, 11\n\t"
"ds_read_b128 v[128:131], v8 offset:4096\n\t"
"v_cvt_pk_bf16_f32 v227, v158, v159\n\t"
"v_mfma_f32_16x16x32_bf16 v[164:167], a[212:215], v[132:135], v[164:167]\n\t"
"ds_read_b128 v[132:135], v8 offset:5120\n\t"
"buffer_load_dwordx4 v[112:115], v234, s[56:59], s22 offen sc1\n\t"
"v_mfma_f32_16x16x32_bf16 v[164:167], a[216:219], v[136:139], v[164:167]\n\t"
"buffer_store_dwordx4 v[224:227], v250, s[56:59], s98 offen sc1\n\t"
"v_mfma_f32_16x16x32_bf16 v[164:167], a[220:223], v[140:143], v[164:167]\n\t"
"s_waitcnt lgkmcnt(5)\n\t"
"s_add_u32 s98, s98, 32768\n\t"
"ds_read_b128 v[136:139], v8 offset:6144\n\t"
"v_add_u32 v48, s81, v250\n\t"
"v_mfma_f32_16x16x32_bf16 v[160:163], a[160:163], v[144:147], v[160:163]\n\t"
"s_waitcnt lgkmcnt(5)\n\t"
"s_add_u32 s81, s97, 341\n\t"
"s_and_b32 s81, s81, 1023\n\t"
"ds_read_b128 v[140:143], v8 offset:7168\n\t"
"buffer_load_dwordx4 v[116:119], v234, s[56:59], s22 offen offset:1024 sc1\n\t"
"v_mfma_f32_16x16x32_bf16 v[160:163], a[164:167], v[148:151], v[160:163]\n\t"
"s_waitcnt lgkmcnt(5)\n\t"
"v_mov_b32 v52, s81\n\t"
"s_add_u32 s81, s65, 1\n\t"
"s_and_b32 s81, s81, 3\n\t"
"s_lshl_b32 s81, s81, 11\n\t"
"v_add_u32 v49, s81, v250\n\t"
"v_mfma_f32_16x16x32_bf16 v[160:163], a[168:171], v[200:203], v[160:163]\n\t"
"s_waitcnt lgkmcnt(4)\n\t"
"s_add_u32 s81, s97, 682\n\t"
"s_and_b32 s81, s81, 1023\n\t"
"v_mov_b32 v53, s81\n\t"
"s_add_u32 s81, s65, 2\n\t"
"s_and_b32 s81, s81, 3\n\t"
"s_lshl_b32 s81, s81, 11\n\t"
"v_add_u32 v50, s81, v250\n\t"
"v_mfma_f32_16x16x32_bf16 v[160:163], a[172:175], v[204:207], v[160:163]\n\t"
"s_add_u32 s81, s97, 243\n\t"
"s_and_b32 s81, s81, 1023\n\t"
"buffer_load_dwordx4 v[120:123], v235, s[56:59], s22 offen sc1\n\t"
"v_mfma_f32_16x16x32_bf16 v[164:167], a[224:227], v[144:147], v[164:167]\n\t"
"ds_read_b64_tr_b16 a[160:161], v238 offset:32768\n\t"
"v_mov_b32 v54, s81\n\t"
"v_mfma_f32_16x16x32_bf16 v[164:167], a[228:231], v[148:151], v[164:167]\n\t"
"s_waitcnt vmcnt(38)\n\t"
"s_add_u32 s81, s65, 3\n\t"
"s_and_b32 s81, s81, 3\n\t"
"ds_read_b64_tr_b16 a[162:163], v238 offset:24576\n\t"
"s_lshl_b32 s81, s81, 11\n\t"
"v_add_u32 v51, s81, v250\n\t"
"v_mfma_f32_16x16x32_bf16 v[164:167], a[232:235], v[200:203], v[164:167]\n\t"
"s_add_u32 s81, s97, 781\n\t"
"s_and_b32 s81, s81, 1023\n\t"
"buffer_load_dwordx4 v[124:127], v235, s[56:59], s22 offen offset:1024 sc1\n\t"
"v_mfma_f32_16x16x32_bf16 v[164:167], a[236:239], v[204:207], v[164:167]\n\t"
"s_waitcnt lgkmcnt(5)\n\t"
"s_waitcnt vmcnt(35)\n\t"
"ds_read_b64_tr_b16 a[164:165], v238 offset:36864\n\t"
"v_mov_b32 v55, s81\n\t"
"v_mfma_f32_16x16x32_bf16 v[160:163], a[176:179], v[128:131], v[160:163]\n\t"
"s_waitcnt lgkmcnt(5)\n\t"
"s_mov_b32 s27, 1023\n\t"
"ds_read_b128 v[220:223], v249 offset:41984\n\t"
"v_and_b32 v56, 15, v1\n\t"
"v_mfma_f32_16x16x32_bf16 v[160:163], a[180:183], v[132:135], v[160:163]\n\t"
"s_waitcnt lgkmcnt(5)\n\t"
"s_waitcnt vmcnt(30)\n\t"
"ds_read_b64_tr_b16 a[166:167], v238 offset:4096\n\t"
"v_lshlrev_b32 v56, 11, v56\n\t"
"v_mfma_f32_16x16x32_bf16 v[160:163], a[184:187], v[136:139], v[160:163]\n\t"
"s_waitcnt lgkmcnt(5)\n\t"
"s_waitcnt vmcnt(26)\n\t"
"ds_read_b64_tr_b16 a[168:169], v238 offset:0\n\t"
"v_add_u32 v9, -4096, v9\n\t"
"v_mfma_f32_16x16x32_bf16 v[160:163], a[188:191], v[140:143], v[160:163]\n\t"
"s_waitcnt vmcnt(20)\n\t"
"ds_read_b64_tr_b16 a[170:171], v238 offset:28672\n\t"
"v_add_u32 v6, -4096, v6\n\t"
"v_mfma_f32_16x16x32_bf16 v[164:167], a[240:243], v[128:131], v[164:167]\n\t"
"ds_read_b64_tr_b16 a[176:177], v239 offset:32768\n\t"
"v_add_u32 v7, -2048, v7\n\t"
"v_mfma_f32_16x16x32_bf16 v[164:167], a[244:247], v[132:135], v[164:167]\n\t"
"s_waitcnt lgkmcnt(4)\n\t"
"ds_read_b64_tr_b16 a[178:179], v239 offset:24576\n\t"
"v_add_f32 v220, v220, v221\n\t"
"v_mfma_f32_16x16x32_bf16 v[164:167], a[248:251], v[136:139], v[164:167]\n\t"
"ds_read_b64_tr_b16 a[180:181], v239 offset:36864\n\t"
"v_add_f32 v222, v222, v223\n\t"
"v_mfma_f32_16x16x32_bf16 v[164:167], a[252:255], v[140:143], v[164:167]\n\t"
"s_waitcnt vmcnt(17)\n\t"
"ds_read_b64_tr_b16 a[182:183], v239 offset:4096\n\t"
"v_add_f32 v220, v220, v222\n\t"
"v_mfma_f32_16x16x32_bf16 v[128:131], a[8:11], v[168:171], 0\n\t"
"s_waitcnt vmcnt(16)\n\t"
"ds_read_b64_tr_b16 a[184:185], v239 offset:0\n\t"
"v_add_u32 v8, 0, v8\n\t"
"v_mfma_f32_16x16x32_bf16 v[128:131], a[40:43], v[172:175], v[128:131]\n\t"
"s_waitcnt vmcnt(15)\n\t"
"ds_read_b64_tr_b16 a[186:187], v239 offset:28672\n\t"
"v_lshrrev_b32 v57, 4, v1\n\t"
"v_mfma_f32_16x16x32_bf16 v[128:131], a[72:75], v[176:179], v[128:131]\n\t"
"s_waitcnt vmcnt(13)\n\t"
"ds_read_b64_tr_b16 a[192:193], v240 offset:32768\n\t"
"v_lshl_add_u32 v56, v57, 3, v56\n\t"
"v_mfma_f32_16x16x32_bf16 v[128:131], a[104:107], v[180:183], v[128:131]\n\t"
"v_rcp_f32 v230, v220\n\t"
"v_subrev_u32 v221, s24, v220\n\t"
"v_cmp_lt_u32 vcc, s25, v221\n\t"
"s_nop 0\n\t"
"s_cmp_eq_u64 vcc, 0\n\t"
"s_cbranch_scc0 LCK9_%=\n\t"
"s_or_b32 s90, s86, 4\n\t"
"v_cmp_le_u64 vcc, s[90:91], v[2:3]\n\t"
"v_cmp_ge_u64 s[92:93], s[88:89], v[2:3]\n\t"
"s_nop 0\n\t"
"s_and_b64 vcc, vcc, s[92:93]\n\t"
"s_cmp_eq_u64 vcc, -1\n\t"
"s_cbranch_scc0 LPS11_%=\n\t"
"LPR11_%=:\n\t"
"s_waitcnt vmcnt(12)\n\t"
"v_mfma_f32_16x16x32_bf16 v[128:131], v[72:75], v[184:187], v[128:131]\n\t"
"s_waitcnt vmcnt(11)\n\t"
"s_mul_i32 s21, s66, 327680\n\t"
"s_lshl_b32 s81, s85, 8\n\t"
"ds_read_b64_tr_b16 a[194:195], v240 offset:24576\n\t"
"v_mfma_f32_16x16x32_bf16 v[128:131], a[160:163], v[188:191], v[128:131]\n\t"
"s_waitcnt vmcnt(10) lgkmcnt(10)\n\t"
"s_add_u32 s22, s94, 65536\n\t"
"ds_read_b64_tr_b16 a[196:197], v240 offset:36864\n\t"
"v_mul_f32 v160, v160, v230\n\t"
"v_mfma_f32_16x16x32_bf16 v[128:131], a[164:167], v[192:195], v[128:131]\n\t"
"s_waitcnt vmcnt(9) lgkmcnt(9)\n\t"
"s_add_u32 s21, s21, s81\n\t"
"s_lshl_b32 s81, s69, 6\n\t"
"ds_read_b64_tr_b16 a[198:199], v240 offset:4096\n\t"
"v_mfma_f32_16x16x32_bf16 v[128:131], a[168:171], v[196:199], v[128:131]\n\t"
"s_add_u32 s21, s21, s81\n\t"
"s_and_b32 s81, s65, 1\n\t"
"ds_read_b64_tr_b16 a[200:201], v240 offset:0\n\t"
"v_mfma_f32_16x16x32_bf16 v[132:135], a[12:15], v[168:171], 0\n\t"
"s_lshl_b32 s81, s81, 1\n\t"
"s_add_u32 s81, s81, s70\n\t"
"ds_read_b64_tr_b16 a[202:203], v240 offset:28672\n\t"
"v_mfma_f32_16x16x32_bf16 v[132:135], a[44:47], v[172:175], v[132:135]\n\t"
"s_sub_i32 s19, s81, s69\n\t"
"buffer_load_dwordx4 a[140:143], v232, s[56:59], s22 offen sc1\n\t"
"v_mfma_f32_16x16x32_bf16 v[132:135], a[76:79], v[176:179], v[132:135]\n\t"
"ds_write_b64 v6, v[128:129] offset:0\n\t"
"v_mfma_f32_16x16x32_bf16 v[132:135], a[108:111], v[180:183], v[132:135]\n\t"
"buffer_load_dwordx4 a[156:159], v232, s[56:59], s22 offen offset:1024 sc1\n\t"
"v_mfma_f32_16x16x32_bf16 v[132:135], v[76:79], v[184:187], v[132:135]\n\t"
"s_waitcnt lgkmcnt(11)\n\t"
"buffer_load_dwordx4 a[172:175], v233, s[56:59], s22 offen sc1\n\t"
"v_mfma_f32_16x16x32_bf16 v[132:135], a[176:179], v[188:191], v[132:135]\n\t"
"s_waitcnt lgkmcnt(9)\n\t"
"ds_read_b64_tr_b16 a[208:209], v241 offset:32768\n\t"
"v_mul_f32 v161, v161, v230\n\t"
"v_mfma_f32_16x16x32_bf16 v[132:135], a[180:183], v[192:195], v[132:135]\n\t"
"v_mul_f32 v162, v162, v230\n\t"
"v_mul_f32 v163, v163, v230\n\t"
"v_mul_f32 v164, v164, v230\n\t"
"v_mul_f32 v165, v165, v230\n\t"
"v_mul_f32 v166, v166, v230\n\t"
"v_mul_f32 v167, v167, v230\n\t"
"v_cvt_pk_bf16_f32 v224, v160, v161\n\t"
"v_cvt_pk_bf16_f32 v225, v162, v163\n\t"
"v_cvt_pk_bf16_f32 v226, v164, v165\n\t"
"v_cvt_pk_bf16_f32 v227, v166, v167\n\t"
"s_waitcnt lgkmcnt(8)\n\t"
"v_mfma_f32_16x16x32_bf16 v[132:135], a[184:187], v[196:199], v[132:135]\n\t"
"buffer_store_dwordx4 v[224:227], v250, s[56:59], s98 offen sc1\n\t"
"v_mfma_f32_16x16x32_bf16 v[136:139], a[16:19], v[168:171], 0\n\t"
"s_add_u32 s98, s98, 32768\n\t"
"ds_read_b64_tr_b16 a[210:211], v241 offset:24576\n\t"
"v_mfma_f32_16x16x32_bf16 v[136:139], a[48:51], v[172:175], v[136:139]\n\t"
"ds_write_b64 v6, v[130:131] offset:8\n\t"
"v_mfma_f32_16x16x32_bf16 v[136:139], a[80:83], v[176:179], v[136:139]\n\t"
"ds_read_b64_tr_b16 a[212:213], v241 offset:36864\n\t"
"v_mfma_f32_16x16x32_bf16 v[136:139], a[112:115], v[180:183], v[136:139]\n\t"
"ds_read_b64_tr_b16 a[214:215], v241 offset:4096\n\t"
"v_mfma_f32_16x16x32_bf16 v[136:139], v[80:83], v[184:187], v[136:139]\n\t"
"s_waitcnt lgkmcnt(10)\n\t"
"ds_read_b64_tr_b16 a[216:217], v241 offset:0\n\t"
"v_mfma_f32_16x16x32_bf16 v[136:139], a[192:195], v[188:191], v[136:139]\n\t"
"s_waitcnt lgkmcnt(9)\n\t"
"ds_read_b64_tr_b16 a[218:219], v241 offset:28672\n\t"
"v_mfma_f32_16x16x32_bf16 v[136:139], a[196:199], v[192:195], v[136:139]\n\t"
"s_waitcnt lgkmcnt(8)\n\t"
"buffer_load_dwordx4 a[188:191], v233, s[56:59], s22 offen offset:1024 sc1\n\t"
"v_mfma_f32_16x16x32_bf16 v[136:139], a[200:203], v[196:199], v[136:139]\n\t"
"ds_write_b64 v6, v[132:133] offset:1024\n\t"
"v_mfma_f32_16x16x32_bf16 v[140:143], a[20:23], v[168:171], 0\n\t"
"ds_read_b64_tr_b16 a[224:225], v242 offset:32768\n\t"
"v_mfma_f32_16x16x32_bf16 v[140:143], a[52:55], v[172:175], v[140:143]\n\t"
"ds_read_b64_tr_b16 a[226:227], v242 offset:24576\n\t"
"v_mfma_f32_16x16x32_bf16 v[140:143], a[84:87], v[176:179], v[140:143]\n\t"
"buffer_load_dwordx4 a[204:207], v234, s[56:59], s22 offen sc1\n\t"
"ds_write_b64 v6, v[134:135] offset:1032\n\t"
"ds_read_b64_tr_b16 a[228:229], v242 offset:36864\n\t"
"v_mfma_f32_16x16x32_bf16 v[140:143], a[116:119], v[180:183], v[140:143]\n\t"
"ds_read_b64_tr_b16 a[230:231], v242 offset:4096\n\t"
"v_mfma_f32_16x16x32_bf16 v[140:143], v[84:87], v[184:187], v[140:143]\n\t"
"s_waitcnt lgkmcnt(11)\n\t"
"ds_read_b64_tr_b16 a[232:233], v242 offset:0\n\t"
"v_mfma_f32_16x16x32_bf16 v[140:143], a[208:211], v[188:191], v[140:143]\n\t"
"s_waitcnt lgkmcnt(9)\n\t"
"buffer_load_dwordx4 a[220:223], v234, s[56:59], s22 offen offset:1024 sc1\n\t"
"ds_read_b64_tr_b16 a[234:235], v242 offset:28672\n\t"
"v_mfma_f32_16x16x32_bf16 v[140:143], a[212:215], v[192:195], v[140:143]\n\t"
"s_waitcnt lgkmcnt(8)\n\t"
"buffer_load_dwordx4 a[236:239], v235, s[56:59], s22 offen sc1\n\t"
"v_mfma_f32_16x16x32_bf16 v[140:143], a[216:219], v[196:199], v[140:143]\n\t"
"ds_write_b64 v7, v[136:137] offset:0\n\t"
"v_mfma_f32_16x16x32_bf16 v[144:147], a[24:27], v[168:171], 0\n\t"
"ds_read_b64_tr_b16 a[240:241], v243 offset:32768\n\t"
"v_or_b32 v228, 7, v4\n\t"
"v_mfma_f32_16x16x32_bf16 v[144:147], a[56:59], v[172:175], v[144:147]\n\t"
"ds_read_b64_tr_b16 a[242:243], v243 offset:24576\n\t"
"v_mfma_f32_16x16x32_bf16 v[144:147], a[88:91], v[176:179], v[144:147]\n\t"
"ds_write_b64 v7, v[138:139] offset:8\n\t"
"ds_read_b64_tr_b16 a[244:245], v243 offset:36864\n\t"
"v_mfma_f32_16x16x32_bf16 v[144:147], a[120:123], v[180:183], v[144:147]\n\t"
"buffer_load_dwordx4 a[252:255], v235, s[56:59], s22 offen offset:1024 sc1\n\t"
"ds_read_b64_tr_b16 a[246:247], v243 offset:4096\n\t"
"v_mfma_f32_16x16x32_bf16 v[144:147], v[88:91], v[184:187], v[144:147]\n\t"
"s_waitcnt lgkmcnt(11)\n\t"
"ds_read_b64_tr_b16 a[248:249], v243 offset:0\n\t"
"v_mfma_f32_16x16x32_bf16 v[144:147], a[224:227], v[188:191], v[144:147]\n\t"
"s_waitcnt lgkmcnt(9)\n\t"
"ds_read_b64_tr_b16 a[250:251], v243 offset:28672\n\t"
"v_mfma_f32_16x16x32_bf16 v[144:147], a[228:231], v[192:195], v[144:147]\n\t"
"s_waitcnt lgkmcnt(8)\n\t"
"buffer_store_dwordx2 v[228:229], v5, s[56:59], s17 offen sc1\n\t"
"v_mfma_f32_16x16x32_bf16 v[144:147], a[232:235], v[196:199], v[144:147]\n\t"
"ds_write_b64 v7, v[140:141] offset:1024\n\t"
"v_mfma_f32_16x16x32_bf16 v[148:151], a[28:31], v[168:171], 0\n\t"
"ds_read_b64_tr_b16 a[128:129], v236 offset:32768\n\t"
"v_mfma_f32_16x16x32_bf16 v[148:151], a[60:63], v[172:175], v[148:151]\n\t"
"ds_read_b64_tr_b16 a[130:131], v236 offset:24576\n\t"
"v_mfma_f32_16x16x32_bf16 v[148:151], a[92:95], v[176:179], v[148:151]\n\t"
"ds_write_b64 v7, v[142:143] offset:1032\n\t"
"ds_read_b64_tr_b16 a[132:133], v236 offset:36864\n\t"
"v_mfma_f32_16x16x32_bf16 v[148:151], a[124:127], v[180:183], v[148:151]\n\t"
"ds_read_b64_tr_b16 a[134:135], v236 offset:4096\n\t"
"v_mfma_f32_16x16x32_bf16 v[148:151], v[92:95], v[184:187], v[148:151]\n\t"
"s_waitcnt lgkmcnt(11)\n\t"
"ds_read_b64_tr_b16 a[136:137], v236 offset:0\n\t"
"v_mfma_f32_16x16x32_bf16 v[148:151], a[240:243], v[188:191], v[148:151]\n\t"
"s_waitcnt lgkmcnt(9)\n\t"
"ds_read_b64_tr_b16 a[138:139], v236 offset:28672\n\t"
"v_mfma_f32_16x16x32_bf16 v[148:151], a[244:247], v[192:195], v[148:151]\n\t"
"s_waitcnt lgkmcnt(8)\n\t"
"ds_write_b64 v8, v[144:145] offset:0\n\t"
"v_mfma_f32_16x16x32_bf16 v[148:151], a[248:251], v[196:199], v[148:151]\n\t"
"v_mfma_f32_16x16x32_bf16 v[152:155], a[0:3], v[168:171], 0\n\t"
"ds_read_b64_tr_b16 a[144:145], v237 offset:32768\n\t"
"v_mfma_f32_16x16x32_bf16 v[152:155], a[32:35], v[172:175], v[152:155]\n\t"
"ds_read_b64_tr_b16 a[146:147], v237 offset:24576\n\t"
"v_mfma_f32_16x16x32_bf16 v[152:155], a[64:67], v[176:179], v[152:155]\n\t"
"ds_write_b64 v8, v[146:147] offset:8\n\t"
"ds_read_b64_tr_b16 a[148:149], v237 offset:36864\n\t"
"v_mfma_f32_16x16x32_bf16 v[152:155], a[96:99], v[180:183], v[152:155]\n\t"
"ds_read_b64_tr_b16 a[150:151], v237 offset:4096\n\t"
"v_mfma_f32_16x16x32_bf16 v[152:155], v[64:67], v[184:187], v[152:155]\n\t"
"s_waitcnt lgkmcnt(11)\n\t"
"ds_read_b64_tr_b16 a[152:153], v237 offset:0\n\t"
"v_mfma_f32_16x16x32_bf16 v[152:155], a[128:131], v[188:191], v[152:155]\n\t"
"s_waitcnt lgkmcnt(9)\n\t"
"ds_read_b64_tr_b16 a[154:155], v237 offset:28672\n\t"
"v_mfma_f32_16x16x32_bf16 v[152:155], a[132:135], v[192:195], v[152:155]\n\t"
"s_waitcnt lgkmcnt(8)\n\t"
"ds_write_b64 v8, v[148:149] offset:1024\n\t"
"v_mfma_f32_16x16x32_bf16 v[152:155], a[136:139], v[196:199], v[152:155]\n\t"
"v_mfma_f32_16x16x32_bf16 v[156:159], a[4:7], v[168:171], 0\n\t"
"v_mfma_f32_16x16x32_bf16 v[156:159], a[36:39], v[172:175], v[156:159]\n\t"
"ds_write_b64 v8, v[150:151] offset:1032\n\t"
"v_mfma_f32_16x16x32_bf16 v[156:159], a[68:71], v[176:179], v[156:159]\n\t"
"v_mfma_f32_16x16x32_bf16 v[156:159], a[100:103], v[180:183], v[156:159]\n\t"
"v_mfma_f32_16x16x32_bf16 v[156:159], v[68:71], v[184:187], v[156:159]\n\t"
"s_waitcnt lgkmcnt(7)\n\t"
"v_mfma_f32_16x16x32_bf16 v[156:159], a[144:147], v[188:191], v[156:159]\n\t"
"s_waitcnt lgkmcnt(4)\n\t"
"v_mfma_f32_16x16x32_bf16 v[156:159], a[148:151], v[192:195], v[156:159]\n\t"
"s_waitcnt lgkmcnt(2)\n\t"
"v_mfma_f32_16x16x32_bf16 v[156:159], a[152:155], v[196:199], v[156:159]\n\t"
"s_waitcnt lgkmcnt(0)\n\t"
"s_barrier\n\t"
"ds_read_b128 v[208:211], v9 offset:2048\n\t"
"s_waitcnt vmcnt(18)\n\t"
"v_mfma_f32_16x16x32_bf16 v[128:131], a[8:11], v[96:99], 0\n\t"
"s_waitcnt vmcnt(17)\n\t"
"s_lshr_b64 s[80:81], s[82:83], 0\n\t"
"s_and_b32 s80, s80, 15\n\t"
"ds_read_b128 v[212:215], v9 offset:3072\n\t"
"v_mfma_f32_16x16x32_bf16 v[128:131], a[40:43], v[100:103], v[128:131]\n\t"
"s_waitcnt vmcnt(16)\n\t"
"s_mul_i32 s80, s80, 32768\n\t"
"s_add_u32 s20, s80, s95\n\t"
"ds_read_b128 v[216:219], v9 offset:4096\n\t"
"v_mfma_f32_16x16x32_bf16 v[128:131], a[72:75], v[104:107], v[128:131]\n\t"
"s_waitcnt vmcnt(15)\n\t"
"s_add_u32 s22, s94, 98304\n\t"
"ds_read_b128 v[220:223], v9 offset:5120\n\t"
"v_mfma_f32_16x16x32_bf16 v[128:131], a[104:107], v[108:111], v[128:131]\n\t"
"s_waitcnt vmcnt(14)\n\t"
"ds_read_b128 v[200:203], v9 offset:0\n\t"
"v_mfma_f32_16x16x32_bf16 v[128:131], v[72:75], v[112:115], v[128:131]\n\t"
"s_waitcnt vmcnt(12)\n\t"
"ds_read_b128 v[204:207], v9 offset:1024\n\t"
"v_mfma_f32_16x16x32_bf16 v[128:131], a[160:163], v[116:119], v[128:131]\n\t"
"s_waitcnt vmcnt(11)\n\t"
"buffer_load_dwordx4 v[168:171], v232, s[56:59], s22 offen sc1\n\t"
"v_mfma_f32_16x16x32_bf16 v[128:131], a[164:167], v[120:123], v[128:131]\n\t"
"s_waitcnt vmcnt(11)\n\t"
"buffer_load_dwordx4 v[172:175], v232, s[56:59], s22 offen offset:1024 sc1\n\t"
"v_mfma_f32_16x16x32_bf16 v[128:131], a[168:171], v[124:127], v[128:131]\n\t"
"s_waitcnt lgkmcnt(3)\n\t"
"v_add_f32 v208, v208, v216\n\t"
"v_add_f32 v209, v209, v217\n\t"
"v_mfma_f32_16x16x32_bf16 v[132:135], a[12:15], v[96:99], 0\n\t"
"v_add_f32 v210, v210, v218\n\t"
"v_add_f32 v211, v211, v219\n\t"
"v_mfma_f32_16x16x32_bf16 v[132:135], a[44:47], v[100:103], v[132:135]\n\t"
"buffer_load_dwordx4 v[176:179], v233, s[56:59], s22 offen sc1\n\t"
"v_mfma_f32_16x16x32_bf16 v[132:135], a[76:79], v[104:107], v[132:135]\n\t"
"ds_write_b64 v6, v[128:129] offset:6144\n\t"
"v_mfma_f32_16x16x32_bf16 v[132:135], a[108:111], v[108:111], v[132:135]\n\t"
"s_waitcnt lgkmcnt(3)\n\t"
"v_add_f32 v212, v212, v220\n\t"
"v_add_f32 v213, v213, v221\n\t"
"v_mfma_f32_16x16x32_bf16 v[132:135], v[76:79], v[112:115], v[132:135]\n\t"
"buffer_load_dwordx4 v[180:183], v233, s[56:59], s22 offen offset:1024 sc1\n\t"
"v_mfma_f32_16x16x32_bf16 v[132:135], a[176:179], v[116:119], v[132:135]\n\t"
"ds_write_b64 v6, v[130:131] offset:6152\n\t"
"v_mfma_f32_16x16x32_bf16 v[132:135], a[180:183], v[120:123], v[132:135]\n\t"
"v_add_f32 v214, v214, v222\n\t"
"v_add_f32 v215, v215, v223\n\t"
"v_mfma_f32_16x16x32_bf16 v[132:135], a[184:187], v[124:127], v[132:135]\n\t"
"buffer_load_dwordx4 v[184:187], v234, s[56:59], s22 offen sc1\n\t"
"v_mfma_f32_16x16x32_bf16 v[136:139], a[16:19], v[96:99], 0\n\t"
"s_waitcnt lgkmcnt(3)\n\t"
"v_add_f32 v208, v208, v200\n\t"
"v_add_f32 v209, v209, v201\n\t"
"v_mfma_f32_16x16x32_bf16 v[136:139], a[48:51], v[100:103], v[136:139]\n\t"
"v_add_f32 v210, v210, v202\n\t"
"v_add_f32 v211, v211, v203\n\t"
"v_mfma_f32_16x16x32_bf16 v[136:139], a[80:83], v[104:107], v[136:139]\n\t"
"ds_write_b64 v6, v[132:133] offset:7168\n\t"
"v_mfma_f32_16x16x32_bf16 v[136:139], a[112:115], v[108:111], v[136:139]\n\t"
"buffer_load_dwordx4 v[188:191], v234, s[56:59], s22 offen offset:1024 sc1\n\t"
"v_mfma_f32_16x16x32_bf16 v[136:139], v[80:83], v[112:115], v[136:139]\n\t"
"s_waitcnt lgkmcnt(3)\n\t"
"v_add_f32 v212, v212, v204\n\t"
"v_add_f32 v213, v213, v205\n\t"
"v_mfma_f32_16x16x32_bf16 v[136:139], a[192:195], v[116:119], v[136:139]\n\t"
"ds_write_b64 v6, v[134:135] offset:7176\n\t"
"v_mfma_f32_16x16x32_bf16 v[136:139], a[196:199], v[120:123], v[136:139]\n\t"
"buffer_load_dwordx4 v[192:195], v235, s[56:59], s22 offen sc1\n\t"
"v_mfma_f32_16x16x32_bf16 v[136:139], a[200:203], v[124:127], v[136:139]\n\t"
"v_add_f32 v214, v214, v206\n\t"
"v_add_f32 v215, v215, v207\n\t"
"v_mfma_f32_16x16x32_bf16 v[140:143], a[20:23], v[96:99], 0\n\t"
"buffer_load_dwordx4 v[196:199], v235, s[56:59], s22 offen offset:1024 sc1\n\t"
"v_mfma_f32_16x16x32_bf16 v[140:143], a[52:55], v[100:103], v[140:143]\n\t"
"v_add_f32 v152, v152, v208\n\t"
"v_add_f32 v153, v153, v209\n\t"
"v_mfma_f32_16x16x32_bf16 v[140:143], a[84:87], v[104:107], v[140:143]\n\t"
"ds_write_b64 v7, v[136:137] offset:6144\n\t"
"v_mfma_f32_16x16x32_bf16 v[140:143], a[116:119], v[108:111], v[140:143]\n\t"
"v_add_f32 v154, v154, v210\n\t"
"v_add_f32 v155, v155, v211\n\t"
"v_mfma_f32_16x16x32_bf16 v[140:143], v[84:87], v[112:115], v[140:143]\n\t"
"buffer_load_dwordx2 v[2:3], v15, s[56:59], s16 offen sc1\n\t"
"v_mfma_f32_16x16x32_bf16 v[140:143], a[208:211], v[116:119], v[140:143]\n\t"
"ds_write_b64 v7, v[138:139] offset:6152\n\t"
"v_mfma_f32_16x16x32_bf16 v[140:143], a[212:215], v[120:123], v[140:143]\n\t"
"v_add_f32 v156, v156, v212\n\t"
"v_add_f32 v157, v157, v213\n\t"
"v_add_f32 v158, v158, v214\n\t"
"v_add_f32 v159, v159, v215\n\t"
"v_mfma_f32_16x16x32_bf16 v[140:143], a[216:219], v[124:127], v[140:143]\n\t"
"v_bfi_b32 v152, s27, v52, v152\n\t"
"v_bfi_b32 v153, s27, v53, v153\n\t"
"v_mfma_f32_16x16x32_bf16 v[144:147], a[24:27], v[96:99], 0\n\t"
"v_bfi_b32 v154, s27, v54, v154\n\t"
"v_bfi_b32 v155, s27, v55, v155\n\t"
"v_mfma_f32_16x16x32_bf16 v[144:147], a[56:59], v[100:103], v[144:147]\n\t"
"v_bfi_b32 v156, s27, v52, v156\n\t"
"v_bfi_b32 v157, s27, v53, v157\n\t"
"v_mfma_f32_16x16x32_bf16 v[144:147], a[88:91], v[104:107], v[144:147]\n\t"
"v_bfi_b32 v158, s27, v54, v158\n\t"
"v_bfi_b32 v159, s27, v55, v159\n\t"
"v_mfma_f32_16x16x32_bf16 v[144:147], a[120:123], v[108:111], v[144:147]\n\t"
"buffer_store_dwordx4 v[152:155], v48, s[56:59], s20 offen sc1\n\t"
"v_mfma_f32_16x16x32_bf16 v[144:147], v[88:91], v[112:115], v[144:147]\n\t"
"buffer_store_dwordx4 v[156:159], v48, s[56:59], s20 offen offset:1024 sc1\n\t"
"v_mfma_f32_16x16x32_bf16 v[144:147], a[224:227], v[116:119], v[144:147]\n\t"
"ds_write_b64 v7, v[140:141] offset:7168\n\t"
"v_mfma_f32_16x16x32_bf16 v[144:147], a[228:231], v[120:123], v[144:147]\n\t"
"v_mfma_f32_16x16x32_bf16 v[144:147], a[232:235], v[124:127], v[144:147]\n\t"
"v_mfma_f32_16x16x32_bf16 v[148:151], a[28:31], v[96:99], 0\n\t"
"ds_write_b64 v7, v[142:143] offset:7176\n\t"
"v_mfma_f32_16x16x32_bf16 v[148:151], a[60:63], v[100:103], v[148:151]\n\t"
"v_mfma_f32_16x16x32_bf16 v[148:151], a[92:95], v[104:107], v[148:151]\n\t"
"v_mfma_f32_16x16x32_bf16 v[148:151], a[124:127], v[108:111], v[148:151]\n\t"
"v_mfma_f32_16x16x32_bf16 v[148:151], v[92:95], v[112:115], v[148:151]\n\t"
"v_mfma_f32_16x16x32_bf16 v[148:151], a[240:243], v[116:119], v[148:151]\n\t"
"v_mfma_f32_16x16x32_bf16 v[148:151], a[244:247], v[120:123], v[148:151]\n\t"
"ds_write_b64 v8, v[144:145] offset:6144\n\t"
"v_mfma_f32_16x16x32_bf16 v[148:151], a[248:251], v[124:127], v[148:151]\n\t"
"v_mfma_f32_16x16x32_bf16 v[160:163], a[0:3], v[96:99], 0\n\t"
"v_mfma_f32_16x16x32_bf16 v[160:163], a[32:35], v[100:103], v[160:163]\n\t"
"ds_write_b64 v8, v[146:147] offset:6152\n\t"
"v_mfma_f32_16x16x32_bf16 v[160:163], a[64:67], v[104:107], v[160:163]\n\t"
"v_mfma_f32_16x16x32_bf16 v[160:163], a[96:99], v[108:111], v[160:163]\n\t"
"v_mfma_f32_16x16x32_bf16 v[160:163], v[64:67], v[112:115], v[160:163]\n\t"
"v_mfma_f32_16x16x32_bf16 v[160:163], a[128:131], v[116:119], v[160:163]\n\t"
"v_mfma_f32_16x16x32_bf16 v[160:163], a[132:135], v[120:123], v[160:163]\n\t"
"ds_write_b64 v8, v[148:149] offset:7168\n\t"
"v_mfma_f32_16x16x32_bf16 v[160:163], a[136:139], v[124:127], v[160:163]\n\t"
"v_mfma_f32_16x16x32_bf16 v[164:167], a[4:7], v[96:99], 0\n\t"
"v_mfma_f32_16x16x32_bf16 v[164:167], a[36:39], v[100:103], v[164:167]\n\t"
"ds_write_b64 v8, v[150:151] offset:7176\n\t"
"v_mfma_f32_16x16x32_bf16 v[164:167], a[68:71], v[104:107], v[164:167]\n\t"
"v_mfma_f32_16x16x32_bf16 v[164:167], a[100:103], v[108:111], v[164:167]\n\t"
"v_mfma_f32_16x16x32_bf16 v[164:167], v[68:71], v[112:115], v[164:167]\n\t"
"v_mfma_f32_16x16x32_bf16 v[164:167], a[144:147], v[116:119], v[164:167]\n\t"
"v_mfma_f32_16x16x32_bf16 v[164:167], a[148:151], v[120:123], v[164:167]\n\t"
"v_mfma_f32_16x16x32_bf16 v[164:167], a[152:155], v[124:127], v[164:167]\n\t"
"s_waitcnt lgkmcnt(0)\n\t"
"s_barrier\n\t"
"ds_read_b128 v[208:211], v9 offset:8192\n\t"
"s_waitcnt vmcnt(20)\n\t"
"v_mfma_f32_16x16x32_bf16 v[128:131], a[8:11], a[140:143], 0\n\t"
"s_waitcnt vmcnt(19)\n\t"
"s_lshr_b64 s[80:81], s[82:83], 4\n\t"
"s_and_b32 s80, s80, 15\n\t"
"ds_read_b128 v[212:215], v9 offset:9216\n\t"
"v_mfma_f32_16x16x32_bf16 v[128:131], a[40:43], a[156:159], v[128:131]\n\t"
"s_waitcnt vmcnt(18)\n\t"
"s_mul_i32 s80, s80, 32768\n\t"
"s_add_u32 s20, s80, s95\n\t"
"ds_read_b128 v[216:219], v9 offset:10240\n\t"
"v_mfma_f32_16x16x32_bf16 v[128:131], a[72:75], a[172:175], v[128:131]\n\t"
"s_waitcnt vmcnt(16)\n\t"
"ds_read_b128 v[220:223], v9 offset:11264\n\t"
"v_mfma_f32_16x16x32_bf16 v[128:131], a[104:107], a[188:191], v[128:131]\n\t"
"s_waitcnt vmcnt(15)\n\t"
"ds_read_b128 v[200:203], v9 offset:6144\n\t"
"v_mfma_f32_16x16x32_bf16 v[128:131], v[72:75], a[204:207], v[128:131]\n\t"
"s_waitcnt vmcnt(14)\n\t"
"ds_read_b128 v[204:207], v9 offset:7168\n\t"
"v_mfma_f32_16x16x32_bf16 v[128:131], a[160:163], a[220:223], v[128:131]\n\t"
"s_waitcnt vmcnt(13)\n\t"
"s_waitcnt lgkmcnt(3)\n\t"
"v_add_f32 v208, v208, v216\n\t"
"v_add_f32 v209, v209, v217\n\t"
"v_mfma_f32_16x16x32_bf16 v[128:131], a[164:167], a[236:239], v[128:131]\n\t"
"s_waitcnt vmcnt(12)\n\t"
"v_add_f32 v210, v210, v218\n\t"
"v_add_f32 v211, v211, v219\n\t"
"v_mfma_f32_16x16x32_bf16 v[128:131], a[168:171], a[252:255], v[128:131]\n\t"
"s_waitcnt lgkmcnt(2)\n\t"
"v_add_f32 v212, v212, v220\n\t"
"v_add_f32 v213, v213, v221\n\t"
"v_mfma_f32_16x16x32_bf16 v[132:135], a[12:15], a[140:143], 0\n\t"
"v_add_f32 v214, v214, v222\n\t"
"v_add_f32 v215, v215, v223\n\t"
"v_mfma_f32_16x16x32_bf16 v[132:135], a[44:47], a[156:159], v[132:135]\n\t"
"s_waitcnt lgkmcnt(1)\n\t"
"v_add_f32 v208, v208, v200\n\t"
"v_add_f32 v209, v209, v201\n\t"
"v_mfma_f32_16x16x32_bf16 v[132:135], a[76:79], a[172:175], v[132:135]\n\t"
"ds_write_b64 v6, v[128:129] offset:0\n\t"
"v_mfma_f32_16x16x32_bf16 v[132:135], a[108:111], a[188:191], v[132:135]\n\t"
"v_add_f32 v210, v210, v202\n\t"
"v_add_f32 v211, v211, v203\n\t"
"v_mfma_f32_16x16x32_bf16 v[132:135], v[76:79], a[204:207], v[132:135]\n\t"
"s_waitcnt lgkmcnt(1)\n\t"
"v_add_f32 v212, v212, v204\n\t"
"v_add_f32 v213, v213, v205\n\t"
"v_mfma_f32_16x16x32_bf16 v[132:135], a[176:179], a[220:223], v[132:135]\n\t"
"ds_write_b64 v6, v[130:131] offset:8\n\t"
"v_mfma_f32_16x16x32_bf16 v[132:135], a[180:183], a[236:239], v[132:135]\n\t"
"v_add_f32 v214, v214, v206\n\t"
"v_add_f32 v215, v215, v207\n\t"
"v_mfma_f32_16x16x32_bf16 v[132:135], a[184:187], a[252:255], v[132:135]\n\t"
"v_add_f32 v160, v160, v208\n\t"
"v_add_f32 v161, v161, v209\n\t"
"v_mfma_f32_16x16x32_bf16 v[136:139], a[16:19], a[140:143], 0\n\t"
"v_add_f32 v162, v162, v210\n\t"
"v_add_f32 v163, v163, v211\n\t"
"v_mfma_f32_16x16x32_bf16 v[136:139], a[48:51], a[156:159], v[136:139]\n\t"
"v_add_f32 v164, v164, v212\n\t"
"v_add_f32 v165, v165, v213\n\t"
"v_mfma_f32_16x16x32_bf16 v[136:139], a[80:83], a[172:175], v[136:139]\n\t"
"ds_write_b64 v6, v[132:133] offset:1024\n\t"
"v_mfma_f32_16x16x32_bf16 v[136:139], a[112:115], a[188:191], v[136:139]\n\t"
"v_add_f32 v166, v166, v214\n\t"
"v_add_f32 v167, v167, v215\n\t"
"v_mfma_f32_16x16x32_bf16 v[136:139], v[80:83], a[204:207], v[136:139]\n\t"
"v_bfi_b32 v160, s27, v52, v160\n\t"
"v_bfi_b32 v161, s27, v53, v161\n\t"
"v_mfma_f32_16x16x32_bf16 v[136:139], a[192:195], a[220:223], v[136:139]\n\t"
"ds_write_b64 v6, v[134:135] offset:1032\n\t"
"v_mfma_f32_16x16x32_bf16 v[136:139], a[196:199], a[236:239], v[136:139]\n\t"
"v_bfi_b32 v162, s27, v54, v162\n\t"
"v_bfi_b32 v163, s27, v55, v163\n\t"
"v_mfma_f32_16x16x32_bf16 v[136:139], a[200:203], a[252:255], v[136:139]\n\t"
"v_bfi_b32 v164, s27, v52, v164\n\t"
"v_bfi_b32 v165, s27, v53, v165\n\t"
"v_mfma_f32_16x16x32_bf16 v[140:143], a[20:23], a[140:143], 0\n\t"
"v_bfi_b32 v166, s27, v54, v166\n\t"
"v_bfi_b32 v167, s27, v55, v167\n\t"
"v_mfma_f32_16x16x32_bf16 v[140:143], a[52:55], a[156:159], v[140:143]\n\t"
"buffer_store_dwordx4 v[160:163], v48, s[56:59], s20 offen sc1\n\t"
"v_mfma_f32_16x16x32_bf16 v[140:143], a[84:87], a[172:175], v[140:143]\n\t"
"ds_write_b64 v7, v[136:137] offset:0\n\t"
"v_mfma_f32_16x16x32_bf16 v[140:143], a[116:119], a[188:191], v[140:143]\n\t"
"buffer_store_dwordx4 v[164:167], v48, s[56:59], s20 offen offset:1024 sc1\n\t"
"v_mfma_f32_16x16x32_bf16 v[140:143], v[84:87], a[204:207], v[140:143]\n\t"
"v_mfma_f32_16x16x32_bf16 v[140:143], a[208:211], a[220:223], v[140:143]\n\t"
"ds_write_b64 v7, v[138:139] offset:8\n\t"
"v_mfma_f32_16x16x32_bf16 v[140:143], a[212:215], a[236:239], v[140:143]\n\t"
"v_mfma_f32_16x16x32_bf16 v[140:143], a[216:219], a[252:255], v[140:143]\n\t"
"v_mfma_f32_16x16x32_bf16 v[144:147], a[24:27], a[140:143], 0\n\t"
"v_or_b32 v228, 10, v4\n\t"
"v_mfma_f32_16x16x32_bf16 v[144:147], a[56:59], a[156:159], v[144:147]\n\t"
"buffer_store_dwordx2 v[228:229], v5, s[56:59], s17 offen sc1\n\t"
"v_mfma_f32_16x16x32_bf16 v[144:147], a[88:91], a[172:175], v[144:147]\n\t"
"v_mfma_f32_16x16x32_bf16 v[144:147], a[120:123], a[188:191], v[144:147]\n\t"
"v_mfma_f32_16x16x32_bf16 v[144:147], v[88:91], a[204:207], v[144:147]\n\t"
"v_mfma_f32_16x16x32_bf16 v[144:147], a[224:227], a[220:223], v[144:147]\n\t"
"ds_write_b64 v7, v[140:141] offset:1024\n\t"
"v_mfma_f32_16x16x32_bf16 v[144:147], a[228:231], a[236:239], v[144:147]\n\t"
"v_mfma_f32_16x16x32_bf16 v[144:147], a[232:235], a[252:255], v[144:147]\n\t"
"v_mfma_f32_16x16x32_bf16 v[148:151], a[28:31], a[140:143], 0\n\t"
"s_waitcnt vmcnt(5)\n\t"
"s_or_b32 s90, s86, 7\n\t"
"v_cmp_le_u64 vcc, s[90:91], v[2:3]\n\t"
"v_cmp_ge_u64 s[92:93], s[88:89], v[2:3]\n\t"
"s_nop 0\n\t"
"s_and_b64 vcc, vcc, s[92:93]\n\t"
"s_cmp_eq_u64 vcc, -1\n\t"
"s_cbranch_scc0 LPS12_%=\n\t"
"LPR12_%=:\n\t"
"v_mfma_f32_16x16x32_bf16 v[148:151], a[60:63], a[156:159], v[148:151]\n\t"
"s_add_u32 s22, s94, 131072\n\t"
"ds_write_b64 v7, v[142:143] offset:1032\n\t"
"v_mfma_f32_16x16x32_bf16 v[148:151], a[92:95], a[172:175], v[148:151]\n\t"
"buffer_load_dwordx4 v[96:99], v232, s[56:59], s22 offen sc1\n\t"
"v_mfma_f32_16x16x32_bf16 v[148:151], a[124:127], a[188:191], v[148:151]\n\t"
"v_mfma_f32_16x16x32_bf16 v[148:151], v[92:95], a[204:207], v[148:151]\n\t"
"ds_write_b64 v8, v[144:145] offset:0\n\t"
"v_mfma_f32_16x16x32_bf16 v[148:151], a[240:243], a[220:223], v[148:151]\n\t"
"buffer_load_dwordx4 v[100:103], v232, s[56:59], s22 offen offset:1024 sc1\n\t"
"v_mfma_f32_16x16x32_bf16 v[148:151], a[244:247], a[236:239], v[148:151]\n\t"
"v_mfma_f32_16x16x32_bf16 v[148:151], a[248:251], a[252:255], v[148:151]\n\t"
"ds_write_b64 v8, v[146:147] offset:8\n\t"
"v_mfma_f32_16x16x32_bf16 v[152:155], a[0:3], a[140:143], 0\n\t"
"buffer_load_dwordx4 v[104:107], v233, s[56:59], s22 offen sc1\n\t"
"v_mfma_f32_16x16x32_bf16 v[152:155], a[32:35], a[156:159], v[152:155]\n\t"
"v_mfma_f32_16x16x32_bf16 v[152:155], a[64:67], a[172:175], v[152:155]\n\t"
"buffer_load_dwordx4 v[108:111], v233, s[56:59], s22 offen offset:1024 sc1\n\t"
"v_mfma_f32_16x16x32_bf16 v[152:155], a[96:99], a[188:191], v[152:155]\n\t"
"v_mfma_f32_16x16x32_bf16 v[152:155], v[64:67], a[204:207], v[152:155]\n\t"
"ds_write_b64 v8, v[148:149] offset:1024\n\t"
"v_mfma_f32_16x16x32_bf16 v[152:155], a[128:131], a[220:223], v[152:155]\n\t"
"buffer_load_dwordx4 v[112:115], v234, s[56:59], s22 offen sc1\n\t"
"v_mfma_f32_16x16x32_bf16 v[152:155], a[132:135], a[236:239], v[152:155]\n\t"
"v_mfma_f32_16x16x32_bf16 v[152:155], a[136:139], a[252:255], v[152:155]\n\t"
"ds_write_b64 v8, v[150:151] offset:1032\n\t"
"v_mfma_f32_16x16x32_bf16 v[156:159], a[4:7], a[140:143], 0\n\t"
"buffer_load_dwordx4 v[116:119], v234, s[56:59], s22 offen offset:1024 sc1\n\t"
"v_mfma_f32_16x16x32_bf16 v[156:159], a[36:39], a[156:159], v[156:159]\n\t"
"v_mfma_f32_16x16x32_bf16 v[156:159], a[68:71], a[172:175], v[156:159]\n\t"
"v_mfma_f32_16x16x32_bf16 v[156:159], a[100:103], a[188:191], v[156:159]\n\t"
"buffer_load_dwordx4 v[120:123], v235, s[56:59], s22 offen sc1\n\t"
"v_mfma_f32_16x16x32_bf16 v[156:159], v[68:71], a[204:207], v[156:159]\n\t"
"v_mfma_f32_16x16x32_bf16 v[156:159], a[144:147], a[220:223], v[156:159]\n\t"
"v_mfma_f32_16x16x32_bf16 v[156:159], a[148:151], a[236:239], v[156:159]\n\t"
"buffer_load_dwordx4 v[124:127], v235, s[56:59], s22 offen offset:1024 sc1\n\t"
"v_mfma_f32_16x16x32_bf16 v[156:159], a[152:155], a[252:255], v[156:159]\n\t"
"s_waitcnt lgkmcnt(0)\n\t"
"s_barrier\n\t"
"ds_read_b128 v[208:211], v9 offset:2048\n\t"
"v_mfma_f32_16x16x32_bf16 v[128:131], a[8:11], v[168:171], 0\n\t"
"s_lshr_b64 s[80:81], s[82:83], 8\n\t"
"s_and_b32 s80, s80, 15\n\t"
"ds_read_b128 v[212:215], v9 offset:3072\n\t"
"v_mfma_f32_16x16x32_bf16 v[128:131], a[40:43], v[172:175], v[128:131]\n\t"
"s_mul_i32 s80, s80, 32768\n\t"
"s_add_u32 s20, s80, s95\n\t"
"ds_read_b128 v[216:219], v9 offset:4096\n\t"
"v_mfma_f32_16x16x32_bf16 v[128:131], a[72:75], v[176:179], v[128:131]\n\t"
"s_add_u32 s22, s94, 163840\n\t"
"ds_read_b128 v[220:223], v9 offset:5120\n\t"
"v_mfma_f32_16x16x32_bf16 v[128:131], a[104:107], v[180:183], v[128:131]\n\t"
"ds_read_b128 v[200:203], v9 offset:0\n\t"
"v_mfma_f32_16x16x32_bf16 v[128:131], v[72:75], v[184:187], v[128:131]\n\t"
"ds_read_b128 v[204:207], v9 offset:1024\n\t"
"v_mfma_f32_16x16x32_bf16 v[128:131], a[160:163], v[188:191], v[128:131]\n\t"
"buffer_load_dwordx4 a[140:143], v232, s[56:59], s22 offen sc1\n\t"
"v_mfma_f32_16x16x32_bf16 v[128:131], a[164:167], v[192:195], v[128:131]\n\t"
"buffer_load_dwordx4 a[156:159], v232, s[56:59], s22 offen offset:1024 sc1\n\t"
"v_mfma_f32_16x16x32_bf16 v[128:131], a[168:171], v[196:199], v[128:131]\n\t"
"s_waitcnt lgkmcnt(3)\n\t"
"v_add_f32 v208, v208, v216\n\t"
"v_add_f32 v209, v209, v217\n\t"
"v_mfma_f32_16x16x32_bf16 v[132:135], a[12:15], v[168:171], 0\n\t"
"v_add_f32 v210, v210, v218\n\t"
"v_add_f32 v211, v211, v219\n\t"
"v_mfma_f32_16x16x32_bf16 v[132:135], a[44:47], v[172:175], v[132:135]\n\t"
"buffer_load_dwordx4 a[172:175], v233, s[56:59], s22 offen sc1\n\t"
"v_mfma_f32_16x16x32_bf16 v[132:135], a[76:79], v[176:179], v[132:135]\n\t"
"ds_write_b64 v6, v[128:129] offset:6144\n\t"
"v_mfma_f32_16x16x32_bf16 v[132:135], a[108:111], v[180:183], v[132:135]\n\t"
"s_waitcnt lgkmcnt(3)\n\t"
"v_add_f32 v212, v212, v220\n\t"
"v_add_f32 v213, v213, v221\n\t"
"v_mfma_f32_16x16x32_bf16 v[132:135], v[76:79], v[184:187], v[132:135]\n\t"
"buffer_load_dwordx4 a[188:191], v233, s[56:59], s22 offen offset:1024 sc1\n\t"
"v_mfma_f32_16x16x32_bf16 v[132:135], a[176:179], v[188:191], v[132:135]\n\t"
"ds_write_b64 v6, v[130:131] offset:6152\n\t"
"v_mfma_f32_16x16x32_bf16 v[132:135], a[180:183], v[192:195], v[132:135]\n\t"
"v_add_f32 v214, v214, v222\n\t"
"v_add_f32 v215, v215, v223\n\t"
"v_mfma_f32_16x16x32_bf16 v[132:135], a[184:187], v[196:199], v[132:135]\n\t"
"buffer_load_dwordx4 a[204:207], v234, s[56:59], s22 offen sc1\n\t"
"v_mfma_f32_16x16x32_bf16 v[136:139], a[16:19], v[168:171], 0\n\t"
"s_waitcnt lgkmcnt(3)\n\t"
"v_add_f32 v208, v208, v200\n\t"
"v_add_f32 v209, v209, v201\n\t"
"v_mfma_f32_16x16x32_bf16 v[136:139], a[48:51], v[172:175], v[136:139]\n\t"
"v_add_f32 v210, v210, v202\n\t"
"v_add_f32 v211, v211, v203\n\t"
"v_mfma_f32_16x16x32_bf16 v[136:139], a[80:83], v[176:179], v[136:139]\n\t"
"ds_write_b64 v6, v[132:133] offset:7168\n\t"
"v_mfma_f32_16x16x32_bf16 v[136:139], a[112:115], v[180:183], v[136:139]\n\t"
"buffer_load_dwordx4 a[220:223], v234, s[56:59], s22 offen offset:1024 sc1\n\t"
"v_mfma_f32_16x16x32_bf16 v[136:139], v[80:83], v[184:187], v[136:139]\n\t"
"s_waitcnt lgkmcnt(3)\n\t"
"v_add_f32 v212, v212, v204\n\t"
"v_add_f32 v213, v213, v205\n\t"
"v_mfma_f32_16x16x32_bf16 v[136:139], a[192:195], v[188:191], v[136:139]\n\t"
"ds_write_b64 v6, v[134:135] offset:7176\n\t"
"v_mfma_f32_16x16x32_bf16 v[136:139], a[196:199], v[192:195], v[136:139]\n\t"
"buffer_load_dwordx4 a[236:239], v235, s[56:59], s22 offen sc1\n\t"
"v_mfma_f32_16x16x32_bf16 v[136:139], a[200:203], v[196:199], v[136:139]\n\t"
"v_add_f32 v214, v214, v206\n\t"
"v_add_f32 v215, v215, v207\n\t"
"v_mfma_f32_16x16x32_bf16 v[140:143], a[20:23], v[168:171], 0\n\t"
"buffer_load_dwordx4 a[252:255], v235, s[56:59], s22 offen offset:1024 sc1\n\t"
"v_mfma_f32_16x16x32_bf16 v[140:143], a[52:55], v[172:175], v[140:143]\n\t"
"v_add_f32 v152, v152, v208\n\t"
"v_add_f32 v153, v153, v209\n\t"
"v_mfma_f32_16x16x32_bf16 v[140:143], a[84:87], v[176:179], v[140:143]\n\t"
"ds_write_b64 v7, v[136:137] offset:6144\n\t"
"v_mfma_f32_16x16x32_bf16 v[140:143], a[116:119], v[180:183], v[140:143]\n\t"
"v_add_f32 v154, v154, v210\n\t"
"v_add_f32 v155, v155, v211\n\t"
"v_mfma_f32_16x16x32_bf16 v[140:143], v[84:87], v[184:187], v[140:143]\n\t"
"buffer_load_dwordx2 v[2:3], v15, s[56:59], s16 offen sc1\n\t"
"v_mfma_f32_16x16x32_bf16 v[140:143], a[208:211], v[188:191], v[140:143]\n\t"
"ds_write_b64 v7, v[138:139] offset:6152\n\t"
"v_mfma_f32_16x16x32_bf16 v[140:143], a[212:215], v[192:195], v[140:143]\n\t"
"v_add_f32 v156, v156, v212\n\t"
"v_add_f32 v157, v157, v213\n\t"
"v_add_f32 v158, v158, v214\n\t"
"v_add_f32 v159, v159, v215\n\t"
"v_mfma_f32_16x16x32_bf16 v[140:143], a[216:219], v[196:199], v[140:143]\n\t"
"v_bfi_b32 v152, s27, v52, v152\n\t"
"v_bfi_b32 v153, s27, v53, v153\n\t"
"v_mfma_f32_16x16x32_bf16 v[144:147], a[24:27], v[168:171], 0\n\t"
"v_bfi_b32 v154, s27, v54, v154\n\t"
"v_bfi_b32 v155, s27, v55, v155\n\t"
"v_mfma_f32_16x16x32_bf16 v[144:147], a[56:59], v[172:175], v[144:147]\n\t"
"v_bfi_b32 v156, s27, v52, v156\n\t"
"v_bfi_b32 v157, s27, v53, v157\n\t"
"v_mfma_f32_16x16x32_bf16 v[144:147], a[88:91], v[176:179], v[144:147]\n\t"
"v_bfi_b32 v158, s27, v54, v158\n\t"
"v_bfi_b32 v159, s27, v55, v159\n\t"
"v_mfma_f32_16x16x32_bf16 v[144:147], a[120:123], v[180:183], v[144:147]\n\t"
"buffer_store_dwordx4 v[152:155], v48, s[56:59], s20 offen sc1\n\t"
"v_mfma_f32_16x16x32_bf16 v[144:147], v[88:91], v[184:187], v[144:147]\n\t"
"buffer_store_dwordx4 v[156:159], v48, s[56:59], s20 offen offset:1024 sc1\n\t"
"v_mfma_f32_16x16x32_bf16 v[144:147], a[224:227], v[188:191], v[144:147]\n\t"
"ds_write_b64 v7, v[140:141] offset:7168\n\t"
"v_mfma_f32_16x16x32_bf16 v[144:147], a[228:231], v[192:195], v[144:147]\n\t"
"v_mfma_f32_16x16x32_bf16 v[144:147], a[232:235], v[196:199], v[144:147]\n\t"
"v_mfma_f32_16x16x32_bf16 v[148:151], a[28:31], v[168:171], 0\n\t"
"ds_write_b64 v7, v[142:143] offset:7176\n\t"
"v_mfma_f32_16x16x32_bf16 v[148:151], a[60:63], v[172:175], v[148:151]\n\t"
"v_mfma_f32_16x16x32_bf16 v[148:151], a[92:95], v[176:179], v[148:151]\n\t"
"v_mfma_f32_16x16x32_bf16 v[148:151], a[124:127], v[180:183], v[148:151]\n\t"
"v_mfma_f32_16x16x32_bf16 v[148:151], v[92:95], v[184:187], v[148:151]\n\t"
"v_mfma_f32_16x16x32_bf16 v[148:151], a[240:243], v[188:191], v[148:151]\n\t"
"v_mfma_f32_16x16x32_bf16 v[148:151], a[244:247], v[192:195], v[148:151]\n\t"
"ds_write_b64 v8, v[144:145] offset:6144\n\t"
"v_mfma_f32_16x16x32_bf16 v[148:151], a[248:251], v[196:199], v[148:151]\n\t"
"v_mfma_f32_16x16x32_bf16 v[160:163], a[0:3], v[168:171], 0\n\t"
"v_mfma_f32_16x16x32_bf16 v[160:163], a[32:35], v[172:175], v[160:163]\n\t"
"ds_write_b64 v8, v[146:147] offset:6152\n\t"
"v_mfma_f32_16x16x32_bf16 v[160:163], a[64:67], v[176:179], v[160:163]\n\t"
"v_mfma_f32_16x16x32_bf16 v[160:163], a[96:99], v[180:183], v[160:163]\n\t"
"v_mfma_f32_16x16x32_bf16 v[160:163], v[64:67], v[184:187], v[160:163]\n\t"
"v_mfma_f32_16x16x32_bf16 v[160:163], a[128:131], v[188:191], v[160:163]\n\t"
"v_mfma_f32_16x16x32_bf16 v[160:163], a[132:135], v[192:195], v[160:163]\n\t"
"ds_write_b64 v8, v[148:149] offset:7168\n\t"
"v_mfma_f32_16x16x32_bf16 v[160:163], a[136:139], v[196:199], v[160:163]\n\t"
"v_mfma_f32_16x16x32_bf16 v[164:167], a[4:7], v[168:171], 0\n\t"
"v_mfma_f32_16x16x32_bf16 v[164:167], a[36:39], v[172:175], v[164:167]\n\t"
"ds_write_b64 v8, v[150:151] offset:7176\n\t"
"v_mfma_f32_16x16x32_bf16 v[164:167], a[68:71], v[176:179], v[164:167]\n\t"
"v_mfma_f32_16x16x32_bf16 v[164:167], a[100:103], v[180:183], v[164:167]\n\t"
"v_mfma_f32_16x16x32_bf16 v[164:167], v[68:71], v[184:187], v[164:167]\n\t"
"v_mfma_f32_16x16x32_bf16 v[164:167], a[144:147], v[188:191], v[164:167]\n\t"
"v_mfma_f32_16x16x32_bf16 v[164:167], a[148:151], v[192:195], v[164:167]\n\t"
"v_mfma_f32_16x16x32_bf16 v[164:167], a[152:155], v[196:199], v[164:167]\n\t"
"s_waitcnt lgkmcnt(0)\n\t"
"s_barrier\n\t"
"ds_read_b128 v[208:211], v9 offset:8192\n\t"
"s_waitcnt vmcnt(18)\n\t"
"v_mfma_f32_16x16x32_bf16 v[128:131], a[8:11], v[96:99], 0\n\t"
"s_waitcnt vmcnt(17)\n\t"
"s_lshr_b64 s[80:81], s[82:83], 12\n\t"
"s_and_b32 s80, s80, 15\n\t"
"ds_read_b128 v[212:215], v9 offset:9216\n\t"
"v_mfma_f32_16x16x32_bf16 v[128:131], a[40:43], v[100:103], v[128:131]\n\t"
"s_waitcnt vmcnt(16)\n\t"
"s_mul_i32 s80, s80, 32768\n\t"
"s_add_u32 s20, s80, s95\n\t"
"ds_read_b128 v[216:219], v9 offset:10240\n\t"
"v_mfma_f32_16x16x32_bf16 v[128:131], a[72:75], v[104:107], v[128:131]\n\t"
"s_waitcnt vmcnt(15)\n\t"
"s_add_u32 s22, s94, 196608\n\t"
"ds_read_b128 v[220:223], v9 offset:11264\n\t"
"v_mfma_f32_16x16x32_bf16 v[128:131], a[104:107], v[108:111], v[128:131]\n\t"
"s_waitcnt vmcnt(14)\n\t"
"ds_read_b128 v[200:203], v9 offset:6144\n\t"
"v_mfma_f32_16x16x32_bf16 v[128:131], v[72:75], v[112:115], v[128:131]\n\t"
"s_waitcnt vmcnt(13)\n\t"
"ds_read_b128 v[204:207], v9 offset:7168\n\t"
"v_mfma_f32_16x16x32_bf16 v[128:131], a[160:163], v[116:119], v[128:131]\n\t"
"s_waitcnt vmcnt(12)\n\t"
"buffer_load_dwordx4 v[168:171], v232, s[56:59], s22 offen sc1\n\t"
"v_mfma_f32_16x16x32_bf16 v[128:131], a[164:167], v[120:123], v[128:131]\n\t"
"s_waitcnt vmcnt(12)\n\t"
"buffer_load_dwordx4 v[172:175], v232, s[56:59], s22 offen offset:1024 sc1\n\t"
"v_mfma_f32_16x16x32_bf16 v[128:131], a[168:171], v[124:127], v[128:131]\n\t"
"s_waitcnt lgkmcnt(3)\n\t"
"v_add_f32 v208, v208, v216\n\t"
"v_add_f32 v209, v209, v217\n\t"
"v_mfma_f32_16x16x32_bf16 v[132:135], a[12:15], v[96:99], 0\n\t"
"v_add_f32 v210, v210, v218\n\t"
"v_add_f32 v211, v211, v219\n\t"
"v_mfma_f32_16x16x32_bf16 v[132:135], a[44:47], v[100:103], v[132:135]\n\t"
"buffer_load_dwordx4 v[176:179], v233, s[56:59], s22 offen sc1\n\t"
"v_mfma_f32_16x16x32_bf16 v[132:135], a[76:79], v[104:107], v[132:135]\n\t"
"ds_write_b64 v6, v[128:129] offset:0\n\t"
"v_mfma_f32_16x16x32_bf16 v[132:135], a[108:111], v[108:111], v[132:135]\n\t"
"s_waitcnt lgkmcnt(3)\n\t"
"v_add_f32 v212, v212, v220\n\t"
"v_add_f32 v213, v213, v221\n\t"
"v_mfma_f32_16x16x32_bf16 v[132:135], v[76:79], v[112:115], v[132:135]\n\t"
"buffer_load_dwordx4 v[180:183], v233, s[56:59], s22 offen offset:1024 sc1\n\t"
"v_mfma_f32_16x16x32_bf16 v[132:135], a[176:179], v[116:119], v[132:135]\n\t"
"ds_write_b64 v6, v[130:131] offset:8\n\t"
"v_mfma_f32_16x16x32_bf16 v[132:135], a[180:183], v[120:123], v[132:135]\n\t"
"v_add_f32 v214, v214, v222\n\t"
"v_add_f32 v215, v215, v223\n\t"
"v_mfma_f32_16x16x32_bf16 v[132:135], a[184:187], v[124:127], v[132:135]\n\t"
"buffer_load_dwordx4 v[184:187], v234, s[56:59], s22 offen sc1\n\t"
"v_mfma_f32_16x16x32_bf16 v[136:139], a[16:19], v[96:99], 0\n\t"
"s_waitcnt lgkmcnt(3)\n\t"
"v_add_f32 v208, v208, v200\n\t"
"v_add_f32 v209, v209, v201\n\t"
"v_mfma_f32_16x16x32_bf16 v[136:139], a[48:51], v[100:103], v[136:139]\n\t"
"v_add_f32 v210, v210, v202\n\t"
"v_add_f32 v211, v211, v203\n\t"
"v_mfma_f32_16x16x32_bf16 v[136:139], a[80:83], v[104:107], v[136:139]\n\t"
"ds_write_b64 v6, v[132:133] offset:1024\n\t"
"v_mfma_f32_16x16x32_bf16 v[136:139], a[112:115], v[108:111], v[136:139]\n\t"
"buffer_load_dwordx4 v[188:191], v234, s[56:59], s22 offen offset:1024 sc1\n\t"
"v_mfma_f32_16x16x32_bf16 v[136:139], v[80:83], v[112:115], v[136:139]\n\t"
"s_waitcnt lgkmcnt(3)\n\t"
"v_add_f32 v212, v212, v204\n\t"
"v_add_f32 v213, v213, v205\n\t"
"v_mfma_f32_16x16x32_bf16 v[136:139], a[192:195], v[116:119], v[136:139]\n\t"
"ds_write_b64 v6, v[134:135] offset:1032\n\t"
"v_mfma_f32_16x16x32_bf16 v[136:139], a[196:199], v[120:123], v[136:139]\n\t"
"buffer_load_dwordx4 v[192:195], v235, s[56:59], s22 offen sc1\n\t"
"v_mfma_f32_16x16x32_bf16 v[136:139], a[200:203], v[124:127], v[136:139]\n\t"
"v_add_f32 v214, v214, v206\n\t"
"v_add_f32 v215, v215, v207\n\t"
"v_mfma_f32_16x16x32_bf16 v[140:143], a[20:23], v[96:99], 0\n\t"
"buffer_load_dwordx4 v[196:199], v235, s[56:59], s22 offen offset:1024 sc1\n\t"
"v_mfma_f32_16x16x32_bf16 v[140:143], a[52:55], v[100:103], v[140:143]\n\t"
"v_add_f32 v160, v160, v208\n\t"
"v_add_f32 v161, v161, v209\n\t"
"v_mfma_f32_16x16x32_bf16 v[140:143], a[84:87], v[104:107], v[140:143]\n\t"
"ds_write_b64 v7, v[136:137] offset:0\n\t"
"v_mfma_f32_16x16x32_bf16 v[140:143], a[116:119], v[108:111], v[140:143]\n\t"
"v_add_f32 v162, v162, v210\n\t"
"v_add_f32 v163, v163, v211\n\t"
"v_mfma_f32_16x16x32_bf16 v[140:143], v[84:87], v[112:115], v[140:143]\n\t"
"v_add_f32 v164, v164, v212\n\t"
"v_add_f32 v165, v165, v213\n\t"
"v_mfma_f32_16x16x32_bf16 v[140:143], a[208:211], v[116:119], v[140:143]\n\t"
"ds_write_b64 v7, v[138:139] offset:8\n\t"
"v_mfma_f32_16x16x32_bf16 v[140:143], a[212:215], v[120:123], v[140:143]\n\t"
"v_add_f32 v166, v166, v214\n\t"
"v_add_f32 v167, v167, v215\n\t"
"v_mfma_f32_16x16x32_bf16 v[140:143], a[216:219], v[124:127], v[140:143]\n\t"
"v_bfi_b32 v160, s27, v52, v160\n\t"
"v_bfi_b32 v161, s27, v53, v161\n\t"
"v_mfma_f32_16x16x32_bf16 v[144:147], a[24:27], v[96:99], 0\n\t"
"v_bfi_b32 v162, s27, v54, v162\n\t"
"v_bfi_b32 v163, s27, v55, v163\n\t"
"v_mfma_f32_16x16x32_bf16 v[144:147], a[56:59], v[100:103], v[144:147]\n\t"
"v_bfi_b32 v164, s27, v52, v164\n\t"
"v_bfi_b32 v165, s27, v53, v165\n\t"
"v_mfma_f32_16x16x32_bf16 v[144:147], a[88:91], v[104:107], v[144:147]\n\t"
"v_bfi_b32 v166, s27, v54, v166\n\t"
"v_bfi_b32 v167, s27, v55, v167\n\t"
"v_mfma_f32_16x16x32_bf16 v[144:147], a[120:123], v[108:111], v[144:147]\n\t"
"buffer_store_dwordx4 v[160:163], v48, s[56:59], s20 offen sc1\n\t"
"v_mfma_f32_16x16x32_bf16 v[144:147], v[88:91], v[112:115], v[144:147]\n\t"
"buffer_store_dwordx4 v[164:167], v48, s[56:59], s20 offen offset:1024 sc1\n\t"
"v_mfma_f32_16x16x32_bf16 v[144:147], a[224:227], v[116:119], v[144:147]\n\t"
"ds_write_b64 v7, v[140:141] offset:1024\n\t"
"v_mfma_f32_16x16x32_bf16 v[144:147], a[228:231], v[120:123], v[144:147]\n\t"
"v_mfma_f32_16x16x32_bf16 v[144:147], a[232:235], v[124:127], v[144:147]\n\t"
"v_mfma_f32_16x16x32_bf16 v[148:151], a[28:31], v[96:99], 0\n\t"
"ds_write_b64 v7, v[142:143] offset:1032\n\t"
"v_mfma_f32_16x16x32_bf16 v[148:151], a[60:63], v[100:103], v[148:151]\n\t"
"v_mfma_f32_16x16x32_bf16 v[148:151], a[92:95], v[104:107], v[148:151]\n\t"
"v_mfma_f32_16x16x32_bf16 v[148:151], a[124:127], v[108:111], v[148:151]\n\t"
"v_mfma_f32_16x16x32_bf16 v[148:151], v[92:95], v[112:115], v[148:151]\n\t"
"v_mfma_f32_16x16x32_bf16 v[148:151], a[240:243], v[116:119], v[148:151]\n\t"
"v_mfma_f32_16x16x32_bf16 v[148:151], a[244:247], v[120:123], v[148:151]\n\t"
"ds_write_b64 v8, v[144:145] offset:0\n\t"
"v_mfma_f32_16x16x32_bf16 v[148:151], a[248:251], v[124:127], v[148:151]\n\t"
"v_mfma_f32_16x16x32_bf16 v[152:155], a[0:3], v[96:99], 0\n\t"
"v_mfma_f32_16x16x32_bf16 v[152:155], a[32:35], v[100:103], v[152:155]\n\t"
"ds_write_b64 v8, v[146:147] offset:8\n\t"
"v_mfma_f32_16x16x32_bf16 v[152:155], a[64:67], v[104:107], v[152:155]\n\t"
"v_mfma_f32_16x16x32_bf16 v[152:155], a[96:99], v[108:111], v[152:155]\n\t"
"v_mfma_f32_16x16x32_bf16 v[152:155], v[64:67], v[112:115], v[152:155]\n\t"
"v_mfma_f32_16x16x32_bf16 v[152:155], a[128:131], v[116:119], v[152:155]\n\t"
"v_mfma_f32_16x16x32_bf16 v[152:155], a[132:135], v[120:123], v[152:155]\n\t"
"ds_write_b64 v8, v[148:149] offset:1024\n\t"
"v_mfma_f32_16x16x32_bf16 v[152:155], a[136:139], v[124:127], v[152:155]\n\t"
"v_mfma_f32_16x16x32_bf16 v[156:159], a[4:7], v[96:99], 0\n\t"
"v_mfma_f32_16x16x32_bf16 v[156:159], a[36:39], v[100:103], v[156:159]\n\t"
"ds_write_b64 v8, v[150:151] offset:1032\n\t"
"v_mfma_f32_16x16x32_bf16 v[156:159], a[68:71], v[104:107], v[156:159]\n\t"
"v_mfma_f32_16x16x32_bf16 v[156:159], a[100:103], v[108:111], v[156:159]\n\t"
"s_waitcnt vmcnt(12)\n\t"
"s_or_b32 s90, s86, 10\n\t"
"v_cmp_le_u64 vcc, s[90:91], v[2:3]\n\t"
"v_cmp_ge_u64 s[92:93], s[88:89], v[2:3]\n\t"
"s_nop 0\n\t"
"s_and_b64 vcc, vcc, s[92:93]\n\t"
"s_cmp_eq_u64 vcc, -1\n\t"
"s_cbranch_scc0 LPS13_%=\n\t"
"LPR13_%=:\n\t"
"v_mfma_f32_16x16x32_bf16 v[156:159], v[68:71], v[112:115], v[156:159]\n\t"
"v_mfma_f32_16x16x32_bf16 v[156:159], a[144:147], v[116:119], v[156:159]\n\t"
"v_mfma_f32_16x16x32_bf16 v[156:159], a[148:151], v[120:123], v[156:159]\n\t"
"v_mfma_f32_16x16x32_bf16 v[156:159], a[152:155], v[124:127], v[156:159]\n\t"
"s_waitcnt lgkmcnt(0)\n\t"
"s_barrier\n\t"
"ds_read_b128 v[208:211], v9 offset:2048\n\t"
"v_mfma_f32_16x16x32_bf16 v[128:131], a[8:11], a[140:143], 0\n\t"
"s_lshr_b64 s[80:81], s[82:83], 16\n\t"
"s_and_b32 s80, s80, 15\n\t"
"ds_read_b128 v[212:215], v9 offset:3072\n\t"
"v_mfma_f32_16x16x32_bf16 v[128:131], a[40:43], a[156:159], v[128:131]\n\t"
"s_mul_i32 s80, s80, 32768\n\t"
"s_add_u32 s20, s80, s95\n\t"
"ds_read_b128 v[216:219], v9 offset:4096\n\t"
"v_mfma_f32_16x16x32_bf16 v[128:131], a[72:75], a[172:175], v[128:131]\n\t"
"s_add_u32 s22, s94, 229376\n\t"
"ds_read_b128 v[220:223], v9 offset:5120\n\t"
"v_mfma_f32_16x16x32_bf16 v[128:131], a[104:107], a[188:191], v[128:131]\n\t"
"ds_read_b128 v[200:203], v9 offset:0\n\t"
"v_mfma_f32_16x16x32_bf16 v[128:131], v[72:75], a[204:207], v[128:131]\n\t"
"ds_read_b128 v[204:207], v9 offset:1024\n\t"
"v_mfma_f32_16x16x32_bf16 v[128:131], a[160:163], a[220:223], v[128:131]\n\t"
"buffer_load_dwordx4 v[96:99], v232, s[56:59], s22 offen sc1\n\t"
"v_mfma_f32_16x16x32_bf16 v[128:131], a[164:167], a[236:239], v[128:131]\n\t"
"buffer_load_dwordx4 v[100:103], v232, s[56:59], s22 offen offset:1024 sc1\n\t"
"v_mfma_f32_16x16x32_bf16 v[128:131], a[168:171], a[252:255], v[128:131]\n\t"
"s_waitcnt lgkmcnt(3)\n\t"
"v_add_f32 v208, v208, v216\n\t"
"v_add_f32 v209, v209, v217\n\t"
"v_mfma_f32_16x16x32_bf16 v[132:135], a[12:15], a[140:143], 0\n\t"
"v_add_f32 v210, v210, v218\n\t"
"v_add_f32 v211, v211, v219\n\t"
"v_mfma_f32_16x16x32_bf16 v[132:135], a[44:47], a[156:159], v[132:135]\n\t"
"buffer_load_dwordx4 v[104:107], v233, s[56:59], s22 offen sc1\n\t"
"v_mfma_f32_16x16x32_bf16 v[132:135], a[76:79], a[172:175], v[132:135]\n\t"
"ds_write_b64 v6, v[128:129] offset:6144\n\t"
"v_mfma_f32_16x16x32_bf16 v[132:135], a[108:111], a[188:191], v[132:135]\n\t"
"s_waitcnt lgkmcnt(3)\n\t"
"v_add_f32 v212, v212, v220\n\t"
"v_add_f32 v213, v213, v221\n\t"
"v_mfma_f32_16x16x32_bf16 v[132:135], v[76:79], a[204:207], v[132:135]\n\t"
"buffer_load_dwordx4 v[108:111], v233, s[56:59], s22 offen offset:1024 sc1\n\t"
"v_mfma_f32_16x16x32_bf16 v[132:135], a[176:179], a[220:223], v[132:135]\n\t"
"ds_write_b64 v6, v[130:131] offset:6152\n\t"
"v_mfma_f32_16x16x32_bf16 v[132:135], a[180:183], a[236:239], v[132:135]\n\t"
"v_add_f32 v214, v214, v222\n\t"
"v_add_f32 v215, v215, v223\n\t"
"v_mfma_f32_16x16x32_bf16 v[132:135], a[184:187], a[252:255], v[132:135]\n\t"
"buffer_load_dwordx4 v[112:115], v234, s[56:59], s22 offen sc1\n\t"
"v_mfma_f32_16x16x32_bf16 v[136:139], a[16:19], a[140:143], 0\n\t"
"s_waitcnt lgkmcnt(3)\n\t"
"v_add_f32 v208, v208, v200\n\t"
"v_add_f32 v209, v209, v201\n\t"
"v_mfma_f32_16x16x32_bf16 v[136:139], a[48:51], a[156:159], v[136:139]\n\t"
"v_add_f32 v210, v210, v202\n\t"
"v_add_f32 v211, v211, v203\n\t"
"v_mfma_f32_16x16x32_bf16 v[136:139], a[80:83], a[172:175], v[136:139]\n\t"
"ds_write_b64 v6, v[132:133] offset:7168\n\t"
"v_mfma_f32_16x16x32_bf16 v[136:139], a[112:115], a[188:191], v[136:139]\n\t"
"buffer_load_dwordx4 v[116:119], v234, s[56:59], s22 offen offset:1024 sc1\n\t"
"v_mfma_f32_16x16x32_bf16 v[136:139], v[80:83], a[204:207], v[136:139]\n\t"
"s_waitcnt lgkmcnt(3)\n\t"
"v_add_f32 v212, v212, v204\n\t"
"v_add_f32 v213, v213, v205\n\t"
"v_mfma_f32_16x16x32_bf16 v[136:139], a[192:195], a[220:223], v[136:139]\n\t"
"ds_write_b64 v6, v[134:135] offset:7176\n\t"
"v_mfma_f32_16x16x32_bf16 v[136:139], a[196:199], a[236:239], v[136:139]\n\t"
"buffer_load_dwordx4 v[120:123], v235, s[56:59], s22 offen sc1\n\t"
"v_mfma_f32_16x16x32_bf16 v[136:139], a[200:203], a[252:255], v[136:139]\n\t"
"v_add_f32 v214, v214, v206\n\t"
"v_add_f32 v215, v215, v207\n\t"
"v_mfma_f32_16x16x32_bf16 v[140:143], a[20:23], a[140:143], 0\n\t"
"buffer_load_dwordx4 v[124:127], v235, s[56:59], s22 offen offset:1024 sc1\n\t"
"v_mfma_f32_16x16x32_bf16 v[140:143], a[52:55], a[156:159], v[140:143]\n\t"
"v_add_f32 v152, v152, v208\n\t"
"v_add_f32 v153, v153, v209\n\t"
"v_mfma_f32_16x16x32_bf16 v[140:143], a[84:87], a[172:175], v[140:143]\n\t"
"ds_write_b64 v7, v[136:137] offset:6144\n\t"
"v_mfma_f32_16x16x32_bf16 v[140:143], a[116:119], a[188:191], v[140:143]\n\t"
"v_add_f32 v154, v154, v210\n\t"
"v_add_f32 v155, v155, v211\n\t"
"v_mfma_f32_16x16x32_bf16 v[140:143], v[84:87], a[204:207], v[140:143]\n\t"
"v_add_f32 v156, v156, v212\n\t"
"v_add_f32 v157, v157, v213\n\t"
"v_mfma_f32_16x16x32_bf16 v[140:143], a[208:211], a[220:223], v[140:143]\n\t"
"ds_write_b64 v7, v[138:139] offset:6152\n\t"
"v_mfma_f32_16x16x32_bf16 v[140:143], a[212:215], a[236:239], v[140:143]\n\t"
"v_add_f32 v158, v158, v214\n\t"
"v_add_f32 v159, v159, v215\n\t"
"v_mfma_f32_16x16x32_bf16 v[140:143], a[216:219], a[252:255], v[140:143]\n\t"
"v_bfi_b32 v152, s27, v52, v152\n\t"
"v_bfi_b32 v153, s27, v53, v153\n\t"
"v_mfma_f32_16x16x32_bf16 v[144:147], a[24:27], a[140:143], 0\n\t"
"v_bfi_b32 v154, s27, v54, v154\n\t"
"v_bfi_b32 v155, s27, v55, v155\n\t"
"v_mfma_f32_16x16x32_bf16 v[144:147], a[56:59], a[156:159], v[144:147]\n\t"
"v_bfi_b32 v156, s27, v52, v156\n\t"
"v_bfi_b32 v157, s27, v53, v157\n\t"
"v_mfma_f32_16x16x32_bf16 v[144:147], a[88:91], a[172:175], v[144:147]\n\t"
"v_bfi_b32 v158, s27, v54, v158\n\t"
"v_bfi_b32 v159, s27, v55, v159\n\t"
"v_mfma_f32_16x16x32_bf16 v[144:147], a[120:123], a[188:191], v[144:147]\n\t"
"buffer_store_dwordx4 v[152:155], v48, s[56:59], s20 offen sc1\n\t"
"v_mfma_f32_16x16x32_bf16 v[144:147], v[88:91], a[204:207], v[144:147]\n\t"
"buffer_store_dwordx4 v[156:159], v48, s[56:59], s20 offen offset:1024 sc1\n\t"
"v_mfma_f32_16x16x32_bf16 v[144:147], a[224:227], a[220:223], v[144:147]\n\t"
"ds_write_b64 v7, v[140:141] offset:7168\n\t"
"v_mfma_f32_16x16x32_bf16 v[144:147], a[228:231], a[236:239], v[144:147]\n\t"
"v_mfma_f32_16x16x32_bf16 v[144:147], a[232:235], a[252:255], v[144:147]\n\t"
"v_mfma_f32_16x16x32_bf16 v[148:151], a[28:31], a[140:143], 0\n\t"
"ds_write_b64 v7, v[142:143] offset:7176\n\t"
"v_mfma_f32_16x16x32_bf16 v[148:151], a[60:63], a[156:159], v[148:151]\n\t"
"v_mfma_f32_16x16x32_bf16 v[148:151], a[92:95], a[172:175], v[148:151]\n\t"
"v_mfma_f32_16x16x32_bf16 v[148:151], a[124:127], a[188:191], v[148:151]\n\t"
"v_mfma_f32_16x16x32_bf16 v[148:151], v[92:95], a[204:207], v[148:151]\n\t"
"v_mfma_f32_16x16x32_bf16 v[148:151], a[240:243], a[220:223], v[148:151]\n\t"
"v_mfma_f32_16x16x32_bf16 v[148:151], a[244:247], a[236:239], v[148:151]\n\t"
"ds_write_b64 v8, v[144:145] offset:6144\n\t"
"v_mfma_f32_16x16x32_bf16 v[148:151], a[248:251], a[252:255], v[148:151]\n\t"
"v_mfma_f32_16x16x32_bf16 v[160:163], a[0:3], a[140:143], 0\n\t"
"v_mfma_f32_16x16x32_bf16 v[160:163], a[32:35], a[156:159], v[160:163]\n\t"
"ds_write_b64 v8, v[146:147] offset:6152\n\t"
"v_mfma_f32_16x16x32_bf16 v[160:163], a[64:67], a[172:175], v[160:163]\n\t"
"v_mfma_f32_16x16x32_bf16 v[160:163], a[96:99], a[188:191], v[160:163]\n\t"
"v_mfma_f32_16x16x32_bf16 v[160:163], v[64:67], a[204:207], v[160:163]\n\t"
"v_mfma_f32_16x16x32_bf16 v[160:163], a[128:131], a[220:223], v[160:163]\n\t"
"v_mfma_f32_16x16x32_bf16 v[160:163], a[132:135], a[236:239], v[160:163]\n\t"
"ds_write_b64 v8, v[148:149] offset:7168\n\t"
"v_mfma_f32_16x16x32_bf16 v[160:163], a[136:139], a[252:255], v[160:163]\n\t"
"v_mfma_f32_16x16x32_bf16 v[164:167], a[4:7], a[140:143], 0\n\t"
"v_mfma_f32_16x16x32_bf16 v[164:167], a[36:39], a[156:159], v[164:167]\n\t"
"ds_write_b64 v8, v[150:151] offset:7176\n\t"
"v_mfma_f32_16x16x32_bf16 v[164:167], a[68:71], a[172:175], v[164:167]\n\t"
"v_mfma_f32_16x16x32_bf16 v[164:167], a[100:103], a[188:191], v[164:167]\n\t"
"v_mfma_f32_16x16x32_bf16 v[164:167], v[68:71], a[204:207], v[164:167]\n\t"
"v_mfma_f32_16x16x32_bf16 v[164:167], a[144:147], a[220:223], v[164:167]\n\t"
"v_mfma_f32_16x16x32_bf16 v[164:167], a[148:151], a[236:239], v[164:167]\n\t"
"v_mfma_f32_16x16x32_bf16 v[164:167], a[152:155], a[252:255], v[164:167]\n\t"
"s_waitcnt lgkmcnt(0)\n\t"
"s_barrier\n\t"
"ds_read_b128 v[208:211], v9 offset:8192\n\t"
"s_waitcnt vmcnt(19)\n\t"
"v_mfma_f32_16x16x32_bf16 v[128:131], a[8:11], v[168:171], 0\n\t"
"s_waitcnt vmcnt(18)\n\t"
"s_lshr_b64 s[80:81], s[82:83], 20\n\t"
"s_and_b32 s80, s80, 15\n\t"
"ds_read_b128 v[212:215], v9 offset:9216\n\t"
"v_mfma_f32_16x16x32_bf16 v[128:131], a[40:43], v[172:175], v[128:131]\n\t"
"s_waitcnt vmcnt(17)\n\t"
"s_mul_i32 s80, s80, 32768\n\t"
"s_add_u32 s20, s80, s95\n\t"
"ds_read_b128 v[216:219], v9 offset:10240\n\t"
"v_mfma_f32_16x16x32_bf16 v[128:131], a[72:75], v[176:179], v[128:131]\n\t"
"s_waitcnt vmcnt(16)\n\t"
"s_add_u32 s22, s94, 262144\n\t"
"ds_read_b128 v[220:223], v9 offset:11264\n\t"
"v_mfma_f32_16x16x32_bf16 v[128:131], a[104:107], v[180:183], v[128:131]\n\t"
"s_waitcnt vmcnt(15)\n\t"
"ds_read_b128 v[200:203], v9 offset:6144\n\t"
"v_mfma_f32_16x16x32_bf16 v[128:131], v[72:75], v[184:187], v[128:131]\n\t"
"s_waitcnt vmcnt(14)\n\t"
"ds_read_b128 v[204:207], v9 offset:7168\n\t"
"v_mfma_f32_16x16x32_bf16 v[128:131], a[160:163], v[188:191], v[128:131]\n\t"
"s_waitcnt vmcnt(13)\n\t"
"buffer_load_dwordx4 a[140:143], v232, s[56:59], s22 offen sc1\n\t"
"v_mfma_f32_16x16x32_bf16 v[128:131], a[164:167], v[192:195], v[128:131]\n\t"
"s_waitcnt vmcnt(13)\n\t"
"buffer_load_dwordx4 a[156:159], v232, s[56:59], s22 offen offset:1024 sc1\n\t"
"v_mfma_f32_16x16x32_bf16 v[128:131], a[168:171], v[196:199], v[128:131]\n\t"
"s_waitcnt lgkmcnt(3)\n\t"
"v_add_f32 v208, v208, v216\n\t"
"v_add_f32 v209, v209, v217\n\t"
"v_mfma_f32_16x16x32_bf16 v[132:135], a[12:15], v[168:171], 0\n\t"
"v_add_f32 v210, v210, v218\n\t"
"v_add_f32 v211, v211, v219\n\t"
"v_mfma_f32_16x16x32_bf16 v[132:135], a[44:47], v[172:175], v[132:135]\n\t"
"buffer_load_dwordx4 a[172:175], v233, s[56:59], s22 offen sc1\n\t"
"v_mfma_f32_16x16x32_bf16 v[132:135], a[76:79], v[176:179], v[132:135]\n\t"
"ds_write_b64 v6, v[128:129] offset:0\n\t"
"v_mfma_f32_16x16x32_bf16 v[132:135], a[108:111], v[180:183], v[132:135]\n\t"
"s_waitcnt lgkmcnt(3)\n\t"
"v_add_f32 v212, v212, v220\n\t"
"v_add_f32 v213, v213, v221\n\t"
"v_mfma_f32_16x16x32_bf16 v[132:135], v[76:79], v[184:187], v[132:135]\n\t"
"buffer_load_dwordx4 a[188:191], v233, s[56:59], s22 offen offset:1024 sc1\n\t"
"v_mfma_f32_16x16x32_bf16 v[132:135], a[176:179], v[188:191], v[132:135]\n\t"
"ds_write_b64 v6, v[130:131] offset:8\n\t"
"v_mfma_f32_16x16x32_bf16 v[132:135], a[180:183], v[192:195], v[132:135]\n\t"
"v_add_f32 v214, v214, v222\n\t"
"v_add_f32 v215, v215, v223\n\t"
"v_mfma_f32_16x16x32_bf16 v[132:135], a[184:187], v[196:199], v[132:135]\n\t"
"buffer_load_dwordx4 a[204:207], v234, s[56:59], s22 offen sc1\n\t"
"v_mfma_f32_16x16x32_bf16 v[136:139], a[16:19], v[168:171], 0\n\t"
"s_waitcnt lgkmcnt(3)\n\t"
"v_add_f32 v208, v208, v200\n\t"
"v_add_f32 v209, v209, v201\n\t"
"v_mfma_f32_16x16x32_bf16 v[136:139], a[48:51], v[172:175], v[136:139]\n\t"
"v_add_f32 v210, v210, v202\n\t"
"v_add_f32 v211, v211, v203\n\t"
"v_mfma_f32_16x16x32_bf16 v[136:139], a[80:83], v[176:179], v[136:139]\n\t"
"ds_write_b64 v6, v[132:133] offset:1024\n\t"
"v_mfma_f32_16x16x32_bf16 v[136:139], a[112:115], v[180:183], v[136:139]\n\t"
"buffer_load_dwordx4 a[220:223], v234, s[56:59], s22 offen offset:1024 sc1\n\t"
"v_mfma_f32_16x16x32_bf16 v[136:139], v[80:83], v[184:187], v[136:139]\n\t"
"s_waitcnt lgkmcnt(3)\n\t"
"v_add_f32 v212, v212, v204\n\t"
"v_add_f32 v213, v213, v205\n\t"
"v_mfma_f32_16x16x32_bf16 v[136:139], a[192:195], v[188:191], v[136:139]\n\t"
"ds_write_b64 v6, v[134:135] offset:1032\n\t"
"v_mfma_f32_16x16x32_bf16 v[136:139], a[196:199], v[192:195], v[136:139]\n\t"
"buffer_load_dwordx4 a[236:239], v235, s[56:59], s22 offen sc1\n\t"
"v_mfma_f32_16x16x32_bf16 v[136:139], a[200:203], v[196:199], v[136:139]\n\t"
"v_add_f32 v214, v214, v206\n\t"
"v_add_f32 v215, v215, v207\n\t"
"v_mfma_f32_16x16x32_bf16 v[140:143], a[20:23], v[168:171], 0\n\t"
"buffer_load_dwordx4 a[252:255], v235, s[56:59], s22 offen offset:1024 sc1\n\t"
"v_mfma_f32_16x16x32_bf16 v[140:143], a[52:55], v[172:175], v[140:143]\n\t"
"v_add_f32 v160, v160, v208\n\t"
"v_add_f32 v161, v161, v209\n\t"
"v_mfma_f32_16x16x32_bf16 v[140:143], a[84:87], v[176:179], v[140:143]\n\t"
"ds_write_b64 v7, v[136:137] offset:0\n\t"
"v_mfma_f32_16x16x32_bf16 v[140:143], a[116:119], v[180:183], v[140:143]\n\t"
"v_add_f32 v162, v162, v210\n\t"
"v_add_f32 v163, v163, v211\n\t"
"v_mfma_f32_16x16x32_bf16 v[140:143], v[84:87], v[184:187], v[140:143]\n\t"
"v_add_f32 v164, v164, v212\n\t"
"v_add_f32 v165, v165, v213\n\t"
"v_mfma_f32_16x16x32_bf16 v[140:143], a[208:211], v[188:191], v[140:143]\n\t"
"ds_write_b64 v7, v[138:139] offset:8\n\t"
"v_mfma_f32_16x16x32_bf16 v[140:143], a[212:215], v[192:195], v[140:143]\n\t"
"v_add_f32 v166, v166, v214\n\t"
"v_add_f32 v167, v167, v215\n\t"
"v_mfma_f32_16x16x32_bf16 v[140:143], a[216:219], v[196:199], v[140:143]\n\t"
"v_bfi_b32 v160, s27, v52, v160\n\t"
"v_bfi_b32 v161, s27, v53, v161\n\t"
"v_mfma_f32_16x16x32_bf16 v[144:147], a[24:27], v[168:171], 0\n\t"
"v_bfi_b32 v162, s27, v54, v162\n\t"
"v_bfi_b32 v163, s27, v55, v163\n\t"
"v_mfma_f32_16x16x32_bf16 v[144:147], a[56:59], v[172:175], v[144:147]\n\t"
"v_bfi_b32 v164, s27, v52, v164\n\t"
"v_bfi_b32 v165, s27, v53, v165\n\t"
"v_mfma_f32_16x16x32_bf16 v[144:147], a[88:91], v[176:179], v[144:147]\n\t"
"v_bfi_b32 v166, s27, v54, v166\n\t"
"v_bfi_b32 v167, s27, v55, v167\n\t"
"v_mfma_f32_16x16x32_bf16 v[144:147], a[120:123], v[180:183], v[144:147]\n\t"
"buffer_store_dwordx4 v[160:163], v48, s[56:59], s20 offen sc1\n\t"
"v_mfma_f32_16x16x32_bf16 v[144:147], v[88:91], v[184:187], v[144:147]\n\t"
"buffer_store_dwordx4 v[164:167], v48, s[56:59], s20 offen offset:1024 sc1\n\t"
"v_mfma_f32_16x16x32_bf16 v[144:147], a[224:227], v[188:191], v[144:147]\n\t"
"ds_write_b64 v7, v[140:141] offset:1024\n\t"
"v_mfma_f32_16x16x32_bf16 v[144:147], a[228:231], v[192:195], v[144:147]\n\t"
"v_mfma_f32_16x16x32_bf16 v[144:147], a[232:235], v[196:199], v[144:147]\n\t"
"v_mfma_f32_16x16x32_bf16 v[148:151], a[28:31], v[168:171], 0\n\t"
"ds_write_b64 v7, v[142:143] offset:1032\n\t"
"v_mfma_f32_16x16x32_bf16 v[148:151], a[60:63], v[172:175], v[148:151]\n\t"
"v_mfma_f32_16x16x32_bf16 v[148:151], a[92:95], v[176:179], v[148:151]\n\t"
"v_mfma_f32_16x16x32_bf16 v[148:151], a[124:127], v[180:183], v[148:151]\n\t"
"v_mfma_f32_16x16x32_bf16 v[148:151], v[92:95], v[184:187], v[148:151]\n\t"
"v_mfma_f32_16x16x32_bf16 v[148:151], a[240:243], v[188:191], v[148:151]\n\t"
"v_mfma_f32_16x16x32_bf16 v[148:151], a[244:247], v[192:195], v[148:151]\n\t"
"ds_write_b64 v8, v[144:145] offset:0\n\t"
"v_mfma_f32_16x16x32_bf16 v[148:151], a[248:251], v[196:199], v[148:151]\n\t"
"v_mfma_f32_16x16x32_bf16 v[152:155], a[0:3], v[168:171], 0\n\t"
"v_mfma_f32_16x16x32_bf16 v[152:155], a[32:35], v[172:175], v[152:155]\n\t"
"ds_write_b64 v8, v[146:147] offset:8\n\t"
"v_mfma_f32_16x16x32_bf16 v[152:155], a[64:67], v[176:179], v[152:155]\n\t"
"v_mfma_f32_16x16x32_bf16 v[152:155], a[96:99], v[180:183], v[152:155]\n\t"
"v_mfma_f32_16x16x32_bf16 v[152:155], v[64:67], v[184:187], v[152:155]\n\t"
"v_mfma_f32_16x16x32_bf16 v[152:155], a[128:131], v[188:191], v[152:155]\n\t"
"v_mfma_f32_16x16x32_bf16 v[152:155], a[132:135], v[192:195], v[152:155]\n\t"
"ds_write_b64 v8, v[148:149] offset:1024\n\t"
"v_mfma_f32_16x16x32_bf16 v[152:155], a[136:139], v[196:199], v[152:155]\n\t"
"v_mfma_f32_16x16x32_bf16 v[156:159], a[4:7], v[168:171], 0\n\t"
"v_mfma_f32_16x16x32_bf16 v[156:159], a[36:39], v[172:175], v[156:159]\n\t"
"ds_write_b64 v8, v[150:151] offset:1032\n\t"
"v_mfma_f32_16x16x32_bf16 v[156:159], a[68:71], v[176:179], v[156:159]\n\t"
"v_mfma_f32_16x16x32_bf16 v[156:159], a[100:103], v[180:183], v[156:159]\n\t"
"v_mfma_f32_16x16x32_bf16 v[156:159], v[68:71], v[184:187], v[156:159]\n\t"
"v_mfma_f32_16x16x32_bf16 v[156:159], a[144:147], v[188:191], v[156:159]\n\t"
"v_mfma_f32_16x16x32_bf16 v[156:159], a[148:151], v[192:195], v[156:159]\n\t"
"v_mfma_f32_16x16x32_bf16 v[156:159], a[152:155], v[196:199], v[156:159]\n\t"
"s_waitcnt lgkmcnt(0)\n\t"
"s_barrier\n\t"
"ds_read_b128 v[208:211], v9 offset:2048\n\t"
"s_waitcnt vmcnt(19)\n\t"
"v_mfma_f32_16x16x32_bf16 v[128:131], a[8:11], v[96:99], 0\n\t"
"s_waitcnt vmcnt(18)\n\t"
"s_lshr_b64 s[80:81], s[82:83], 24\n\t"
"s_and_b32 s80, s80, 15\n\t"
"ds_read_b128 v[212:215], v9 offset:3072\n\t"
"v_mfma_f32_16x16x32_bf16 v[128:131], a[40:43], v[100:103], v[128:131]\n\t"
"s_waitcnt vmcnt(17)\n\t"
"s_mul_i32 s80, s80, 32768\n\t"
"s_add_u32 s20, s80, s95\n\t"
"ds_read_b128 v[216:219], v9 offset:4096\n\t"
"v_mfma_f32_16x16x32_bf16 v[128:131], a[72:75], v[104:107], v[128:131]\n\t"
"s_waitcnt vmcnt(16)\n\t"
"s_add_u32 s22, s94, 294912\n\t"
"ds_read_b128 v[220:223], v9 offset:5120\n\t"
"v_mfma_f32_16x16x32_bf16 v[128:131], a[104:107], v[108:111], v[128:131]\n\t"
"s_waitcnt vmcnt(15)\n\t"
"ds_read_b128 v[200:203], v9 offset:0\n\t"
"v_mfma_f32_16x16x32_bf16 v[128:131], v[72:75], v[112:115], v[128:131]\n\t"
"s_waitcnt vmcnt(14)\n\t"
"ds_read_b128 v[204:207], v9 offset:1024\n\t"
"v_mfma_f32_16x16x32_bf16 v[128:131], a[160:163], v[116:119], v[128:131]\n\t"
"s_waitcnt vmcnt(13)\n\t"
"buffer_load_dwordx4 v[168:171], v232, s[56:59], s22 offen sc1\n\t"
"v_mfma_f32_16x16x32_bf16 v[128:131], a[164:167], v[120:123], v[128:131]\n\t"
"s_waitcnt vmcnt(13)\n\t"
"buffer_load_dwordx4 v[172:175], v232, s[56:59], s22 offen offset:1024 sc1\n\t"
"v_mfma_f32_16x16x32_bf16 v[128:131], a[168:171], v[124:127], v[128:131]\n\t"
"s_waitcnt lgkmcnt(3)\n\t"
"v_add_f32 v208, v208, v216\n\t"
"v_add_f32 v209, v209, v217\n\t"
"v_mfma_f32_16x16x32_bf16 v[132:135], a[12:15], v[96:99], 0\n\t"
"v_add_f32 v210, v210, v218\n\t"
"v_add_f32 v211, v211, v219\n\t"
"v_mfma_f32_16x16x32_bf16 v[132:135], a[44:47], v[100:103], v[132:135]\n\t"
"buffer_load_dwordx4 v[176:179], v233, s[56:59], s22 offen sc1\n\t"
"v_mfma_f32_16x16x32_bf16 v[132:135], a[76:79], v[104:107], v[132:135]\n\t"
"ds_write_b64 v6, v[128:129] offset:6144\n\t"
"v_mfma_f32_16x16x32_bf16 v[132:135], a[108:111], v[108:111], v[132:135]\n\t"
"s_waitcnt lgkmcnt(3)\n\t"
"v_add_f32 v212, v212, v220\n\t"
"v_add_f32 v213, v213, v221\n\t"
"v_mfma_f32_16x16x32_bf16 v[132:135], v[76:79], v[112:115], v[132:135]\n\t"
"buffer_load_dwordx4 v[180:183], v233, s[56:59], s22 offen offset:1024 sc1\n\t"
"v_mfma_f32_16x16x32_bf16 v[132:135], a[176:179], v[116:119], v[132:135]\n\t"
"ds_write_b64 v6, v[130:131] offset:6152\n\t"
"v_mfma_f32_16x16x32_bf16 v[132:135], a[180:183], v[120:123], v[132:135]\n\t"
"v_add_f32 v214, v214, v222\n\t"
"v_add_f32 v215, v215, v223\n\t"
"v_mfma_f32_16x16x32_bf16 v[132:135], a[184:187], v[124:127], v[132:135]\n\t"
"buffer_load_dwordx4 v[184:187], v234, s[56:59], s22 offen sc1\n\t"
"v_mfma_f32_16x16x32_bf16 v[136:139], a[16:19], v[96:99], 0\n\t"
"s_waitcnt lgkmcnt(3)\n\t"
"v_add_f32 v208, v208, v200\n\t"
"v_add_f32 v209, v209, v201\n\t"
"v_mfma_f32_16x16x32_bf16 v[136:139], a[48:51], v[100:103], v[136:139]\n\t"
"v_add_f32 v210, v210, v202\n\t"
"v_add_f32 v211, v211, v203\n\t"
"v_mfma_f32_16x16x32_bf16 v[136:139], a[80:83], v[104:107], v[136:139]\n\t"
"ds_write_b64 v6, v[132:133] offset:7168\n\t"
"v_mfma_f32_16x16x32_bf16 v[136:139], a[112:115], v[108:111], v[136:139]\n\t"
"buffer_load_dwordx4 v[188:191], v234, s[56:59], s22 offen offset:1024 sc1\n\t"
"v_mfma_f32_16x16x32_bf16 v[136:139], v[80:83], v[112:115], v[136:139]\n\t"
"s_waitcnt lgkmcnt(3)\n\t"
"s_lshr_b32 s80, s65, 1\n\t"
"s_add_u32 s80, s80, 8\n\t"
"v_add_f32 v212, v212, v204\n\t"
"v_mfma_f32_16x16x32_bf16 v[136:139], a[192:195], v[116:119], v[136:139]\n\t"
"s_lshl_b32 s80, s80, 15\n\t"
"s_add_u32 s14, s80, s95\n\t"
"ds_write_b64 v6, v[134:135] offset:7176\n\t"
"v_mfma_f32_16x16x32_bf16 v[136:139], a[196:199], v[120:123], v[136:139]\n\t"
"s_mul_i32 s80, s19, 8192\n\t"
"s_add_u32 s14, s14, s80\n\t"
"v_add_f32 v213, v213, v205\n\t"
"v_mfma_f32_16x16x32_bf16 v[136:139], a[200:203], v[124:127], v[136:139]\n\t"
"s_lshl_b32 s80, s71, 10\n\t"
"s_add_u32 s14, s14, s80\n\t"
"v_add_f32 v214, v214, v206\n\t"
"v_mfma_f32_16x16x32_bf16 v[140:143], a[20:23], v[96:99], 0\n\t"
"buffer_load_dwordx4 v[192:195], v235, s[56:59], s22 offen sc1\n\t"
"v_mfma_f32_16x16x32_bf16 v[140:143], a[52:55], v[100:103], v[140:143]\n\t"
"buffer_load_dwordx4 v[28:31], v51, s[56:59], s14 offen sc1\n\t"
"v_mfma_f32_16x16x32_bf16 v[140:143], a[84:87], v[104:107], v[140:143]\n\t"
"buffer_load_dwordx4 v[196:199], v235, s[56:59], s22 offen offset:1024 sc1\n\t"
"v_mfma_f32_16x16x32_bf16 v[140:143], a[116:119], v[108:111], v[140:143]\n\t"
"ds_write_b64 v7, v[136:137] offset:6144\n\t"
"v_mfma_f32_16x16x32_bf16 v[140:143], v[84:87], v[112:115], v[140:143]\n\t"
"buffer_load_dwordx4 v[24:27], v50, s[56:59], s14 offen sc1\n\t"
"v_mfma_f32_16x16x32_bf16 v[140:143], a[208:211], v[116:119], v[140:143]\n\t"
"buffer_load_dwordx4 v[20:23], v49, s[56:59], s14 offen sc1\n\t"
"v_mfma_f32_16x16x32_bf16 v[140:143], a[212:215], v[120:123], v[140:143]\n\t"
"v_add_f32 v215, v215, v207\n\t"
"v_add_f32 v152, v152, v208\n\t"
"v_add_f32 v153, v153, v209\n\t"
"v_add_f32 v154, v154, v210\n\t"
"v_add_f32 v155, v155, v211\n\t"
"v_add_f32 v156, v156, v212\n\t"
"v_add_f32 v157, v157, v213\n\t"
"v_add_f32 v158, v158, v214\n\t"
"v_add_f32 v159, v159, v215\n\t"
"v_mfma_f32_16x16x32_bf16 v[140:143], a[216:219], v[124:127], v[140:143]\n\t"
"ds_write_b64 v7, v[138:139] offset:6152\n\t"
"v_mfma_f32_16x16x32_bf16 v[144:147], a[24:27], v[96:99], 0\n\t"
"buffer_load_dwordx4 v[16:19], v48, s[56:59], s14 offen sc1\n\t"
"v_mfma_f32_16x16x32_bf16 v[144:147], a[56:59], v[100:103], v[144:147]\n\t"
"v_bfi_b32 v152, s27, v52, v152\n\t"
"v_bfi_b32 v153, s27, v53, v153\n\t"
"v_mfma_f32_16x16x32_bf16 v[144:147], a[88:91], v[104:107], v[144:147]\n\t"
"v_bfi_b32 v154, s27, v54, v154\n\t"
"v_bfi_b32 v155, s27, v55, v155\n\t"
"v_bfi_b32 v156, s27, v52, v156\n\t"
"v_bfi_b32 v157, s27, v53, v157\n\t"
"v_bfi_b32 v158, s27, v54, v158\n\t"
"v_bfi_b32 v159, s27, v55, v159\n\t"
"v_mfma_f32_16x16x32_bf16 v[144:147], a[120:123], v[108:111], v[144:147]\n\t"
"buffer_store_dwordx4 v[152:155], v48, s[56:59], s20 offen sc1\n\t"
"v_mfma_f32_16x16x32_bf16 v[144:147], v[88:91], v[112:115], v[144:147]\n\t"
"buffer_store_dwordx4 v[156:159], v48, s[56:59], s20 offen offset:1024 sc1\n\t"
"v_mfma_f32_16x16x32_bf16 v[144:147], a[224:227], v[116:119], v[144:147]\n\t"
"ds_write_b64 v7, v[140:141] offset:7168\n\t"
"v_mfma_f32_16x16x32_bf16 v[144:147], a[228:231], v[120:123], v[144:147]\n\t"
"v_mfma_f32_16x16x32_bf16 v[144:147], a[232:235], v[124:127], v[144:147]\n\t"
"v_mfma_f32_16x16x32_bf16 v[148:151], a[28:31], v[96:99], 0\n\t"
"ds_write_b64 v7, v[142:143] offset:7176\n\t"
"v_mfma_f32_16x16x32_bf16 v[148:151], a[60:63], v[100:103], v[148:151]\n\t"
"v_mfma_f32_16x16x32_bf16 v[148:151], a[92:95], v[104:107], v[148:151]\n\t"
"v_mfma_f32_16x16x32_bf16 v[148:151], a[124:127], v[108:111], v[148:151]\n\t"
"v_mfma_f32_16x16x32_bf16 v[148:151], v[92:95], v[112:115], v[148:151]\n\t"
"v_mfma_f32_16x16x32_bf16 v[148:151], a[240:243], v[116:119], v[148:151]\n\t"
"v_mfma_f32_16x16x32_bf16 v[148:151], a[244:247], v[120:123], v[148:151]\n\t"
"ds_write_b64 v8, v[144:145] offset:6144\n\t"
"v_mfma_f32_16x16x32_bf16 v[148:151], a[248:251], v[124:127], v[148:151]\n\t"
"v_mfma_f32_16x16x32_bf16 v[160:163], a[0:3], v[96:99], 0\n\t"
"v_mfma_f32_16x16x32_bf16 v[160:163], a[32:35], v[100:103], v[160:163]\n\t"
"ds_write_b64 v8, v[146:147] offset:6152\n\t"
"v_mfma_f32_16x16x32_bf16 v[160:163], a[64:67], v[104:107], v[160:163]\n\t"
"v_mfma_f32_16x16x32_bf16 v[160:163], a[96:99], v[108:111], v[160:163]\n\t"
"v_mfma_f32_16x16x32_bf16 v[160:163], v[64:67], v[112:115], v[160:163]\n\t"
"v_mfma_f32_16x16x32_bf16 v[160:163], a[128:131], v[116:119], v[160:163]\n\t"
"v_mfma_f32_16x16x32_bf16 v[160:163], a[132:135], v[120:123], v[160:163]\n\t"
"ds_write_b64 v8, v[148:149] offset:7168\n\t"
"v_mfma_f32_16x16x32_bf16 v[160:163], a[136:139], v[124:127], v[160:163]\n\t"
"v_mfma_f32_16x16x32_bf16 v[164:167], a[4:7], v[96:99], 0\n\t"
"v_mfma_f32_16x16x32_bf16 v[164:167], a[36:39], v[100:103], v[164:167]\n\t"
"ds_write_b64 v8, v[150:151] offset:7176\n\t"
"v_mfma_f32_16x16x32_bf16 v[164:167], a[68:71], v[104:107], v[164:167]\n\t"
"v_mfma_f32_16x16x32_bf16 v[164:167], a[100:103], v[108:111], v[164:167]\n\t"
"v_mfma_f32_16x16x32_bf16 v[164:167], v[68:71], v[112:115], v[164:167]\n\t"
"v_mfma_f32_16x16x32_bf16 v[164:167], a[144:147], v[116:119], v[164:167]\n\t"
"v_mfma_f32_16x16x32_bf16 v[164:167], a[148:151], v[120:123], v[164:167]\n\t"
"v_mfma_f32_16x16x32_bf16 v[164:167], a[152:155], v[124:127], v[164:167]\n\t"
"s_waitcnt lgkmcnt(0)\n\t"
"s_barrier\n\t"
"ds_read_b128 v[208:211], v9 offset:8192\n\t"
"s_waitcnt vmcnt(23)\n\t"
"v_mfma_f32_16x16x32_bf16 v[128:131], a[8:11], a[140:143], 0\n\t"
"s_waitcnt vmcnt(22)\n\t"
"s_lshr_b64 s[80:81], s[82:83], 28\n\t"
"s_and_b32 s80, s80, 15\n\t"
"ds_read_b128 v[212:215], v9 offset:9216\n\t"
"v_mfma_f32_16x16x32_bf16 v[128:131], a[40:43], a[156:159], v[128:131]\n\t"
"s_waitcnt vmcnt(21)\n\t"
"s_mul_i32 s80, s80, 32768\n\t"
"s_add_u32 s20, s80, s95\n\t"
"ds_read_b128 v[216:219], v9 offset:10240\n\t"
"v_mfma_f32_16x16x32_bf16 v[128:131], a[72:75], a[172:175], v[128:131]\n\t"
"s_waitcnt vmcnt(20)\n\t"
"s_lshr_b64 s[80:81], s[82:83], 32\n\t"
"s_and_b32 s80, s80, 15\n\t"
"ds_read_b128 v[220:223], v9 offset:11264\n\t"
"v_mfma_f32_16x16x32_bf16 v[128:131], a[104:107], a[188:191], v[128:131]\n\t"
"s_waitcnt vmcnt(19)\n\t"
"s_mul_i32 s80, s80, 32768\n\t"
"s_add_u32 s15, s80, s95\n\t"
"ds_read_b128 v[200:203], v9 offset:6144\n\t"
"v_mfma_f32_16x16x32_bf16 v[128:131], v[72:75], a[204:207], v[128:131]\n\t"
"s_waitcnt vmcnt(18)\n\t"
"ds_read_b128 v[204:207], v9 offset:7168\n\t"
"v_mfma_f32_16x16x32_bf16 v[128:131], a[160:163], a[220:223], v[128:131]\n\t"
"s_waitcnt vmcnt(17)\n\t"
"buffer_load_dwordx4 v[40:43], v51, s[56:59], s15 offen sc1\n\t"
"v_mfma_f32_16x16x32_bf16 v[128:131], a[164:167], a[236:239], v[128:131]\n\t"
"s_waitcnt vmcnt(17)\n\t"
"buffer_load_dwordx4 v[44:47], v51, s[56:59], s15 offen offset:1024 sc1\n\t"
"v_mfma_f32_16x16x32_bf16 v[128:131], a[168:171], a[252:255], v[128:131]\n\t"
"buffer_load_dwordx4 v[32:35], v50, s[56:59], s15 offen sc1\n\t"
"v_mfma_f32_16x16x32_bf16 v[132:135], a[12:15], a[140:143], 0\n\t"
"buffer_load_dwordx4 v[36:39], v50, s[56:59], s15 offen offset:1024 sc1\n\t"
"v_mfma_f32_16x16x32_bf16 v[132:135], a[44:47], a[156:159], v[132:135]\n\t"
"buffer_load_dwordx4 v[96:99], v49, s[56:59], s15 offen sc1\n\t"
"v_mfma_f32_16x16x32_bf16 v[132:135], a[76:79], a[172:175], v[132:135]\n\t"
"s_waitcnt lgkmcnt(3)\n\t"
"v_add_f32 v208, v208, v216\n\t"
"v_add_f32 v209, v209, v217\n\t"
"v_mfma_f32_16x16x32_bf16 v[132:135], a[108:111], a[188:191], v[132:135]\n\t"
"buffer_load_dwordx4 v[100:103], v49, s[56:59], s15 offen offset:1024 sc1\n\t"
"v_mfma_f32_16x16x32_bf16 v[132:135], v[76:79], a[204:207], v[132:135]\n\t"
"ds_write_b64 v6, v[128:129] offset:0\n\t"
"v_mfma_f32_16x16x32_bf16 v[132:135], a[176:179], a[220:223], v[132:135]\n\t"
"v_add_f32 v210, v210, v218\n\t"
"v_add_f32 v211, v211, v219\n\t"
"v_mfma_f32_16x16x32_bf16 v[132:135], a[180:183], a[236:239], v[132:135]\n\t"
"s_waitcnt lgkmcnt(3)\n\t"
"v_add_f32 v212, v212, v220\n\t"
"v_add_f32 v213, v213, v221\n\t"
"v_mfma_f32_16x16x32_bf16 v[132:135], a[184:187], a[252:255], v[132:135]\n\t"
"ds_write_b64 v6, v[130:131] offset:8\n\t"
"v_mfma_f32_16x16x32_bf16 v[136:139], a[16:19], a[140:143], 0\n\t"
"s_waitcnt lgkmcnt(2)\n\t"
"s_waitcnt lgkmcnt(3)\n\t"
"v_add_f32 v214, v214, v222\n\t"
"v_add_f32 v215, v215, v223\n\t"
"v_add_f32 v208, v208, v200\n\t"
"v_add_f32 v209, v209, v201\n\t"
"v_add_f32 v210, v210, v202\n\t"
"v_add_f32 v211, v211, v203\n\t"
"v_add_f32 v212, v212, v204\n\t"
"v_add_f32 v213, v213, v205\n\t"
"v_add_f32 v214, v214, v206\n\t"
"v_add_f32 v215, v215, v207\n\t"
"v_add_f32 v160, v160, v208\n\t"
"v_add_f32 v161, v161, v209\n\t"
"v_add_f32 v162, v162, v210\n\t"
"v_add_f32 v163, v163, v211\n\t"
"v_add_f32 v164, v164, v212\n\t"
"v_add_f32 v165, v165, v213\n\t"
"v_add_f32 v166, v166, v214\n\t"
"v_add_f32 v167, v167, v215\n\t"
"v_mfma_f32_16x16x32_bf16 v[136:139], a[48:51], a[156:159], v[136:139]\n\t"
"v_bfi_b32 v160, s27, v52, v160\n\t"
"v_bfi_b32 v161, s27, v53, v161\n\t"
"v_mfma_f32_16x16x32_bf16 v[136:139], a[80:83], a[172:175], v[136:139]\n\t"
"ds_write_b64 v6, v[132:133] offset:1024\n\t"
"v_mfma_f32_16x16x32_bf16 v[136:139], a[112:115], a[188:191], v[136:139]\n\t"
"v_bfi_b32 v162, s27, v54, v162\n\t"
"v_bfi_b32 v163, s27, v55, v163\n\t"
"v_mfma_f32_16x16x32_bf16 v[136:139], v[80:83], a[204:207], v[136:139]\n\t"
"v_bfi_b32 v164, s27, v52, v164\n\t"
"v_bfi_b32 v165, s27, v53, v165\n\t"
"v_bfi_b32 v166, s27, v54, v166\n\t"
"v_bfi_b32 v167, s27, v55, v167\n\t"
"v_mfma_f32_16x16x32_bf16 v[136:139], a[192:195], a[220:223], v[136:139]\n\t"
"buffer_store_dwordx4 v[160:163], v48, s[56:59], s20 offen sc1\n\t"
"v_mfma_f32_16x16x32_bf16 v[136:139], a[196:199], a[236:239], v[136:139]\n\t"
"buffer_store_dwordx4 v[164:167], v48, s[56:59], s20 offen offset:1024 sc1\n\t"
"v_mfma_f32_16x16x32_bf16 v[136:139], a[200:203], a[252:255], v[136:139]\n\t"
"ds_write_b64 v6, v[134:135] offset:1032\n\t"
"v_mfma_f32_16x16x32_bf16 v[140:143], a[20:23], a[140:143], 0\n\t"
"v_mfma_f32_16x16x32_bf16 v[140:143], a[52:55], a[156:159], v[140:143]\n\t"
"v_mfma_f32_16x16x32_bf16 v[140:143], a[84:87], a[172:175], v[140:143]\n\t"
"v_mfma_f32_16x16x32_bf16 v[140:143], a[116:119], a[188:191], v[140:143]\n\t"
"v_mfma_f32_16x16x32_bf16 v[140:143], v[84:87], a[204:207], v[140:143]\n\t"
"v_mfma_f32_16x16x32_bf16 v[140:143], a[208:211], a[220:223], v[140:143]\n\t"
"v_mfma_f32_16x16x32_bf16 v[140:143], a[212:215], a[236:239], v[140:143]\n\t"
"ds_write_b64 v7, v[136:137] offset:0\n\t"
"v_mfma_f32_16x16x32_bf16 v[140:143], a[216:219], a[252:255], v[140:143]\n\t"
"v_mfma_f32_16x16x32_bf16 v[144:147], a[24:27], a[140:143], 0\n\t"
"v_mfma_f32_16x16x32_bf16 v[144:147], a[56:59], a[156:159], v[144:147]\n\t"
"ds_write_b64 v7, v[138:139] offset:8\n\t"
"v_mfma_f32_16x16x32_bf16 v[144:147], a[88:91], a[172:175], v[144:147]\n\t"
"v_mfma_f32_16x16x32_bf16 v[144:147], a[120:123], a[188:191], v[144:147]\n\t"
"v_mfma_f32_16x16x32_bf16 v[144:147], v[88:91], a[204:207], v[144:147]\n\t"
"v_mfma_f32_16x16x32_bf16 v[144:147], a[224:227], a[220:223], v[144:147]\n\t"
"v_mfma_f32_16x16x32_bf16 v[144:147], a[228:231], a[236:239], v[144:147]\n\t"
"ds_write_b64 v7, v[140:141] offset:1024\n\t"
"v_mfma_f32_16x16x32_bf16 v[144:147], a[232:235], a[252:255], v[144:147]\n\t"
"v_mfma_f32_16x16x32_bf16 v[148:151], a[28:31], a[140:143], 0\n\t"
"v_mfma_f32_16x16x32_bf16 v[148:151], a[60:63], a[156:159], v[148:151]\n\t"
"ds_write_b64 v7, v[142:143] offset:1032\n\t"
"v_mfma_f32_16x16x32_bf16 v[148:151], a[92:95], a[172:175], v[148:151]\n\t"
"v_mfma_f32_16x16x32_bf16 v[148:151], a[124:127], a[188:191], v[148:151]\n\t"
"v_mfma_f32_16x16x32_bf16 v[148:151], v[92:95], a[204:207], v[148:151]\n\t"
"v_mfma_f32_16x16x32_bf16 v[148:151], a[240:243], a[220:223], v[148:151]\n\t"
"v_mfma_f32_16x16x32_bf16 v[148:151], a[244:247], a[236:239], v[148:151]\n\t"
"ds_write_b64 v8, v[144:145] offset:0\n\t"
"v_mfma_f32_16x16x32_bf16 v[148:151], a[248:251], a[252:255], v[148:151]\n\t"
"v_mfma_f32_16x16x32_bf16 v[152:155], a[0:3], a[140:143], 0\n\t"
"v_mfma_f32_16x16x32_bf16 v[152:155], a[32:35], a[156:159], v[152:155]\n\t"
"ds_write_b64 v8, v[146:147] offset:8\n\t"
"v_mfma_f32_16x16x32_bf16 v[152:155], a[64:67], a[172:175], v[152:155]\n\t"
"v_mfma_f32_16x16x32_bf16 v[152:155], a[96:99], a[188:191], v[152:155]\n\t"
"v_mfma_f32_16x16x32_bf16 v[152:155], v[64:67], a[204:207], v[152:155]\n\t"
"v_mfma_f32_16x16x32_bf16 v[152:155], a[128:131], a[220:223], v[152:155]\n\t"
"v_mfma_f32_16x16x32_bf16 v[152:155], a[132:135], a[236:239], v[152:155]\n\t"
"ds_write_b64 v8, v[148:149] offset:1024\n\t"
"v_mfma_f32_16x16x32_bf16 v[152:155], a[136:139], a[252:255], v[152:155]\n\t"
"v_mfma_f32_16x16x32_bf16 v[156:159], a[4:7], a[140:143], 0\n\t"
"v_mfma_f32_16x16x32_bf16 v[156:159], a[36:39], a[156:159], v[156:159]\n\t"
"s_lshr_b32 s80, s65, 1\n\t"
"s_add_u32 s80, s80, 8\n\t"
"ds_write_b64 v8, v[150:151] offset:1032\n\t"
"v_mfma_f32_16x16x32_bf16 v[156:159], a[68:71], a[172:175], v[156:159]\n\t"
"s_waitcnt vmcnt(14)\n\t"
"s_lshl_b32 s80, s80, 15\n\t"
"s_add_u32 s79, s80, s21\n\t"
"v_xor_b32 v28, v28, v52\n\t"
"v_mfma_f32_16x16x32_bf16 v[156:159], a[100:103], a[188:191], v[156:159]\n\t"
"s_mul_i32 s80, s19, 64\n\t"
"s_add_u32 s79, s79, s80\n\t"
"v_xor_b32 v29, v29, v53\n\t"
"v_mfma_f32_16x16x32_bf16 v[156:159], v[68:71], a[204:207], v[156:159]\n\t"
"s_lshl_b32 s80, s71, 5\n\t"
"s_add_u32 s79, s79, s80\n\t"
"v_xor_b32 v30, v30, v54\n\t"
"v_mfma_f32_16x16x32_bf16 v[156:159], a[144:147], a[220:223], v[156:159]\n\t"
"v_xor_b32 v31, v31, v55\n\t"
"v_or3_b32 v58, v28, v29, v30\n\t"
"v_mfma_f32_16x16x32_bf16 v[156:159], a[148:151], a[236:239], v[156:159]\n\t"
"s_waitcnt vmcnt(12)\n\t"
"v_or_b32 v58, v31, v58\n\t"
"v_xor_b32 v24, v24, v52\n\t"
"v_mfma_f32_16x16x32_bf16 v[156:159], a[152:155], a[252:255], v[156:159]\n\t"
"s_waitcnt lgkmcnt(0)\n\t"
"s_barrier\n\t"
"ds_read_b128 v[208:211], v9 offset:2048\n\t"
"v_xor_b32 v25, v25, v53\n\t"
"v_mfma_f32_16x16x32_bf16 v[128:131], a[8:11], v[168:171], 0\n\t"
"ds_read_b128 v[212:215], v9 offset:3072\n\t"
"v_xor_b32 v26, v26, v54\n\t"
"v_mfma_f32_16x16x32_bf16 v[128:131], a[40:43], v[172:175], v[128:131]\n\t"
"ds_read_b128 v[216:219], v9 offset:4096\n\t"
"v_xor_b32 v27, v27, v55\n\t"
"v_mfma_f32_16x16x32_bf16 v[128:131], a[72:75], v[176:179], v[128:131]\n\t"
"ds_read_b128 v[220:223], v9 offset:5120\n\t"
"v_or3_b32 v58, v24, v25, v58\n\t"
"v_mfma_f32_16x16x32_bf16 v[128:131], a[104:107], v[180:183], v[128:131]\n\t"
"ds_read_b128 v[200:203], v9 offset:0\n\t"
"v_or3_b32 v58, v26, v27, v58\n\t"
"v_mfma_f32_16x16x32_bf16 v[128:131], v[72:75], v[184:187], v[128:131]\n\t"
"s_waitcnt vmcnt(11)\n\t"
"ds_read_b128 v[204:207], v9 offset:1024\n\t"
"v_xor_b32 v20, v20, v52\n\t"
"v_mfma_f32_16x16x32_bf16 v[128:131], a[160:163], v[188:191], v[128:131]\n\t"
"v_xor_b32 v21, v21, v53\n\t"
"v_xor_b32 v22, v22, v54\n\t"
"v_mfma_f32_16x16x32_bf16 v[128:131], a[164:167], v[192:195], v[128:131]\n\t"
"v_xor_b32 v23, v23, v55\n\t"
"v_or3_b32 v58, v20, v21, v58\n\t"
"v_mfma_f32_16x16x32_bf16 v[128:131], a[168:171], v[196:199], v[128:131]\n\t"
"s_waitcnt vmcnt(10)\n\t"
"v_or3_b32 v58, v22, v23, v58\n\t"
"v_xor_b32 v16, v16, v52\n\t"
"v_mfma_f32_16x16x32_bf16 v[132:135], a[12:15], v[168:171], 0\n\t"
"v_xor_b32 v17, v17, v53\n\t"
"v_xor_b32 v18, v18, v54\n\t"
"v_mfma_f32_16x16x32_bf16 v[132:135], a[44:47], v[172:175], v[132:135]\n\t"
"v_xor_b32 v19, v19, v55\n\t"
"v_or3_b32 v58, v16, v17, v58\n\t"
"v_mfma_f32_16x16x32_bf16 v[132:135], a[76:79], v[176:179], v[132:135]\n\t"
"ds_write_b64 v6, v[128:129] offset:6144\n\t"
"v_mfma_f32_16x16x32_bf16 v[132:135], a[108:111], v[180:183], v[132:135]\n\t"
"s_waitcnt lgkmcnt(4)\n\t"
"v_or3_b32 v58, v18, v19, v58\n\t"
"v_add_f32 v208, v208, v216\n\t"
"v_mfma_f32_16x16x32_bf16 v[132:135], v[76:79], v[184:187], v[132:135]\n\t"
"v_and_b32 v58, s27, v58\n\t"
"v_cmp_ne_u32 vcc, 0, v58\n\t"
"s_nop 0\n\t"
"s_cmp_eq_u64 vcc, 0\n\t"
"s_cbranch_scc0 LTS14_%=\n\t"
"LTR14_%=:\n\t"
"v_mfma_f32_16x16x32_bf16 v[132:135], a[176:179], v[188:191], v[132:135]\n\t"
"ds_write_b64 v6, v[130:131] offset:6152\n\t"
"v_mfma_f32_16x16x32_bf16 v[132:135], a[180:183], v[192:195], v[132:135]\n\t"
"v_add_f32 v24, v24, v28\n\t"
"v_add_f32 v25, v25, v29\n\t"
"v_mfma_f32_16x16x32_bf16 v[132:135], a[184:187], v[196:199], v[132:135]\n\t"
"s_lshr_b64 s[80:81], s[82:83], 36\n\t"
"s_and_b32 s80, s80, 15\n\t"
"v_add_f32 v26, v26, v30\n\t"
"v_mfma_f32_16x16x32_bf16 v[136:139], a[16:19], v[168:171], 0\n\t"
"s_mul_i32 s80, s80, 32768\n\t"
"s_add_u32 s23, s80, s95\n\t"
"v_add_f32 v27, v27, v31\n\t"
"v_mfma_f32_16x16x32_bf16 v[136:139], a[48:51], v[172:175], v[136:139]\n\t"
"ds_write_b64 v6, v[132:133] offset:7168\n\t"
"v_mfma_f32_16x16x32_bf16 v[136:139], a[80:83], v[176:179], v[136:139]\n\t"
"v_add_f32 v24, v24, v20\n\t"
"v_add_f32 v25, v25, v21\n\t"
"v_mfma_f32_16x16x32_bf16 v[136:139], a[112:115], v[180:183], v[136:139]\n\t"
"v_add_f32 v26, v26, v22\n\t"
"v_add_f32 v27, v27, v23\n\t"
"v_add_f32 v16, v16, v24\n\t"
"v_add_f32 v17, v17, v25\n\t"
"v_add_f32 v18, v18, v26\n\t"
"v_add_f32 v19, v19, v27\n\t"
"v_cvt_pk_bf16_f32 v224, v16, v17\n\t"
"v_cvt_pk_bf16_f32 v225, v18, v19\n\t"
"buffer_store_dwordx2 v[224:225], v56, s[60:63], s79 offen\n\t"
"v_mfma_f32_16x16x32_bf16 v[136:139], v[80:83], v[184:187], v[136:139]\n\t"
"buffer_load_dwordx4 v[120:123], v51, s[56:59], s23 offen sc1\n\t"
"v_mfma_f32_16x16x32_bf16 v[136:139], a[192:195], v[188:191], v[136:139]\n\t"
"ds_write_b64 v6, v[134:135] offset:7176\n\t"
"v_mfma_f32_16x16x32_bf16 v[136:139], a[196:199], v[192:195], v[136:139]\n\t"
"buffer_load_dwordx4 v[124:127], v51, s[56:59], s23 offen offset:1024 sc1\n\t"
"v_mfma_f32_16x16x32_bf16 v[136:139], a[200:203], v[196:199], v[136:139]\n\t"
"buffer_load_dwordx4 v[112:115], v50, s[56:59], s23 offen sc1\n\t"
"v_mfma_f32_16x16x32_bf16 v[140:143], a[20:23], v[168:171], 0\n\t"
"v_add_f32 v209, v209, v217\n\t"
"v_add_f32 v210, v210, v218\n\t"
"v_mfma_f32_16x16x32_bf16 v[140:143], a[52:55], v[172:175], v[140:143]\n\t"
"s_lshr_b64 s[80:81], s[82:83], 32\n\t"
"s_and_b32 s80, s80, 15\n\t"
"v_add_f32 v211, v211, v219\n\t"
"v_mfma_f32_16x16x32_bf16 v[140:143], a[84:87], v[176:179], v[140:143]\n\t"
"s_mul_i32 s80, s80, 32768\n\t"
"s_add_u32 s79, s80, s21\n\t"
"ds_write_b64 v7, v[136:137] offset:6144\n\t"
"v_mfma_f32_16x16x32_bf16 v[140:143], a[116:119], v[180:183], v[140:143]\n\t"
"buffer_load_dwordx4 v[116:119], v50, s[56:59], s23 offen offset:1024 sc1\n\t"
"v_mfma_f32_16x16x32_bf16 v[140:143], v[84:87], v[184:187], v[140:143]\n\t"
"s_waitcnt lgkmcnt(7)\n\t"
"v_add_f32 v212, v212, v220\n\t"
"v_add_f32 v213, v213, v221\n\t"
"v_mfma_f32_16x16x32_bf16 v[140:143], a[208:211], v[188:191], v[140:143]\n\t"
"ds_write_b64 v7, v[138:139] offset:6152\n\t"
"v_mfma_f32_16x16x32_bf16 v[140:143], a[212:215], v[192:195], v[140:143]\n\t"
"s_waitcnt lgkmcnt(6)\n\t"
"s_waitcnt lgkmcnt(7)\n\t"
"v_add_f32 v214, v214, v222\n\t"
"v_add_f32 v215, v215, v223\n\t"
"v_add_f32 v208, v208, v200\n\t"
"v_add_f32 v209, v209, v201\n\t"
"v_add_f32 v210, v210, v202\n\t"
"v_add_f32 v211, v211, v203\n\t"
"v_add_f32 v212, v212, v204\n\t"
"v_add_f32 v213, v213, v205\n\t"
"v_add_f32 v214, v214, v206\n\t"
"v_add_f32 v215, v215, v207\n\t"
"v_add_f32 v152, v152, v208\n\t"
"v_add_f32 v153, v153, v209\n\t"
"v_add_f32 v154, v154, v210\n\t"
"v_add_f32 v155, v155, v211\n\t"
"v_add_f32 v156, v156, v212\n\t"
"v_add_f32 v157, v157, v213\n\t"
"v_add_f32 v158, v158, v214\n\t"
"v_add_f32 v159, v159, v215\n\t"
"v_mfma_f32_16x16x32_bf16 v[140:143], a[216:219], v[196:199], v[140:143]\n\t"
"s_waitcnt vmcnt(12)\n\t"
"v_xor_b32 v40, v40, v52\n\t"
"v_xor_b32 v41, v41, v53\n\t"
"v_mfma_f32_16x16x32_bf16 v[144:147], a[24:27], v[168:171], 0\n\t"
"v_xor_b32 v42, v42, v54\n\t"
"v_xor_b32 v43, v43, v55\n\t"
"v_mfma_f32_16x16x32_bf16 v[144:147], a[56:59], v[172:175], v[144:147]\n\t"
"v_or3_b32 v58, v40, v41, v42\n\t"
"v_or_b32 v58, v43, v58\n\t"
"v_mfma_f32_16x16x32_bf16 v[144:147], a[88:91], v[176:179], v[144:147]\n\t"
"ds_write_b64 v7, v[140:141] offset:7168\n\t"
"v_mfma_f32_16x16x32_bf16 v[144:147], a[120:123], v[180:183], v[144:147]\n\t"
"s_waitcnt vmcnt(11)\n\t"
"v_xor_b32 v44, v44, v52\n\t"
"v_xor_b32 v45, v45, v53\n\t"
"v_mfma_f32_16x16x32_bf16 v[144:147], v[88:91], v[184:187], v[144:147]\n\t"
"v_xor_b32 v46, v46, v54\n\t"
"v_xor_b32 v47, v47, v55\n\t"
"v_mfma_f32_16x16x32_bf16 v[144:147], a[224:227], v[188:191], v[144:147]\n\t"
"ds_write_b64 v7, v[142:143] offset:7176\n\t"
"v_mfma_f32_16x16x32_bf16 v[144:147], a[228:231], v[192:195], v[144:147]\n\t"
"v_or3_b32 v58, v44, v45, v58\n\t"
"v_or3_b32 v58, v46, v47, v58\n\t"
"v_mfma_f32_16x16x32_bf16 v[144:147], a[232:235], v[196:199], v[144:147]\n\t"
"s_waitcnt vmcnt(10)\n\t"
"v_xor_b32 v32, v32, v52\n\t"
"v_xor_b32 v33, v33, v53\n\t"
"v_mfma_f32_16x16x32_bf16 v[148:151], a[28:31], v[168:171], 0\n\t"
"v_xor_b32 v34, v34, v54\n\t"
"v_xor_b32 v35, v35, v55\n\t"
"v_mfma_f32_16x16x32_bf16 v[148:151], a[60:63], v[172:175], v[148:151]\n\t"
"v_or3_b32 v58, v32, v33, v58\n\t"
"v_or3_b32 v58, v34, v35, v58\n\t"
"v_mfma_f32_16x16x32_bf16 v[148:151], a[92:95], v[176:179], v[148:151]\n\t"
"ds_write_b64 v8, v[144:145] offset:6144\n\t"
"v_mfma_f32_16x16x32_bf16 v[148:151], a[124:127], v[180:183], v[148:151]\n\t"
"s_waitcnt vmcnt(9)\n\t"
"v_xor_b32 v36, v36, v52\n\t"
"v_xor_b32 v37, v37, v53\n\t"
"v_mfma_f32_16x16x32_bf16 v[148:151], v[92:95], v[184:187], v[148:151]\n\t"
"v_xor_b32 v38, v38, v54\n\t"
"v_xor_b32 v39, v39, v55\n\t"
"v_mfma_f32_16x16x32_bf16 v[148:151], a[240:243], v[188:191], v[148:151]\n\t"
"ds_write_b64 v8, v[146:147] offset:6152\n\t"
"v_mfma_f32_16x16x32_bf16 v[148:151], a[244:247], v[192:195], v[148:151]\n\t"
"v_or3_b32 v58, v36, v37, v58\n\t"
"v_or3_b32 v58, v38, v39, v58\n\t"
"v_mfma_f32_16x16x32_bf16 v[148:151], a[248:251], v[196:199], v[148:151]\n\t"
"s_waitcnt vmcnt(8)\n\t"
"v_xor_b32 v96, v96, v52\n\t"
"v_xor_b32 v97, v97, v53\n\t"
"v_mfma_f32_16x16x32_bf16 v[160:163], a[0:3], v[168:171], 0\n\t"
"v_xor_b32 v98, v98, v54\n\t"
"v_xor_b32 v99, v99, v55\n\t"
"v_mfma_f32_16x16x32_bf16 v[160:163], a[32:35], v[172:175], v[160:163]\n\t"
"v_or3_b32 v58, v96, v97, v58\n\t"
"v_or3_b32 v58, v98, v99, v58\n\t"
"v_mfma_f32_16x16x32_bf16 v[160:163], a[64:67], v[176:179], v[160:163]\n\t"
"ds_write_b64 v8, v[148:149] offset:7168\n\t"
"v_mfma_f32_16x16x32_bf16 v[160:163], a[96:99], v[180:183], v[160:163]\n\t"
"s_waitcnt vmcnt(7)\n\t"
"v_xor_b32 v100, v100, v52\n\t"
"v_xor_b32 v101, v101, v53\n\t"
"v_mfma_f32_16x16x32_bf16 v[160:163], v[64:67], v[184:187], v[160:163]\n\t"
"v_xor_b32 v102, v102, v54\n\t"
"v_xor_b32 v103, v103, v55\n\t"
"v_mfma_f32_16x16x32_bf16 v[160:163], a[128:131], v[188:191], v[160:163]\n\t"
"ds_write_b64 v8, v[150:151] offset:7176\n\t"
"v_mfma_f32_16x16x32_bf16 v[160:163], a[132:135], v[192:195], v[160:163]\n\t"
"v_or3_b32 v58, v100, v101, v58\n\t"
"v_or3_b32 v58, v102, v103, v58\n\t"
"v_mfma_f32_16x16x32_bf16 v[160:163], a[136:139], v[196:199], v[160:163]\n\t"
"v_and_b32 v58, s27, v58\n\t"
"v_cmp_ne_u32 vcc, 0, v58\n\t"
"s_nop 0\n\t"
"s_cmp_eq_u64 vcc, 0\n\t"
"s_cbranch_scc0 LTS15_%=\n\t"
"LTR15_%=:\n\t"
"v_add_f32 v32, v32, v40\n\t"
"v_add_f32 v33, v33, v41\n\t"
"v_add_f32 v34, v34, v42\n\t"
"v_add_f32 v35, v35, v43\n\t"
"v_add_f32 v36, v36, v44\n\t"
"v_add_f32 v37, v37, v45\n\t"
"v_add_f32 v38, v38, v46\n\t"
"v_add_f32 v39, v39, v47\n\t"
"v_add_f32 v32, v32, v96\n\t"
"v_add_f32 v33, v33, v97\n\t"
"v_add_f32 v34, v34, v98\n\t"
"v_add_f32 v35, v35, v99\n\t"
"v_add_f32 v36, v36, v100\n\t"
"v_add_f32 v37, v37, v101\n\t"
"v_add_f32 v38, v38, v102\n\t"
"v_add_f32 v39, v39, v103\n\t"
"v_mfma_f32_16x16x32_bf16 v[164:167], a[4:7], v[168:171], 0\n\t"
"v_add_f32 v152, v152, v32\n\t"
"v_add_f32 v153, v153, v33\n\t"
"v_mfma_f32_16x16x32_bf16 v[164:167], a[36:39], v[172:175], v[164:167]\n\t"
"buffer_load_dwordx4 v[104:107], v49, s[56:59], s23 offen sc1\n\t"
"v_mfma_f32_16x16x32_bf16 v[164:167], a[68:71], v[176:179], v[164:167]\n\t"
"v_add_f32 v154, v154, v34\n\t"
"v_add_f32 v155, v155, v35\n\t"
"v_mfma_f32_16x16x32_bf16 v[164:167], a[100:103], v[180:183], v[164:167]\n\t"
"buffer_load_dwordx4 v[108:111], v49, s[56:59], s23 offen offset:1024 sc1\n\t"
"v_mfma_f32_16x16x32_bf16 v[164:167], v[68:71], v[184:187], v[164:167]\n\t"
"v_add_f32 v156, v156, v36\n\t"
"v_add_f32 v157, v157, v37\n\t"
"v_mfma_f32_16x16x32_bf16 v[164:167], a[144:147], v[188:191], v[164:167]\n\t"
"v_add_f32 v158, v158, v38\n\t"
"v_add_f32 v159, v159, v39\n\t"
"v_mfma_f32_16x16x32_bf16 v[164:167], a[148:151], v[192:195], v[164:167]\n\t"
"v_cvt_pk_bf16_f32 v224, v152, v153\n\t"
"v_cvt_pk_bf16_f32 v225, v154, v155\n\t"
"v_mfma_f32_16x16x32_bf16 v[164:167], a[152:155], v[196:199], v[164:167]\n\t"
"s_waitcnt lgkmcnt(0)\n\t"
"s_barrier\n\t"
"ds_read_b128 v[208:211], v9 offset:8192\n\t"
"ds_read_b128 v[212:215], v9 offset:9216\n\t"
"ds_read_b128 v[216:219], v9 offset:10240\n\t"
"ds_read_b128 v[220:223], v9 offset:11264\n\t"
"ds_read_b128 v[200:203], v9 offset:6144\n\t"
"ds_read_b128 v[204:207], v9 offset:7168\n\t"
"v_cvt_pk_bf16_f32 v226, v156, v157\n\t"
"v_cvt_pk_bf16_f32 v227, v158, v159\n\t"
"buffer_store_dwordx2 v[224:225], v56, s[60:63], s79 offen\n\t"
"buffer_store_dwordx2 v[226:227], v56, s[60:63], s79 offen offset:32\n\t"
"s_waitcnt lgkmcnt(3)\n\t"
"v_add_f32 v208, v208, v216\n\t"
"v_add_f32 v209, v209, v217\n\t"
"v_add_f32 v210, v210, v218\n\t"
"v_add_f32 v211, v211, v219\n\t"
"s_waitcnt lgkmcnt(2)\n\t"
"v_add_f32 v212, v212, v220\n\t"
"v_add_f32 v213, v213, v221\n\t"
"v_add_f32 v214, v214, v222\n\t"
"v_add_f32 v215, v215, v223\n\t"
"s_waitcnt lgkmcnt(1)\n\t"
"v_add_f32 v208, v208, v200\n\t"
"v_add_f32 v209, v209, v201\n\t"
"v_add_f32 v210, v210, v202\n\t"
"v_add_f32 v211, v211, v203\n\t"
"s_waitcnt lgkmcnt(0)\n\t"
"v_add_f32 v212, v212, v204\n\t"
"v_add_f32 v213, v213, v205\n\t"
"v_add_f32 v214, v214, v206\n\t"
"v_add_f32 v215, v215, v207\n\t"
"v_add_f32 v160, v160, v208\n\t"
"v_add_f32 v161, v161, v209\n\t"
"v_add_f32 v162, v162, v210\n\t"
"v_add_f32 v163, v163, v211\n\t"
"v_add_f32 v164, v164, v212\n\t"
"v_add_f32 v165, v165, v213\n\t"
"v_add_f32 v166, v166, v214\n\t"
"v_add_f32 v167, v167, v215\n\t"
"s_or_b32 s78, s67, s65\n\t"
"s_or_b32 s78, s78, s69\n\t"
"s_cmp_eq_u32 s78, 0\n\t"
"s_cselect_b32 s78, 0, 1073741824\n\t"
"s_add_u32 s81, s97, 1\n\t"
"v_mov_b32 v254, s81\n\t"
"v_add_u32 v255, s78, v5\n\t"
"buffer_store_dword v254, v255, s[56:59], s99 offen sc1\n\t"
"s_waitcnt vmcnt(8)\n\t"
"v_xor_b32 v120, v120, v52\n\t"
"v_xor_b32 v121, v121, v53\n\t"
"v_xor_b32 v122, v122, v54\n\t"
"v_xor_b32 v123, v123, v55\n\t"
"v_or3_b32 v58, v120, v121, v122\n\t"
"v_or_b32 v58, v123, v58\n\t"
"s_waitcnt vmcnt(7)\n\t"
"v_xor_b32 v124, v124, v52\n\t"
"v_xor_b32 v125, v125, v53\n\t"
"v_xor_b32 v126, v126, v54\n\t"
"v_xor_b32 v127, v127, v55\n\t"
"v_or3_b32 v58, v124, v125, v58\n\t"
"v_or3_b32 v58, v126, v127, v58\n\t"
"s_waitcnt vmcnt(6)\n\t"
"v_xor_b32 v112, v112, v52\n\t"
"v_xor_b32 v113, v113, v53\n\t"
"v_xor_b32 v114, v114, v54\n\t"
"v_xor_b32 v115, v115, v55\n\t"
"v_or3_b32 v58, v112, v113, v58\n\t"
"v_or3_b32 v58, v114, v115, v58\n\t"
"s_waitcnt vmcnt(5)\n\t"
"v_xor_b32 v116, v116, v52\n\t"
"v_xor_b32 v117, v117, v53\n\t"
"v_xor_b32 v118, v118, v54\n\t"
"v_xor_b32 v119, v119, v55\n\t"
"v_or3_b32 v58, v116, v117, v58\n\t"
"v_or3_b32 v58, v118, v119, v58\n\t"
"v_and_b32 v58, s27, v58\n\t"
"v_cmp_ne_u32 vcc, 0, v58\n\t"
"s_nop 0\n\t"
"s_cmp_eq_u64 vcc, 0\n\t"
"s_cbranch_scc0 LTS16_%=\n\t"
"LTR16_%=:\n\t"
"v_add_f32 v112, v112, v120\n\t"
"v_add_f32 v113, v113, v121\n\t"
"v_add_f32 v114, v114, v122\n\t"
"v_add_f32 v115, v115, v123\n\t"
"v_add_f32 v116, v116, v124\n\t"
"v_add_f32 v117, v117, v125\n\t"
"v_add_f32 v118, v118, v126\n\t"
"v_add_f32 v119, v119, v127\n\t"
"s_waitcnt vmcnt(4)\n\t"
"v_xor_b32 v104, v104, v52\n\t"
"v_xor_b32 v105, v105, v53\n\t"
"v_xor_b32 v106, v106, v54\n\t"
"v_xor_b32 v107, v107, v55\n\t"
"v_or3_b32 v58, v104, v105, v106\n\t"
"v_or_b32 v58, v107, v58\n\t"
"s_waitcnt vmcnt(3)\n\t"
"v_xor_b32 v108, v108, v52\n\t"
"v_xor_b32 v109, v109, v53\n\t"
"v_xor_b32 v110, v110, v54\n\t"
"v_xor_b32 v111, v111, v55\n\t"
"v_or3_b32 v58, v108, v109, v58\n\t"
"v_or3_b32 v58, v110, v111, v58\n\t"
"v_and_b32 v58, s27, v58\n\t"
"v_cmp_ne_u32 vcc, 0, v58\n\t"
"s_nop 0\n\t"
"s_cmp_eq_u64 vcc, 0\n\t"
"s_cbranch_scc0 LTS17_%=\n\t"
"LTR17_%=:\n\t"
"v_add_f32 v112, v112, v104\n\t"
"v_add_f32 v113, v113, v105\n\t"
"v_add_f32 v114, v114, v106\n\t"
"v_add_f32 v115, v115, v107\n\t"
"v_add_f32 v116, v116, v108\n\t"
"v_add_f32 v117, v117, v109\n\t"
"v_add_f32 v118, v118, v110\n\t"
"v_add_f32 v119, v119, v111\n\t"
"v_add_f32 v160, v160, v112\n\t"
"v_add_f32 v161, v161, v113\n\t"
"v_add_f32 v162, v162, v114\n\t"
"v_add_f32 v163, v163, v115\n\t"
"v_add_f32 v164, v164, v116\n\t"
"v_add_f32 v165, v165, v117\n\t"
"v_add_f32 v166, v166, v118\n\t"
"v_add_f32 v167, v167, v119\n\t"
"v_cvt_pk_bf16_f32 v224, v160, v161\n\t"
"v_cvt_pk_bf16_f32 v225, v162, v163\n\t"
"v_cvt_pk_bf16_f32 v226, v164, v165\n\t"
"v_cvt_pk_bf16_f32 v227, v166, v167\n\t"
"s_lshr_b64 s[80:81], s[82:83], 36\n\t"
"s_and_b32 s80, s80, 15\n\t"
"s_mul_i32 s80, s80, 32768\n\t"
"s_add_u32 s79, s80, s21\n\t"
"buffer_store_dwordx2 v[224:225], v56, s[60:63], s79 offen\n\t"
"buffer_store_dwordx2 v[226:227], v56, s[60:63], s79 offen offset:32\n\t"
"s_waitcnt vmcnt(0) lgkmcnt(0)\n\t"
"s_branch LEND_%=\n\t"
"LCK0_%=:\n\t"
"s_mov_b32 s9, 0\n\t"
"s_mov_b32 s10, 0\n\t"
"s_branch LRESTART_%=\n\t"
"LCK1_%=:\n\t"
"s_mov_b32 s9, 0\n\t"
"s_mov_b32 s10, 1\n\t"
"s_branch LRESTART_%=\n\t"
"LCK2_%=:\n\t"
"s_mov_b32 s9, 0\n\t"
"s_mov_b32 s10, 2\n\t"
"s_branch LRESTART_%=\n\t"
"LCK3_%=:\n\t"
"s_mov_b32 s9, 0\n\t"
"s_mov_b32 s10, 3\n\t"
"s_branch LRESTART_%=\n\t"
"LCK4_%=:\n\t"
"s_mov_b32 s9, 2\n\t"
"s_mov_b32 s10, 4\n\t"
"s_branch LRESTART_%=\n\t"
"LCK5_%=:\n\t"
"s_mov_b32 s9, 2\n\t"
"s_mov_b32 s10, 5\n\t"
"s_branch LRESTART_%=\n\t"
"LCK6_%=:\n\t"
"s_mov_b32 s9, 4\n\t"
"s_mov_b32 s10, 6\n\t"
"s_branch LRESTART_%=\n\t"
"LCK7_%=:\n\t"
"s_mov_b32 s9, 4\n\t"
"s_mov_b32 s10, 7\n\t"
"s_branch LRESTART_%=\n\t"
"LPS8_%=:\n\t"
"s_mov_b32 s96, 100000\n\t"
"LPS8_%=x:\n\t"
"s_sleep 1\n\t"
"buffer_load_dwordx2 v[2:3], v15, s[56:59], s16 offen sc1\n\t"
"s_waitcnt vmcnt(0) lgkmcnt(0)\n\t"
"v_cmp_le_u64 vcc, s[90:91], v[2:3]\n\t"
"v_cmp_ge_u64 s[92:93], s[88:89], v[2:3]\n\t"
"s_nop 0\n\t"
"s_and_b64 vcc, vcc, s[92:93]\n\t"
"s_cmp_eq_u64 vcc, -1\n\t"
"s_cbranch_scc1 LPR8_%=\n\t"
"s_sub_u32 s96, s96, 1\n\t"
"s_cmp_lg_u32 s96, 0\n\t"
"s_cbranch_scc1 LPS8_%=x\n\t"
"s_branch LPR8_%=\n\t"
"LCK8_%=:\n\t"
"s_mov_b32 s9, 4\n\t"
"s_mov_b32 s10, 8\n\t"
"s_branch LRESTART_%=\n\t"
"LCK9_%=:\n\t"
"s_mov_b32 s9, 4\n\t"
"s_mov_b32 s10, 9\n\t"
"s_branch LRESTART_%=\n\t"
"LPS11_%=:\n\t"
"s_mov_b32 s96, 100000\n\t"
"LPS11_%=x:\n\t"
"s_sleep 1\n\t"
"buffer_load_dwordx2 v[2:3], v15, s[56:59], s16 offen sc1\n\t"
"s_waitcnt vmcnt(0) lgkmcnt(0)\n\t"
"v_cmp_le_u64 vcc, s[90:91], v[2:3]\n\t"
"v_cmp_ge_u64 s[92:93], s[88:89], v[2:3]\n\t"
"s_nop 0\n\t"
"s_and_b64 vcc, vcc, s[92:93]\n\t"
"s_cmp_eq_u64 vcc, -1\n\t"
"s_cbranch_scc1 LPR11_%=\n\t"
"s_sub_u32 s96, s96, 1\n\t"
"s_cmp_lg_u32 s96, 0\n\t"
"s_cbranch_scc1 LPS11_%=x\n\t"
"s_branch LPR11_%=\n\t"
"LPS12_%=:\n\t"
"s_mov_b32 s96, 100000\n\t"
"LPS12_%=x:\n\t"
"s_sleep 1\n\t"
"buffer_load_dwordx2 v[2:3], v15, s[56:59], s16 offen sc1\n\t"
"s_waitcnt vmcnt(0) lgkmcnt(0)\n\t"
"v_cmp_le_u64 vcc, s[90:91], v[2:3]\n\t"
"v_cmp_ge_u64 s[92:93], s[88:89], v[2:3]\n\t"
"s_nop 0\n\t"
"s_and_b64 vcc, vcc, s[92:93]\n\t"
"s_cmp_eq_u64 vcc, -1\n\t"
"s_cbranch_scc1 LPR12_%=\n\t"
"s_sub_u32 s96, s96, 1\n\t"
"s_cmp_lg_u32 s96, 0\n\t"
"s_cbranch_scc1 LPS12_%=x\n\t"
"s_branch LPR12_%=\n\t"
"LPS13_%=:\n\t"
"s_mov_b32 s96, 100000\n\t"
"LPS13_%=x:\n\t"
"s_sleep 1\n\t"
"buffer_load_dwordx2 v[2:3], v15, s[56:59], s16 offen sc1\n\t"
"s_waitcnt vmcnt(0) lgkmcnt(0)\n\t"
"v_cmp_le_u64 vcc, s[90:91], v[2:3]\n\t"
"v_cmp_ge_u64 s[92:93], s[88:89], v[2:3]\n\t"
"s_nop 0\n\t"
"s_and_b64 vcc, vcc, s[92:93]\n\t"
"s_cmp_eq_u64 vcc, -1\n\t"
"s_cbranch_scc1 LPR13_%=\n\t"
"s_sub_u32 s96, s96, 1\n\t"
"s_cmp_lg_u32 s96, 0\n\t"
"s_cbranch_scc1 LPS13_%=x\n\t"
"s_branch LPR13_%=\n\t"
"LTS14_%=:\n\t"
"s_mov_b32 s96, 100000\n\t"
"LTS14_%=x:\n\t"
"s_waitcnt vmcnt(0) lgkmcnt(0)\n\t"
"buffer_load_dwordx4 v[28:31], v51, s[56:59], s14 offen sc1\n\t"
"buffer_load_dwordx4 v[24:27], v50, s[56:59], s14 offen sc1\n\t"
"buffer_load_dwordx4 v[20:23], v49, s[56:59], s14 offen sc1\n\t"
"buffer_load_dwordx4 v[16:19], v48, s[56:59], s14 offen sc1\n\t"
"s_waitcnt vmcnt(3)\n\t"
"v_xor_b32 v28, v28, v52\n\t"
"v_xor_b32 v29, v29, v53\n\t"
"v_xor_b32 v30, v30, v54\n\t"
"v_xor_b32 v31, v31, v55\n\t"
"v_or3_b32 v58, v28, v29, v30\n\t"
"v_or_b32 v58, v31, v58\n\t"
"s_waitcnt vmcnt(2)\n\t"
"v_xor_b32 v24, v24, v52\n\t"
"v_xor_b32 v25, v25, v53\n\t"
"v_xor_b32 v26, v26, v54\n\t"
"v_xor_b32 v27, v27, v55\n\t"
"v_or3_b32 v58, v24, v25, v58\n\t"
"v_or3_b32 v58, v26, v27, v58\n\t"
"s_waitcnt vmcnt(1)\n\t"
"v_xor_b32 v20, v20, v52\n\t"
"v_xor_b32 v21, v21, v53\n\t"
"v_xor_b32 v22, v22, v54\n\t"
"v_xor_b32 v23, v23, v55\n\t"
"v_or3_b32 v58, v20, v21, v58\n\t"
"v_or3_b32 v58, v22, v23, v58\n\t"
"s_waitcnt vmcnt(0)\n\t"
"v_xor_b32 v16, v16, v52\n\t"
"v_xor_b32 v17, v17, v53\n\t"
"v_xor_b32 v18, v18, v54\n\t"
"v_xor_b32 v19, v19, v55\n\t"
"v_or3_b32 v58, v16, v17, v58\n\t"
"v_or3_b32 v58, v18, v19, v58\n\t"
"v_and_b32 v58, s27, v58\n\t"
"v_cmp_ne_u32 vcc, 0, v58\n\t"
"s_nop 0\n\t"
"s_cmp_eq_u64 vcc, 0\n\t"
"s_cbranch_scc1 LTR14_%=\n\t"
"s_sub_u32 s96, s96, 1\n\t"
"s_cmp_lg_u32 s96, 0\n\t"
"s_cbranch_scc1 LTS14_%=x\n\t"
"s_branch LTR14_%=\n\t"
"LTS15_%=:\n\t"
"s_mov_b32 s96, 100000\n\t"
"LTS15_%=x:\n\t"
"s_waitcnt vmcnt(0) lgkmcnt(0)\n\t"
"buffer_load_dwordx4 v[40:43], v51, s[56:59], s15 offen sc1\n\t"
"buffer_load_dwordx4 v[44:47], v51, s[56:59], s15 offen offset:1024 sc1\n\t"
"buffer_load_dwordx4 v[32:35], v50, s[56:59], s15 offen sc1\n\t"
"buffer_load_dwordx4 v[36:39], v50, s[56:59], s15 offen offset:1024 sc1\n\t"
"buffer_load_dwordx4 v[96:99], v49, s[56:59], s15 offen sc1\n\t"
"buffer_load_dwordx4 v[100:103], v49, s[56:59], s15 offen offset:1024 sc1\n\t"
"s_waitcnt vmcnt(5)\n\t"
"v_xor_b32 v40, v40, v52\n\t"
"v_xor_b32 v41, v41, v53\n\t"
"v_xor_b32 v42, v42, v54\n\t"
"v_xor_b32 v43, v43, v55\n\t"
"v_or3_b32 v58, v40, v41, v42\n\t"
"v_or_b32 v58, v43, v58\n\t"
"s_waitcnt vmcnt(4)\n\t"
"v_xor_b32 v44, v44, v52\n\t"
"v_xor_b32 v45, v45, v53\n\t"
"v_xor_b32 v46, v46, v54\n\t"
"v_xor_b32 v47, v47, v55\n\t"
"v_or3_b32 v58, v44, v45, v58\n\t"
"v_or3_b32 v58, v46, v47, v58\n\t"
"s_waitcnt vmcnt(3)\n\t"
"v_xor_b32 v32, v32, v52\n\t"
"v_xor_b32 v33, v33, v53\n\t"
"v_xor_b32 v34, v34, v54\n\t"
"v_xor_b32 v35, v35, v55\n\t"
"v_or3_b32 v58, v32, v33, v58\n\t"
"v_or3_b32 v58, v34, v35, v58\n\t"
"s_waitcnt vmcnt(2)\n\t"
"v_xor_b32 v36, v36, v52\n\t"
"v_xor_b32 v37, v37, v53\n\t"
"v_xor_b32 v38, v38, v54\n\t"
"v_xor_b32 v39, v39, v55\n\t"
"v_or3_b32 v58, v36, v37, v58\n\t"
"v_or3_b32 v58, v38, v39, v58\n\t"
"s_waitcnt vmcnt(1)\n\t"
"v_xor_b32 v96, v96, v52\n\t"
"v_xor_b32 v97, v97, v53\n\t"
"v_xor_b32 v98, v98, v54\n\t"
"v_xor_b32 v99, v99, v55\n\t"
"v_or3_b32 v58, v96, v97, v58\n\t"
"v_or3_b32 v58, v98, v99, v58\n\t"
"s_waitcnt vmcnt(0)\n\t"
"v_xor_b32 v100, v100, v52\n\t"
"v_xor_b32 v101, v101, v53\n\t"
"v_xor_b32 v102, v102, v54\n\t"
"v_xor_b32 v103, v103, v55\n\t"
"v_or3_b32 v58, v100, v101, v58\n\t"
"v_or3_b32 v58, v102, v103, v58\n\t"
"v_and_b32 v58, s27, v58\n\t"
"v_cmp_ne_u32 vcc, 0, v58\n\t"
"s_nop 0\n\t"
"s_cmp_eq_u64 vcc, 0\n\t"
"s_cbranch_scc1 LTR15_%=\n\t"
"s_sub_u32 s96, s96, 1\n\t"
"s_cmp_lg_u32 s96, 0\n\t"
"s_cbranch_scc1 LTS15_%=x\n\t"
"s_branch LTR15_%=\n\t"
"LTS16_%=:\n\t"
"s_mov_b32 s96, 100000\n\t"
"LTS16_%=x:\n\t"
"s_waitcnt vmcnt(0) lgkmcnt(0)\n\t"
"buffer_load_dwordx4 v[120:123], v51, s[56:59], s23 offen sc1\n\t"
"buffer_load_dwordx4 v[124:127], v51, s[56:59], s23 offen offset:1024 sc1\n\t"
"buffer_load_dwordx4 v[112:115], v50, s[56:59], s23 offen sc1\n\t"
"buffer_load_dwordx4 v[116:119], v50, s[56:59], s23 offen offset:1024 sc1\n\t"
"s_waitcnt vmcnt(3)\n\t"
"v_xor_b32 v120, v120, v52\n\t"
"v_xor_b32 v121, v121, v53\n\t"
"v_xor_b32 v122, v122, v54\n\t"
"v_xor_b32 v123, v123, v55\n\t"
"v_or3_b32 v58, v120, v121, v122\n\t"
"v_or_b32 v58, v123, v58\n\t"
"s_waitcnt vmcnt(2)\n\t"
"v_xor_b32 v124, v124, v52\n\t"
"v_xor_b32 v125, v125, v53\n\t"
"v_xor_b32 v126, v126, v54\n\t"
"v_xor_b32 v127, v127, v55\n\t"
"v_or3_b32 v58, v124, v125, v58\n\t"
"v_or3_b32 v58, v126, v127, v58\n\t"
"s_waitcnt vmcnt(1)\n\t"
"v_xor_b32 v112, v112, v52\n\t"
"v_xor_b32 v113, v113, v53\n\t"
"v_xor_b32 v114, v114, v54\n\t"
"v_xor_b32 v115, v115, v55\n\t"
"v_or3_b32 v58, v112, v113, v58\n\t"
"v_or3_b32 v58, v114, v115, v58\n\t"
"s_waitcnt vmcnt(0)\n\t"
"v_xor_b32 v116, v116, v52\n\t"
"v_xor_b32 v117, v117, v53\n\t"
"v_xor_b32 v118, v118, v54\n\t"
"v_xor_b32 v119, v119, v55\n\t"
"v_or3_b32 v58, v116, v117, v58\n\t"
"v_or3_b32 v58, v118, v119, v58\n\t"
"v_and_b32 v58, s27, v58\n\t"
"v_cmp_ne_u32 vcc, 0, v58\n\t"
"s_nop 0\n\t"
"s_cmp_eq_u64 vcc, 0\n\t"
"s_cbranch_scc1 LTR16_%=\n\t"
"s_sub_u32 s96, s96, 1\n\t"
"s_cmp_lg_u32 s96, 0\n\t"
"s_cbranch_scc1 LTS16_%=x\n\t"
"s_branch LTR16_%=\n\t"
"LTS17_%=:\n\t"
"s_mov_b32 s96, 100000\n\t"
"LTS17_%=x:\n\t"
"s_waitcnt vmcnt(0) lgkmcnt(0)\n\t"
"buffer_load_dwordx4 v[104:107], v49, s[56:59], s23 offen sc1\n\t"
"buffer_load_dwordx4 v[108:111], v49, s[56:59], s23 offen offset:1024 sc1\n\t"
"s_waitcnt vmcnt(1)\n\t"
"v_xor_b32 v104, v104, v52\n\t"
"v_xor_b32 v105, v105, v53\n\t"
"v_xor_b32 v106, v106, v54\n\t"
"v_xor_b32 v107, v107, v55\n\t"
"v_or3_b32 v58, v104, v105, v106\n\t"
"v_or_b32 v58, v107, v58\n\t"
"s_waitcnt vmcnt(0)\n\t"
"v_xor_b32 v108, v108, v52\n\t"
"v_xor_b32 v109, v109, v53\n\t"
"v_xor_b32 v110, v110, v54\n\t"
"v_xor_b32 v111, v111, v55\n\t"
"v_or3_b32 v58, v108, v109, v58\n\t"
"v_or3_b32 v58, v110, v111, v58\n\t"
"v_and_b32 v58, s27, v58\n\t"
"v_cmp_ne_u32 vcc, 0, v58\n\t"
"s_nop 0\n\t"
"s_cmp_eq_u64 vcc, 0\n\t"
"s_cbranch_scc1 LTR17_%=\n\t"
"s_sub_u32 s96, s96, 1\n\t"
"s_cmp_lg_u32 s96, 0\n\t"
"s_cbranch_scc1 LTS17_%=x\n\t"
"s_branch LTR17_%=\n\t"
"LSAFE_%=:\n\t"
"s_waitcnt vmcnt(0) lgkmcnt(0)\n\t"
"ds_read_b64_tr_b16 a[128:129], v236 offset:36864\n\t"
"ds_read_b64_tr_b16 a[192:193], v237 offset:36864\n\t"
"ds_read_b64_tr_b16 a[144:145], v238 offset:36864\n\t"
"ds_read_b64_tr_b16 a[208:209], v239 offset:36864\n\t"
"ds_read_b64_tr_b16 a[160:161], v240 offset:36864\n\t"
"ds_read_b64_tr_b16 a[224:225], v241 offset:36864\n\t"
"ds_read_b64_tr_b16 a[176:177], v242 offset:36864\n\t"
"ds_read_b64_tr_b16 a[240:241], v243 offset:36864\n\t"
"ds_read_b64_tr_b16 a[130:131], v236 offset:28672\n\t"
"ds_read_b64_tr_b16 a[194:195], v237 offset:28672\n\t"
"ds_read_b64_tr_b16 a[146:147], v238 offset:28672\n\t"
"ds_read_b64_tr_b16 a[210:211], v239 offset:28672\n\t"
"ds_read_b64_tr_b16 a[162:163], v240 offset:28672\n\t"
"ds_read_b64_tr_b16 a[226:227], v241 offset:28672\n\t"
"ds_read_b64_tr_b16 a[178:179], v242 offset:28672\n\t"
"ds_read_b64_tr_b16 a[242:243], v243 offset:28672\n\t"
"s_waitcnt vmcnt(0) lgkmcnt(0)\n\t"
"s_barrier\n\t"
"ds_read_b64_tr_b16 a[132:133], v236 offset:32768\n\t"
"ds_read_b64_tr_b16 a[196:197], v237 offset:32768\n\t"
"ds_read_b64_tr_b16 a[148:149], v238 offset:32768\n\t"
"ds_read_b64_tr_b16 a[212:213], v239 offset:32768\n\t"
"ds_read_b64_tr_b16 a[164:165], v240 offset:32768\n\t"
"ds_read_b64_tr_b16 a[228:229], v241 offset:32768\n\t"
"ds_read_b64_tr_b16 a[180:181], v242 offset:32768\n\t"
"ds_read_b64_tr_b16 a[244:245], v243 offset:32768\n\t"
"ds_read_b64_tr_b16 a[134:135], v236 offset:12288\n\t"
"ds_read_b64_tr_b16 a[198:199], v237 offset:12288\n\t"
"ds_read_b64_tr_b16 a[150:151], v238 offset:12288\n\t"
"ds_read_b64_tr_b16 a[214:215], v239 offset:12288\n\t"
"ds_read_b64_tr_b16 a[166:167], v240 offset:12288\n\t"
"ds_read_b64_tr_b16 a[230:231], v241 offset:12288\n\t"
"ds_read_b64_tr_b16 a[182:183], v242 offset:12288\n\t"
"ds_read_b64_tr_b16 a[246:247], v243 offset:12288\n\t"
"ds_read_b64_tr_b16 a[136:137], v236 offset:16384\n\t"
"ds_read_b64_tr_b16 a[200:201], v237 offset:16384\n\t"
"ds_read_b64_tr_b16 a[152:153], v238 offset:16384\n\t"
"ds_read_b64_tr_b16 a[216:217], v239 offset:16384\n\t"
"ds_read_b64_tr_b16 a[168:169], v240 offset:16384\n\t"
"ds_read_b64_tr_b16 a[232:233], v241 offset:16384\n\t"
"ds_read_b64_tr_b16 a[184:185], v242 offset:16384\n\t"
"ds_read_b64_tr_b16 a[248:249], v243 offset:16384\n\t"
"ds_read_b64_tr_b16 a[138:139], v236 offset:24576\n\t"
"ds_read_b64_tr_b16 a[202:203], v237 offset:24576\n\t"
"ds_read_b64_tr_b16 a[154:155], v238 offset:24576\n\t"
"ds_read_b64_tr_b16 a[218:219], v239 offset:24576\n\t"
"ds_read_b64_tr_b16 a[170:171], v240 offset:24576\n\t"
"ds_read_b64_tr_b16 a[234:235], v241 offset:24576\n\t"
"ds_read_b64_tr_b16 a[186:187], v242 offset:24576\n\t"
"ds_read_b64_tr_b16 a[250:251], v243 offset:24576\n\t"
"s_waitcnt vmcnt(0) lgkmcnt(0)\n\t"
"s_barrier\n\t"
"s_add_u32 m0, s72, 20480\n\t"
"s_nop 0\n\t"
"buffer_load_dwordx4 v244, s[48:51], s76 offen nt lds\n\t"
"buffer_load_dwordx4 v245, s[48:51], s76 offen offset:1024 nt lds\n\t"
"buffer_load_dwordx4 v14, s[48:51], s76 offen offset:2048 nt lds\n\t"
"buffer_load_dwordx4 v251, s[48:51], s76 offen offset:3072 nt lds\n\t"
"s_add_u32 s76, s76, 4096\n\t"
"s_and_b32 s76, s76, 32767\n\t"
"s_add_u32 m0, s72, 36864\n\t"
"s_nop 0\n\t"
"buffer_load_dwordx4 v244, s[48:51], s76 offen nt lds\n\t"
"buffer_load_dwordx4 v245, s[48:51], s76 offen offset:1024 nt lds\n\t"
"buffer_load_dwordx4 v14, s[48:51], s76 offen offset:2048 nt lds\n\t"
"buffer_load_dwordx4 v251, s[48:51], s76 offen offset:3072 nt lds\n\t"
"s_add_u32 s76, s76, 4096\n\t"
"s_and_b32 s76, s76, 32767\n\t"
"s_waitcnt vmcnt(0) lgkmcnt(0)\n\t"
"s_barrier\n\t"
"ds_read_b64_tr_b16 a[140:141], v236 offset:20480\n\t"
"ds_read_b64_tr_b16 a[204:205], v237 offset:20480\n\t"
"ds_read_b64_tr_b16 a[156:157], v238 offset:20480\n\t"
"ds_read_b64_tr_b16 a[220:221], v239 offset:20480\n\t"
"ds_read_b64_tr_b16 a[172:173], v240 offset:20480\n\t"
"ds_read_b64_tr_b16 a[236:237], v241 offset:20480\n\t"
"ds_read_b64_tr_b16 a[188:189], v242 offset:20480\n\t"
"ds_read_b64_tr_b16 a[252:253], v243 offset:20480\n\t"
"ds_read_b64_tr_b16 a[142:143], v236 offset:36864\n\t"
"ds_read_b64_tr_b16 a[206:207], v237 offset:36864\n\t"
"ds_read_b64_tr_b16 a[158:159], v238 offset:36864\n\t"
"ds_read_b64_tr_b16 a[222:223], v239 offset:36864\n\t"
"ds_read_b64_tr_b16 a[174:175], v240 offset:36864\n\t"
"ds_read_b64_tr_b16 a[238:239], v241 offset:36864\n\t"
"ds_read_b64_tr_b16 a[190:191], v242 offset:36864\n\t"
"ds_read_b64_tr_b16 a[254:255], v243 offset:36864\n\t"
"s_waitcnt vmcnt(0) lgkmcnt(0)\n\t"
"s_barrier\n\t"
"v_mov_b32 v88, v216\n\t"
"v_mov_b32 v89, v217\n\t"
"v_mov_b32 v90, v218\n\t"
"v_mov_b32 v91, v219\n\t"
"v_mov_b32 v216, v88\n\t"
"v_mov_b32 v217, v89\n\t"
"v_mov_b32 v218, v90\n\t"
"v_mov_b32 v219, v91\n\t"
"v_add_u32 v92, 81920, v248\n\t"
"v_add_u32 v93, 81920, v249\n\t"
"ds_read_b128 v[168:171], v246 offset:0\n\t"
"ds_read_b128 v[172:175], v246 offset:40960\n\t"
"ds_read_b128 v[176:179], v247 offset:0\n\t"
"ds_read_b128 v[180:183], v247 offset:40960\n\t"
"s_waitcnt lgkmcnt(3)\n\t"
"v_mfma_f32_16x16x32_bf16 v[200:203], a[0:3], v[168:171], 0\n\t"
"s_waitcnt lgkmcnt(2)\n\t"
"v_mfma_f32_16x16x32_bf16 v[200:203], a[4:7], v[172:175], v[200:203]\n\t"
"s_waitcnt lgkmcnt(1)\n\t"
"v_mfma_f32_16x16x32_bf16 v[200:203], a[8:11], v[176:179], v[200:203]\n\t"
"s_waitcnt lgkmcnt(0)\n\t"
"v_mfma_f32_16x16x32_bf16 v[200:203], a[12:15], v[180:183], v[200:203]\n\t"
"s_nop 7\n\t"
"v_max_f32 v88, v200, v201\n\t"
"v_max3_f32 v88, v202, v203, v88\n\t"
"v_mfma_f32_16x16x32_bf16 v[204:207], a[16:19], v[168:171], 0\n\t"
"v_mfma_f32_16x16x32_bf16 v[204:207], a[20:23], v[172:175], v[204:207]\n\t"
"v_mfma_f32_16x16x32_bf16 v[204:207], a[24:27], v[176:179], v[204:207]\n\t"
"v_mfma_f32_16x16x32_bf16 v[204:207], a[28:31], v[180:183], v[204:207]\n\t"
"s_nop 7\n\t"
"v_max3_f32 v88, v204, v205, v88\n\t"
"v_max3_f32 v88, v206, v207, v88\n\t"
"v_mfma_f32_16x16x32_bf16 v[208:211], a[32:35], v[168:171], 0\n\t"
"v_mfma_f32_16x16x32_bf16 v[208:211], a[36:39], v[172:175], v[208:211]\n\t"
"v_mfma_f32_16x16x32_bf16 v[208:211], a[40:43], v[176:179], v[208:211]\n\t"
"v_mfma_f32_16x16x32_bf16 v[208:211], a[44:47], v[180:183], v[208:211]\n\t"
"s_nop 7\n\t"
"v_max3_f32 v88, v208, v209, v88\n\t"
"v_max3_f32 v88, v210, v211, v88\n\t"
"v_mfma_f32_16x16x32_bf16 v[200:203], a[48:51], v[168:171], 0\n\t"
"v_mfma_f32_16x16x32_bf16 v[200:203], a[52:55], v[172:175], v[200:203]\n\t"
"v_mfma_f32_16x16x32_bf16 v[200:203], a[56:59], v[176:179], v[200:203]\n\t"
"v_mfma_f32_16x16x32_bf16 v[200:203], a[60:63], v[180:183], v[200:203]\n\t"
"s_nop 7\n\t"
"v_max3_f32 v88, v200, v201, v88\n\t"
"v_max3_f32 v88, v202, v203, v88\n\t"
"v_mfma_f32_16x16x32_bf16 v[204:207], a[64:67], v[168:171], 0\n\t"
"v_mfma_f32_16x16x32_bf16 v[204:207], a[68:71], v[172:175], v[204:207]\n\t"
"v_mfma_f32_16x16x32_bf16 v[204:207], a[72:75], v[176:179], v[204:207]\n\t"
"v_mfma_f32_16x16x32_bf16 v[204:207], a[76:79], v[180:183], v[204:207]\n\t"
"s_nop 7\n\t"
"v_max3_f32 v88, v204, v205, v88\n\t"
"v_max3_f32 v88, v206, v207, v88\n\t"
"v_mfma_f32_16x16x32_bf16 v[208:211], a[80:83], v[168:171], 0\n\t"
"v_mfma_f32_16x16x32_bf16 v[208:211], a[84:87], v[172:175], v[208:211]\n\t"
"v_mfma_f32_16x16x32_bf16 v[208:211], a[88:91], v[176:179], v[208:211]\n\t"
"v_mfma_f32_16x16x32_bf16 v[208:211], a[92:95], v[180:183], v[208:211]\n\t"
"s_nop 7\n\t"
"v_max3_f32 v88, v208, v209, v88\n\t"
"v_max3_f32 v88, v210, v211, v88\n\t"
"v_mfma_f32_16x16x32_bf16 v[200:203], a[96:99], v[168:171], 0\n\t"
"v_mfma_f32_16x16x32_bf16 v[200:203], a[100:103], v[172:175], v[200:203]\n\t"
"v_mfma_f32_16x16x32_bf16 v[200:203], a[104:107], v[176:179], v[200:203]\n\t"
"v_mfma_f32_16x16x32_bf16 v[200:203], a[108:111], v[180:183], v[200:203]\n\t"
"s_nop 7\n\t"
"v_max3_f32 v88, v200, v201, v88\n\t"
"v_max3_f32 v88, v202, v203, v88\n\t"
"v_mfma_f32_16x16x32_bf16 v[204:207], a[112:115], v[168:171], 0\n\t"
"v_mfma_f32_16x16x32_bf16 v[204:207], a[116:119], v[172:175], v[204:207]\n\t"
"v_mfma_f32_16x16x32_bf16 v[204:207], a[120:123], v[176:179], v[204:207]\n\t"
"v_mfma_f32_16x16x32_bf16 v[204:207], a[124:127], v[180:183], v[204:207]\n\t"
"s_nop 7\n\t"
"v_max3_f32 v88, v204, v205, v88\n\t"
"v_max3_f32 v88, v206, v207, v88\n\t"
"v_mov_b32 v89, v88\n\t"
"s_nop 1\n\t"
"v_permlane32_swap_b32 v88, v89\n\t"
"s_nop 1\n\t"
"v_max_f32 v88, v88, v89\n\t"
"v_mov_b32 v89, v88\n\t"
"s_nop 1\n\t"
"v_permlane16_swap_b32 v88, v89\n\t"
"s_nop 1\n\t"
"v_max_f32 v88, v88, v89\n\t"
"ds_write_b32 v92, v88 offset:0\n\t"
"ds_read_b128 v[184:187], v246 offset:1024\n\t"
"ds_read_b128 v[188:191], v246 offset:41984\n\t"
"ds_read_b128 v[192:195], v247 offset:1024\n\t"
"ds_read_b128 v[196:199], v247 offset:41984\n\t"
"s_waitcnt lgkmcnt(3)\n\t"
"v_mfma_f32_16x16x32_bf16 v[200:203], a[0:3], v[184:187], 0\n\t"
"s_waitcnt lgkmcnt(2)\n\t"
"v_mfma_f32_16x16x32_bf16 v[200:203], a[4:7], v[188:191], v[200:203]\n\t"
"s_waitcnt lgkmcnt(1)\n\t"
"v_mfma_f32_16x16x32_bf16 v[200:203], a[8:11], v[192:195], v[200:203]\n\t"
"s_waitcnt lgkmcnt(0)\n\t"
"v_mfma_f32_16x16x32_bf16 v[200:203], a[12:15], v[196:199], v[200:203]\n\t"
"s_nop 7\n\t"
"v_max_f32 v88, v200, v201\n\t"
"v_max3_f32 v88, v202, v203, v88\n\t"
"v_mfma_f32_16x16x32_bf16 v[204:207], a[16:19], v[184:187], 0\n\t"
"v_mfma_f32_16x16x32_bf16 v[204:207], a[20:23], v[188:191], v[204:207]\n\t"
"v_mfma_f32_16x16x32_bf16 v[204:207], a[24:27], v[192:195], v[204:207]\n\t"
"v_mfma_f32_16x16x32_bf16 v[204:207], a[28:31], v[196:199], v[204:207]\n\t"
"s_nop 7\n\t"
"v_max3_f32 v88, v204, v205, v88\n\t"
"v_max3_f32 v88, v206, v207, v88\n\t"
"v_mfma_f32_16x16x32_bf16 v[208:211], a[32:35], v[184:187], 0\n\t"
"v_mfma_f32_16x16x32_bf16 v[208:211], a[36:39], v[188:191], v[208:211]\n\t"
"v_mfma_f32_16x16x32_bf16 v[208:211], a[40:43], v[192:195], v[208:211]\n\t"
"v_mfma_f32_16x16x32_bf16 v[208:211], a[44:47], v[196:199], v[208:211]\n\t"
"s_nop 7\n\t"
"v_max3_f32 v88, v208, v209, v88\n\t"
"v_max3_f32 v88, v210, v211, v88\n\t"
"v_mfma_f32_16x16x32_bf16 v[200:203], a[48:51], v[184:187], 0\n\t"
"v_mfma_f32_16x16x32_bf16 v[200:203], a[52:55], v[188:191], v[200:203]\n\t"
"v_mfma_f32_16x16x32_bf16 v[200:203], a[56:59], v[192:195], v[200:203]\n\t"
"v_mfma_f32_16x16x32_bf16 v[200:203], a[60:63], v[196:199], v[200:203]\n\t"
"s_nop 7\n\t"
"v_max3_f32 v88, v200, v201, v88\n\t"
"v_max3_f32 v88, v202, v203, v88\n\t"
"v_mfma_f32_16x16x32_bf16 v[204:207], a[64:67], v[184:187], 0\n\t"
"v_mfma_f32_16x16x32_bf16 v[204:207], a[68:71], v[188:191], v[204:207]\n\t"
"v_mfma_f32_16x16x32_bf16 v[204:207], a[72:75], v[192:195], v[204:207]\n\t"
"v_mfma_f32_16x16x32_bf16 v[204:207], a[76:79], v[196:199], v[204:207]\n\t"
"s_nop 7\n\t"
"v_max3_f32 v88, v204, v205, v88\n\t"
"v_max3_f32 v88, v206, v207, v88\n\t"
"v_mfma_f32_16x16x32_bf16 v[208:211], a[80:83], v[184:187], 0\n\t"
"v_mfma_f32_16x16x32_bf16 v[208:211], a[84:87], v[188:191], v[208:211]\n\t"
"v_mfma_f32_16x16x32_bf16 v[208:211], a[88:91], v[192:195], v[208:211]\n\t"
"v_mfma_f32_16x16x32_bf16 v[208:211], a[92:95], v[196:199], v[208:211]\n\t"
"s_nop 7\n\t"
"v_max3_f32 v88, v208, v209, v88\n\t"
"v_max3_f32 v88, v210, v211, v88\n\t"
"v_mfma_f32_16x16x32_bf16 v[200:203], a[96:99], v[184:187], 0\n\t"
"v_mfma_f32_16x16x32_bf16 v[200:203], a[100:103], v[188:191], v[200:203]\n\t"
"v_mfma_f32_16x16x32_bf16 v[200:203], a[104:107], v[192:195], v[200:203]\n\t"
"v_mfma_f32_16x16x32_bf16 v[200:203], a[108:111], v[196:199], v[200:203]\n\t"
"s_nop 7\n\t"
"v_max3_f32 v88, v200, v201, v88\n\t"
"v_max3_f32 v88, v202, v203, v88\n\t"
"v_mfma_f32_16x16x32_bf16 v[204:207], a[112:115], v[184:187], 0\n\t"
"v_mfma_f32_16x16x32_bf16 v[204:207], a[116:119], v[188:191], v[204:207]\n\t"
"v_mfma_f32_16x16x32_bf16 v[204:207], a[120:123], v[192:195], v[204:207]\n\t"
"v_mfma_f32_16x16x32_bf16 v[204:207], a[124:127], v[196:199], v[204:207]\n\t"
"s_nop 7\n\t"
"v_max3_f32 v88, v204, v205, v88\n\t"
"v_max3_f32 v88, v206, v207, v88\n\t"
"v_mov_b32 v89, v88\n\t"
"s_nop 1\n\t"
"v_permlane32_swap_b32 v88, v89\n\t"
"s_nop 1\n\t"
"v_max_f32 v88, v88, v89\n\t"
"v_mov_b32 v89, v88\n\t"
"s_nop 1\n\t"
"v_permlane16_swap_b32 v88, v89\n\t"
"s_nop 1\n\t"
"v_max_f32 v88, v88, v89\n\t"
"ds_write_b32 v92, v88 offset:256\n\t"
"ds_read_b128 v[168:171], v246 offset:2048\n\t"
"ds_read_b128 v[172:175], v246 offset:43008\n\t"
"ds_read_b128 v[176:179], v247 offset:2048\n\t"
"ds_read_b128 v[180:183], v247 offset:43008\n\t"
"s_waitcnt lgkmcnt(3)\n\t"
"v_mfma_f32_16x16x32_bf16 v[200:203], a[0:3], v[168:171], 0\n\t"
"s_waitcnt lgkmcnt(2)\n\t"
"v_mfma_f32_16x16x32_bf16 v[200:203], a[4:7], v[172:175], v[200:203]\n\t"
"s_waitcnt lgkmcnt(1)\n\t"
"v_mfma_f32_16x16x32_bf16 v[200:203], a[8:11], v[176:179], v[200:203]\n\t"
"s_waitcnt lgkmcnt(0)\n\t"
"v_mfma_f32_16x16x32_bf16 v[200:203], a[12:15], v[180:183], v[200:203]\n\t"
"s_nop 7\n\t"
"v_max_f32 v88, v200, v201\n\t"
"v_max3_f32 v88, v202, v203, v88\n\t"
"v_mfma_f32_16x16x32_bf16 v[204:207], a[16:19], v[168:171], 0\n\t"
"v_mfma_f32_16x16x32_bf16 v[204:207], a[20:23], v[172:175], v[204:207]\n\t"
"v_mfma_f32_16x16x32_bf16 v[204:207], a[24:27], v[176:179], v[204:207]\n\t"
"v_mfma_f32_16x16x32_bf16 v[204:207], a[28:31], v[180:183], v[204:207]\n\t"
"s_nop 7\n\t"
"v_max3_f32 v88, v204, v205, v88\n\t"
"v_max3_f32 v88, v206, v207, v88\n\t"
"v_mfma_f32_16x16x32_bf16 v[208:211], a[32:35], v[168:171], 0\n\t"
"v_mfma_f32_16x16x32_bf16 v[208:211], a[36:39], v[172:175], v[208:211]\n\t"
"v_mfma_f32_16x16x32_bf16 v[208:211], a[40:43], v[176:179], v[208:211]\n\t"
"v_mfma_f32_16x16x32_bf16 v[208:211], a[44:47], v[180:183], v[208:211]\n\t"
"s_nop 7\n\t"
"v_max3_f32 v88, v208, v209, v88\n\t"
"v_max3_f32 v88, v210, v211, v88\n\t"
"v_mfma_f32_16x16x32_bf16 v[200:203], a[48:51], v[168:171], 0\n\t"
"v_mfma_f32_16x16x32_bf16 v[200:203], a[52:55], v[172:175], v[200:203]\n\t"
"v_mfma_f32_16x16x32_bf16 v[200:203], a[56:59], v[176:179], v[200:203]\n\t"
"v_mfma_f32_16x16x32_bf16 v[200:203], a[60:63], v[180:183], v[200:203]\n\t"
"s_nop 7\n\t"
"v_max3_f32 v88, v200, v201, v88\n\t"
"v_max3_f32 v88, v202, v203, v88\n\t"
"v_mfma_f32_16x16x32_bf16 v[204:207], a[64:67], v[168:171], 0\n\t"
"v_mfma_f32_16x16x32_bf16 v[204:207], a[68:71], v[172:175], v[204:207]\n\t"
"v_mfma_f32_16x16x32_bf16 v[204:207], a[72:75], v[176:179], v[204:207]\n\t"
"v_mfma_f32_16x16x32_bf16 v[204:207], a[76:79], v[180:183], v[204:207]\n\t"
"s_nop 7\n\t"
"v_max3_f32 v88, v204, v205, v88\n\t"
"v_max3_f32 v88, v206, v207, v88\n\t"
"v_mfma_f32_16x16x32_bf16 v[208:211], a[80:83], v[168:171], 0\n\t"
"v_mfma_f32_16x16x32_bf16 v[208:211], a[84:87], v[172:175], v[208:211]\n\t"
"v_mfma_f32_16x16x32_bf16 v[208:211], a[88:91], v[176:179], v[208:211]\n\t"
"v_mfma_f32_16x16x32_bf16 v[208:211], a[92:95], v[180:183], v[208:211]\n\t"
"s_nop 7\n\t"
"v_max3_f32 v88, v208, v209, v88\n\t"
"v_max3_f32 v88, v210, v211, v88\n\t"
"v_mfma_f32_16x16x32_bf16 v[200:203], a[96:99], v[168:171], 0\n\t"
"v_mfma_f32_16x16x32_bf16 v[200:203], a[100:103], v[172:175], v[200:203]\n\t"
"v_mfma_f32_16x16x32_bf16 v[200:203], a[104:107], v[176:179], v[200:203]\n\t"
"v_mfma_f32_16x16x32_bf16 v[200:203], a[108:111], v[180:183], v[200:203]\n\t"
"s_nop 7\n\t"
"v_max3_f32 v88, v200, v201, v88\n\t"
"v_max3_f32 v88, v202, v203, v88\n\t"
"v_mfma_f32_16x16x32_bf16 v[204:207], a[112:115], v[168:171], 0\n\t"
"v_mfma_f32_16x16x32_bf16 v[204:207], a[116:119], v[172:175], v[204:207]\n\t"
"v_mfma_f32_16x16x32_bf16 v[204:207], a[120:123], v[176:179], v[204:207]\n\t"
"v_mfma_f32_16x16x32_bf16 v[204:207], a[124:127], v[180:183], v[204:207]\n\t"
"s_nop 7\n\t"
"v_max3_f32 v88, v204, v205, v88\n\t"
"v_max3_f32 v88, v206, v207, v88\n\t"
"v_mov_b32 v89, v88\n\t"
"s_nop 1\n\t"
"v_permlane32_swap_b32 v88, v89\n\t"
"s_nop 1\n\t"
"v_max_f32 v88, v88, v89\n\t"
"v_mov_b32 v89, v88\n\t"
"s_nop 1\n\t"
"v_permlane16_swap_b32 v88, v89\n\t"
"s_nop 1\n\t"
"v_max_f32 v88, v88, v89\n\t"
"ds_write_b32 v92, v88 offset:512\n\t"
"ds_read_b128 v[184:187], v246 offset:3072\n\t"
"ds_read_b128 v[188:191], v246 offset:44032\n\t"
"ds_read_b128 v[192:195], v247 offset:3072\n\t"
"ds_read_b128 v[196:199], v247 offset:44032\n\t"
"s_waitcnt lgkmcnt(3)\n\t"
"v_mfma_f32_16x16x32_bf16 v[200:203], a[0:3], v[184:187], 0\n\t"
"s_waitcnt lgkmcnt(2)\n\t"
"v_mfma_f32_16x16x32_bf16 v[200:203], a[4:7], v[188:191], v[200:203]\n\t"
"s_waitcnt lgkmcnt(1)\n\t"
"v_mfma_f32_16x16x32_bf16 v[200:203], a[8:11], v[192:195], v[200:203]\n\t"
"s_waitcnt lgkmcnt(0)\n\t"
"v_mfma_f32_16x16x32_bf16 v[200:203], a[12:15], v[196:199], v[200:203]\n\t"
"s_nop 7\n\t"
"v_max_f32 v88, v200, v201\n\t"
"v_max3_f32 v88, v202, v203, v88\n\t"
"v_mfma_f32_16x16x32_bf16 v[204:207], a[16:19], v[184:187], 0\n\t"
"v_mfma_f32_16x16x32_bf16 v[204:207], a[20:23], v[188:191], v[204:207]\n\t"
"v_mfma_f32_16x16x32_bf16 v[204:207], a[24:27], v[192:195], v[204:207]\n\t"
"v_mfma_f32_16x16x32_bf16 v[204:207], a[28:31], v[196:199], v[204:207]\n\t"
"s_nop 7\n\t"
"v_max3_f32 v88, v204, v205, v88\n\t"
"v_max3_f32 v88, v206, v207, v88\n\t"
"v_mfma_f32_16x16x32_bf16 v[208:211], a[32:35], v[184:187], 0\n\t"
"v_mfma_f32_16x16x32_bf16 v[208:211], a[36:39], v[188:191], v[208:211]\n\t"
"v_mfma_f32_16x16x32_bf16 v[208:211], a[40:43], v[192:195], v[208:211]\n\t"
"v_mfma_f32_16x16x32_bf16 v[208:211], a[44:47], v[196:199], v[208:211]\n\t"
"s_nop 7\n\t"
"v_max3_f32 v88, v208, v209, v88\n\t"
"v_max3_f32 v88, v210, v211, v88\n\t"
"v_mfma_f32_16x16x32_bf16 v[200:203], a[48:51], v[184:187], 0\n\t"
"v_mfma_f32_16x16x32_bf16 v[200:203], a[52:55], v[188:191], v[200:203]\n\t"
"v_mfma_f32_16x16x32_bf16 v[200:203], a[56:59], v[192:195], v[200:203]\n\t"
"v_mfma_f32_16x16x32_bf16 v[200:203], a[60:63], v[196:199], v[200:203]\n\t"
"s_nop 7\n\t"
"v_max3_f32 v88, v200, v201, v88\n\t"
"v_max3_f32 v88, v202, v203, v88\n\t"
"v_mfma_f32_16x16x32_bf16 v[204:207], a[64:67], v[184:187], 0\n\t"
"v_mfma_f32_16x16x32_bf16 v[204:207], a[68:71], v[188:191], v[204:207]\n\t"
"v_mfma_f32_16x16x32_bf16 v[204:207], a[72:75], v[192:195], v[204:207]\n\t"
"v_mfma_f32_16x16x32_bf16 v[204:207], a[76:79], v[196:199], v[204:207]\n\t"
"s_nop 7\n\t"
"v_max3_f32 v88, v204, v205, v88\n\t"
"v_max3_f32 v88, v206, v207, v88\n\t"
"v_mfma_f32_16x16x32_bf16 v[208:211], a[80:83], v[184:187], 0\n\t"
"v_mfma_f32_16x16x32_bf16 v[208:211], a[84:87], v[188:191], v[208:211]\n\t"
"v_mfma_f32_16x16x32_bf16 v[208:211], a[88:91], v[192:195], v[208:211]\n\t"
"v_mfma_f32_16x16x32_bf16 v[208:211], a[92:95], v[196:199], v[208:211]\n\t"
"s_nop 7\n\t"
"v_max3_f32 v88, v208, v209, v88\n\t"
"v_max3_f32 v88, v210, v211, v88\n\t"
"v_mfma_f32_16x16x32_bf16 v[200:203], a[96:99], v[184:187], 0\n\t"
"v_mfma_f32_16x16x32_bf16 v[200:203], a[100:103], v[188:191], v[200:203]\n\t"
"v_mfma_f32_16x16x32_bf16 v[200:203], a[104:107], v[192:195], v[200:203]\n\t"
"v_mfma_f32_16x16x32_bf16 v[200:203], a[108:111], v[196:199], v[200:203]\n\t"
"s_nop 7\n\t"
"v_max3_f32 v88, v200, v201, v88\n\t"
"v_max3_f32 v88, v202, v203, v88\n\t"
"v_mfma_f32_16x16x32_bf16 v[204:207], a[112:115], v[184:187], 0\n\t"
"v_mfma_f32_16x16x32_bf16 v[204:207], a[116:119], v[188:191], v[204:207]\n\t"
"v_mfma_f32_16x16x32_bf16 v[204:207], a[120:123], v[192:195], v[204:207]\n\t"
"v_mfma_f32_16x16x32_bf16 v[204:207], a[124:127], v[196:199], v[204:207]\n\t"
"s_nop 7\n\t"
"v_max3_f32 v88, v204, v205, v88\n\t"
"v_max3_f32 v88, v206, v207, v88\n\t"
"v_mov_b32 v89, v88\n\t"
"s_nop 1\n\t"
"v_permlane32_swap_b32 v88, v89\n\t"
"s_nop 1\n\t"
"v_max_f32 v88, v88, v89\n\t"
"v_mov_b32 v89, v88\n\t"
"s_nop 1\n\t"
"v_permlane16_swap_b32 v88, v89\n\t"
"s_nop 1\n\t"
"v_max_f32 v88, v88, v89\n\t"
"ds_write_b32 v92, v88 offset:768\n\t"
"ds_read_b128 v[168:171], v246 offset:4096\n\t"
"ds_read_b128 v[172:175], v246 offset:45056\n\t"
"ds_read_b128 v[176:179], v247 offset:4096\n\t"
"ds_read_b128 v[180:183], v247 offset:45056\n\t"
"s_waitcnt lgkmcnt(3)\n\t"
"v_mfma_f32_16x16x32_bf16 v[200:203], a[0:3], v[168:171], 0\n\t"
"s_waitcnt lgkmcnt(2)\n\t"
"v_mfma_f32_16x16x32_bf16 v[200:203], a[4:7], v[172:175], v[200:203]\n\t"
"s_waitcnt lgkmcnt(1)\n\t"
"v_mfma_f32_16x16x32_bf16 v[200:203], a[8:11], v[176:179], v[200:203]\n\t"
"s_waitcnt lgkmcnt(0)\n\t"
"v_mfma_f32_16x16x32_bf16 v[200:203], a[12:15], v[180:183], v[200:203]\n\t"
"s_nop 7\n\t"
"v_max_f32 v88, v200, v201\n\t"
"v_max3_f32 v88, v202, v203, v88\n\t"
"v_mfma_f32_16x16x32_bf16 v[204:207], a[16:19], v[168:171], 0\n\t"
"v_mfma_f32_16x16x32_bf16 v[204:207], a[20:23], v[172:175], v[204:207]\n\t"
"v_mfma_f32_16x16x32_bf16 v[204:207], a[24:27], v[176:179], v[204:207]\n\t"
"v_mfma_f32_16x16x32_bf16 v[204:207], a[28:31], v[180:183], v[204:207]\n\t"
"s_nop 7\n\t"
"v_max3_f32 v88, v204, v205, v88\n\t"
"v_max3_f32 v88, v206, v207, v88\n\t"
"v_mfma_f32_16x16x32_bf16 v[208:211], a[32:35], v[168:171], 0\n\t"
"v_mfma_f32_16x16x32_bf16 v[208:211], a[36:39], v[172:175], v[208:211]\n\t"
"v_mfma_f32_16x16x32_bf16 v[208:211], a[40:43], v[176:179], v[208:211]\n\t"
"v_mfma_f32_16x16x32_bf16 v[208:211], a[44:47], v[180:183], v[208:211]\n\t"
"s_nop 7\n\t"
"v_max3_f32 v88, v208, v209, v88\n\t"
"v_max3_f32 v88, v210, v211, v88\n\t"
"v_mfma_f32_16x16x32_bf16 v[200:203], a[48:51], v[168:171], 0\n\t"
"v_mfma_f32_16x16x32_bf16 v[200:203], a[52:55], v[172:175], v[200:203]\n\t"
"v_mfma_f32_16x16x32_bf16 v[200:203], a[56:59], v[176:179], v[200:203]\n\t"
"v_mfma_f32_16x16x32_bf16 v[200:203], a[60:63], v[180:183], v[200:203]\n\t"
"s_nop 7\n\t"
"v_max3_f32 v88, v200, v201, v88\n\t"
"v_max3_f32 v88, v202, v203, v88\n\t"
"v_mfma_f32_16x16x32_bf16 v[204:207], a[64:67], v[168:171], 0\n\t"
"v_mfma_f32_16x16x32_bf16 v[204:207], a[68:71], v[172:175], v[204:207]\n\t"
"v_mfma_f32_16x16x32_bf16 v[204:207], a[72:75], v[176:179], v[204:207]\n\t"
"v_mfma_f32_16x16x32_bf16 v[204:207], a[76:79], v[180:183], v[204:207]\n\t"
"s_nop 7\n\t"
"v_max3_f32 v88, v204, v205, v88\n\t"
"v_max3_f32 v88, v206, v207, v88\n\t"
"v_mfma_f32_16x16x32_bf16 v[208:211], a[80:83], v[168:171], 0\n\t"
"v_mfma_f32_16x16x32_bf16 v[208:211], a[84:87], v[172:175], v[208:211]\n\t"
"v_mfma_f32_16x16x32_bf16 v[208:211], a[88:91], v[176:179], v[208:211]\n\t"
"v_mfma_f32_16x16x32_bf16 v[208:211], a[92:95], v[180:183], v[208:211]\n\t"
"s_nop 7\n\t"
"v_max3_f32 v88, v208, v209, v88\n\t"
"v_max3_f32 v88, v210, v211, v88\n\t"
"v_mfma_f32_16x16x32_bf16 v[200:203], a[96:99], v[168:171], 0\n\t"
"v_mfma_f32_16x16x32_bf16 v[200:203], a[100:103], v[172:175], v[200:203]\n\t"
"v_mfma_f32_16x16x32_bf16 v[200:203], a[104:107], v[176:179], v[200:203]\n\t"
"v_mfma_f32_16x16x32_bf16 v[200:203], a[108:111], v[180:183], v[200:203]\n\t"
"s_nop 7\n\t"
"v_max3_f32 v88, v200, v201, v88\n\t"
"v_max3_f32 v88, v202, v203, v88\n\t"
"v_mfma_f32_16x16x32_bf16 v[204:207], a[112:115], v[168:171], 0\n\t"
"v_mfma_f32_16x16x32_bf16 v[204:207], a[116:119], v[172:175], v[204:207]\n\t"
"v_mfma_f32_16x16x32_bf16 v[204:207], a[120:123], v[176:179], v[204:207]\n\t"
"v_mfma_f32_16x16x32_bf16 v[204:207], a[124:127], v[180:183], v[204:207]\n\t"
"s_nop 7\n\t"
"v_max3_f32 v88, v204, v205, v88\n\t"
"v_max3_f32 v88, v206, v207, v88\n\t"
"v_mov_b32 v89, v88\n\t"
"s_nop 1\n\t"
"v_permlane32_swap_b32 v88, v89\n\t"
"s_nop 1\n\t"
"v_max_f32 v88, v88, v89\n\t"
"v_mov_b32 v89, v88\n\t"
"s_nop 1\n\t"
"v_permlane16_swap_b32 v88, v89\n\t"
"s_nop 1\n\t"
"v_max_f32 v88, v88, v89\n\t"
"ds_write_b32 v92, v88 offset:1024\n\t"
"ds_read_b128 v[184:187], v246 offset:5120\n\t"
"ds_read_b128 v[188:191], v246 offset:46080\n\t"
"ds_read_b128 v[192:195], v247 offset:5120\n\t"
"ds_read_b128 v[196:199], v247 offset:46080\n\t"
"s_waitcnt lgkmcnt(3)\n\t"
"v_mfma_f32_16x16x32_bf16 v[200:203], a[0:3], v[184:187], 0\n\t"
"s_waitcnt lgkmcnt(2)\n\t"
"v_mfma_f32_16x16x32_bf16 v[200:203], a[4:7], v[188:191], v[200:203]\n\t"
"s_waitcnt lgkmcnt(1)\n\t"
"v_mfma_f32_16x16x32_bf16 v[200:203], a[8:11], v[192:195], v[200:203]\n\t"
"s_waitcnt lgkmcnt(0)\n\t"
"v_mfma_f32_16x16x32_bf16 v[200:203], a[12:15], v[196:199], v[200:203]\n\t"
"s_nop 7\n\t"
"v_max_f32 v88, v200, v201\n\t"
"v_max3_f32 v88, v202, v203, v88\n\t"
"v_mfma_f32_16x16x32_bf16 v[204:207], a[16:19], v[184:187], 0\n\t"
"v_mfma_f32_16x16x32_bf16 v[204:207], a[20:23], v[188:191], v[204:207]\n\t"
"v_mfma_f32_16x16x32_bf16 v[204:207], a[24:27], v[192:195], v[204:207]\n\t"
"v_mfma_f32_16x16x32_bf16 v[204:207], a[28:31], v[196:199], v[204:207]\n\t"
"s_nop 7\n\t"
"v_max3_f32 v88, v204, v205, v88\n\t"
"v_max3_f32 v88, v206, v207, v88\n\t"
"v_mfma_f32_16x16x32_bf16 v[208:211], a[32:35], v[184:187], 0\n\t"
"v_mfma_f32_16x16x32_bf16 v[208:211], a[36:39], v[188:191], v[208:211]\n\t"
"v_mfma_f32_16x16x32_bf16 v[208:211], a[40:43], v[192:195], v[208:211]\n\t"
"v_mfma_f32_16x16x32_bf16 v[208:211], a[44:47], v[196:199], v[208:211]\n\t"
"s_nop 7\n\t"
"v_max3_f32 v88, v208, v209, v88\n\t"
"v_max3_f32 v88, v210, v211, v88\n\t"
"v_mfma_f32_16x16x32_bf16 v[200:203], a[48:51], v[184:187], 0\n\t"
"v_mfma_f32_16x16x32_bf16 v[200:203], a[52:55], v[188:191], v[200:203]\n\t"
"v_mfma_f32_16x16x32_bf16 v[200:203], a[56:59], v[192:195], v[200:203]\n\t"
"v_mfma_f32_16x16x32_bf16 v[200:203], a[60:63], v[196:199], v[200:203]\n\t"
"s_nop 7\n\t"
"v_max3_f32 v88, v200, v201, v88\n\t"
"v_max3_f32 v88, v202, v203, v88\n\t"
"v_mfma_f32_16x16x32_bf16 v[204:207], a[64:67], v[184:187], 0\n\t"
"v_mfma_f32_16x16x32_bf16 v[204:207], a[68:71], v[188:191], v[204:207]\n\t"
"v_mfma_f32_16x16x32_bf16 v[204:207], a[72:75], v[192:195], v[204:207]\n\t"
"v_mfma_f32_16x16x32_bf16 v[204:207], a[76:79], v[196:199], v[204:207]\n\t"
"s_nop 7\n\t"
"v_max3_f32 v88, v204, v205, v88\n\t"
"v_max3_f32 v88, v206, v207, v88\n\t"
"v_mfma_f32_16x16x32_bf16 v[208:211], a[80:83], v[184:187], 0\n\t"
"v_mfma_f32_16x16x32_bf16 v[208:211], a[84:87], v[188:191], v[208:211]\n\t"
"v_mfma_f32_16x16x32_bf16 v[208:211], a[88:91], v[192:195], v[208:211]\n\t"
"v_mfma_f32_16x16x32_bf16 v[208:211], a[92:95], v[196:199], v[208:211]\n\t"
"s_nop 7\n\t"
"v_max3_f32 v88, v208, v209, v88\n\t"
"v_max3_f32 v88, v210, v211, v88\n\t"
"v_mfma_f32_16x16x32_bf16 v[200:203], a[96:99], v[184:187], 0\n\t"
"v_mfma_f32_16x16x32_bf16 v[200:203], a[100:103], v[188:191], v[200:203]\n\t"
"v_mfma_f32_16x16x32_bf16 v[200:203], a[104:107], v[192:195], v[200:203]\n\t"
"v_mfma_f32_16x16x32_bf16 v[200:203], a[108:111], v[196:199], v[200:203]\n\t"
"s_nop 7\n\t"
"v_max3_f32 v88, v200, v201, v88\n\t"
"v_max3_f32 v88, v202, v203, v88\n\t"
"v_mfma_f32_16x16x32_bf16 v[204:207], a[112:115], v[184:187], 0\n\t"
"v_mfma_f32_16x16x32_bf16 v[204:207], a[116:119], v[188:191], v[204:207]\n\t"
"v_mfma_f32_16x16x32_bf16 v[204:207], a[120:123], v[192:195], v[204:207]\n\t"
"v_mfma_f32_16x16x32_bf16 v[204:207], a[124:127], v[196:199], v[204:207]\n\t"
"s_nop 7\n\t"
"v_max3_f32 v88, v204, v205, v88\n\t"
"v_max3_f32 v88, v206, v207, v88\n\t"
"v_mov_b32 v89, v88\n\t"
"s_nop 1\n\t"
"v_permlane32_swap_b32 v88, v89\n\t"
"s_nop 1\n\t"
"v_max_f32 v88, v88, v89\n\t"
"v_mov_b32 v89, v88\n\t"
"s_nop 1\n\t"
"v_permlane16_swap_b32 v88, v89\n\t"
"s_nop 1\n\t"
"v_max_f32 v88, v88, v89\n\t"
"ds_write_b32 v92, v88 offset:40960\n\t"
"ds_read_b128 v[168:171], v246 offset:6144\n\t"
"ds_read_b128 v[172:175], v246 offset:47104\n\t"
"ds_read_b128 v[176:179], v247 offset:6144\n\t"
"ds_read_b128 v[180:183], v247 offset:47104\n\t"
"s_waitcnt lgkmcnt(3)\n\t"
"v_mfma_f32_16x16x32_bf16 v[200:203], a[0:3], v[168:171], 0\n\t"
"s_waitcnt lgkmcnt(2)\n\t"
"v_mfma_f32_16x16x32_bf16 v[200:203], a[4:7], v[172:175], v[200:203]\n\t"
"s_waitcnt lgkmcnt(1)\n\t"
"v_mfma_f32_16x16x32_bf16 v[200:203], a[8:11], v[176:179], v[200:203]\n\t"
"s_waitcnt lgkmcnt(0)\n\t"
"v_mfma_f32_16x16x32_bf16 v[200:203], a[12:15], v[180:183], v[200:203]\n\t"
"s_nop 7\n\t"
"v_max_f32 v88, v200, v201\n\t"
"v_max3_f32 v88, v202, v203, v88\n\t"
"v_mfma_f32_16x16x32_bf16 v[204:207], a[16:19], v[168:171], 0\n\t"
"v_mfma_f32_16x16x32_bf16 v[204:207], a[20:23], v[172:175], v[204:207]\n\t"
"v_mfma_f32_16x16x32_bf16 v[204:207], a[24:27], v[176:179], v[204:207]\n\t"
"v_mfma_f32_16x16x32_bf16 v[204:207], a[28:31], v[180:183], v[204:207]\n\t"
"s_nop 7\n\t"
"v_max3_f32 v88, v204, v205, v88\n\t"
"v_max3_f32 v88, v206, v207, v88\n\t"
"v_mfma_f32_16x16x32_bf16 v[208:211], a[32:35], v[168:171], 0\n\t"
"v_mfma_f32_16x16x32_bf16 v[208:211], a[36:39], v[172:175], v[208:211]\n\t"
"v_mfma_f32_16x16x32_bf16 v[208:211], a[40:43], v[176:179], v[208:211]\n\t"
"v_mfma_f32_16x16x32_bf16 v[208:211], a[44:47], v[180:183], v[208:211]\n\t"
"s_nop 7\n\t"
"v_max3_f32 v88, v208, v209, v88\n\t"
"v_max3_f32 v88, v210, v211, v88\n\t"
"v_mfma_f32_16x16x32_bf16 v[200:203], a[48:51], v[168:171], 0\n\t"
"v_mfma_f32_16x16x32_bf16 v[200:203], a[52:55], v[172:175], v[200:203]\n\t"
"v_mfma_f32_16x16x32_bf16 v[200:203], a[56:59], v[176:179], v[200:203]\n\t"
"v_mfma_f32_16x16x32_bf16 v[200:203], a[60:63], v[180:183], v[200:203]\n\t"
"s_nop 7\n\t"
"v_max3_f32 v88, v200, v201, v88\n\t"
"v_max3_f32 v88, v202, v203, v88\n\t"
"v_mfma_f32_16x16x32_bf16 v[204:207], a[64:67], v[168:171], 0\n\t"
"v_mfma_f32_16x16x32_bf16 v[204:207], a[68:71], v[172:175], v[204:207]\n\t"
"v_mfma_f32_16x16x32_bf16 v[204:207], a[72:75], v[176:179], v[204:207]\n\t"
"v_mfma_f32_16x16x32_bf16 v[204:207], a[76:79], v[180:183], v[204:207]\n\t"
"s_nop 7\n\t"
"v_max3_f32 v88, v204, v205, v88\n\t"
"v_max3_f32 v88, v206, v207, v88\n\t"
"v_mfma_f32_16x16x32_bf16 v[208:211], a[80:83], v[168:171], 0\n\t"
"v_mfma_f32_16x16x32_bf16 v[208:211], a[84:87], v[172:175], v[208:211]\n\t"
"v_mfma_f32_16x16x32_bf16 v[208:211], a[88:91], v[176:179], v[208:211]\n\t"
"v_mfma_f32_16x16x32_bf16 v[208:211], a[92:95], v[180:183], v[208:211]\n\t"
"s_nop 7\n\t"
"v_max3_f32 v88, v208, v209, v88\n\t"
"v_max3_f32 v88, v210, v211, v88\n\t"
"v_mfma_f32_16x16x32_bf16 v[200:203], a[96:99], v[168:171], 0\n\t"
"v_mfma_f32_16x16x32_bf16 v[200:203], a[100:103], v[172:175], v[200:203]\n\t"
"v_mfma_f32_16x16x32_bf16 v[200:203], a[104:107], v[176:179], v[200:203]\n\t"
"v_mfma_f32_16x16x32_bf16 v[200:203], a[108:111], v[180:183], v[200:203]\n\t"
"s_nop 7\n\t"
"v_max3_f32 v88, v200, v201, v88\n\t"
"v_max3_f32 v88, v202, v203, v88\n\t"
"v_mfma_f32_16x16x32_bf16 v[204:207], a[112:115], v[168:171], 0\n\t"
"v_mfma_f32_16x16x32_bf16 v[204:207], a[116:119], v[172:175], v[204:207]\n\t"
"v_mfma_f32_16x16x32_bf16 v[204:207], a[120:123], v[176:179], v[204:207]\n\t"
"v_mfma_f32_16x16x32_bf16 v[204:207], a[124:127], v[180:183], v[204:207]\n\t"
"s_nop 7\n\t"
"v_max3_f32 v88, v204, v205, v88\n\t"
"v_max3_f32 v88, v206, v207, v88\n\t"
"v_mov_b32 v89, v88\n\t"
"s_nop 1\n\t"
"v_permlane32_swap_b32 v88, v89\n\t"
"s_nop 1\n\t"
"v_max_f32 v88, v88, v89\n\t"
"v_mov_b32 v89, v88\n\t"
"s_nop 1\n\t"
"v_permlane16_swap_b32 v88, v89\n\t"
"s_nop 1\n\t"
"v_max_f32 v88, v88, v89\n\t"
"ds_write_b32 v92, v88 offset:41216\n\t"
"ds_read_b128 v[184:187], v246 offset:7168\n\t"
"ds_read_b128 v[188:191], v246 offset:48128\n\t"
"ds_read_b128 v[192:195], v247 offset:7168\n\t"
"ds_read_b128 v[196:199], v247 offset:48128\n\t"
"s_waitcnt lgkmcnt(3)\n\t"
"v_mfma_f32_16x16x32_bf16 v[200:203], a[0:3], v[184:187], 0\n\t"
"s_waitcnt lgkmcnt(2)\n\t"
"v_mfma_f32_16x16x32_bf16 v[200:203], a[4:7], v[188:191], v[200:203]\n\t"
"s_waitcnt lgkmcnt(1)\n\t"
"v_mfma_f32_16x16x32_bf16 v[200:203], a[8:11], v[192:195], v[200:203]\n\t"
"s_waitcnt lgkmcnt(0)\n\t"
"v_mfma_f32_16x16x32_bf16 v[200:203], a[12:15], v[196:199], v[200:203]\n\t"
"s_nop 7\n\t"
"v_max_f32 v88, v200, v201\n\t"
"v_max3_f32 v88, v202, v203, v88\n\t"
"v_mfma_f32_16x16x32_bf16 v[204:207], a[16:19], v[184:187], 0\n\t"
"v_mfma_f32_16x16x32_bf16 v[204:207], a[20:23], v[188:191], v[204:207]\n\t"
"v_mfma_f32_16x16x32_bf16 v[204:207], a[24:27], v[192:195], v[204:207]\n\t"
"v_mfma_f32_16x16x32_bf16 v[204:207], a[28:31], v[196:199], v[204:207]\n\t"
"s_nop 7\n\t"
"v_max3_f32 v88, v204, v205, v88\n\t"
"v_max3_f32 v88, v206, v207, v88\n\t"
"v_mfma_f32_16x16x32_bf16 v[208:211], a[32:35], v[184:187], 0\n\t"
"v_mfma_f32_16x16x32_bf16 v[208:211], a[36:39], v[188:191], v[208:211]\n\t"
"v_mfma_f32_16x16x32_bf16 v[208:211], a[40:43], v[192:195], v[208:211]\n\t"
"v_mfma_f32_16x16x32_bf16 v[208:211], a[44:47], v[196:199], v[208:211]\n\t"
"s_nop 7\n\t"
"v_max3_f32 v88, v208, v209, v88\n\t"
"v_max3_f32 v88, v210, v211, v88\n\t"
"v_mfma_f32_16x16x32_bf16 v[200:203], a[48:51], v[184:187], 0\n\t"
"v_mfma_f32_16x16x32_bf16 v[200:203], a[52:55], v[188:191], v[200:203]\n\t"
"v_mfma_f32_16x16x32_bf16 v[200:203], a[56:59], v[192:195], v[200:203]\n\t"
"v_mfma_f32_16x16x32_bf16 v[200:203], a[60:63], v[196:199], v[200:203]\n\t"
"s_nop 7\n\t"
"v_max3_f32 v88, v200, v201, v88\n\t"
"v_max3_f32 v88, v202, v203, v88\n\t"
"v_mfma_f32_16x16x32_bf16 v[204:207], a[64:67], v[184:187], 0\n\t"
"v_mfma_f32_16x16x32_bf16 v[204:207], a[68:71], v[188:191], v[204:207]\n\t"
"v_mfma_f32_16x16x32_bf16 v[204:207], a[72:75], v[192:195], v[204:207]\n\t"
"v_mfma_f32_16x16x32_bf16 v[204:207], a[76:79], v[196:199], v[204:207]\n\t"
"s_nop 7\n\t"
"v_max3_f32 v88, v204, v205, v88\n\t"
"v_max3_f32 v88, v206, v207, v88\n\t"
"v_mfma_f32_16x16x32_bf16 v[208:211], a[80:83], v[184:187], 0\n\t"
"v_mfma_f32_16x16x32_bf16 v[208:211], a[84:87], v[188:191], v[208:211]\n\t"
"v_mfma_f32_16x16x32_bf16 v[208:211], a[88:91], v[192:195], v[208:211]\n\t"
"v_mfma_f32_16x16x32_bf16 v[208:211], a[92:95], v[196:199], v[208:211]\n\t"
"s_nop 7\n\t"
"v_max3_f32 v88, v208, v209, v88\n\t"
"v_max3_f32 v88, v210, v211, v88\n\t"
"v_mfma_f32_16x16x32_bf16 v[200:203], a[96:99], v[184:187], 0\n\t"
"v_mfma_f32_16x16x32_bf16 v[200:203], a[100:103], v[188:191], v[200:203]\n\t"
"v_mfma_f32_16x16x32_bf16 v[200:203], a[104:107], v[192:195], v[200:203]\n\t"
"v_mfma_f32_16x16x32_bf16 v[200:203], a[108:111], v[196:199], v[200:203]\n\t"
"s_nop 7\n\t"
"v_max3_f32 v88, v200, v201, v88\n\t"
"v_max3_f32 v88, v202, v203, v88\n\t"
"v_mfma_f32_16x16x32_bf16 v[204:207], a[112:115], v[184:187], 0\n\t"
"v_mfma_f32_16x16x32_bf16 v[204:207], a[116:119], v[188:191], v[204:207]\n\t"
"v_mfma_f32_16x16x32_bf16 v[204:207], a[120:123], v[192:195], v[204:207]\n\t"
"v_mfma_f32_16x16x32_bf16 v[204:207], a[124:127], v[196:199], v[204:207]\n\t"
"s_nop 7\n\t"
"v_max3_f32 v88, v204, v205, v88\n\t"
"v_max3_f32 v88, v206, v207, v88\n\t"
"v_mov_b32 v89, v88\n\t"
"s_nop 1\n\t"
"v_permlane32_swap_b32 v88, v89\n\t"
"s_nop 1\n\t"
"v_max_f32 v88, v88, v89\n\t"
"v_mov_b32 v89, v88\n\t"
"s_nop 1\n\t"
"v_permlane16_swap_b32 v88, v89\n\t"
"s_nop 1\n\t"
"v_max_f32 v88, v88, v89\n\t"
"ds_write_b32 v92, v88 offset:41472\n\t"
"ds_read_b128 v[168:171], v246 offset:8192\n\t"
"ds_read_b128 v[172:175], v246 offset:49152\n\t"
"ds_read_b128 v[176:179], v247 offset:8192\n\t"
"ds_read_b128 v[180:183], v247 offset:49152\n\t"
"s_waitcnt lgkmcnt(3)\n\t"
"v_mfma_f32_16x16x32_bf16 v[200:203], a[0:3], v[168:171], 0\n\t"
"s_waitcnt lgkmcnt(2)\n\t"
"v_mfma_f32_16x16x32_bf16 v[200:203], a[4:7], v[172:175], v[200:203]\n\t"
"s_waitcnt lgkmcnt(1)\n\t"
"v_mfma_f32_16x16x32_bf16 v[200:203], a[8:11], v[176:179], v[200:203]\n\t"
"s_waitcnt lgkmcnt(0)\n\t"
"v_mfma_f32_16x16x32_bf16 v[200:203], a[12:15], v[180:183], v[200:203]\n\t"
"s_nop 7\n\t"
"v_max_f32 v88, v200, v201\n\t"
"v_max3_f32 v88, v202, v203, v88\n\t"
"v_mfma_f32_16x16x32_bf16 v[204:207], a[16:19], v[168:171], 0\n\t"
"v_mfma_f32_16x16x32_bf16 v[204:207], a[20:23], v[172:175], v[204:207]\n\t"
"v_mfma_f32_16x16x32_bf16 v[204:207], a[24:27], v[176:179], v[204:207]\n\t"
"v_mfma_f32_16x16x32_bf16 v[204:207], a[28:31], v[180:183], v[204:207]\n\t"
"s_nop 7\n\t"
"v_max3_f32 v88, v204, v205, v88\n\t"
"v_max3_f32 v88, v206, v207, v88\n\t"
"v_mfma_f32_16x16x32_bf16 v[208:211], a[32:35], v[168:171], 0\n\t"
"v_mfma_f32_16x16x32_bf16 v[208:211], a[36:39], v[172:175], v[208:211]\n\t"
"v_mfma_f32_16x16x32_bf16 v[208:211], a[40:43], v[176:179], v[208:211]\n\t"
"v_mfma_f32_16x16x32_bf16 v[208:211], a[44:47], v[180:183], v[208:211]\n\t"
"s_nop 7\n\t"
"v_max3_f32 v88, v208, v209, v88\n\t"
"v_max3_f32 v88, v210, v211, v88\n\t"
"v_mfma_f32_16x16x32_bf16 v[200:203], a[48:51], v[168:171], 0\n\t"
"v_mfma_f32_16x16x32_bf16 v[200:203], a[52:55], v[172:175], v[200:203]\n\t"
"v_mfma_f32_16x16x32_bf16 v[200:203], a[56:59], v[176:179], v[200:203]\n\t"
"v_mfma_f32_16x16x32_bf16 v[200:203], a[60:63], v[180:183], v[200:203]\n\t"
"s_nop 7\n\t"
"v_max3_f32 v88, v200, v201, v88\n\t"
"v_max3_f32 v88, v202, v203, v88\n\t"
"v_mfma_f32_16x16x32_bf16 v[204:207], a[64:67], v[168:171], 0\n\t"
"v_mfma_f32_16x16x32_bf16 v[204:207], a[68:71], v[172:175], v[204:207]\n\t"
"v_mfma_f32_16x16x32_bf16 v[204:207], a[72:75], v[176:179], v[204:207]\n\t"
"v_mfma_f32_16x16x32_bf16 v[204:207], a[76:79], v[180:183], v[204:207]\n\t"
"s_nop 7\n\t"
"v_max3_f32 v88, v204, v205, v88\n\t"
"v_max3_f32 v88, v206, v207, v88\n\t"
"v_mfma_f32_16x16x32_bf16 v[208:211], a[80:83], v[168:171], 0\n\t"
"v_mfma_f32_16x16x32_bf16 v[208:211], a[84:87], v[172:175], v[208:211]\n\t"
"v_mfma_f32_16x16x32_bf16 v[208:211], a[88:91], v[176:179], v[208:211]\n\t"
"v_mfma_f32_16x16x32_bf16 v[208:211], a[92:95], v[180:183], v[208:211]\n\t"
"s_nop 7\n\t"
"v_max3_f32 v88, v208, v209, v88\n\t"
"v_max3_f32 v88, v210, v211, v88\n\t"
"v_mfma_f32_16x16x32_bf16 v[200:203], a[96:99], v[168:171], 0\n\t"
"v_mfma_f32_16x16x32_bf16 v[200:203], a[100:103], v[172:175], v[200:203]\n\t"
"v_mfma_f32_16x16x32_bf16 v[200:203], a[104:107], v[176:179], v[200:203]\n\t"
"v_mfma_f32_16x16x32_bf16 v[200:203], a[108:111], v[180:183], v[200:203]\n\t"
"s_nop 7\n\t"
"v_max3_f32 v88, v200, v201, v88\n\t"
"v_max3_f32 v88, v202, v203, v88\n\t"
"v_mfma_f32_16x16x32_bf16 v[204:207], a[112:115], v[168:171], 0\n\t"
"v_mfma_f32_16x16x32_bf16 v[204:207], a[116:119], v[172:175], v[204:207]\n\t"
"v_mfma_f32_16x16x32_bf16 v[204:207], a[120:123], v[176:179], v[204:207]\n\t"
"v_mfma_f32_16x16x32_bf16 v[204:207], a[124:127], v[180:183], v[204:207]\n\t"
"s_nop 7\n\t"
"v_max3_f32 v88, v204, v205, v88\n\t"
"v_max3_f32 v88, v206, v207, v88\n\t"
"v_mov_b32 v89, v88\n\t"
"s_nop 1\n\t"
"v_permlane32_swap_b32 v88, v89\n\t"
"s_nop 1\n\t"
"v_max_f32 v88, v88, v89\n\t"
"v_mov_b32 v89, v88\n\t"
"s_nop 1\n\t"
"v_permlane16_swap_b32 v88, v89\n\t"
"s_nop 1\n\t"
"v_max_f32 v88, v88, v89\n\t"
"ds_write_b32 v92, v88 offset:41728\n\t"
"ds_read_b128 v[184:187], v246 offset:9216\n\t"
"ds_read_b128 v[188:191], v246 offset:50176\n\t"
"ds_read_b128 v[192:195], v247 offset:9216\n\t"
"ds_read_b128 v[196:199], v247 offset:50176\n\t"
"s_waitcnt lgkmcnt(3)\n\t"
"v_mfma_f32_16x16x32_bf16 v[200:203], a[0:3], v[184:187], 0\n\t"
"s_waitcnt lgkmcnt(2)\n\t"
"v_mfma_f32_16x16x32_bf16 v[200:203], a[4:7], v[188:191], v[200:203]\n\t"
"s_waitcnt lgkmcnt(1)\n\t"
"v_mfma_f32_16x16x32_bf16 v[200:203], a[8:11], v[192:195], v[200:203]\n\t"
"s_waitcnt lgkmcnt(0)\n\t"
"v_mfma_f32_16x16x32_bf16 v[200:203], a[12:15], v[196:199], v[200:203]\n\t"
"s_nop 7\n\t"
"v_max_f32 v88, v200, v201\n\t"
"v_max3_f32 v88, v202, v203, v88\n\t"
"v_mfma_f32_16x16x32_bf16 v[204:207], a[16:19], v[184:187], 0\n\t"
"v_mfma_f32_16x16x32_bf16 v[204:207], a[20:23], v[188:191], v[204:207]\n\t"
"v_mfma_f32_16x16x32_bf16 v[204:207], a[24:27], v[192:195], v[204:207]\n\t"
"v_mfma_f32_16x16x32_bf16 v[204:207], a[28:31], v[196:199], v[204:207]\n\t"
"s_nop 7\n\t"
"v_max3_f32 v88, v204, v205, v88\n\t"
"v_max3_f32 v88, v206, v207, v88\n\t"
"v_mfma_f32_16x16x32_bf16 v[208:211], a[32:35], v[184:187], 0\n\t"
"v_mfma_f32_16x16x32_bf16 v[208:211], a[36:39], v[188:191], v[208:211]\n\t"
"v_mfma_f32_16x16x32_bf16 v[208:211], a[40:43], v[192:195], v[208:211]\n\t"
"v_mfma_f32_16x16x32_bf16 v[208:211], a[44:47], v[196:199], v[208:211]\n\t"
"s_nop 7\n\t"
"v_max3_f32 v88, v208, v209, v88\n\t"
"v_max3_f32 v88, v210, v211, v88\n\t"
"v_mfma_f32_16x16x32_bf16 v[200:203], a[48:51], v[184:187], 0\n\t"
"v_mfma_f32_16x16x32_bf16 v[200:203], a[52:55], v[188:191], v[200:203]\n\t"
"v_mfma_f32_16x16x32_bf16 v[200:203], a[56:59], v[192:195], v[200:203]\n\t"
"v_mfma_f32_16x16x32_bf16 v[200:203], a[60:63], v[196:199], v[200:203]\n\t"
"s_nop 7\n\t"
"v_max3_f32 v88, v200, v201, v88\n\t"
"v_max3_f32 v88, v202, v203, v88\n\t"
"v_mfma_f32_16x16x32_bf16 v[204:207], a[64:67], v[184:187], 0\n\t"
"v_mfma_f32_16x16x32_bf16 v[204:207], a[68:71], v[188:191], v[204:207]\n\t"
"v_mfma_f32_16x16x32_bf16 v[204:207], a[72:75], v[192:195], v[204:207]\n\t"
"v_mfma_f32_16x16x32_bf16 v[204:207], a[76:79], v[196:199], v[204:207]\n\t"
"s_nop 7\n\t"
"v_max3_f32 v88, v204, v205, v88\n\t"
"v_max3_f32 v88, v206, v207, v88\n\t"
"v_mfma_f32_16x16x32_bf16 v[208:211], a[80:83], v[184:187], 0\n\t"
"v_mfma_f32_16x16x32_bf16 v[208:211], a[84:87], v[188:191], v[208:211]\n\t"
"v_mfma_f32_16x16x32_bf16 v[208:211], a[88:91], v[192:195], v[208:211]\n\t"
"v_mfma_f32_16x16x32_bf16 v[208:211], a[92:95], v[196:199], v[208:211]\n\t"
"s_nop 7\n\t"
"v_max3_f32 v88, v208, v209, v88\n\t"
"v_max3_f32 v88, v210, v211, v88\n\t"
"v_mfma_f32_16x16x32_bf16 v[200:203], a[96:99], v[184:187], 0\n\t"
"v_mfma_f32_16x16x32_bf16 v[200:203], a[100:103], v[188:191], v[200:203]\n\t"
"v_mfma_f32_16x16x32_bf16 v[200:203], a[104:107], v[192:195], v[200:203]\n\t"
"v_mfma_f32_16x16x32_bf16 v[200:203], a[108:111], v[196:199], v[200:203]\n\t"
"s_nop 7\n\t"
"v_max3_f32 v88, v200, v201, v88\n\t"
"v_max3_f32 v88, v202, v203, v88\n\t"
"v_mfma_f32_16x16x32_bf16 v[204:207], a[112:115], v[184:187], 0\n\t"
"v_mfma_f32_16x16x32_bf16 v[204:207], a[116:119], v[188:191], v[204:207]\n\t"
"v_mfma_f32_16x16x32_bf16 v[204:207], a[120:123], v[192:195], v[204:207]\n\t"
"v_mfma_f32_16x16x32_bf16 v[204:207], a[124:127], v[196:199], v[204:207]\n\t"
"s_nop 7\n\t"
"v_max3_f32 v88, v204, v205, v88\n\t"
"v_max3_f32 v88, v206, v207, v88\n\t"
"v_mov_b32 v89, v88\n\t"
"s_nop 1\n\t"
"v_permlane32_swap_b32 v88, v89\n\t"
"s_nop 1\n\t"
"v_max_f32 v88, v88, v89\n\t"
"v_mov_b32 v89, v88\n\t"
"s_nop 1\n\t"
"v_permlane16_swap_b32 v88, v89\n\t"
"s_nop 1\n\t"
"v_max_f32 v88, v88, v89\n\t"
"ds_write_b32 v92, v88 offset:41984\n\t"
"s_waitcnt lgkmcnt(0)\n\t"
"s_barrier\n\t"
"ds_read_b128 v[168:171], v246 offset:0\n\t"
"ds_read_b128 v[172:175], v246 offset:40960\n\t"
"ds_read_b128 v[176:179], v247 offset:0\n\t"
"ds_read_b128 v[180:183], v247 offset:40960\n\t"
"ds_read_b128 v[88:91], v93 offset:0\n\t"
"s_waitcnt lgkmcnt(0)\n\t"
"v_max_f32 v88, v88, v89\n\t"
"v_max3_f32 v88, v90, v91, v88\n\t"
"v_sub_f32 v80, 0, v88\n\t"
"v_sub_f32 v81, 0, v88\n\t"
"v_sub_f32 v82, 0, v88\n\t"
"v_sub_f32 v83, 0, v88\n\t"
"s_nop 1\n\t"
"v_mfma_f32_16x16x32_bf16 v[200:203], a[0:3], v[168:171], v[80:83]\n\t"
"v_mfma_f32_16x16x32_bf16 v[200:203], a[4:7], v[172:175], v[200:203]\n\t"
"v_mfma_f32_16x16x32_bf16 v[200:203], a[8:11], v[176:179], v[200:203]\n\t"
"v_mfma_f32_16x16x32_bf16 v[200:203], a[12:15], v[180:183], v[200:203]\n\t"
"v_mfma_f32_16x16x32_bf16 v[204:207], a[16:19], v[168:171], v[80:83]\n\t"
"v_mfma_f32_16x16x32_bf16 v[204:207], a[20:23], v[172:175], v[204:207]\n\t"
"v_mfma_f32_16x16x32_bf16 v[204:207], a[24:27], v[176:179], v[204:207]\n\t"
"v_mfma_f32_16x16x32_bf16 v[204:207], a[28:31], v[180:183], v[204:207]\n\t"
"v_mfma_f32_16x16x32_bf16 v[208:211], a[32:35], v[168:171], v[80:83]\n\t"
"v_mfma_f32_16x16x32_bf16 v[208:211], a[36:39], v[172:175], v[208:211]\n\t"
"v_mfma_f32_16x16x32_bf16 v[208:211], a[40:43], v[176:179], v[208:211]\n\t"
"v_mfma_f32_16x16x32_bf16 v[208:211], a[44:47], v[180:183], v[208:211]\n\t"
"v_exp_f32 v200, v200\n\t"
"v_exp_f32 v201, v201\n\t"
"v_exp_f32 v202, v202\n\t"
"v_exp_f32 v203, v203\n\t"
"v_cvt_pk_bf16_f32 v16, v200, v201\n\t"
"v_cvt_pk_bf16_f32 v17, v202, v203\n\t"
"v_mfma_f32_16x16x32_bf16 v[200:203], a[48:51], v[168:171], v[80:83]\n\t"
"ds_read_b128 v[184:187], v246 offset:1024\n\t"
"v_exp_f32 v204, v204\n\t"
"v_mfma_f32_16x16x32_bf16 v[200:203], a[52:55], v[172:175], v[200:203]\n\t"
"v_exp_f32 v205, v205\n\t"
"v_mfma_f32_16x16x32_bf16 v[200:203], a[56:59], v[176:179], v[200:203]\n\t"
"v_exp_f32 v206, v206\n\t"
"v_mfma_f32_16x16x32_bf16 v[200:203], a[60:63], v[180:183], v[200:203]\n\t"
"v_exp_f32 v207, v207\n\t"
"v_cvt_pk_bf16_f32 v18, v204, v205\n\t"
"v_cvt_pk_bf16_f32 v19, v206, v207\n\t"
"v_mfma_f32_16x16x32_bf16 v[204:207], a[64:67], v[168:171], v[80:83]\n\t"
"ds_read_b128 v[188:191], v246 offset:41984\n\t"
"v_exp_f32 v208, v208\n\t"
"v_mfma_f32_16x16x32_bf16 v[204:207], a[68:71], v[172:175], v[204:207]\n\t"
"v_exp_f32 v209, v209\n\t"
"v_mfma_f32_16x16x32_bf16 v[204:207], a[72:75], v[176:179], v[204:207]\n\t"
"v_exp_f32 v210, v210\n\t"
"v_mfma_f32_16x16x32_bf16 v[204:207], a[76:79], v[180:183], v[204:207]\n\t"
"v_exp_f32 v211, v211\n\t"
"v_mfma_f32_16x16x32_bf16 v[212:215], v[216:219], v[16:19], 0\n\t"
"ds_read_b128 v[192:195], v247 offset:1024\n\t"
"v_cvt_pk_bf16_f32 v20, v208, v209\n\t"
"v_cvt_pk_bf16_f32 v21, v210, v211\n\t"
"v_mfma_f32_16x16x32_bf16 v[208:211], a[80:83], v[168:171], v[80:83]\n\t"
"v_exp_f32 v200, v200\n\t"
"v_mfma_f32_16x16x32_bf16 v[208:211], a[84:87], v[172:175], v[208:211]\n\t"
"v_exp_f32 v201, v201\n\t"
"v_mfma_f32_16x16x32_bf16 v[208:211], a[88:91], v[176:179], v[208:211]\n\t"
"v_exp_f32 v202, v202\n\t"
"v_mfma_f32_16x16x32_bf16 v[208:211], a[92:95], v[180:183], v[208:211]\n\t"
"ds_read_b128 v[196:199], v247 offset:41984\n\t"
"ds_read_b128 v[88:91], v93 offset:256\n\t"
"v_exp_f32 v203, v203\n\t"
"v_cvt_pk_bf16_f32 v22, v200, v201\n\t"
"v_cvt_pk_bf16_f32 v23, v202, v203\n\t"
"v_mfma_f32_16x16x32_bf16 v[200:203], a[96:99], v[168:171], v[80:83]\n\t"
"s_waitcnt lgkmcnt(0)\n\t"
"v_max_f32 v88, v88, v89\n\t"
"v_max3_f32 v88, v90, v91, v88\n\t"
"v_mfma_f32_16x16x32_bf16 v[200:203], a[100:103], v[172:175], v[200:203]\n\t"
"v_sub_f32 v84, 0, v88\n\t"
"v_sub_f32 v85, 0, v88\n\t"
"v_mfma_f32_16x16x32_bf16 v[200:203], a[104:107], v[176:179], v[200:203]\n\t"
"v_sub_f32 v86, 0, v88\n\t"
"v_sub_f32 v87, 0, v88\n\t"
"v_mfma_f32_16x16x32_bf16 v[200:203], a[108:111], v[180:183], v[200:203]\n\t"
"v_exp_f32 v204, v204\n\t"
"v_exp_f32 v205, v205\n\t"
"v_exp_f32 v206, v206\n\t"
"v_exp_f32 v207, v207\n\t"
"v_cvt_pk_bf16_f32 v24, v204, v205\n\t"
"v_cvt_pk_bf16_f32 v25, v206, v207\n\t"
"v_mfma_f32_16x16x32_bf16 v[212:215], v[216:219], v[20:23], v[212:215]\n\t"
"v_exp_f32 v208, v208\n\t"
"v_mfma_f32_16x16x32_bf16 v[204:207], a[112:115], v[168:171], v[80:83]\n\t"
"v_exp_f32 v209, v209\n\t"
"v_mfma_f32_16x16x32_bf16 v[204:207], a[116:119], v[172:175], v[204:207]\n\t"
"v_exp_f32 v210, v210\n\t"
"v_mfma_f32_16x16x32_bf16 v[204:207], a[120:123], v[176:179], v[204:207]\n\t"
"v_exp_f32 v211, v211\n\t"
"v_cvt_pk_bf16_f32 v26, v208, v209\n\t"
"v_mfma_f32_16x16x32_bf16 v[204:207], a[124:127], v[180:183], v[204:207]\n\t"
"s_barrier\n\t"
"v_cvt_pk_bf16_f32 v27, v210, v211\n\t"
"v_exp_f32 v200, v200\n\t"
"v_mfma_f32_16x16x32_bf16 v[208:211], a[0:3], v[184:187], v[84:87]\n\t"
"v_exp_f32 v201, v201\n\t"
"v_mfma_f32_16x16x32_bf16 v[208:211], a[4:7], v[188:191], v[208:211]\n\t"
"v_exp_f32 v202, v202\n\t"
"v_mfma_f32_16x16x32_bf16 v[208:211], a[8:11], v[192:195], v[208:211]\n\t"
"v_exp_f32 v203, v203\n\t"
"v_cvt_pk_bf16_f32 v28, v200, v201\n\t"
"v_mfma_f32_16x16x32_bf16 v[208:211], a[12:15], v[196:199], v[208:211]\n\t"
"v_cvt_pk_bf16_f32 v29, v202, v203\n\t"
"v_exp_f32 v204, v204\n\t"
"v_mfma_f32_16x16x32_bf16 v[212:215], v[216:219], v[24:27], v[212:215]\n\t"
"v_exp_f32 v205, v205\n\t"
"v_mfma_f32_16x16x32_bf16 v[200:203], a[16:19], v[184:187], v[84:87]\n\t"
"v_exp_f32 v206, v206\n\t"
"v_mfma_f32_16x16x32_bf16 v[200:203], a[20:23], v[188:191], v[200:203]\n\t"
"v_exp_f32 v207, v207\n\t"
"v_cvt_pk_bf16_f32 v30, v204, v205\n\t"
"v_mfma_f32_16x16x32_bf16 v[200:203], a[24:27], v[192:195], v[200:203]\n\t"
"v_cvt_pk_bf16_f32 v31, v206, v207\n\t"
"v_exp_f32 v208, v208\n\t"
"v_mfma_f32_16x16x32_bf16 v[200:203], a[28:31], v[196:199], v[200:203]\n\t"
"v_exp_f32 v209, v209\n\t"
"v_mfma_f32_16x16x32_bf16 v[204:207], a[32:35], v[184:187], v[84:87]\n\t"
"v_exp_f32 v210, v210\n\t"
"v_mfma_f32_16x16x32_bf16 v[204:207], a[36:39], v[188:191], v[204:207]\n\t"
"v_exp_f32 v211, v211\n\t"
"v_cvt_pk_bf16_f32 v32, v208, v209\n\t"
"v_mfma_f32_16x16x32_bf16 v[204:207], a[40:43], v[192:195], v[204:207]\n\t"
"v_cvt_pk_bf16_f32 v33, v210, v211\n\t"
"v_exp_f32 v200, v200\n\t"
"v_mfma_f32_16x16x32_bf16 v[204:207], a[44:47], v[196:199], v[204:207]\n\t"
"v_exp_f32 v201, v201\n\t"
"v_mfma_f32_16x16x32_bf16 v[212:215], v[216:219], v[28:31], v[212:215]\n\t"
"ds_read_b128 v[168:171], v246 offset:2048\n\t"
"v_exp_f32 v202, v202\n\t"
"v_mfma_f32_16x16x32_bf16 v[208:211], a[48:51], v[184:187], v[84:87]\n\t"
"v_exp_f32 v203, v203\n\t"
"v_mfma_f32_16x16x32_bf16 v[208:211], a[52:55], v[188:191], v[208:211]\n\t"
"v_cvt_pk_bf16_f32 v34, v200, v201\n\t"
"v_cvt_pk_bf16_f32 v35, v202, v203\n\t"
"v_mfma_f32_16x16x32_bf16 v[208:211], a[56:59], v[192:195], v[208:211]\n\t"
"ds_write_b32 v248, v212 offset:0\n\t"
"v_mov_b32 v4, s86\n\t"
"v_mfma_f32_16x16x32_bf16 v[208:211], a[60:63], v[196:199], v[208:211]\n\t"
"ds_read_b128 v[172:175], v246 offset:43008\n\t"
"v_exp_f32 v204, v204\n\t"
"v_mfma_f32_16x16x32_bf16 v[200:203], a[64:67], v[184:187], v[84:87]\n\t"
"v_exp_f32 v205, v205\n\t"
"v_mfma_f32_16x16x32_bf16 v[200:203], a[68:71], v[188:191], v[200:203]\n\t"
"v_exp_f32 v206, v206\n\t"
"v_mfma_f32_16x16x32_bf16 v[200:203], a[72:75], v[192:195], v[200:203]\n\t"
"v_exp_f32 v207, v207\n\t"
"v_cvt_pk_bf16_f32 v36, v204, v205\n\t"
"v_mfma_f32_16x16x32_bf16 v[200:203], a[76:79], v[196:199], v[200:203]\n\t"
"ds_read_b128 v[176:179], v247 offset:2048\n\t"
"v_cvt_pk_bf16_f32 v37, v206, v207\n\t"
"v_mfma_f32_16x16x32_bf16 v[212:215], v[216:219], v[32:35], 0\n\t"
"v_exp_f32 v208, v208\n\t"
"v_mov_b32 v229, s87\n\t"
"v_mfma_f32_16x16x32_bf16 v[204:207], a[80:83], v[184:187], v[84:87]\n\t"
"v_exp_f32 v209, v209\n\t"
"v_mfma_f32_16x16x32_bf16 v[204:207], a[84:87], v[188:191], v[204:207]\n\t"
"v_exp_f32 v210, v210\n\t"
"v_mov_b32 v5, 1073741824\n\t"
"v_mfma_f32_16x16x32_bf16 v[204:207], a[88:91], v[192:195], v[204:207]\n\t"
"ds_read_b128 v[180:183], v247 offset:43008\n\t"
"ds_read_b128 v[88:91], v93 offset:512\n\t"
"v_mfma_f32_16x16x32_bf16 v[204:207], a[92:95], v[196:199], v[204:207]\n\t"
"v_exp_f32 v211, v211\n\t"
"v_cvt_pk_bf16_f32 v38, v208, v209\n\t"
"v_cvt_pk_bf16_f32 v39, v210, v211\n\t"
"v_mfma_f32_16x16x32_bf16 v[208:211], a[96:99], v[184:187], v[84:87]\n\t"
"s_waitcnt lgkmcnt(0)\n\t"
"v_max_f32 v88, v88, v89\n\t"
"v_max3_f32 v88, v90, v91, v88\n\t"
"v_mfma_f32_16x16x32_bf16 v[208:211], a[100:103], v[188:191], v[208:211]\n\t"
"v_sub_f32 v80, 0, v88\n\t"
"v_sub_f32 v81, 0, v88\n\t"
"v_sub_f32 v82, 0, v88\n\t"
"v_sub_f32 v83, 0, v88\n\t"
"v_mfma_f32_16x16x32_bf16 v[208:211], a[104:107], v[192:195], v[208:211]\n\t"
"v_exp_f32 v200, v200\n\t"
"v_mfma_f32_16x16x32_bf16 v[208:211], a[108:111], v[196:199], v[208:211]\n\t"
"v_exp_f32 v201, v201\n\t"
"v_exp_f32 v202, v202\n\t"
"v_exp_f32 v203, v203\n\t"
"v_cvt_pk_bf16_f32 v40, v200, v201\n\t"
"v_cvt_pk_bf16_f32 v41, v202, v203\n\t"
"v_mfma_f32_16x16x32_bf16 v[212:215], v[216:219], v[36:39], v[212:215]\n\t"
"v_exp_f32 v204, v204\n\t"
"v_mfma_f32_16x16x32_bf16 v[200:203], a[112:115], v[184:187], v[84:87]\n\t"
"v_exp_f32 v205, v205\n\t"
"v_mfma_f32_16x16x32_bf16 v[200:203], a[116:119], v[188:191], v[200:203]\n\t"
"v_exp_f32 v206, v206\n\t"
"v_mfma_f32_16x16x32_bf16 v[200:203], a[120:123], v[192:195], v[200:203]\n\t"
"v_exp_f32 v207, v207\n\t"
"v_cvt_pk_bf16_f32 v42, v204, v205\n\t"
"v_mfma_f32_16x16x32_bf16 v[200:203], a[124:127], v[196:199], v[200:203]\n\t"
"s_barrier\n\t"
"v_cvt_pk_bf16_f32 v43, v206, v207\n\t"
"v_exp_f32 v208, v208\n\t"
"v_mfma_f32_16x16x32_bf16 v[204:207], a[0:3], v[168:171], v[80:83]\n\t"
"v_exp_f32 v209, v209\n\t"
"v_mfma_f32_16x16x32_bf16 v[204:207], a[4:7], v[172:175], v[204:207]\n\t"
"v_exp_f32 v210, v210\n\t"
"v_mfma_f32_16x16x32_bf16 v[204:207], a[8:11], v[176:179], v[204:207]\n\t"
"v_exp_f32 v211, v211\n\t"
"v_cvt_pk_bf16_f32 v44, v208, v209\n\t"
"v_mfma_f32_16x16x32_bf16 v[204:207], a[12:15], v[180:183], v[204:207]\n\t"
"v_cvt_pk_bf16_f32 v45, v210, v211\n\t"
"v_exp_f32 v200, v200\n\t"
"v_mfma_f32_16x16x32_bf16 v[212:215], v[216:219], v[40:43], v[212:215]\n\t"
"v_exp_f32 v201, v201\n\t"
"v_mfma_f32_16x16x32_bf16 v[208:211], a[16:19], v[168:171], v[80:83]\n\t"
"v_exp_f32 v202, v202\n\t"
"v_mfma_f32_16x16x32_bf16 v[208:211], a[20:23], v[172:175], v[208:211]\n\t"
"v_exp_f32 v203, v203\n\t"
"v_cvt_pk_bf16_f32 v46, v200, v201\n\t"
"v_mfma_f32_16x16x32_bf16 v[208:211], a[24:27], v[176:179], v[208:211]\n\t"
"v_cvt_pk_bf16_f32 v47, v202, v203\n\t"
"v_exp_f32 v204, v204\n\t"
"v_mfma_f32_16x16x32_bf16 v[208:211], a[28:31], v[180:183], v[208:211]\n\t"
"v_exp_f32 v205, v205\n\t"
"v_mfma_f32_16x16x32_bf16 v[200:203], a[32:35], v[168:171], v[80:83]\n\t"
"v_exp_f32 v206, v206\n\t"
"v_mfma_f32_16x16x32_bf16 v[200:203], a[36:39], v[172:175], v[200:203]\n\t"
"v_exp_f32 v207, v207\n\t"
"v_cvt_pk_bf16_f32 v48, v204, v205\n\t"
"v_mfma_f32_16x16x32_bf16 v[200:203], a[40:43], v[176:179], v[200:203]\n\t"
"v_cvt_pk_bf16_f32 v49, v206, v207\n\t"
"v_exp_f32 v208, v208\n\t"
"v_mfma_f32_16x16x32_bf16 v[200:203], a[44:47], v[180:183], v[200:203]\n\t"
"v_exp_f32 v209, v209\n\t"
"v_mfma_f32_16x16x32_bf16 v[212:215], v[216:219], v[44:47], v[212:215]\n\t"
"ds_read_b128 v[184:187], v246 offset:3072\n\t"
"v_exp_f32 v210, v210\n\t"
"v_mfma_f32_16x16x32_bf16 v[204:207], a[48:51], v[168:171], v[80:83]\n\t"
"v_exp_f32 v211, v211\n\t"
"v_mfma_f32_16x16x32_bf16 v[204:207], a[52:55], v[172:175], v[204:207]\n\t"
"v_cvt_pk_bf16_f32 v50, v208, v209\n\t"
"v_cvt_pk_bf16_f32 v51, v210, v211\n\t"
"v_mfma_f32_16x16x32_bf16 v[204:207], a[56:59], v[176:179], v[204:207]\n\t"
"ds_write_b32 v248, v212 offset:256\n\t"
"v_writelane_b32 v5, 0, 0\n\t"
"v_mfma_f32_16x16x32_bf16 v[204:207], a[60:63], v[180:183], v[204:207]\n\t"
"s_add_u32 s81, s26, 0\n\t"
"s_and_b32 s81, s81, 7\n\t"
"ds_read_b128 v[188:191], v246 offset:44032\n\t"
"v_exp_f32 v200, v200\n\t"
"v_mfma_f32_16x16x32_bf16 v[208:211], a[64:67], v[168:171], v[80:83]\n\t"
"s_lshl_b32 s81, s81, 10\n\t"
"ds_write_b128 v9, v[16:19] offset:0\n\t"
"v_mfma_f32_16x16x32_bf16 v[208:211], a[68:71], v[172:175], v[208:211]\n\t"
"v_exp_f32 v201, v201\n\t"
"v_mfma_f32_16x16x32_bf16 v[208:211], a[72:75], v[176:179], v[208:211]\n\t"
"v_exp_f32 v202, v202\n\t"
"v_mfma_f32_16x16x32_bf16 v[208:211], a[76:79], v[180:183], v[208:211]\n\t"
"ds_read_b128 v[192:195], v247 offset:3072\n\t"
"v_exp_f32 v203, v203\n\t"
"v_cvt_pk_bf16_f32 v52, v200, v201\n\t"
"v_cvt_pk_bf16_f32 v53, v202, v203\n\t"
"v_mfma_f32_16x16x32_bf16 v[212:215], v[216:219], v[48:51], 0\n\t"
"ds_write_b128 v9, v[20:23] offset:1024\n\t"
"v_mfma_f32_16x16x32_bf16 v[200:203], a[80:83], v[168:171], v[80:83]\n\t"
"v_exp_f32 v204, v204\n\t"
"v_mfma_f32_16x16x32_bf16 v[200:203], a[84:87], v[172:175], v[200:203]\n\t"
"v_exp_f32 v205, v205\n\t"
"v_mfma_f32_16x16x32_bf16 v[200:203], a[88:91], v[176:179], v[200:203]\n\t"
"ds_read_b128 v[196:199], v247 offset:44032\n\t"
"ds_read_b128 v[88:91], v93 offset:768\n\t"
"v_mfma_f32_16x16x32_bf16 v[200:203], a[92:95], v[180:183], v[200:203]\n\t"
"ds_write_b128 v9, v[24:27] offset:2048\n\t"
"v_exp_f32 v206, v206\n\t"
"v_exp_f32 v207, v207\n\t"
"v_cvt_pk_bf16_f32 v54, v204, v205\n\t"
"v_cvt_pk_bf16_f32 v55, v206, v207\n\t"
"v_mfma_f32_16x16x32_bf16 v[204:207], a[96:99], v[168:171], v[80:83]\n\t"
"s_waitcnt lgkmcnt(1)\n\t"
"v_max_f32 v88, v88, v89\n\t"
"v_max3_f32 v88, v90, v91, v88\n\t"
"v_mfma_f32_16x16x32_bf16 v[204:207], a[100:103], v[172:175], v[204:207]\n\t"
"v_sub_f32 v84, 0, v88\n\t"
"v_sub_f32 v85, 0, v88\n\t"
"v_sub_f32 v86, 0, v88\n\t"
"v_sub_f32 v87, 0, v88\n\t"
"v_mfma_f32_16x16x32_bf16 v[204:207], a[104:107], v[176:179], v[204:207]\n\t"
"ds_write_b128 v9, v[28:31] offset:3072\n\t"
"v_mfma_f32_16x16x32_bf16 v[204:207], a[108:111], v[180:183], v[204:207]\n\t"
"v_exp_f32 v208, v208\n\t"
"v_exp_f32 v209, v209\n\t"
"v_exp_f32 v210, v210\n\t"
"v_exp_f32 v211, v211\n\t"
"v_cvt_pk_bf16_f32 v56, v208, v209\n\t"
"v_cvt_pk_bf16_f32 v57, v210, v211\n\t"
"v_mfma_f32_16x16x32_bf16 v[212:215], v[216:219], v[52:55], v[212:215]\n\t"
"v_exp_f32 v200, v200\n\t"
"v_mfma_f32_16x16x32_bf16 v[208:211], a[112:115], v[168:171], v[80:83]\n\t"
"v_exp_f32 v201, v201\n\t"
"v_mfma_f32_16x16x32_bf16 v[208:211], a[116:119], v[172:175], v[208:211]\n\t"
"v_exp_f32 v202, v202\n\t"
"v_mfma_f32_16x16x32_bf16 v[208:211], a[120:123], v[176:179], v[208:211]\n\t"
"v_exp_f32 v203, v203\n\t"
"v_cvt_pk_bf16_f32 v58, v200, v201\n\t"
"v_mfma_f32_16x16x32_bf16 v[208:211], a[124:127], v[180:183], v[208:211]\n\t"
"s_waitcnt lgkmcnt(0)\n\t"
"s_barrier\n\t"
"v_cvt_pk_bf16_f32 v59, v202, v203\n\t"
"v_exp_f32 v204, v204\n\t"
"v_mfma_f32_16x16x32_bf16 v[200:203], a[0:3], v[184:187], v[84:87]\n\t"
"ds_read_b128 v[128:131], v6 offset:0\n\t"
"v_exp_f32 v205, v205\n\t"
"v_mfma_f32_16x16x32_bf16 v[200:203], a[4:7], v[188:191], v[200:203]\n\t"
"ds_read_b128 v[132:135], v6 offset:1024\n\t"
"v_exp_f32 v206, v206\n\t"
"v_mfma_f32_16x16x32_bf16 v[200:203], a[8:11], v[192:195], v[200:203]\n\t"
"ds_read_b128 v[136:139], v6 offset:2048\n\t"
"v_exp_f32 v207, v207\n\t"
"v_mfma_f32_16x16x32_bf16 v[200:203], a[12:15], v[196:199], v[200:203]\n\t"
"v_cvt_pk_bf16_f32 v60, v204, v205\n\t"
"v_cvt_pk_bf16_f32 v61, v206, v207\n\t"
"v_mfma_f32_16x16x32_bf16 v[212:215], v[216:219], v[56:59], v[212:215]\n\t"
"ds_read_b128 v[140:143], v6 offset:3072\n\t"
"v_exp_f32 v208, v208\n\t"
"v_mfma_f32_16x16x32_bf16 v[152:155], a[128:131], v[16:19], 0\n\t"
"ds_read_b128 v[144:147], v7 offset:0\n\t"
"v_exp_f32 v209, v209\n\t"
"v_mfma_f32_16x16x32_bf16 v[152:155], a[132:135], v[20:23], v[152:155]\n\t"
"ds_read_b128 v[148:151], v7 offset:1024\n\t"
"v_exp_f32 v210, v210\n\t"
"v_mfma_f32_16x16x32_bf16 v[152:155], a[136:139], v[24:27], v[152:155]\n\t"
"v_exp_f32 v211, v211\n\t"
"v_mfma_f32_16x16x32_bf16 v[152:155], a[140:143], v[28:31], v[152:155]\n\t"
"v_cvt_pk_bf16_f32 v62, v208, v209\n\t"
"v_cvt_pk_bf16_f32 v63, v210, v211\n\t"
"v_mfma_f32_16x16x32_bf16 v[204:207], a[16:19], v[184:187], v[84:87]\n\t"
"v_add_u32 v232, s81, v250\n\t"
"s_add_u32 s81, s26, 2\n\t"
"s_and_b32 s81, s81, 7\n\t"
"v_exp_f32 v200, v200\n\t"
"v_mfma_f32_16x16x32_bf16 v[204:207], a[20:23], v[188:191], v[204:207]\n\t"
"s_lshl_b32 s81, s81, 10\n\t"
"v_add_u32 v233, s81, v250\n\t"
"v_exp_f32 v201, v201\n\t"
"v_mfma_f32_16x16x32_bf16 v[204:207], a[24:27], v[192:195], v[204:207]\n\t"
"s_add_u32 s81, s26, 4\n\t"
"s_and_b32 s81, s81, 7\n\t"
"v_exp_f32 v202, v202\n\t"
"v_mfma_f32_16x16x32_bf16 v[204:207], a[28:31], v[196:199], v[204:207]\n\t"
"s_lshl_b32 s81, s81, 10\n\t"
"v_add_u32 v234, s81, v250\n\t"
"v_exp_f32 v203, v203\n\t"
"v_mfma_f32_16x16x32_bf16 v[156:159], a[192:195], v[16:19], 0\n\t"
"s_add_u32 s81, s26, 6\n\t"
"s_and_b32 s81, s81, 7\n\t"
"v_cvt_pk_bf16_f32 v64, v200, v201\n\t"
"v_cvt_pk_bf16_f32 v65, v202, v203\n\t"
"v_mfma_f32_16x16x32_bf16 v[156:159], a[196:199], v[20:23], v[156:159]\n\t"
"s_lshl_b32 s81, s81, 10\n\t"
"ds_write_b128 v9, v[32:35] offset:4096\n\t"
"v_mfma_f32_16x16x32_bf16 v[156:159], a[200:203], v[24:27], v[156:159]\n\t"
"v_add_u32 v235, s81, v250\n\t"
"v_exp_f32 v204, v204\n\t"
"v_mfma_f32_16x16x32_bf16 v[156:159], a[204:207], v[28:31], v[156:159]\n\t"
"v_exp_f32 v205, v205\n\t"
"v_mfma_f32_16x16x32_bf16 v[208:211], a[32:35], v[184:187], v[84:87]\n\t"
"v_exp_f32 v206, v206\n\t"
"v_mfma_f32_16x16x32_bf16 v[208:211], a[36:39], v[188:191], v[208:211]\n\t"
"ds_write_b128 v9, v[36:39] offset:5120\n\t"
"v_mfma_f32_16x16x32_bf16 v[208:211], a[40:43], v[192:195], v[208:211]\n\t"
"v_exp_f32 v207, v207\n\t"
"v_mfma_f32_16x16x32_bf16 v[208:211], a[44:47], v[196:199], v[208:211]\n\t"
"v_cvt_pk_bf16_f32 v66, v204, v205\n\t"
"v_cvt_pk_bf16_f32 v67, v206, v207\n\t"
"v_mfma_f32_16x16x32_bf16 v[212:215], v[216:219], v[60:63], v[212:215]\n\t"
"s_waitcnt lgkmcnt(7)\n\t"
"v_xor_b32 v238, 64, v236\n\t"
"v_xor_b32 v239, 96, v236\n\t"
"v_mfma_f32_16x16x32_bf16 v[152:155], a[144:147], v[128:131], v[152:155]\n\t"
"s_waitcnt lgkmcnt(6)\n\t"
"ds_write_b128 v9, v[40:43] offset:6144\n\t"
"v_mfma_f32_16x16x32_bf16 v[152:155], a[148:151], v[132:135], v[152:155]\n\t"
"s_waitcnt lgkmcnt(6)\n\t"
"v_xor_b32 v240, 128, v236\n\t"
"v_xor_b32 v241, 160, v236\n\t"
"v_mfma_f32_16x16x32_bf16 v[152:155], a[152:155], v[136:139], v[152:155]\n\t"
"s_waitcnt lgkmcnt(5)\n\t"
"v_xor_b32 v242, 192, v236\n\t"
"v_xor_b32 v243, 224, v236\n\t"
"v_mfma_f32_16x16x32_bf16 v[152:155], a[156:159], v[140:143], v[152:155]\n\t"
"v_exp_f32 v208, v208\n\t"
"v_mfma_f32_16x16x32_bf16 v[200:203], a[48:51], v[184:187], v[84:87]\n\t"
"ds_read_b128 v[168:171], v246 offset:4096\n\t"
"v_exp_f32 v209, v209\n\t"
"v_mfma_f32_16x16x32_bf16 v[200:203], a[52:55], v[188:191], v[200:203]\n\t"
"ds_write_b128 v9, v[44:47] offset:7168\n\t"
"v_mfma_f32_16x16x32_bf16 v[200:203], a[56:59], v[192:195], v[200:203]\n\t"
"v_exp_f32 v210, v210\n\t"
"v_mfma_f32_16x16x32_bf16 v[200:203], a[60:63], v[196:199], v[200:203]\n\t"
"ds_read_b128 v[172:175], v246 offset:45056\n\t"
"v_exp_f32 v211, v211\n\t"
"v_mfma_f32_16x16x32_bf16 v[156:159], a[208:211], v[128:131], v[156:159]\n\t"
"ds_write_b32 v248, v212 offset:512\n\t"
"v_mfma_f32_16x16x32_bf16 v[156:159], a[212:215], v[132:135], v[156:159]\n\t"
"ds_read_b128 v[128:131], v7 offset:2048\n\t"
"v_cvt_pk_bf16_f32 v68, v208, v209\n\t"
"v_mfma_f32_16x16x32_bf16 v[156:159], a[216:219], v[136:139], v[156:159]\n\t"
"ds_read_b128 v[132:135], v7 offset:3072\n\t"
"v_cvt_pk_bf16_f32 v69, v210, v211\n\t"
"v_mfma_f32_16x16x32_bf16 v[156:159], a[220:223], v[140:143], v[156:159]\n\t"
"ds_read_b128 v[136:139], v8 offset:0\n\t"
"v_exp_f32 v200, v200\n\t"
"v_mfma_f32_16x16x32_bf16 v[204:207], a[64:67], v[184:187], v[84:87]\n\t"
"ds_read_b128 v[140:143], v8 offset:1024\n\t"
"v_exp_f32 v201, v201\n\t"
"v_mfma_f32_16x16x32_bf16 v[204:207], a[68:71], v[188:191], v[204:207]\n\t"
"v_exp_f32 v202, v202\n\t"
"v_mfma_f32_16x16x32_bf16 v[204:207], a[72:75], v[192:195], v[204:207]\n\t"
"v_exp_f32 v203, v203\n\t"
"v_mfma_f32_16x16x32_bf16 v[204:207], a[76:79], v[196:199], v[204:207]\n\t"
"ds_read_b128 v[176:179], v247 offset:4096\n\t"
"v_cvt_pk_bf16_f32 v70, v200, v201\n\t"
"v_mfma_f32_16x16x32_bf16 v[212:215], v[216:219], v[64:67], 0\n\t"
"s_waitcnt lgkmcnt(13)\n\t"
"v_cvt_pk_bf16_f32 v71, v202, v203\n\t"
"v_mfma_f32_16x16x32_bf16 v[152:155], a[160:163], v[144:147], v[152:155]\n\t"
"s_waitcnt lgkmcnt(12)\n\t"
"v_mfma_f32_16x16x32_bf16 v[152:155], a[164:167], v[148:151], v[152:155]\n\t"
"s_waitcnt lgkmcnt(4)\n\t"
"v_mfma_f32_16x16x32_bf16 v[152:155], a[168:171], v[128:131], v[152:155]\n\t"
"s_waitcnt lgkmcnt(3)\n\t"
"v_mfma_f32_16x16x32_bf16 v[152:155], a[172:175], v[132:135], v[152:155]\n\t"
"v_exp_f32 v204, v204\n\t"
"v_mfma_f32_16x16x32_bf16 v[208:211], a[80:83], v[184:187], v[84:87]\n\t"
"v_exp_f32 v205, v205\n\t"
"v_mfma_f32_16x16x32_bf16 v[208:211], a[84:87], v[188:191], v[208:211]\n\t"
"v_exp_f32 v206, v206\n\t"
"v_mfma_f32_16x16x32_bf16 v[208:211], a[88:91], v[192:195], v[208:211]\n\t"
"ds_read_b128 v[180:183], v247 offset:45056\n\t"
"ds_read_b128 v[88:91], v93 offset:1024\n\t"
"s_waitcnt lgkmcnt(0)\n\t"
"v_max_f32 v88, v88, v89\n\t"
"v_max3_f32 v88, v90, v91, v88\n\t"
"v_sub_f32 v80, 0, v88\n\t"
"v_sub_f32 v81, 0, v88\n\t"
"v_sub_f32 v82, 0, v88\n\t"
"v_sub_f32 v83, 0, v88\n\t"
"v_mfma_f32_16x16x32_bf16 v[208:211], a[92:95], v[196:199], v[208:211]\n\t"
"v_exp_f32 v207, v207\n\t"
"v_mfma_f32_16x16x32_bf16 v[156:159], a[224:227], v[144:147], v[156:159]\n\t"
"ds_read_b128 v[144:147], v8 offset:2048\n\t"
"v_cvt_pk_bf16_f32 v72, v204, v205\n\t"
"v_mfma_f32_16x16x32_bf16 v[156:159], a[228:231], v[148:151], v[156:159]\n\t"
"ds_read_b128 v[148:151], v8 offset:3072\n\t"
"v_cvt_pk_bf16_f32 v73, v206, v207\n\t"
"v_mfma_f32_16x16x32_bf16 v[156:159], a[232:235], v[128:131], v[156:159]\n\t"
"v_exp_f32 v208, v208\n\t"
"v_mfma_f32_16x16x32_bf16 v[156:159], a[236:239], v[132:135], v[156:159]\n\t"
"v_exp_f32 v209, v209\n\t"
"v_mfma_f32_16x16x32_bf16 v[200:203], a[96:99], v[184:187], v[84:87]\n\t"
"ds_read_b128 v[220:223], v249 offset:0\n\t"
"v_exp_f32 v210, v210\n\t"
"v_mfma_f32_16x16x32_bf16 v[200:203], a[100:103], v[188:191], v[200:203]\n\t"
"v_exp_f32 v211, v211\n\t"
"v_mfma_f32_16x16x32_bf16 v[200:203], a[104:107], v[192:195], v[200:203]\n\t"
"v_cvt_pk_bf16_f32 v74, v208, v209\n\t"
"v_cvt_pk_bf16_f32 v75, v210, v211\n\t"
"v_mfma_f32_16x16x32_bf16 v[200:203], a[108:111], v[196:199], v[200:203]\n\t"
"v_mfma_f32_16x16x32_bf16 v[212:215], v[216:219], v[68:71], v[212:215]\n\t"
"v_mfma_f32_16x16x32_bf16 v[152:155], a[176:179], v[136:139], v[152:155]\n\t"
"s_waitcnt lgkmcnt(0)\n\t"
"v_add_f32 v220, v220, v221\n\t"
"v_add_f32 v222, v222, v223\n\t"
"v_add_f32 v220, v220, v222\n\t"
"v_mfma_f32_16x16x32_bf16 v[152:155], a[180:183], v[140:143], v[152:155]\n\t"
"v_rcp_f32 v230, v220\n\t"
"v_mfma_f32_16x16x32_bf16 v[152:155], a[184:187], v[144:147], v[152:155]\n\t"
"v_exp_f32 v200, v200\n\t"
"v_mfma_f32_16x16x32_bf16 v[152:155], a[188:191], v[148:151], v[152:155]\n\t"
"v_exp_f32 v201, v201\n\t"
"v_mfma_f32_16x16x32_bf16 v[204:207], a[112:115], v[184:187], v[84:87]\n\t"
"v_exp_f32 v202, v202\n\t"
"v_mfma_f32_16x16x32_bf16 v[204:207], a[116:119], v[188:191], v[204:207]\n\t"
"v_exp_f32 v203, v203\n\t"
"v_cvt_pk_bf16_f32 v76, v200, v201\n\t"
"v_mfma_f32_16x16x32_bf16 v[204:207], a[120:123], v[192:195], v[204:207]\n\t"
"v_cvt_pk_bf16_f32 v77, v202, v203\n\t"
"v_mfma_f32_16x16x32_bf16 v[204:207], a[124:127], v[196:199], v[204:207]\n\t"
"v_mfma_f32_16x16x32_bf16 v[156:159], a[240:243], v[136:139], v[156:159]\n\t"
"v_mfma_f32_16x16x32_bf16 v[156:159], a[244:247], v[140:143], v[156:159]\n\t"
"v_mfma_f32_16x16x32_bf16 v[156:159], a[248:251], v[144:147], v[156:159]\n\t"
"v_mfma_f32_16x16x32_bf16 v[156:159], a[252:255], v[148:151], v[156:159]\n\t"
"s_barrier\n\t"
"ds_read_b128 v[128:131], v6 offset:4096\n\t"
"v_mfma_f32_16x16x32_bf16 v[208:211], a[0:3], v[168:171], v[80:83]\n\t"
"ds_read_b128 v[132:135], v6 offset:5120\n\t"
"v_exp_f32 v204, v204\n\t"
"v_mfma_f32_16x16x32_bf16 v[208:211], a[4:7], v[172:175], v[208:211]\n\t"
"ds_read_b128 v[136:139], v6 offset:6144\n\t"
"v_exp_f32 v205, v205\n\t"
"v_mfma_f32_16x16x32_bf16 v[208:211], a[8:11], v[176:179], v[208:211]\n\t"
"s_add_u32 m0, s72, 24576\n\t"
"ds_read_b128 v[140:143], v6 offset:7168\n\t"
"v_exp_f32 v206, v206\n\t"
"v_mfma_f32_16x16x32_bf16 v[208:211], a[12:15], v[180:183], v[208:211]\n\t"
"ds_read_b128 v[144:147], v7 offset:4096\n\t"
"buffer_load_dwordx4 v10, s[52:55], s77 offen lds\n\t"
"v_mfma_f32_16x16x32_bf16 v[212:215], v[216:219], v[72:75], v[212:215]\n\t"
"ds_read_b128 v[148:151], v7 offset:5120\n\t"
"v_exp_f32 v207, v207\n\t"
"v_mfma_f32_16x16x32_bf16 v[160:163], a[128:131], v[32:35], 0\n\t"
"v_cvt_pk_bf16_f32 v78, v204, v205\n\t"
"v_cvt_pk_bf16_f32 v79, v206, v207\n\t"
"v_mfma_f32_16x16x32_bf16 v[160:163], a[132:135], v[36:39], v[160:163]\n\t"
"v_exp_f32 v208, v208\n\t"
"v_mfma_f32_16x16x32_bf16 v[160:163], a[136:139], v[40:43], v[160:163]\n\t"
"v_exp_f32 v209, v209\n\t"
"v_mfma_f32_16x16x32_bf16 v[160:163], a[140:143], v[44:47], v[160:163]\n\t"
"v_exp_f32 v210, v210\n\t"
"v_mfma_f32_16x16x32_bf16 v[200:203], a[16:19], v[168:171], v[80:83]\n\t"
"buffer_load_dwordx4 v11, s[52:55], s77 offen offset:1024 lds\n\t"
"v_mfma_f32_16x16x32_bf16 v[200:203], a[20:23], v[172:175], v[200:203]\n\t"
"v_exp_f32 v211, v211\n\t"
"v_mfma_f32_16x16x32_bf16 v[200:203], a[24:27], v[176:179], v[200:203]\n\t"
"v_cvt_pk_bf16_f32 v16, v208, v209\n\t"
"v_cvt_pk_bf16_f32 v17, v210, v211\n\t"
"v_mfma_f32_16x16x32_bf16 v[200:203], a[28:31], v[180:183], v[200:203]\n\t"
"v_mul_f32 v152, v152, v230\n\t"
"v_mul_f32 v153, v153, v230\n\t"
"v_mfma_f32_16x16x32_bf16 v[164:167], a[192:195], v[32:35], 0\n\t"
"v_mul_f32 v154, v154, v230\n\t"
"v_mul_f32 v155, v155, v230\n\t"
"v_mfma_f32_16x16x32_bf16 v[164:167], a[196:199], v[36:39], v[164:167]\n\t"
"ds_write_b128 v9, v[48:51] offset:0\n\t"
"v_mfma_f32_16x16x32_bf16 v[164:167], a[200:203], v[40:43], v[164:167]\n\t"
"buffer_load_dwordx4 v12, s[52:55], s77 offen offset:2048 lds\n\t"
"v_mfma_f32_16x16x32_bf16 v[164:167], a[204:207], v[44:47], v[164:167]\n\t"
"v_mul_f32 v156, v156, v230\n\t"
"v_mul_f32 v157, v157, v230\n\t"
"v_mfma_f32_16x16x32_bf16 v[204:207], a[32:35], v[168:171], v[80:83]\n\t"
"v_mul_f32 v158, v158, v230\n\t"
"v_mul_f32 v159, v159, v230\n\t"
"v_mfma_f32_16x16x32_bf16 v[204:207], a[36:39], v[172:175], v[204:207]\n\t"
"ds_write_b128 v9, v[52:55] offset:1024\n\t"
"v_mfma_f32_16x16x32_bf16 v[204:207], a[40:43], v[176:179], v[204:207]\n\t"
"v_cvt_pk_bf16_f32 v224, v152, v153\n\t"
"v_cvt_pk_bf16_f32 v225, v154, v155\n\t"
"v_mfma_f32_16x16x32_bf16 v[204:207], a[44:47], v[180:183], v[204:207]\n\t"
"v_cvt_pk_bf16_f32 v226, v156, v157\n\t"
"v_cvt_pk_bf16_f32 v227, v158, v159\n\t"
"v_mfma_f32_16x16x32_bf16 v[212:215], v[216:219], v[76:79], v[212:215]\n\t"
"s_waitcnt lgkmcnt(7)\n\t"
"buffer_load_dwordx4 v13, s[52:55], s77 offen offset:3072 lds\n\t"
"v_mfma_f32_16x16x32_bf16 v[160:163], a[144:147], v[128:131], v[160:163]\n\t"
"s_waitcnt lgkmcnt(6)\n\t"
"s_add_u32 s77, s77, 32768\n\t"
"ds_write_b128 v9, v[56:59] offset:2048\n\t"
"v_mfma_f32_16x16x32_bf16 v[160:163], a[148:151], v[132:135], v[160:163]\n\t"
"s_waitcnt lgkmcnt(6)\n\t"
"s_cmp_gt_u32 s10, 0\n\t"
"s_cselect_b32 s78, 1073741824, 0\n\t"
"v_add_u32 v94, s78, v250\n\t"
"v_exp_f32 v200, v200\n\t"
"v_mfma_f32_16x16x32_bf16 v[160:163], a[152:155], v[136:139], v[160:163]\n\t"
"s_waitcnt lgkmcnt(5)\n\t"
"buffer_store_dwordx4 v[224:227], v94, s[56:59], s98 offen sc1\n\t"
"v_mfma_f32_16x16x32_bf16 v[160:163], a[156:159], v[140:143], v[160:163]\n\t"
"s_add_u32 s98, s98, 32768\n\t"
"v_exp_f32 v201, v201\n\t"
"v_mfma_f32_16x16x32_bf16 v[208:211], a[48:51], v[168:171], v[80:83]\n\t"
"ds_read_b128 v[184:187], v246 offset:5120\n\t"
"v_exp_f32 v202, v202\n\t"
"v_mfma_f32_16x16x32_bf16 v[208:211], a[52:55], v[172:175], v[208:211]\n\t"
"ds_write_b128 v9, v[60:63] offset:3072\n\t"
"v_mfma_f32_16x16x32_bf16 v[208:211], a[56:59], v[176:179], v[208:211]\n\t"
"v_exp_f32 v203, v203\n\t"
"v_mfma_f32_16x16x32_bf16 v[208:211], a[60:63], v[180:183], v[208:211]\n\t"
"ds_read_b128 v[188:191], v246 offset:46080\n\t"
"v_cvt_pk_bf16_f32 v18, v200, v201\n\t"
"v_cvt_pk_bf16_f32 v19, v202, v203\n\t"
"v_mfma_f32_16x16x32_bf16 v[164:167], a[208:211], v[128:131], v[164:167]\n\t"
"ds_write_b32 v248, v212 offset:768\n\t"
"v_mfma_f32_16x16x32_bf16 v[164:167], a[212:215], v[132:135], v[164:167]\n\t"
"ds_read_b128 v[128:131], v7 offset:6144\n\t"
"v_exp_f32 v204, v204\n\t"
"v_mfma_f32_16x16x32_bf16 v[164:167], a[216:219], v[136:139], v[164:167]\n\t"
"ds_read_b128 v[132:135], v7 offset:7168\n\t"
"v_exp_f32 v205, v205\n\t"
"v_mfma_f32_16x16x32_bf16 v[164:167], a[220:223], v[140:143], v[164:167]\n\t"
"ds_read_b128 v[136:139], v8 offset:4096\n\t"
"v_exp_f32 v206, v206\n\t"
"v_mfma_f32_16x16x32_bf16 v[200:203], a[64:67], v[168:171], v[80:83]\n\t"
"ds_read_b128 v[140:143], v8 offset:5120\n\t"
"v_exp_f32 v207, v207\n\t"
"v_mfma_f32_16x16x32_bf16 v[200:203], a[68:71], v[172:175], v[200:203]\n\t"
"v_cvt_pk_bf16_f32 v20, v204, v205\n\t"
"v_cvt_pk_bf16_f32 v21, v206, v207\n\t"
"v_mfma_f32_16x16x32_bf16 v[200:203], a[72:75], v[176:179], v[200:203]\n\t"
"v_exp_f32 v208, v208\n\t"
"v_mfma_f32_16x16x32_bf16 v[200:203], a[76:79], v[180:183], v[200:203]\n\t"
"ds_read_b128 v[192:195], v247 offset:5120\n\t"
"v_exp_f32 v209, v209\n\t"
"v_mfma_f32_16x16x32_bf16 v[212:215], v[216:219], v[16:19], 0\n\t"
"s_waitcnt lgkmcnt(13)\n\t"
"v_exp_f32 v210, v210\n\t"
"v_mfma_f32_16x16x32_bf16 v[160:163], a[160:163], v[144:147], v[160:163]\n\t"
"s_waitcnt lgkmcnt(12)\n\t"
"s_add_u32 m0, s72, 28672\n\t"
"v_exp_f32 v211, v211\n\t"
"v_mfma_f32_16x16x32_bf16 v[160:163], a[164:167], v[148:151], v[160:163]\n\t"
"s_waitcnt lgkmcnt(4)\n\t"
"buffer_load_dwordx4 v10, s[52:55], s77 offen lds\n\t"
"v_mfma_f32_16x16x32_bf16 v[160:163], a[168:171], v[128:131], v[160:163]\n\t"
"s_waitcnt lgkmcnt(3)\n\t"
"v_cvt_pk_bf16_f32 v22, v208, v209\n\t"
"v_cvt_pk_bf16_f32 v23, v210, v211\n\t"
"v_mfma_f32_16x16x32_bf16 v[160:163], a[172:175], v[132:135], v[160:163]\n\t"
"v_exp_f32 v200, v200\n\t"
"v_mfma_f32_16x16x32_bf16 v[204:207], a[80:83], v[168:171], v[80:83]\n\t"
"v_exp_f32 v201, v201\n\t"
"v_mfma_f32_16x16x32_bf16 v[204:207], a[84:87], v[172:175], v[204:207]\n\t"
"v_exp_f32 v202, v202\n\t"
"v_mfma_f32_16x16x32_bf16 v[204:207], a[88:91], v[176:179], v[204:207]\n\t"
"ds_read_b128 v[196:199], v247 offset:46080\n\t"
"ds_read_b128 v[88:91], v93 offset:40960\n\t"
"s_waitcnt lgkmcnt(0)\n\t"
"v_max_f32 v88, v88, v89\n\t"
"v_max3_f32 v88, v90, v91, v88\n\t"
"v_sub_f32 v84, 0, v88\n\t"
"v_sub_f32 v85, 0, v88\n\t"
"v_sub_f32 v86, 0, v88\n\t"
"v_sub_f32 v87, 0, v88\n\t"
"v_mfma_f32_16x16x32_bf16 v[204:207], a[92:95], v[180:183], v[204:207]\n\t"
"buffer_load_dwordx4 v11, s[52:55], s77 offen offset:1024 lds\n\t"
"v_mfma_f32_16x16x32_bf16 v[164:167], a[224:227], v[144:147], v[164:167]\n\t"
"ds_read_b128 v[144:147], v8 offset:6144\n\t"
"v_exp_f32 v203, v203\n\t"
"v_mfma_f32_16x16x32_bf16 v[164:167], a[228:231], v[148:151], v[164:167]\n\t"
"ds_read_b128 v[148:151], v8 offset:7168\n\t"
"v_cvt_pk_bf16_f32 v24, v200, v201\n\t"
"v_mfma_f32_16x16x32_bf16 v[164:167], a[232:235], v[128:131], v[164:167]\n\t"
"v_cvt_pk_bf16_f32 v25, v202, v203\n\t"
"v_exp_f32 v204, v204\n\t"
"v_mfma_f32_16x16x32_bf16 v[164:167], a[236:239], v[132:135], v[164:167]\n\t"
"v_exp_f32 v205, v205\n\t"
"v_mfma_f32_16x16x32_bf16 v[208:211], a[96:99], v[168:171], v[80:83]\n\t"
"ds_read_b128 v[220:223], v249 offset:256\n\t"
"v_exp_f32 v206, v206\n\t"
"v_mfma_f32_16x16x32_bf16 v[208:211], a[100:103], v[172:175], v[208:211]\n\t"
"buffer_load_dwordx4 v12, s[52:55], s77 offen offset:2048 lds\n\t"
"v_mfma_f32_16x16x32_bf16 v[208:211], a[104:107], v[176:179], v[208:211]\n\t"
"v_exp_f32 v207, v207\n\t"
"v_mfma_f32_16x16x32_bf16 v[208:211], a[108:111], v[180:183], v[208:211]\n\t"
"v_cvt_pk_bf16_f32 v26, v204, v205\n\t"
"v_cvt_pk_bf16_f32 v27, v206, v207\n\t"
"v_mfma_f32_16x16x32_bf16 v[212:215], v[216:219], v[20:23], v[212:215]\n\t"
"v_mfma_f32_16x16x32_bf16 v[160:163], a[176:179], v[136:139], v[160:163]\n\t"
"s_waitcnt lgkmcnt(0)\n\t"
"v_add_f32 v220, v220, v221\n\t"
"v_add_f32 v222, v222, v223\n\t"
"v_add_f32 v220, v220, v222\n\t"
"v_mfma_f32_16x16x32_bf16 v[160:163], a[180:183], v[140:143], v[160:163]\n\t"
"v_rcp_f32 v230, v220\n\t"
"v_mfma_f32_16x16x32_bf16 v[160:163], a[184:187], v[144:147], v[160:163]\n\t"
"buffer_load_dwordx4 v13, s[52:55], s77 offen offset:3072 lds\n\t"
"v_mfma_f32_16x16x32_bf16 v[160:163], a[188:191], v[148:151], v[160:163]\n\t"
"s_add_u32 s77, s77, 229376\n\t"
"v_exp_f32 v208, v208\n\t"
"v_mfma_f32_16x16x32_bf16 v[200:203], a[112:115], v[168:171], v[80:83]\n\t"
"s_and_b32 s77, s77, 2097151\n\t"
"v_exp_f32 v209, v209\n\t"
"v_mfma_f32_16x16x32_bf16 v[200:203], a[116:119], v[172:175], v[200:203]\n\t"
"v_exp_f32 v210, v210\n\t"
"v_mfma_f32_16x16x32_bf16 v[200:203], a[120:123], v[176:179], v[200:203]\n\t"
"v_exp_f32 v211, v211\n\t"
"v_cvt_pk_bf16_f32 v28, v208, v209\n\t"
"v_mfma_f32_16x16x32_bf16 v[200:203], a[124:127], v[180:183], v[200:203]\n\t"
"v_cvt_pk_bf16_f32 v29, v210, v211\n\t"
"v_mfma_f32_16x16x32_bf16 v[164:167], a[240:243], v[136:139], v[164:167]\n\t"
"v_mfma_f32_16x16x32_bf16 v[164:167], a[244:247], v[140:143], v[164:167]\n\t"
"v_mfma_f32_16x16x32_bf16 v[164:167], a[248:251], v[144:147], v[164:167]\n\t"
"v_mfma_f32_16x16x32_bf16 v[164:167], a[252:255], v[148:151], v[164:167]\n\t"
"s_barrier\n\t"
"ds_read_b128 v[128:131], v6 offset:0\n\t"
"v_mfma_f32_16x16x32_bf16 v[204:207], a[0:3], v[184:187], v[84:87]\n\t"
"ds_read_b128 v[132:135], v6 offset:1024\n\t"
"v_exp_f32 v200, v200\n\t"
"v_mfma_f32_16x16x32_bf16 v[204:207], a[4:7], v[188:191], v[204:207]\n\t"
"ds_read_b128 v[136:139], v6 offset:2048\n\t"
"v_exp_f32 v201, v201\n\t"
"v_mfma_f32_16x16x32_bf16 v[204:207], a[8:11], v[192:195], v[204:207]\n\t"
"ds_read_b128 v[140:143], v6 offset:3072\n\t"
"v_exp_f32 v202, v202\n\t"
"v_mfma_f32_16x16x32_bf16 v[204:207], a[12:15], v[196:199], v[204:207]\n\t"
"ds_read_b128 v[144:147], v7 offset:0\n\t"
"v_exp_f32 v203, v203\n\t"
"v_mfma_f32_16x16x32_bf16 v[212:215], v[216:219], v[24:27], v[212:215]\n\t"
"ds_read_b128 v[148:151], v7 offset:1024\n\t"
"v_cvt_pk_bf16_f32 v30, v200, v201\n\t"
"v_mfma_f32_16x16x32_bf16 v[152:155], a[128:131], v[48:51], 0\n\t"
"v_cvt_pk_bf16_f32 v31, v202, v203\n\t"
"v_mul_f32 v160, v160, v230\n\t"
"v_mfma_f32_16x16x32_bf16 v[152:155], a[132:135], v[52:55], v[152:155]\n\t"
"v_exp_f32 v204, v204\n\t"
"v_mfma_f32_16x16x32_bf16 v[152:155], a[136:139], v[56:59], v[152:155]\n\t"
"v_exp_f32 v205, v205\n\t"
"v_mfma_f32_16x16x32_bf16 v[152:155], a[140:143], v[60:63], v[152:155]\n\t"
"v_exp_f32 v206, v206\n\t"
"v_mfma_f32_16x16x32_bf16 v[208:211], a[16:19], v[184:187], v[84:87]\n\t"
"s_add_u32 m0, s72, 32768\n\t"
"v_exp_f32 v207, v207\n\t"
"v_mfma_f32_16x16x32_bf16 v[208:211], a[20:23], v[188:191], v[208:211]\n\t"
"buffer_load_dwordx4 v10, s[52:55], s77 offen lds\n\t"
"v_mfma_f32_16x16x32_bf16 v[208:211], a[24:27], v[192:195], v[208:211]\n\t"
"v_cvt_pk_bf16_f32 v32, v204, v205\n\t"
"v_cvt_pk_bf16_f32 v33, v206, v207\n\t"
"v_mfma_f32_16x16x32_bf16 v[208:211], a[28:31], v[196:199], v[208:211]\n\t"
"v_mul_f32 v161, v161, v230\n\t"
"v_mul_f32 v162, v162, v230\n\t"
"v_mfma_f32_16x16x32_bf16 v[156:159], a[192:195], v[48:51], 0\n\t"
"v_mul_f32 v163, v163, v230\n\t"
"v_mul_f32 v164, v164, v230\n\t"
"v_mul_f32 v165, v165, v230\n\t"
"v_mfma_f32_16x16x32_bf16 v[156:159], a[196:199], v[52:55], v[156:159]\n\t"
"ds_write_b128 v9, v[64:67] offset:4096\n\t"
"v_mfma_f32_16x16x32_bf16 v[156:159], a[200:203], v[56:59], v[156:159]\n\t"
"v_mul_f32 v166, v166, v230\n\t"
"v_mul_f32 v167, v167, v230\n\t"
"v_mfma_f32_16x16x32_bf16 v[156:159], a[204:207], v[60:63], v[156:159]\n\t"
"buffer_load_dwordx4 v11, s[52:55], s77 offen offset:1024 lds\n\t"
"v_mfma_f32_16x16x32_bf16 v[200:203], a[32:35], v[184:187], v[84:87]\n\t"
"v_cvt_pk_bf16_f32 v224, v160, v161\n\t"
"v_cvt_pk_bf16_f32 v225, v162, v163\n\t"
"v_mfma_f32_16x16x32_bf16 v[200:203], a[36:39], v[188:191], v[200:203]\n\t"
"ds_write_b128 v9, v[68:71] offset:5120\n\t"
"v_mfma_f32_16x16x32_bf16 v[200:203], a[40:43], v[192:195], v[200:203]\n\t"
"v_cvt_pk_bf16_f32 v226, v164, v165\n\t"
"v_cvt_pk_bf16_f32 v227, v166, v167\n\t"
"v_mfma_f32_16x16x32_bf16 v[200:203], a[44:47], v[196:199], v[200:203]\n\t"
"s_cmp_gt_u32 s10, 1\n\t"
"s_cselect_b32 s78, 1073741824, 0\n\t"
"v_add_u32 v94, s78, v250\n\t"
"v_exp_f32 v208, v208\n\t"
"v_mfma_f32_16x16x32_bf16 v[212:215], v[216:219], v[28:31], v[212:215]\n\t"
"s_waitcnt lgkmcnt(7)\n\t"
"buffer_store_dwordx4 v[224:227], v94, s[56:59], s98 offen sc1\n\t"
"v_mfma_f32_16x16x32_bf16 v[152:155], a[144:147], v[128:131], v[152:155]\n\t"
"s_waitcnt lgkmcnt(6)\n\t"
"s_add_u32 s98, s98, 32768\n\t"
"ds_write_b128 v9, v[72:75] offset:6144\n\t"
"v_mfma_f32_16x16x32_bf16 v[152:155], a[148:151], v[132:135], v[152:155]\n\t"
"s_waitcnt lgkmcnt(6)\n\t"
"buffer_load_dwordx4 v12, s[52:55], s77 offen offset:2048 lds\n\t"
"v_mfma_f32_16x16x32_bf16 v[152:155], a[152:155], v[136:139], v[152:155]\n\t"
"s_waitcnt lgkmcnt(5)\n\t"
"v_exp_f32 v209, v209\n\t"
"v_mfma_f32_16x16x32_bf16 v[152:155], a[156:159], v[140:143], v[152:155]\n\t"
"v_exp_f32 v210, v210\n\t"
"v_mfma_f32_16x16x32_bf16 v[204:207], a[48:51], v[184:187], v[84:87]\n\t"
"ds_read_b128 v[168:171], v246 offset:6144\n\t"
"v_exp_f32 v211, v211\n\t"
"v_mfma_f32_16x16x32_bf16 v[204:207], a[52:55], v[188:191], v[204:207]\n\t"
"ds_write_b128 v9, v[76:79] offset:7168\n\t"
"v_mfma_f32_16x16x32_bf16 v[204:207], a[56:59], v[192:195], v[204:207]\n\t"
"buffer_load_dwordx4 v13, s[52:55], s77 offen offset:3072 lds\n\t"
"v_mfma_f32_16x16x32_bf16 v[204:207], a[60:63], v[196:199], v[204:207]\n\t"
"ds_read_b128 v[172:175], v246 offset:47104\n\t"
"v_cvt_pk_bf16_f32 v34, v208, v209\n\t"
"v_cvt_pk_bf16_f32 v35, v210, v211\n\t"
"v_mfma_f32_16x16x32_bf16 v[156:159], a[208:211], v[128:131], v[156:159]\n\t"
"ds_write_b32 v248, v212 offset:1024\n\t"
"v_mfma_f32_16x16x32_bf16 v[156:159], a[212:215], v[132:135], v[156:159]\n\t"
"s_add_u32 s77, s77, 32768\n\t"
"ds_read_b128 v[128:131], v7 offset:2048\n\t"
"v_exp_f32 v200, v200\n\t"
"v_mfma_f32_16x16x32_bf16 v[156:159], a[216:219], v[136:139], v[156:159]\n\t"
"ds_read_b128 v[132:135], v7 offset:3072\n\t"
"v_exp_f32 v201, v201\n\t"
"v_mfma_f32_16x16x32_bf16 v[156:159], a[220:223], v[140:143], v[156:159]\n\t"
"ds_read_b128 v[136:139], v8 offset:0\n\t"
"v_exp_f32 v202, v202\n\t"
"v_mfma_f32_16x16x32_bf16 v[208:211], a[64:67], v[184:187], v[84:87]\n\t"
"ds_read_b128 v[140:143], v8 offset:1024\n\t"
"v_exp_f32 v203, v203\n\t"
"v_mfma_f32_16x16x32_bf16 v[208:211], a[68:71], v[188:191], v[208:211]\n\t"
"v_cvt_pk_bf16_f32 v36, v200, v201\n\t"
"v_cvt_pk_bf16_f32 v37, v202, v203\n\t"
"v_mfma_f32_16x16x32_bf16 v[208:211], a[72:75], v[192:195], v[208:211]\n\t"
"v_exp_f32 v204, v204\n\t"
"v_mfma_f32_16x16x32_bf16 v[208:211], a[76:79], v[196:199], v[208:211]\n\t"
"ds_read_b128 v[176:179], v247 offset:6144\n\t"
"v_exp_f32 v205, v205\n\t"
"v_mfma_f32_16x16x32_bf16 v[212:215], v[216:219], v[32:35], 0\n\t"
"s_waitcnt lgkmcnt(13)\n\t"
"v_exp_f32 v206, v206\n\t"
"v_mfma_f32_16x16x32_bf16 v[152:155], a[160:163], v[144:147], v[152:155]\n\t"
"s_waitcnt lgkmcnt(12)\n\t"
"v_exp_f32 v207, v207\n\t"
"v_mfma_f32_16x16x32_bf16 v[152:155], a[164:167], v[148:151], v[152:155]\n\t"
"s_waitcnt lgkmcnt(4)\n\t"
"v_cvt_pk_bf16_f32 v38, v204, v205\n\t"
"v_cvt_pk_bf16_f32 v39, v206, v207\n\t"
"v_mfma_f32_16x16x32_bf16 v[152:155], a[168:171], v[128:131], v[152:155]\n\t"
"s_waitcnt lgkmcnt(3)\n\t"
"v_exp_f32 v208, v208\n\t"
"v_mfma_f32_16x16x32_bf16 v[152:155], a[172:175], v[132:135], v[152:155]\n\t"
"v_exp_f32 v209, v209\n\t"
"v_mfma_f32_16x16x32_bf16 v[200:203], a[80:83], v[184:187], v[84:87]\n\t"
"v_exp_f32 v210, v210\n\t"
"v_mfma_f32_16x16x32_bf16 v[200:203], a[84:87], v[188:191], v[200:203]\n\t"
"v_exp_f32 v211, v211\n\t"
"v_mfma_f32_16x16x32_bf16 v[200:203], a[88:91], v[192:195], v[200:203]\n\t"
"ds_read_b128 v[180:183], v247 offset:47104\n\t"
"ds_read_b128 v[88:91], v93 offset:41216\n\t"
"s_waitcnt lgkmcnt(0)\n\t"
"v_max_f32 v88, v88, v89\n\t"
"v_max3_f32 v88, v90, v91, v88\n\t"
"v_sub_f32 v80, 0, v88\n\t"
"v_sub_f32 v81, 0, v88\n\t"
"v_sub_f32 v82, 0, v88\n\t"
"v_sub_f32 v83, 0, v88\n\t"
"v_mfma_f32_16x16x32_bf16 v[200:203], a[92:95], v[196:199], v[200:203]\n\t"
"s_add_u32 m0, s72, 36864\n\t"
"v_cvt_pk_bf16_f32 v40, v208, v209\n\t"
"v_cvt_pk_bf16_f32 v41, v210, v211\n\t"
"v_mfma_f32_16x16x32_bf16 v[156:159], a[224:227], v[144:147], v[156:159]\n\t"
"ds_read_b128 v[144:147], v8 offset:2048\n\t"
"buffer_load_dwordx4 v10, s[52:55], s77 offen lds\n\t"
"v_mfma_f32_16x16x32_bf16 v[156:159], a[228:231], v[148:151], v[156:159]\n\t"
"ds_read_b128 v[148:151], v8 offset:3072\n\t"
"v_exp_f32 v200, v200\n\t"
"v_mfma_f32_16x16x32_bf16 v[156:159], a[232:235], v[128:131], v[156:159]\n\t"
"v_exp_f32 v201, v201\n\t"
"v_mfma_f32_16x16x32_bf16 v[156:159], a[236:239], v[132:135], v[156:159]\n\t"
"v_exp_f32 v202, v202\n\t"
"v_mfma_f32_16x16x32_bf16 v[204:207], a[96:99], v[184:187], v[84:87]\n\t"
"ds_read_b128 v[220:223], v249 offset:512\n\t"
"v_exp_f32 v203, v203\n\t"
"v_mfma_f32_16x16x32_bf16 v[204:207], a[100:103], v[188:191], v[204:207]\n\t"
"v_cvt_pk_bf16_f32 v42, v200, v201\n\t"
"v_cvt_pk_bf16_f32 v43, v202, v203\n\t"
"v_mfma_f32_16x16x32_bf16 v[204:207], a[104:107], v[192:195], v[204:207]\n\t"
"buffer_load_dwordx4 v11, s[52:55], s77 offen offset:1024 lds\n\t"
"v_mfma_f32_16x16x32_bf16 v[204:207], a[108:111], v[196:199], v[204:207]\n\t"
"v_mfma_f32_16x16x32_bf16 v[212:215], v[216:219], v[36:39], v[212:215]\n\t"
"v_mfma_f32_16x16x32_bf16 v[152:155], a[176:179], v[136:139], v[152:155]\n\t"
"s_waitcnt lgkmcnt(0)\n\t"
"v_add_f32 v220, v220, v221\n\t"
"v_add_f32 v222, v222, v223\n\t"
"v_add_f32 v220, v220, v222\n\t"
"v_mfma_f32_16x16x32_bf16 v[152:155], a[180:183], v[140:143], v[152:155]\n\t"
"v_rcp_f32 v230, v220\n\t"
"v_mfma_f32_16x16x32_bf16 v[152:155], a[184:187], v[144:147], v[152:155]\n\t"
"v_exp_f32 v204, v204\n\t"
"v_mfma_f32_16x16x32_bf16 v[152:155], a[188:191], v[148:151], v[152:155]\n\t"
"buffer_load_dwordx4 v12, s[52:55], s77 offen offset:2048 lds\n\t"
"v_mfma_f32_16x16x32_bf16 v[208:211], a[112:115], v[184:187], v[84:87]\n\t"
"v_exp_f32 v205, v205\n\t"
"v_mfma_f32_16x16x32_bf16 v[208:211], a[116:119], v[188:191], v[208:211]\n\t"
"v_exp_f32 v206, v206\n\t"
"v_mfma_f32_16x16x32_bf16 v[208:211], a[120:123], v[192:195], v[208:211]\n\t"
"v_exp_f32 v207, v207\n\t"
"v_cvt_pk_bf16_f32 v44, v204, v205\n\t"
"v_mfma_f32_16x16x32_bf16 v[208:211], a[124:127], v[196:199], v[208:211]\n\t"
"v_cvt_pk_bf16_f32 v45, v206, v207\n\t"
"v_mfma_f32_16x16x32_bf16 v[156:159], a[240:243], v[136:139], v[156:159]\n\t"
"v_mfma_f32_16x16x32_bf16 v[156:159], a[244:247], v[140:143], v[156:159]\n\t"
"buffer_load_dwordx4 v13, s[52:55], s77 offen offset:3072 lds\n\t"
"v_mfma_f32_16x16x32_bf16 v[156:159], a[248:251], v[144:147], v[156:159]\n\t"
"s_add_u32 s77, s77, 229376\n\t"
"s_and_b32 s77, s77, 2097151\n\t"
"v_mfma_f32_16x16x32_bf16 v[156:159], a[252:255], v[148:151], v[156:159]\n\t"
"s_barrier\n\t"
"ds_read_b128 v[128:131], v6 offset:4096\n\t"
"v_exp_f32 v208, v208\n\t"
"v_mfma_f32_16x16x32_bf16 v[200:203], a[0:3], v[168:171], v[80:83]\n\t"
"ds_read_b128 v[132:135], v6 offset:5120\n\t"
"v_exp_f32 v209, v209\n\t"
"v_mfma_f32_16x16x32_bf16 v[200:203], a[4:7], v[172:175], v[200:203]\n\t"
"ds_read_b128 v[136:139], v6 offset:6144\n\t"
"v_exp_f32 v210, v210\n\t"
"v_mfma_f32_16x16x32_bf16 v[200:203], a[8:11], v[176:179], v[200:203]\n\t"
"ds_read_b128 v[140:143], v6 offset:7168\n\t"
"v_exp_f32 v211, v211\n\t"
"v_mfma_f32_16x16x32_bf16 v[200:203], a[12:15], v[180:183], v[200:203]\n\t"
"ds_read_b128 v[144:147], v7 offset:4096\n\t"
"v_cvt_pk_bf16_f32 v46, v208, v209\n\t"
"v_mfma_f32_16x16x32_bf16 v[212:215], v[216:219], v[40:43], v[212:215]\n\t"
"ds_read_b128 v[148:151], v7 offset:5120\n\t"
"v_cvt_pk_bf16_f32 v47, v210, v211\n\t"
"v_mfma_f32_16x16x32_bf16 v[160:163], a[128:131], v[64:67], 0\n\t"
"v_mul_f32 v152, v152, v230\n\t"
"v_mul_f32 v153, v153, v230\n\t"
"v_mfma_f32_16x16x32_bf16 v[160:163], a[132:135], v[68:71], v[160:163]\n\t"
"v_exp_f32 v200, v200\n\t"
"v_mfma_f32_16x16x32_bf16 v[160:163], a[136:139], v[72:75], v[160:163]\n\t"
"v_exp_f32 v201, v201\n\t"
"v_mfma_f32_16x16x32_bf16 v[160:163], a[140:143], v[76:79], v[160:163]\n\t"
"v_exp_f32 v202, v202\n\t"
"v_mfma_f32_16x16x32_bf16 v[204:207], a[16:19], v[168:171], v[80:83]\n\t"
"v_exp_f32 v203, v203\n\t"
"v_mfma_f32_16x16x32_bf16 v[204:207], a[20:23], v[172:175], v[204:207]\n\t"
"v_cvt_pk_bf16_f32 v48, v200, v201\n\t"
"v_cvt_pk_bf16_f32 v49, v202, v203\n\t"
"v_mfma_f32_16x16x32_bf16 v[204:207], a[24:27], v[176:179], v[204:207]\n\t"
"v_mul_f32 v154, v154, v230\n\t"
"v_mul_f32 v155, v155, v230\n\t"
"v_mfma_f32_16x16x32_bf16 v[204:207], a[28:31], v[180:183], v[204:207]\n\t"
"v_mul_f32 v156, v156, v230\n\t"
"v_mul_f32 v157, v157, v230\n\t"
"v_mul_f32 v158, v158, v230\n\t"
"v_mfma_f32_16x16x32_bf16 v[164:167], a[192:195], v[64:67], 0\n\t"
"v_mul_f32 v159, v159, v230\n\t"
"v_cvt_pk_bf16_f32 v224, v152, v153\n\t"
"v_mfma_f32_16x16x32_bf16 v[164:167], a[196:199], v[68:71], v[164:167]\n\t"
"ds_write_b128 v9, v[16:19] offset:0\n\t"
"v_mfma_f32_16x16x32_bf16 v[164:167], a[200:203], v[72:75], v[164:167]\n\t"
"v_cvt_pk_bf16_f32 v225, v154, v155\n\t"
"v_cvt_pk_bf16_f32 v226, v156, v157\n\t"
"v_mfma_f32_16x16x32_bf16 v[164:167], a[204:207], v[76:79], v[164:167]\n\t"
"s_add_u32 m0, s72, 12288\n\t"
"v_cvt_pk_bf16_f32 v227, v158, v159\n\t"
"s_cmp_gt_u32 s10, 2\n\t"
"s_cselect_b32 s78, 1073741824, 0\n\t"
"v_add_u32 v94, s78, v250\n\t"
"v_mfma_f32_16x16x32_bf16 v[208:211], a[32:35], v[168:171], v[80:83]\n\t"
"buffer_load_dwordx4 v10, s[52:55], s77 offen lds\n\t"
"v_mfma_f32_16x16x32_bf16 v[208:211], a[36:39], v[172:175], v[208:211]\n\t"
"ds_write_b128 v9, v[20:23] offset:1024\n\t"
"v_mfma_f32_16x16x32_bf16 v[208:211], a[40:43], v[176:179], v[208:211]\n\t"
"buffer_store_dwordx4 v[224:227], v94, s[56:59], s98 offen sc1\n\t"
"v_mfma_f32_16x16x32_bf16 v[208:211], a[44:47], v[180:183], v[208:211]\n\t"
"s_add_u32 s98, s98, 32768\n\t"
"v_exp_f32 v204, v204\n\t"
"v_mfma_f32_16x16x32_bf16 v[212:215], v[216:219], v[44:47], v[212:215]\n\t"
"s_waitcnt lgkmcnt(7)\n\t"
"v_exp_f32 v205, v205\n\t"
"v_mfma_f32_16x16x32_bf16 v[160:163], a[144:147], v[128:131], v[160:163]\n\t"
"s_waitcnt lgkmcnt(6)\n\t"
"ds_write_b128 v9, v[24:27] offset:2048\n\t"
"v_mfma_f32_16x16x32_bf16 v[160:163], a[148:151], v[132:135], v[160:163]\n\t"
"s_waitcnt lgkmcnt(6)\n\t"
"buffer_load_dwordx4 v11, s[52:55], s77 offen offset:1024 lds\n\t"
"v_mfma_f32_16x16x32_bf16 v[160:163], a[152:155], v[136:139], v[160:163]\n\t"
"s_waitcnt lgkmcnt(5)\n\t"
"v_exp_f32 v206, v206\n\t"
"v_mfma_f32_16x16x32_bf16 v[160:163], a[156:159], v[140:143], v[160:163]\n\t"
"v_exp_f32 v207, v207\n\t"
"v_mfma_f32_16x16x32_bf16 v[200:203], a[48:51], v[168:171], v[80:83]\n\t"
"ds_read_b128 v[184:187], v246 offset:7168\n\t"
"v_cvt_pk_bf16_f32 v50, v204, v205\n\t"
"v_mfma_f32_16x16x32_bf16 v[200:203], a[52:55], v[172:175], v[200:203]\n\t"
"ds_write_b128 v9, v[28:31] offset:3072\n\t"
"v_mfma_f32_16x16x32_bf16 v[200:203], a[56:59], v[176:179], v[200:203]\n\t"
"v_cvt_pk_bf16_f32 v51, v206, v207\n\t"
"v_exp_f32 v208, v208\n\t"
"v_mfma_f32_16x16x32_bf16 v[200:203], a[60:63], v[180:183], v[200:203]\n\t"
"ds_read_b128 v[188:191], v246 offset:48128\n\t"
"buffer_load_dwordx4 v12, s[52:55], s77 offen offset:2048 lds\n\t"
"v_mfma_f32_16x16x32_bf16 v[164:167], a[208:211], v[128:131], v[164:167]\n\t"
"ds_write_b32 v248, v212 offset:40960\n\t"
"v_mfma_f32_16x16x32_bf16 v[164:167], a[212:215], v[132:135], v[164:167]\n\t"
"ds_read_b128 v[128:131], v7 offset:6144\n\t"
"v_exp_f32 v209, v209\n\t"
"v_mfma_f32_16x16x32_bf16 v[164:167], a[216:219], v[136:139], v[164:167]\n\t"
"ds_read_b128 v[132:135], v7 offset:7168\n\t"
"v_exp_f32 v210, v210\n\t"
"v_mfma_f32_16x16x32_bf16 v[164:167], a[220:223], v[140:143], v[164:167]\n\t"
"ds_read_b128 v[136:139], v8 offset:4096\n\t"
"v_exp_f32 v211, v211\n\t"
"v_mfma_f32_16x16x32_bf16 v[204:207], a[64:67], v[168:171], v[80:83]\n\t"
"ds_read_b128 v[140:143], v8 offset:5120\n\t"
"v_cvt_pk_bf16_f32 v52, v208, v209\n\t"
"v_mfma_f32_16x16x32_bf16 v[204:207], a[68:71], v[172:175], v[204:207]\n\t"
"buffer_load_dwordx4 v13, s[52:55], s77 offen offset:3072 lds\n\t"
"v_mfma_f32_16x16x32_bf16 v[204:207], a[72:75], v[176:179], v[204:207]\n\t"
"s_add_u32 s77, s77, 32768\n\t"
"v_cvt_pk_bf16_f32 v53, v210, v211\n\t"
"v_exp_f32 v200, v200\n\t"
"v_mfma_f32_16x16x32_bf16 v[204:207], a[76:79], v[180:183], v[204:207]\n\t"
"ds_read_b128 v[192:195], v247 offset:7168\n\t"
"v_exp_f32 v201, v201\n\t"
"v_mfma_f32_16x16x32_bf16 v[212:215], v[216:219], v[48:51], 0\n\t"
"s_waitcnt lgkmcnt(13)\n\t"
"v_exp_f32 v202, v202\n\t"
"v_mfma_f32_16x16x32_bf16 v[160:163], a[160:163], v[144:147], v[160:163]\n\t"
"s_waitcnt lgkmcnt(12)\n\t"
"v_exp_f32 v203, v203\n\t"
"v_mfma_f32_16x16x32_bf16 v[160:163], a[164:167], v[148:151], v[160:163]\n\t"
"s_waitcnt lgkmcnt(4)\n\t"
"v_cvt_pk_bf16_f32 v54, v200, v201\n\t"
"v_cvt_pk_bf16_f32 v55, v202, v203\n\t"
"v_mfma_f32_16x16x32_bf16 v[160:163], a[168:171], v[128:131], v[160:163]\n\t"
"s_waitcnt lgkmcnt(3)\n\t"
"v_exp_f32 v204, v204\n\t"
"v_mfma_f32_16x16x32_bf16 v[160:163], a[172:175], v[132:135], v[160:163]\n\t"
"v_exp_f32 v205, v205\n\t"
"v_mfma_f32_16x16x32_bf16 v[208:211], a[80:83], v[168:171], v[80:83]\n\t"
"v_exp_f32 v206, v206\n\t"
"v_mfma_f32_16x16x32_bf16 v[208:211], a[84:87], v[172:175], v[208:211]\n\t"
"v_exp_f32 v207, v207\n\t"
"v_mfma_f32_16x16x32_bf16 v[208:211], a[88:91], v[176:179], v[208:211]\n\t"
"ds_read_b128 v[196:199], v247 offset:48128\n\t"
"ds_read_b128 v[88:91], v93 offset:41472\n\t"
"s_waitcnt lgkmcnt(0)\n\t"
"v_max_f32 v88, v88, v89\n\t"
"v_max3_f32 v88, v90, v91, v88\n\t"
"v_sub_f32 v84, 0, v88\n\t"
"v_sub_f32 v85, 0, v88\n\t"
"v_sub_f32 v86, 0, v88\n\t"
"v_sub_f32 v87, 0, v88\n\t"
"v_mfma_f32_16x16x32_bf16 v[208:211], a[92:95], v[180:183], v[208:211]\n\t"
"v_cvt_pk_bf16_f32 v56, v204, v205\n\t"
"v_cvt_pk_bf16_f32 v57, v206, v207\n\t"
"v_mfma_f32_16x16x32_bf16 v[164:167], a[224:227], v[144:147], v[164:167]\n\t"
"ds_read_b128 v[144:147], v8 offset:6144\n\t"
"v_mfma_f32_16x16x32_bf16 v[164:167], a[228:231], v[148:151], v[164:167]\n\t"
"ds_read_b128 v[148:151], v8 offset:7168\n\t"
"v_mfma_f32_16x16x32_bf16 v[164:167], a[232:235], v[128:131], v[164:167]\n\t"
"v_mfma_f32_16x16x32_bf16 v[164:167], a[236:239], v[132:135], v[164:167]\n\t"
"v_exp_f32 v208, v208\n\t"
"v_mfma_f32_16x16x32_bf16 v[200:203], a[96:99], v[168:171], v[80:83]\n\t"
"ds_read_b128 v[220:223], v249 offset:768\n\t"
"v_exp_f32 v209, v209\n\t"
"v_mfma_f32_16x16x32_bf16 v[200:203], a[100:103], v[172:175], v[200:203]\n\t"
"v_exp_f32 v210, v210\n\t"
"v_mfma_f32_16x16x32_bf16 v[200:203], a[104:107], v[176:179], v[200:203]\n\t"
"v_exp_f32 v211, v211\n\t"
"v_mfma_f32_16x16x32_bf16 v[200:203], a[108:111], v[180:183], v[200:203]\n\t"
"v_cvt_pk_bf16_f32 v58, v208, v209\n\t"
"v_cvt_pk_bf16_f32 v59, v210, v211\n\t"
"v_mfma_f32_16x16x32_bf16 v[212:215], v[216:219], v[52:55], v[212:215]\n\t"
"v_mfma_f32_16x16x32_bf16 v[160:163], a[176:179], v[136:139], v[160:163]\n\t"
"s_waitcnt lgkmcnt(0)\n\t"
"v_add_f32 v220, v220, v221\n\t"
"v_add_f32 v222, v222, v223\n\t"
"v_add_f32 v220, v220, v222\n\t"
"v_mfma_f32_16x16x32_bf16 v[160:163], a[180:183], v[140:143], v[160:163]\n\t"
"v_rcp_f32 v230, v220\n\t"
"v_mfma_f32_16x16x32_bf16 v[160:163], a[184:187], v[144:147], v[160:163]\n\t"
"v_exp_f32 v200, v200\n\t"
"v_mfma_f32_16x16x32_bf16 v[160:163], a[188:191], v[148:151], v[160:163]\n\t"
"v_exp_f32 v201, v201\n\t"
"v_mfma_f32_16x16x32_bf16 v[204:207], a[112:115], v[168:171], v[80:83]\n\t"
"s_add_u32 m0, s72, 0\n\t"
"v_exp_f32 v202, v202\n\t"
"v_mfma_f32_16x16x32_bf16 v[204:207], a[116:119], v[172:175], v[204:207]\n\t"
"buffer_load_dwordx4 v10, s[52:55], s77 offen lds\n\t"
"v_mfma_f32_16x16x32_bf16 v[204:207], a[120:123], v[176:179], v[204:207]\n\t"
"v_exp_f32 v203, v203\n\t"
"v_cvt_pk_bf16_f32 v60, v200, v201\n\t"
"v_mfma_f32_16x16x32_bf16 v[204:207], a[124:127], v[180:183], v[204:207]\n\t"
"v_cvt_pk_bf16_f32 v61, v202, v203\n\t"
"v_mfma_f32_16x16x32_bf16 v[164:167], a[240:243], v[136:139], v[164:167]\n\t"
"v_mfma_f32_16x16x32_bf16 v[164:167], a[244:247], v[140:143], v[164:167]\n\t"
"v_mfma_f32_16x16x32_bf16 v[164:167], a[248:251], v[144:147], v[164:167]\n\t"
"v_mfma_f32_16x16x32_bf16 v[164:167], a[252:255], v[148:151], v[164:167]\n\t"
"s_barrier\n\t"
"ds_read_b128 v[128:131], v6 offset:0\n\t"
"buffer_load_dwordx4 v11, s[52:55], s77 offen offset:1024 lds\n\t"
"v_mfma_f32_16x16x32_bf16 v[208:211], a[0:3], v[184:187], v[84:87]\n\t"
"ds_read_b128 v[132:135], v6 offset:1024\n\t"
"v_exp_f32 v204, v204\n\t"
"v_mfma_f32_16x16x32_bf16 v[208:211], a[4:7], v[188:191], v[208:211]\n\t"
"ds_read_b128 v[136:139], v6 offset:2048\n\t"
"v_exp_f32 v205, v205\n\t"
"v_mfma_f32_16x16x32_bf16 v[208:211], a[8:11], v[192:195], v[208:211]\n\t"
"ds_read_b128 v[140:143], v6 offset:3072\n\t"
"v_exp_f32 v206, v206\n\t"
"v_mfma_f32_16x16x32_bf16 v[208:211], a[12:15], v[196:199], v[208:211]\n\t"
"ds_read_b128 v[144:147], v7 offset:0\n\t"
"v_exp_f32 v207, v207\n\t"
"v_mfma_f32_16x16x32_bf16 v[212:215], v[216:219], v[56:59], v[212:215]\n\t"
"ds_read_b128 v[148:151], v7 offset:1024\n\t"
"v_cvt_pk_bf16_f32 v62, v204, v205\n\t"
"v_mfma_f32_16x16x32_bf16 v[152:155], a[128:131], v[16:19], 0\n\t"
"buffer_load_dwordx4 v12, s[52:55], s77 offen offset:2048 lds\n\t"
"v_mfma_f32_16x16x32_bf16 v[152:155], a[132:135], v[20:23], v[152:155]\n\t"
"s_waitcnt vmcnt(22)\n\t"
"ds_read_b64_tr_b16 v[96:97], v236 offset:24576\n\t"
"v_cvt_pk_bf16_f32 v63, v206, v207\n\t"
"v_mfma_f32_16x16x32_bf16 v[152:155], a[136:139], v[24:27], v[152:155]\n\t"
"ds_read_b64_tr_b16 v[100:101], v237 offset:24576\n\t"
"v_exp_f32 v208, v208\n\t"
"v_mfma_f32_16x16x32_bf16 v[152:155], a[140:143], v[28:31], v[152:155]\n\t"
"ds_read_b64_tr_b16 v[104:105], v238 offset:24576\n\t"
"v_exp_f32 v209, v209\n\t"
"v_mfma_f32_16x16x32_bf16 v[200:203], a[16:19], v[184:187], v[84:87]\n\t"
"ds_read_b64_tr_b16 v[108:109], v239 offset:24576\n\t"
"v_exp_f32 v210, v210\n\t"
"v_mfma_f32_16x16x32_bf16 v[200:203], a[20:23], v[188:191], v[200:203]\n\t"
"v_exp_f32 v211, v211\n\t"
"v_mfma_f32_16x16x32_bf16 v[200:203], a[24:27], v[192:195], v[200:203]\n\t"
"buffer_load_dwordx4 v13, s[52:55], s77 offen offset:3072 lds\n\t"
"v_mfma_f32_16x16x32_bf16 v[200:203], a[28:31], v[196:199], v[200:203]\n\t"
"s_add_u32 s77, s77, 229376\n\t"
"ds_read_b64_tr_b16 v[112:113], v240 offset:24576\n\t"
"v_cvt_pk_bf16_f32 v64, v208, v209\n\t"
"v_mfma_f32_16x16x32_bf16 v[156:159], a[192:195], v[16:19], 0\n\t"
"s_and_b32 s77, s77, 2097151\n\t"
"v_cvt_pk_bf16_f32 v65, v210, v211\n\t"
"v_mul_f32 v160, v160, v230\n\t"
"v_mfma_f32_16x16x32_bf16 v[156:159], a[196:199], v[20:23], v[156:159]\n\t"
"ds_write_b128 v9, v[32:35] offset:4096\n\t"
"v_mfma_f32_16x16x32_bf16 v[156:159], a[200:203], v[24:27], v[156:159]\n\t"
"v_mul_f32 v161, v161, v230\n\t"
"v_mul_f32 v162, v162, v230\n\t"
"v_mfma_f32_16x16x32_bf16 v[156:159], a[204:207], v[28:31], v[156:159]\n\t"
"v_mul_f32 v163, v163, v230\n\t"
"v_mul_f32 v164, v164, v230\n\t"
"v_mfma_f32_16x16x32_bf16 v[204:207], a[32:35], v[184:187], v[84:87]\n\t"
"v_mul_f32 v165, v165, v230\n\t"
"v_mul_f32 v166, v166, v230\n\t"
"v_mfma_f32_16x16x32_bf16 v[204:207], a[36:39], v[188:191], v[204:207]\n\t"
"ds_write_b128 v9, v[36:39] offset:5120\n\t"
"v_mfma_f32_16x16x32_bf16 v[204:207], a[40:43], v[192:195], v[204:207]\n\t"
"v_mul_f32 v167, v167, v230\n\t"
"v_cvt_pk_bf16_f32 v224, v160, v161\n\t"
"v_mfma_f32_16x16x32_bf16 v[204:207], a[44:47], v[196:199], v[204:207]\n\t"
"v_cvt_pk_bf16_f32 v225, v162, v163\n\t"
"v_cvt_pk_bf16_f32 v226, v164, v165\n\t"
"v_mfma_f32_16x16x32_bf16 v[212:215], v[216:219], v[60:63], v[212:215]\n\t"
"s_waitcnt lgkmcnt(12)\n\t"
"v_cvt_pk_bf16_f32 v227, v166, v167\n\t"
"s_cmp_gt_u32 s10, 3\n\t"
"s_cselect_b32 s78, 1073741824, 0\n\t"
"v_add_u32 v94, s78, v250\n\t"
"v_mfma_f32_16x16x32_bf16 v[152:155], a[144:147], v[128:131], v[152:155]\n\t"
"s_waitcnt lgkmcnt(11)\n\t"
"ds_write_b128 v9, v[40:43] offset:6144\n\t"
"v_mfma_f32_16x16x32_bf16 v[152:155], a[148:151], v[132:135], v[152:155]\n\t"
"s_waitcnt lgkmcnt(11)\n\t"
"buffer_store_dwordx4 v[224:227], v94, s[56:59], s98 offen sc1\n\t"
"v_mfma_f32_16x16x32_bf16 v[152:155], a[152:155], v[136:139], v[152:155]\n\t"
"s_waitcnt lgkmcnt(10)\n\t"
"s_add_u32 s98, s98, 32768\n\t"
"v_exp_f32 v200, v200\n\t"
"v_mfma_f32_16x16x32_bf16 v[152:155], a[156:159], v[140:143], v[152:155]\n\t"
"v_exp_f32 v201, v201\n\t"
"v_mfma_f32_16x16x32_bf16 v[208:211], a[48:51], v[184:187], v[84:87]\n\t"
"ds_read_b128 v[168:171], v246 offset:8192\n\t"
"v_exp_f32 v202, v202\n\t"
"v_mfma_f32_16x16x32_bf16 v[208:211], a[52:55], v[188:191], v[208:211]\n\t"
"ds_write_b128 v9, v[44:47] offset:7168\n\t"
"v_mfma_f32_16x16x32_bf16 v[208:211], a[56:59], v[192:195], v[208:211]\n\t"
"v_exp_f32 v203, v203\n\t"
"v_mfma_f32_16x16x32_bf16 v[208:211], a[60:63], v[196:199], v[208:211]\n\t"
"ds_read_b128 v[172:175], v246 offset:49152\n\t"
"ds_read_b64_tr_b16 v[116:117], v241 offset:24576\n\t"
"v_cvt_pk_bf16_f32 v66, v200, v201\n\t"
"v_cvt_pk_bf16_f32 v67, v202, v203\n\t"
"v_mfma_f32_16x16x32_bf16 v[156:159], a[208:211], v[128:131], v[156:159]\n\t"
"ds_write_b32 v248, v212 offset:41216\n\t"
"v_mfma_f32_16x16x32_bf16 v[156:159], a[212:215], v[132:135], v[156:159]\n\t"
"ds_read_b64_tr_b16 v[120:121], v242 offset:24576\n\t"
"v_exp_f32 v204, v204\n\t"
"v_mfma_f32_16x16x32_bf16 v[156:159], a[216:219], v[136:139], v[156:159]\n\t"
"ds_read_b128 v[128:131], v7 offset:2048\n\t"
"v_exp_f32 v205, v205\n\t"
"v_mfma_f32_16x16x32_bf16 v[156:159], a[220:223], v[140:143], v[156:159]\n\t"
"ds_read_b128 v[132:135], v7 offset:3072\n\t"
"v_exp_f32 v206, v206\n\t"
"v_mfma_f32_16x16x32_bf16 v[200:203], a[64:67], v[184:187], v[84:87]\n\t"
"ds_read_b128 v[136:139], v8 offset:0\n\t"
"v_exp_f32 v207, v207\n\t"
"v_mfma_f32_16x16x32_bf16 v[200:203], a[68:71], v[188:191], v[200:203]\n\t"
"ds_read_b64_tr_b16 v[124:125], v243 offset:24576\n\t"
"ds_read_b128 v[140:143], v8 offset:1024\n\t"
"v_mfma_f32_16x16x32_bf16 v[200:203], a[72:75], v[192:195], v[200:203]\n\t"
"s_waitcnt vmcnt(19)\n\t"
"ds_read_b64_tr_b16 v[98:99], v236 offset:28672\n\t"
"v_cvt_pk_bf16_f32 v68, v204, v205\n\t"
"v_mfma_f32_16x16x32_bf16 v[200:203], a[76:79], v[196:199], v[200:203]\n\t"
"ds_read_b128 v[176:179], v247 offset:8192\n\t"
"v_cvt_pk_bf16_f32 v69, v206, v207\n\t"
"v_mfma_f32_16x16x32_bf16 v[212:215], v[216:219], v[64:67], 0\n\t"
"s_waitcnt lgkmcnt(15)\n\t"
"ds_read_b64_tr_b16 v[102:103], v237 offset:28672\n\t"
"v_exp_f32 v208, v208\n\t"
"v_mfma_f32_16x16x32_bf16 v[152:155], a[160:163], v[144:147], v[152:155]\n\t"
"s_add_u32 m0, s72, 24576\n\t"
"ds_read_b64_tr_b16 v[106:107], v238 offset:28672\n\t"
"v_exp_f32 v209, v209\n\t"
"v_mfma_f32_16x16x32_bf16 v[152:155], a[164:167], v[148:151], v[152:155]\n\t"
"s_waitcnt lgkmcnt(5)\n\t"
"buffer_load_dwordx4 v10, s[52:55], s77 offen lds\n\t"
"v_mfma_f32_16x16x32_bf16 v[152:155], a[168:171], v[128:131], v[152:155]\n\t"
"ds_read_b64_tr_b16 v[110:111], v239 offset:28672\n\t"
"v_exp_f32 v210, v210\n\t"
"v_mfma_f32_16x16x32_bf16 v[152:155], a[172:175], v[132:135], v[152:155]\n\t"
"ds_read_b64_tr_b16 v[114:115], v240 offset:28672\n\t"
"v_exp_f32 v211, v211\n\t"
"v_mfma_f32_16x16x32_bf16 v[204:207], a[80:83], v[184:187], v[84:87]\n\t"
"ds_read_b64_tr_b16 v[118:119], v241 offset:28672\n\t"
"v_cvt_pk_bf16_f32 v70, v208, v209\n\t"
"v_mfma_f32_16x16x32_bf16 v[204:207], a[84:87], v[188:191], v[204:207]\n\t"
"v_cvt_pk_bf16_f32 v71, v210, v211\n\t"
"v_exp_f32 v200, v200\n\t"
"v_mfma_f32_16x16x32_bf16 v[204:207], a[88:91], v[192:195], v[204:207]\n\t"
"ds_read_b128 v[180:183], v247 offset:49152\n\t"
"ds_read_b128 v[88:91], v93 offset:41728\n\t"
"s_waitcnt lgkmcnt(0)\n\t"
"v_max_f32 v88, v88, v89\n\t"
"v_max3_f32 v88, v90, v91, v88\n\t"
"v_sub_f32 v80, 0, v88\n\t"
"v_sub_f32 v81, 0, v88\n\t"
"v_sub_f32 v82, 0, v88\n\t"
"v_sub_f32 v83, 0, v88\n\t"
"v_mfma_f32_16x16x32_bf16 v[204:207], a[92:95], v[196:199], v[204:207]\n\t"
"buffer_load_dwordx4 v11, s[52:55], s77 offen offset:1024 lds\n\t"
"v_mfma_f32_16x16x32_bf16 v[156:159], a[224:227], v[144:147], v[156:159]\n\t"
"ds_read_b128 v[144:147], v8 offset:2048\n\t"
"v_exp_f32 v201, v201\n\t"
"v_mfma_f32_16x16x32_bf16 v[156:159], a[228:231], v[148:151], v[156:159]\n\t"
"ds_read_b128 v[148:151], v8 offset:3072\n\t"
"v_exp_f32 v202, v202\n\t"
"v_mfma_f32_16x16x32_bf16 v[156:159], a[232:235], v[128:131], v[156:159]\n\t"
"ds_read_b64_tr_b16 v[122:123], v242 offset:28672\n\t"
"v_exp_f32 v203, v203\n\t"
"v_mfma_f32_16x16x32_bf16 v[156:159], a[236:239], v[132:135], v[156:159]\n\t"
"ds_read_b64_tr_b16 v[126:127], v243 offset:28672\n\t"
"v_cvt_pk_bf16_f32 v72, v200, v201\n\t"
"v_mfma_f32_16x16x32_bf16 v[208:211], a[96:99], v[184:187], v[84:87]\n\t"
"ds_read_b128 v[220:223], v249 offset:1024\n\t"
"v_cvt_pk_bf16_f32 v73, v202, v203\n\t"
"v_mfma_f32_16x16x32_bf16 v[208:211], a[100:103], v[188:191], v[208:211]\n\t"
"buffer_load_dwordx4 v12, s[52:55], s77 offen offset:2048 lds\n\t"
"v_mfma_f32_16x16x32_bf16 v[208:211], a[104:107], v[192:195], v[208:211]\n\t"
"v_exp_f32 v204, v204\n\t"
"v_mfma_f32_16x16x32_bf16 v[208:211], a[108:111], v[196:199], v[208:211]\n\t"
"v_exp_f32 v205, v205\n\t"
"v_mfma_f32_16x16x32_bf16 v[212:215], v[216:219], v[68:71], v[212:215]\n\t"
"v_exp_f32 v206, v206\n\t"
"v_mfma_f32_16x16x32_bf16 v[152:155], a[176:179], v[136:139], v[152:155]\n\t"
"v_exp_f32 v207, v207\n\t"
"v_mfma_f32_16x16x32_bf16 v[152:155], a[180:183], v[140:143], v[152:155]\n\t"
"s_waitcnt lgkmcnt(4)\n\t"
"v_cvt_pk_bf16_f32 v74, v204, v205\n\t"
"v_cvt_pk_bf16_f32 v75, v206, v207\n\t"
"v_mfma_f32_16x16x32_bf16 v[152:155], a[184:187], v[144:147], v[152:155]\n\t"
"s_waitcnt lgkmcnt(3)\n\t"
"buffer_load_dwordx4 v13, s[52:55], s77 offen offset:3072 lds\n\t"
"v_mfma_f32_16x16x32_bf16 v[152:155], a[188:191], v[148:151], v[152:155]\n\t"
"s_waitcnt lgkmcnt(0)\n\t"
"s_add_u32 s77, s77, 32768\n\t"
"v_add_f32 v220, v220, v221\n\t"
"v_add_f32 v222, v222, v223\n\t"
"v_add_f32 v220, v220, v222\n\t"
"v_mfma_f32_16x16x32_bf16 v[200:203], a[112:115], v[184:187], v[84:87]\n\t"
"v_rcp_f32 v230, v220\n\t"
"v_mfma_f32_16x16x32_bf16 v[200:203], a[116:119], v[188:191], v[200:203]\n\t"
"v_exp_f32 v208, v208\n\t"
"v_mfma_f32_16x16x32_bf16 v[200:203], a[120:123], v[192:195], v[200:203]\n\t"
"v_exp_f32 v209, v209\n\t"
"v_mfma_f32_16x16x32_bf16 v[200:203], a[124:127], v[196:199], v[200:203]\n\t"
"v_exp_f32 v210, v210\n\t"
"v_mfma_f32_16x16x32_bf16 v[156:159], a[240:243], v[136:139], v[156:159]\n\t"
"v_exp_f32 v211, v211\n\t"
"v_cvt_pk_bf16_f32 v76, v208, v209\n\t"
"v_mfma_f32_16x16x32_bf16 v[156:159], a[244:247], v[140:143], v[156:159]\n\t"
"v_cvt_pk_bf16_f32 v77, v210, v211\n\t"
"v_mfma_f32_16x16x32_bf16 v[156:159], a[248:251], v[144:147], v[156:159]\n\t"
"v_mfma_f32_16x16x32_bf16 v[156:159], a[252:255], v[148:151], v[156:159]\n\t"
"s_barrier\n\t"
"ds_read_b128 v[128:131], v6 offset:4096\n\t"
"v_exp_f32 v200, v200\n\t"
"v_mfma_f32_16x16x32_bf16 v[204:207], a[0:3], v[168:171], v[80:83]\n\t"
"ds_read_b128 v[132:135], v6 offset:5120\n\t"
"v_exp_f32 v201, v201\n\t"
"v_mfma_f32_16x16x32_bf16 v[204:207], a[4:7], v[172:175], v[204:207]\n\t"
"ds_read_b128 v[136:139], v6 offset:6144\n\t"
"v_exp_f32 v202, v202\n\t"
"v_mfma_f32_16x16x32_bf16 v[204:207], a[8:11], v[176:179], v[204:207]\n\t"
"ds_read_b128 v[140:143], v6 offset:7168\n\t"
"v_exp_f32 v203, v203\n\t"
"v_mfma_f32_16x16x32_bf16 v[204:207], a[12:15], v[180:183], v[204:207]\n\t"
"ds_read_b128 v[144:147], v7 offset:4096\n\t"
"v_cvt_pk_bf16_f32 v78, v200, v201\n\t"
"v_mfma_f32_16x16x32_bf16 v[212:215], v[216:219], v[72:75], v[212:215]\n\t"
"ds_read_b128 v[148:151], v7 offset:5120\n\t"
"v_cvt_pk_bf16_f32 v79, v202, v203\n\t"
"v_mfma_f32_16x16x32_bf16 v[160:163], a[128:131], v[32:35], 0\n\t"
"v_mul_f32 v152, v152, v230\n\t"
"v_mul_f32 v153, v153, v230\n\t"
"v_mfma_f32_16x16x32_bf16 v[160:163], a[132:135], v[36:39], v[160:163]\n\t"
"v_mul_f32 v154, v154, v230\n\t"
"v_mul_f32 v155, v155, v230\n\t"
"v_mul_f32 v156, v156, v230\n\t"
"v_mul_f32 v157, v157, v230\n\t"
"v_mul_f32 v158, v158, v230\n\t"
"v_mul_f32 v159, v159, v230\n\t"
"v_cvt_pk_bf16_f32 v224, v152, v153\n\t"
"v_cvt_pk_bf16_f32 v225, v154, v155\n\t"
"v_cvt_pk_bf16_f32 v226, v156, v157\n\t"
"v_cvt_pk_bf16_f32 v227, v158, v159\n\t"
"s_cmp_gt_u32 s10, 4\n\t"
"s_cselect_b32 s78, 1073741824, 0\n\t"
"v_add_u32 v94, s78, v250\n\t"
"s_max_u32 s78, s9, 2\n\t"
"v_or_b32 v228, s78, v4\n\t"
"buffer_store_dwordx4 v[224:227], v94, s[56:59], s98 offen sc1\n\t"
"s_waitcnt vmcnt(21)\n\t"
"buffer_store_dwordx2 v[228:229], v5, s[56:59], s17 offen sc1\n\t"
"v_mfma_f32_16x16x32_bf16 v[160:163], a[136:139], v[40:43], v[160:163]\n\t"
"s_add_u32 s98, s98, 32768\n\t"
"v_exp_f32 v204, v204\n\t"
"v_mfma_f32_16x16x32_bf16 v[160:163], a[140:143], v[44:47], v[160:163]\n\t"
"v_exp_f32 v205, v205\n\t"
"v_mfma_f32_16x16x32_bf16 v[208:211], a[16:19], v[168:171], v[80:83]\n\t"
"v_exp_f32 v206, v206\n\t"
"v_mfma_f32_16x16x32_bf16 v[208:211], a[20:23], v[172:175], v[208:211]\n\t"
"v_exp_f32 v207, v207\n\t"
"v_mfma_f32_16x16x32_bf16 v[208:211], a[24:27], v[176:179], v[208:211]\n\t"
"v_cvt_pk_bf16_f32 v16, v204, v205\n\t"
"v_cvt_pk_bf16_f32 v17, v206, v207\n\t"
"v_mfma_f32_16x16x32_bf16 v[208:211], a[28:31], v[180:183], v[208:211]\n\t"
"s_add_u32 m0, s72, 28672\n\t"
"v_mfma_f32_16x16x32_bf16 v[164:167], a[192:195], v[32:35], 0\n\t"
"buffer_load_dwordx4 v10, s[52:55], s77 offen lds\n\t"
"v_mfma_f32_16x16x32_bf16 v[164:167], a[196:199], v[36:39], v[164:167]\n\t"
"ds_write_b128 v9, v[48:51] offset:0\n\t"
"v_mfma_f32_16x16x32_bf16 v[164:167], a[200:203], v[40:43], v[164:167]\n\t"
"v_mfma_f32_16x16x32_bf16 v[164:167], a[204:207], v[44:47], v[164:167]\n\t"
"v_mfma_f32_16x16x32_bf16 v[200:203], a[32:35], v[168:171], v[80:83]\n\t"
"v_exp_f32 v208, v208\n\t"
"v_mfma_f32_16x16x32_bf16 v[200:203], a[36:39], v[172:175], v[200:203]\n\t"
"ds_write_b128 v9, v[52:55] offset:1024\n\t"
"v_mfma_f32_16x16x32_bf16 v[200:203], a[40:43], v[176:179], v[200:203]\n\t"
"buffer_load_dwordx4 v11, s[52:55], s77 offen offset:1024 lds\n\t"
"v_mfma_f32_16x16x32_bf16 v[200:203], a[44:47], v[180:183], v[200:203]\n\t"
"v_exp_f32 v209, v209\n\t"
"v_mfma_f32_16x16x32_bf16 v[212:215], v[216:219], v[76:79], v[212:215]\n\t"
"s_waitcnt lgkmcnt(7)\n\t"
"v_exp_f32 v210, v210\n\t"
"v_mfma_f32_16x16x32_bf16 v[160:163], a[144:147], v[128:131], v[160:163]\n\t"
"s_waitcnt lgkmcnt(6)\n\t"
"ds_write_b128 v9, v[56:59] offset:2048\n\t"
"v_mfma_f32_16x16x32_bf16 v[160:163], a[148:151], v[132:135], v[160:163]\n\t"
"s_waitcnt lgkmcnt(6)\n\t"
"v_exp_f32 v211, v211\n\t"
"v_mfma_f32_16x16x32_bf16 v[160:163], a[152:155], v[136:139], v[160:163]\n\t"
"s_waitcnt lgkmcnt(5)\n\t"
"v_cvt_pk_bf16_f32 v18, v208, v209\n\t"
"v_cvt_pk_bf16_f32 v19, v210, v211\n\t"
"v_mfma_f32_16x16x32_bf16 v[160:163], a[156:159], v[140:143], v[160:163]\n\t"
"buffer_load_dwordx4 v12, s[52:55], s77 offen offset:2048 lds\n\t"
"v_mfma_f32_16x16x32_bf16 v[204:207], a[48:51], v[168:171], v[80:83]\n\t"
"ds_read_b128 v[184:187], v246 offset:9216\n\t"
"v_exp_f32 v200, v200\n\t"
"v_mfma_f32_16x16x32_bf16 v[204:207], a[52:55], v[172:175], v[204:207]\n\t"
"ds_write_b128 v9, v[60:63] offset:3072\n\t"
"v_mfma_f32_16x16x32_bf16 v[204:207], a[56:59], v[176:179], v[204:207]\n\t"
"v_exp_f32 v201, v201\n\t"
"v_mfma_f32_16x16x32_bf16 v[204:207], a[60:63], v[180:183], v[204:207]\n\t"
"ds_read_b128 v[188:191], v246 offset:50176\n\t"
"v_exp_f32 v202, v202\n\t"
"v_mfma_f32_16x16x32_bf16 v[164:167], a[208:211], v[128:131], v[164:167]\n\t"
"ds_write_b32 v248, v212 offset:41472\n\t"
"v_mfma_f32_16x16x32_bf16 v[164:167], a[212:215], v[132:135], v[164:167]\n\t"
"ds_read_b128 v[128:131], v7 offset:6144\n\t"
"buffer_load_dwordx4 v13, s[52:55], s77 offen offset:3072 lds\n\t"
"v_mfma_f32_16x16x32_bf16 v[164:167], a[216:219], v[136:139], v[164:167]\n\t"
"s_add_u32 s77, s77, 229376\n\t"
"ds_read_b128 v[132:135], v7 offset:7168\n\t"
"v_exp_f32 v203, v203\n\t"
"v_mfma_f32_16x16x32_bf16 v[164:167], a[220:223], v[140:143], v[164:167]\n\t"
"s_and_b32 s77, s77, 2097151\n\t"
"ds_read_b128 v[136:139], v8 offset:4096\n\t"
"v_cvt_pk_bf16_f32 v20, v200, v201\n\t"
"v_mfma_f32_16x16x32_bf16 v[208:211], a[64:67], v[168:171], v[80:83]\n\t"
"ds_read_b128 v[140:143], v8 offset:5120\n\t"
"v_cvt_pk_bf16_f32 v21, v202, v203\n\t"
"v_mfma_f32_16x16x32_bf16 v[208:211], a[68:71], v[172:175], v[208:211]\n\t"
"v_exp_f32 v204, v204\n\t"
"v_mfma_f32_16x16x32_bf16 v[208:211], a[72:75], v[176:179], v[208:211]\n\t"
"v_exp_f32 v205, v205\n\t"
"v_mfma_f32_16x16x32_bf16 v[208:211], a[76:79], v[180:183], v[208:211]\n\t"
"ds_read_b128 v[192:195], v247 offset:9216\n\t"
"v_exp_f32 v206, v206\n\t"
"v_mfma_f32_16x16x32_bf16 v[212:215], v[216:219], v[16:19], 0\n\t"
"s_waitcnt lgkmcnt(13)\n\t"
"v_exp_f32 v207, v207\n\t"
"v_mfma_f32_16x16x32_bf16 v[160:163], a[160:163], v[144:147], v[160:163]\n\t"
"s_waitcnt lgkmcnt(12)\n\t"
"v_cvt_pk_bf16_f32 v22, v204, v205\n\t"
"v_cvt_pk_bf16_f32 v23, v206, v207\n\t"
"v_mfma_f32_16x16x32_bf16 v[160:163], a[164:167], v[148:151], v[160:163]\n\t"
"s_waitcnt lgkmcnt(4)\n\t"
"v_exp_f32 v208, v208\n\t"
"v_mfma_f32_16x16x32_bf16 v[160:163], a[168:171], v[128:131], v[160:163]\n\t"
"s_waitcnt lgkmcnt(3)\n\t"
"v_exp_f32 v209, v209\n\t"
"v_mfma_f32_16x16x32_bf16 v[160:163], a[172:175], v[132:135], v[160:163]\n\t"
"v_exp_f32 v210, v210\n\t"
"v_mfma_f32_16x16x32_bf16 v[200:203], a[80:83], v[168:171], v[80:83]\n\t"
"v_exp_f32 v211, v211\n\t"
"v_mfma_f32_16x16x32_bf16 v[200:203], a[84:87], v[172:175], v[200:203]\n\t"
"v_cvt_pk_bf16_f32 v24, v208, v209\n\t"
"v_cvt_pk_bf16_f32 v25, v210, v211\n\t"
"v_mfma_f32_16x16x32_bf16 v[200:203], a[88:91], v[176:179], v[200:203]\n\t"
"ds_read_b128 v[196:199], v247 offset:50176\n\t"
"ds_read_b128 v[88:91], v93 offset:41984\n\t"
"s_waitcnt lgkmcnt(0)\n\t"
"v_max_f32 v88, v88, v89\n\t"
"v_max3_f32 v88, v90, v91, v88\n\t"
"v_sub_f32 v84, 0, v88\n\t"
"v_sub_f32 v85, 0, v88\n\t"
"v_sub_f32 v86, 0, v88\n\t"
"v_sub_f32 v87, 0, v88\n\t"
"v_mfma_f32_16x16x32_bf16 v[200:203], a[92:95], v[180:183], v[200:203]\n\t"
"v_mfma_f32_16x16x32_bf16 v[164:167], a[224:227], v[144:147], v[164:167]\n\t"
"ds_read_b128 v[144:147], v8 offset:6144\n\t"
"v_mfma_f32_16x16x32_bf16 v[164:167], a[228:231], v[148:151], v[164:167]\n\t"
"ds_read_b128 v[148:151], v8 offset:7168\n\t"
"v_mfma_f32_16x16x32_bf16 v[164:167], a[232:235], v[128:131], v[164:167]\n\t"
"v_mfma_f32_16x16x32_bf16 v[164:167], a[236:239], v[132:135], v[164:167]\n\t"
"v_mfma_f32_16x16x32_bf16 v[204:207], a[96:99], v[168:171], v[80:83]\n\t"
"ds_read_b128 v[220:223], v249 offset:40960\n\t"
"v_exp_f32 v200, v200\n\t"
"v_mfma_f32_16x16x32_bf16 v[204:207], a[100:103], v[172:175], v[204:207]\n\t"
"v_exp_f32 v201, v201\n\t"
"v_mfma_f32_16x16x32_bf16 v[204:207], a[104:107], v[176:179], v[204:207]\n\t"
"s_add_u32 m0, s72, 4096\n\t"
"v_exp_f32 v202, v202\n\t"
"v_mfma_f32_16x16x32_bf16 v[204:207], a[108:111], v[180:183], v[204:207]\n\t"
"buffer_load_dwordx4 v10, s[52:55], s77 offen lds\n\t"
"v_mfma_f32_16x16x32_bf16 v[212:215], v[216:219], v[20:23], v[212:215]\n\t"
"v_exp_f32 v203, v203\n\t"
"v_mfma_f32_16x16x32_bf16 v[160:163], a[176:179], v[136:139], v[160:163]\n\t"
"s_waitcnt lgkmcnt(0)\n\t"
"v_cvt_pk_bf16_f32 v26, v200, v201\n\t"
"v_cvt_pk_bf16_f32 v27, v202, v203\n\t"
"v_add_f32 v220, v220, v221\n\t"
"v_mfma_f32_16x16x32_bf16 v[160:163], a[180:183], v[140:143], v[160:163]\n\t"
"v_add_f32 v222, v222, v223\n\t"
"v_add_f32 v220, v220, v222\n\t"
"v_mfma_f32_16x16x32_bf16 v[160:163], a[184:187], v[144:147], v[160:163]\n\t"
"v_rcp_f32 v230, v220\n\t"
"v_mfma_f32_16x16x32_bf16 v[160:163], a[188:191], v[148:151], v[160:163]\n\t"
"v_exp_f32 v204, v204\n\t"
"v_mfma_f32_16x16x32_bf16 v[208:211], a[112:115], v[168:171], v[80:83]\n\t"
"buffer_load_dwordx4 v11, s[52:55], s77 offen offset:1024 lds\n\t"
"v_mfma_f32_16x16x32_bf16 v[208:211], a[116:119], v[172:175], v[208:211]\n\t"
"v_exp_f32 v205, v205\n\t"
"v_mfma_f32_16x16x32_bf16 v[208:211], a[120:123], v[176:179], v[208:211]\n\t"
"v_exp_f32 v206, v206\n\t"
"v_mfma_f32_16x16x32_bf16 v[208:211], a[124:127], v[180:183], v[208:211]\n\t"
"v_exp_f32 v207, v207\n\t"
"v_cvt_pk_bf16_f32 v28, v204, v205\n\t"
"v_mfma_f32_16x16x32_bf16 v[164:167], a[240:243], v[136:139], v[164:167]\n\t"
"v_cvt_pk_bf16_f32 v29, v206, v207\n\t"
"v_mfma_f32_16x16x32_bf16 v[164:167], a[244:247], v[140:143], v[164:167]\n\t"
"v_mfma_f32_16x16x32_bf16 v[164:167], a[248:251], v[144:147], v[164:167]\n\t"
"buffer_load_dwordx4 v12, s[52:55], s77 offen offset:2048 lds\n\t"
"v_mfma_f32_16x16x32_bf16 v[164:167], a[252:255], v[148:151], v[164:167]\n\t"
"s_barrier\n\t"
"ds_read_b128 v[128:131], v6 offset:0\n\t"
"v_exp_f32 v208, v208\n\t"
"v_mfma_f32_16x16x32_bf16 v[200:203], a[0:3], v[184:187], v[84:87]\n\t"
"ds_read_b128 v[132:135], v6 offset:1024\n\t"
"v_exp_f32 v209, v209\n\t"
"v_mfma_f32_16x16x32_bf16 v[200:203], a[4:7], v[188:191], v[200:203]\n\t"
"ds_read_b128 v[136:139], v6 offset:2048\n\t"
"v_exp_f32 v210, v210\n\t"
"v_mfma_f32_16x16x32_bf16 v[200:203], a[8:11], v[192:195], v[200:203]\n\t"
"ds_read_b128 v[140:143], v6 offset:3072\n\t"
"v_exp_f32 v211, v211\n\t"
"v_mfma_f32_16x16x32_bf16 v[200:203], a[12:15], v[196:199], v[200:203]\n\t"
"ds_read_b128 v[144:147], v7 offset:0\n\t"
"v_cvt_pk_bf16_f32 v30, v208, v209\n\t"
"v_mfma_f32_16x16x32_bf16 v[212:215], v[216:219], v[24:27], v[212:215]\n\t"
"buffer_load_dwordx4 v13, s[52:55], s77 offen offset:3072 lds\n\t"
"v_mfma_f32_16x16x32_bf16 v[152:155], a[128:131], v[48:51], 0\n\t"
"s_add_u32 s77, s77, 32768\n\t"
"ds_read_b128 v[148:151], v7 offset:1024\n\t"
"v_cvt_pk_bf16_f32 v31, v210, v211\n\t"
"v_mfma_f32_16x16x32_bf16 v[152:155], a[132:135], v[52:55], v[152:155]\n\t"
"v_exp_f32 v200, v200\n\t"
"v_mfma_f32_16x16x32_bf16 v[152:155], a[136:139], v[56:59], v[152:155]\n\t"
"v_exp_f32 v201, v201\n\t"
"v_mfma_f32_16x16x32_bf16 v[152:155], a[140:143], v[60:63], v[152:155]\n\t"
"v_exp_f32 v202, v202\n\t"
"v_mfma_f32_16x16x32_bf16 v[204:207], a[16:19], v[184:187], v[84:87]\n\t"
"v_exp_f32 v203, v203\n\t"
"v_mfma_f32_16x16x32_bf16 v[204:207], a[20:23], v[188:191], v[204:207]\n\t"
"v_cvt_pk_bf16_f32 v32, v200, v201\n\t"
"v_cvt_pk_bf16_f32 v33, v202, v203\n\t"
"v_mfma_f32_16x16x32_bf16 v[204:207], a[24:27], v[192:195], v[204:207]\n\t"
"v_mul_f32 v160, v160, v230\n\t"
"v_mul_f32 v161, v161, v230\n\t"
"v_mfma_f32_16x16x32_bf16 v[204:207], a[28:31], v[196:199], v[204:207]\n\t"
"v_mul_f32 v162, v162, v230\n\t"
"v_mul_f32 v163, v163, v230\n\t"
"v_mul_f32 v164, v164, v230\n\t"
"v_mfma_f32_16x16x32_bf16 v[156:159], a[192:195], v[48:51], 0\n\t"
"v_mul_f32 v165, v165, v230\n\t"
"v_mul_f32 v166, v166, v230\n\t"
"v_mfma_f32_16x16x32_bf16 v[156:159], a[196:199], v[52:55], v[156:159]\n\t"
"ds_write_b128 v9, v[64:67] offset:4096\n\t"
"v_mfma_f32_16x16x32_bf16 v[156:159], a[200:203], v[56:59], v[156:159]\n\t"
"v_mul_f32 v167, v167, v230\n\t"
"v_cvt_pk_bf16_f32 v224, v160, v161\n\t"
"v_mfma_f32_16x16x32_bf16 v[156:159], a[204:207], v[60:63], v[156:159]\n\t"
"v_cvt_pk_bf16_f32 v225, v162, v163\n\t"
"v_cvt_pk_bf16_f32 v226, v164, v165\n\t"
"v_mfma_f32_16x16x32_bf16 v[208:211], a[32:35], v[184:187], v[84:87]\n\t"
"v_cvt_pk_bf16_f32 v227, v166, v167\n\t"
"s_cmp_gt_u32 s10, 5\n\t"
"s_cselect_b32 s78, 1073741824, 0\n\t"
"v_add_u32 v94, s78, v250\n\t"
"v_mfma_f32_16x16x32_bf16 v[208:211], a[36:39], v[188:191], v[208:211]\n\t"
"ds_write_b128 v9, v[68:71] offset:5120\n\t"
"v_mfma_f32_16x16x32_bf16 v[208:211], a[40:43], v[192:195], v[208:211]\n\t"
"buffer_store_dwordx4 v[224:227], v94, s[56:59], s98 offen sc1\n\t"
"v_mfma_f32_16x16x32_bf16 v[208:211], a[44:47], v[196:199], v[208:211]\n\t"
"s_add_u32 s98, s98, 32768\n\t"
"v_exp_f32 v204, v204\n\t"
"v_mfma_f32_16x16x32_bf16 v[212:215], v[216:219], v[28:31], v[212:215]\n\t"
"s_waitcnt lgkmcnt(7)\n\t"
"v_exp_f32 v205, v205\n\t"
"v_mfma_f32_16x16x32_bf16 v[152:155], a[144:147], v[128:131], v[152:155]\n\t"
"s_waitcnt lgkmcnt(6)\n\t"
"ds_write_b128 v9, v[72:75] offset:6144\n\t"
"v_mfma_f32_16x16x32_bf16 v[152:155], a[148:151], v[132:135], v[152:155]\n\t"
"s_waitcnt lgkmcnt(6)\n\t"
"v_exp_f32 v206, v206\n\t"
"v_mfma_f32_16x16x32_bf16 v[152:155], a[152:155], v[136:139], v[152:155]\n\t"
"s_waitcnt lgkmcnt(5)\n\t"
"v_exp_f32 v207, v207\n\t"
"v_mfma_f32_16x16x32_bf16 v[152:155], a[156:159], v[140:143], v[152:155]\n\t"
"v_cvt_pk_bf16_f32 v34, v204, v205\n\t"
"v_cvt_pk_bf16_f32 v35, v206, v207\n\t"
"v_mfma_f32_16x16x32_bf16 v[200:203], a[48:51], v[184:187], v[84:87]\n\t"
"ds_write_b128 v9, v[76:79] offset:7168\n\t"
"v_mfma_f32_16x16x32_bf16 v[200:203], a[52:55], v[188:191], v[200:203]\n\t"
"v_exp_f32 v208, v208\n\t"
"v_mfma_f32_16x16x32_bf16 v[200:203], a[56:59], v[192:195], v[200:203]\n\t"
"v_exp_f32 v209, v209\n\t"
"v_mfma_f32_16x16x32_bf16 v[200:203], a[60:63], v[196:199], v[200:203]\n\t"
"v_exp_f32 v210, v210\n\t"
"v_mfma_f32_16x16x32_bf16 v[156:159], a[208:211], v[128:131], v[156:159]\n\t"
"ds_write_b32 v248, v212 offset:41728\n\t"
"v_mfma_f32_16x16x32_bf16 v[156:159], a[212:215], v[132:135], v[156:159]\n\t"
"ds_read_b128 v[128:131], v7 offset:2048\n\t"
"v_exp_f32 v211, v211\n\t"
"v_mfma_f32_16x16x32_bf16 v[156:159], a[216:219], v[136:139], v[156:159]\n\t"
"ds_read_b128 v[132:135], v7 offset:3072\n\t"
"v_cvt_pk_bf16_f32 v36, v208, v209\n\t"
"v_mfma_f32_16x16x32_bf16 v[156:159], a[220:223], v[140:143], v[156:159]\n\t"
"ds_read_b128 v[136:139], v8 offset:0\n\t"
"v_cvt_pk_bf16_f32 v37, v210, v211\n\t"
"v_mfma_f32_16x16x32_bf16 v[204:207], a[64:67], v[184:187], v[84:87]\n\t"
"ds_read_b128 v[140:143], v8 offset:1024\n\t"
"v_exp_f32 v200, v200\n\t"
"v_mfma_f32_16x16x32_bf16 v[204:207], a[68:71], v[188:191], v[204:207]\n\t"
"v_exp_f32 v201, v201\n\t"
"v_mfma_f32_16x16x32_bf16 v[204:207], a[72:75], v[192:195], v[204:207]\n\t"
"v_exp_f32 v202, v202\n\t"
"v_mfma_f32_16x16x32_bf16 v[204:207], a[76:79], v[196:199], v[204:207]\n\t"
"v_exp_f32 v203, v203\n\t"
"v_mfma_f32_16x16x32_bf16 v[212:215], v[216:219], v[32:35], 0\n\t"
"s_waitcnt lgkmcnt(10)\n\t"
"v_cvt_pk_bf16_f32 v38, v200, v201\n\t"
"v_cvt_pk_bf16_f32 v39, v202, v203\n\t"
"v_mfma_f32_16x16x32_bf16 v[152:155], a[160:163], v[144:147], v[152:155]\n\t"
"s_waitcnt lgkmcnt(9)\n\t"
"v_mfma_f32_16x16x32_bf16 v[152:155], a[164:167], v[148:151], v[152:155]\n\t"
"s_waitcnt lgkmcnt(3)\n\t"
"v_mfma_f32_16x16x32_bf16 v[152:155], a[168:171], v[128:131], v[152:155]\n\t"
"s_waitcnt lgkmcnt(2)\n\t"
"v_mfma_f32_16x16x32_bf16 v[152:155], a[172:175], v[132:135], v[152:155]\n\t"
"v_exp_f32 v204, v204\n\t"
"v_mfma_f32_16x16x32_bf16 v[208:211], a[80:83], v[184:187], v[84:87]\n\t"
"v_exp_f32 v205, v205\n\t"
"v_mfma_f32_16x16x32_bf16 v[208:211], a[84:87], v[188:191], v[208:211]\n\t"
"v_exp_f32 v206, v206\n\t"
"v_mfma_f32_16x16x32_bf16 v[208:211], a[88:91], v[192:195], v[208:211]\n\t"
"v_exp_f32 v207, v207\n\t"
"v_cvt_pk_bf16_f32 v40, v204, v205\n\t"
"v_mfma_f32_16x16x32_bf16 v[208:211], a[92:95], v[196:199], v[208:211]\n\t"
"v_cvt_pk_bf16_f32 v41, v206, v207\n\t"
"v_mfma_f32_16x16x32_bf16 v[156:159], a[224:227], v[144:147], v[156:159]\n\t"
"ds_read_b128 v[144:147], v8 offset:2048\n\t"
"v_mfma_f32_16x16x32_bf16 v[156:159], a[228:231], v[148:151], v[156:159]\n\t"
"ds_read_b128 v[148:151], v8 offset:3072\n\t"
"v_mfma_f32_16x16x32_bf16 v[156:159], a[232:235], v[128:131], v[156:159]\n\t"
"v_mfma_f32_16x16x32_bf16 v[156:159], a[236:239], v[132:135], v[156:159]\n\t"
"v_mfma_f32_16x16x32_bf16 v[200:203], a[96:99], v[184:187], v[84:87]\n\t"
"ds_read_b128 v[220:223], v249 offset:41216\n\t"
"v_exp_f32 v208, v208\n\t"
"v_mfma_f32_16x16x32_bf16 v[200:203], a[100:103], v[188:191], v[200:203]\n\t"
"v_exp_f32 v209, v209\n\t"
"v_mfma_f32_16x16x32_bf16 v[200:203], a[104:107], v[192:195], v[200:203]\n\t"
"v_exp_f32 v210, v210\n\t"
"v_mfma_f32_16x16x32_bf16 v[200:203], a[108:111], v[196:199], v[200:203]\n\t"
"v_exp_f32 v211, v211\n\t"
"v_mfma_f32_16x16x32_bf16 v[212:215], v[216:219], v[36:39], v[212:215]\n\t"
"s_waitcnt lgkmcnt(4)\n\t"
"v_cvt_pk_bf16_f32 v42, v208, v209\n\t"
"v_cvt_pk_bf16_f32 v43, v210, v211\n\t"
"v_mfma_f32_16x16x32_bf16 v[152:155], a[176:179], v[136:139], v[152:155]\n\t"
"s_waitcnt lgkmcnt(0)\n\t"
"v_add_f32 v220, v220, v221\n\t"
"v_add_f32 v222, v222, v223\n\t"
"v_add_f32 v220, v220, v222\n\t"
"v_mfma_f32_16x16x32_bf16 v[152:155], a[180:183], v[140:143], v[152:155]\n\t"
"buffer_load_dwordx2 v[2:3], v15, s[56:59], s16 offen sc1\n\t"
"v_mfma_f32_16x16x32_bf16 v[152:155], a[184:187], v[144:147], v[152:155]\n\t"
"v_rcp_f32 v230, v220\n\t"
"v_mfma_f32_16x16x32_bf16 v[152:155], a[188:191], v[148:151], v[152:155]\n\t"
"v_exp_f32 v200, v200\n\t"
"v_mfma_f32_16x16x32_bf16 v[204:207], a[112:115], v[184:187], v[84:87]\n\t"
"v_exp_f32 v201, v201\n\t"
"v_mfma_f32_16x16x32_bf16 v[204:207], a[116:119], v[188:191], v[204:207]\n\t"
"v_exp_f32 v202, v202\n\t"
"v_mfma_f32_16x16x32_bf16 v[204:207], a[120:123], v[192:195], v[204:207]\n\t"
"v_exp_f32 v203, v203\n\t"
"v_cvt_pk_bf16_f32 v44, v200, v201\n\t"
"v_mfma_f32_16x16x32_bf16 v[204:207], a[124:127], v[196:199], v[204:207]\n\t"
"v_cvt_pk_bf16_f32 v45, v202, v203\n\t"
"v_mfma_f32_16x16x32_bf16 v[156:159], a[240:243], v[136:139], v[156:159]\n\t"
"s_max_u32 s78, s9, 4\n\t"
"v_or_b32 v228, s78, v4\n\t"
"v_mfma_f32_16x16x32_bf16 v[156:159], a[244:247], v[140:143], v[156:159]\n\t"
"s_waitcnt vmcnt(16)\n\t"
"buffer_store_dwordx2 v[228:229], v5, s[56:59], s17 offen sc1\n\t"
"v_mfma_f32_16x16x32_bf16 v[156:159], a[248:251], v[144:147], v[156:159]\n\t"
"v_mfma_f32_16x16x32_bf16 v[156:159], a[252:255], v[148:151], v[156:159]\n\t"
"s_barrier\n\t"
"ds_read_b128 v[128:131], v6 offset:4096\n\t"
"v_exp_f32 v204, v204\n\t"
"v_mfma_f32_16x16x32_bf16 v[212:215], v[216:219], v[40:43], v[212:215]\n\t"
"ds_read_b128 v[132:135], v6 offset:5120\n\t"
"v_exp_f32 v205, v205\n\t"
"v_mfma_f32_16x16x32_bf16 v[160:163], a[128:131], v[64:67], 0\n\t"
"ds_read_b128 v[136:139], v6 offset:6144\n\t"
"v_exp_f32 v206, v206\n\t"
"v_mfma_f32_16x16x32_bf16 v[160:163], a[132:135], v[68:71], v[160:163]\n\t"
"ds_read_b128 v[140:143], v6 offset:7168\n\t"
"v_exp_f32 v207, v207\n\t"
"v_mfma_f32_16x16x32_bf16 v[160:163], a[136:139], v[72:75], v[160:163]\n\t"
"ds_read_b128 v[144:147], v7 offset:4096\n\t"
"v_cvt_pk_bf16_f32 v46, v204, v205\n\t"
"v_mfma_f32_16x16x32_bf16 v[160:163], a[140:143], v[76:79], v[160:163]\n\t"
"ds_read_b128 v[148:151], v7 offset:5120\n\t"
"v_cvt_pk_bf16_f32 v47, v206, v207\n\t"
"v_mfma_f32_16x16x32_bf16 v[164:167], a[192:195], v[64:67], 0\n\t"
"ds_read_b128 v[200:203], v7 offset:6144\n\t"
"v_mul_f32 v152, v152, v230\n\t"
"v_mfma_f32_16x16x32_bf16 v[164:167], a[196:199], v[68:71], v[164:167]\n\t"
"ds_read_b128 v[204:207], v7 offset:7168\n\t"
"v_mul_f32 v153, v153, v230\n\t"
"v_mfma_f32_16x16x32_bf16 v[164:167], a[200:203], v[72:75], v[164:167]\n\t"
"s_add_u32 m0, s72, 32768\n\t"
"ds_read_b64_tr_b16 a[0:1], v236 offset:32768\n\t"
"v_mul_f32 v154, v154, v230\n\t"
"v_mfma_f32_16x16x32_bf16 v[164:167], a[204:207], v[76:79], v[164:167]\n\t"
"ds_read_b64_tr_b16 a[4:5], v237 offset:32768\n\t"
"v_mul_f32 v155, v155, v230\n\t"
"v_mfma_f32_16x16x32_bf16 v[212:215], v[216:219], v[44:47], v[212:215]\n\t"
"s_waitcnt lgkmcnt(9)\n\t"
"ds_write_b128 v9, v[16:19] offset:0\n\t"
"v_mfma_f32_16x16x32_bf16 v[160:163], a[144:147], v[128:131], v[160:163]\n\t"
"s_waitcnt lgkmcnt(9)\n\t"
"v_mul_f32 v156, v156, v230\n\t"
"v_mul_f32 v157, v157, v230\n\t"
"v_mfma_f32_16x16x32_bf16 v[160:163], a[148:151], v[132:135], v[160:163]\n\t"
"s_waitcnt lgkmcnt(8)\n\t"
"v_mul_f32 v158, v158, v230\n\t"
"v_mul_f32 v159, v159, v230\n\t"
"v_mfma_f32_16x16x32_bf16 v[160:163], a[152:155], v[136:139], v[160:163]\n\t"
"s_waitcnt lgkmcnt(7)\n\t"
"v_cvt_pk_bf16_f32 v224, v152, v153\n\t"
"v_cvt_pk_bf16_f32 v225, v154, v155\n\t"
"v_mfma_f32_16x16x32_bf16 v[160:163], a[156:159], v[140:143], v[160:163]\n\t"
"ds_read_b64_tr_b16 a[8:9], v238 offset:32768\n\t"
"v_cvt_pk_bf16_f32 v226, v156, v157\n\t"
"v_mfma_f32_16x16x32_bf16 v[164:167], a[208:211], v[128:131], v[164:167]\n\t"
"ds_read_b64_tr_b16 a[12:13], v239 offset:32768\n\t"
"ds_read_b64_tr_b16 a[16:17], v240 offset:32768\n\t"
"ds_read_b64_tr_b16 a[20:21], v241 offset:32768\n\t"
"ds_read_b64_tr_b16 a[24:25], v242 offset:32768\n\t"
"ds_read_b64_tr_b16 a[28:29], v243 offset:32768\n\t"
"s_waitcnt lgkmcnt(0)\n\t"
"buffer_load_dwordx4 v10, s[52:55], s77 offen lds\n\t"
"v_cvt_pk_bf16_f32 v227, v158, v159\n\t"
"s_cmp_gt_u32 s10, 6\n\t"
"s_cselect_b32 s78, 1073741824, 0\n\t"
"v_add_u32 v94, s78, v250\n\t"
"v_mfma_f32_16x16x32_bf16 v[164:167], a[212:215], v[132:135], v[164:167]\n\t"
"ds_write_b128 v9, v[20:23] offset:1024\n\t"
"v_mfma_f32_16x16x32_bf16 v[164:167], a[216:219], v[136:139], v[164:167]\n\t"
"buffer_store_dwordx4 v[224:227], v94, s[56:59], s98 offen sc1\n\t"
"buffer_load_dwordx4 v11, s[52:55], s77 offen offset:1024 lds\n\t"
"ds_read_b128 v[128:131], v8 offset:4096\n\t"
"v_mfma_f32_16x16x32_bf16 v[164:167], a[220:223], v[140:143], v[164:167]\n\t"
"ds_read_b128 v[132:135], v8 offset:5120\n\t"
"s_add_u32 s98, s98, 32768\n\t"
"buffer_load_dwordx4 v12, s[52:55], s77 offen offset:2048 lds\n\t"
"v_mfma_f32_16x16x32_bf16 v[160:163], a[160:163], v[144:147], v[160:163]\n\t"
"ds_write_b32 v248, v212 offset:41984\n\t"
"ds_read_b128 v[136:139], v8 offset:6144\n\t"
"v_mfma_f32_16x16x32_bf16 v[160:163], a[164:167], v[148:151], v[160:163]\n\t"
"ds_read_b128 v[140:143], v8 offset:7168\n\t"
"buffer_load_dwordx4 v13, s[52:55], s77 offen offset:3072 lds\n\t"
"v_mfma_f32_16x16x32_bf16 v[160:163], a[168:171], v[200:203], v[160:163]\n\t"
"s_add_u32 m0, s72, 36864\n\t"
"ds_write_b128 v9, v[24:27] offset:2048\n\t"
"v_mfma_f32_16x16x32_bf16 v[160:163], a[172:175], v[204:207], v[160:163]\n\t"
"ds_read_b64_tr_b16 a[2:3], v236 offset:36864\n\t"
"ds_read_b64_tr_b16 a[6:7], v237 offset:36864\n\t"
"ds_read_b64_tr_b16 a[10:11], v238 offset:36864\n\t"
"ds_read_b64_tr_b16 a[14:15], v239 offset:36864\n\t"
"ds_read_b64_tr_b16 a[18:19], v240 offset:36864\n\t"
"ds_read_b64_tr_b16 a[22:23], v241 offset:36864\n\t"
"ds_read_b64_tr_b16 a[26:27], v242 offset:36864\n\t"
"ds_read_b64_tr_b16 a[30:31], v243 offset:36864\n\t"
"s_add_u32 s77, s77, 229376\n\t"
"s_and_b32 s77, s77, 2097151\n\t"
"s_waitcnt lgkmcnt(0)\n\t"
"buffer_load_dwordx4 v10, s[52:55], s77 offen lds\n\t"
"v_mfma_f32_16x16x32_bf16 v[164:167], a[224:227], v[144:147], v[164:167]\n\t"
"buffer_load_dwordx4 v11, s[52:55], s77 offen offset:1024 lds\n\t"
"v_mfma_f32_16x16x32_bf16 v[164:167], a[228:231], v[148:151], v[164:167]\n\t"
"buffer_load_dwordx4 v12, s[52:55], s77 offen offset:2048 lds\n\t"
"v_mfma_f32_16x16x32_bf16 v[164:167], a[232:235], v[200:203], v[164:167]\n\t"
"ds_write_b128 v9, v[28:31] offset:3072\n\t"
"v_mfma_f32_16x16x32_bf16 v[164:167], a[236:239], v[204:207], v[164:167]\n\t"
"buffer_load_dwordx4 v13, s[52:55], s77 offen offset:3072 lds\n\t"
"v_mfma_f32_16x16x32_bf16 v[160:163], a[176:179], v[128:131], v[160:163]\n\t"
"s_add_u32 s77, s77, 32768\n\t"
"v_mfma_f32_16x16x32_bf16 v[160:163], a[180:183], v[132:135], v[160:163]\n\t"
"s_add_u32 m0, s72, 0\n\t"
"v_mfma_f32_16x16x32_bf16 v[160:163], a[184:187], v[136:139], v[160:163]\n\t"
"ds_read_b64_tr_b16 a[32:33], v236 offset:12288\n\t"
"v_mfma_f32_16x16x32_bf16 v[160:163], a[188:191], v[140:143], v[160:163]\n\t"
"ds_read_b64_tr_b16 a[36:37], v237 offset:12288\n\t"
"ds_read_b128 v[220:223], v249 offset:41472\n\t"
"v_mfma_f32_16x16x32_bf16 v[164:167], a[240:243], v[128:131], v[164:167]\n\t"
"ds_read_b64_tr_b16 a[40:41], v238 offset:12288\n\t"
"v_mfma_f32_16x16x32_bf16 v[164:167], a[244:247], v[132:135], v[164:167]\n\t"
"s_waitcnt lgkmcnt(1)\n\t"
"ds_read_b64_tr_b16 a[44:45], v239 offset:12288\n\t"
"v_add_f32 v220, v220, v221\n\t"
"v_add_f32 v222, v222, v223\n\t"
"v_mfma_f32_16x16x32_bf16 v[164:167], a[248:251], v[136:139], v[164:167]\n\t"
"ds_read_b64_tr_b16 a[48:49], v240 offset:12288\n\t"
"v_add_f32 v220, v220, v222\n\t"
"v_mfma_f32_16x16x32_bf16 v[164:167], a[252:255], v[140:143], v[164:167]\n\t"
"s_barrier\n\t"
"ds_read_b64_tr_b16 a[52:53], v241 offset:12288\n\t"
"v_rcp_f32 v230, v220\n\t"
"v_mfma_f32_16x16x32_bf16 v[152:155], a[128:131], v[16:19], 0\n\t"
"ds_read_b64_tr_b16 a[56:57], v242 offset:12288\n\t"
"ds_read_b64_tr_b16 a[60:61], v243 offset:12288\n\t"
"ds_read_b64_tr_b16 a[34:35], v236 offset:0\n\t"
"ds_read_b64_tr_b16 a[38:39], v237 offset:0\n\t"
"ds_read_b64_tr_b16 a[42:43], v238 offset:0\n\t"
"ds_read_b64_tr_b16 a[46:47], v239 offset:0\n\t"
"ds_read_b64_tr_b16 a[50:51], v240 offset:0\n\t"
"ds_read_b64_tr_b16 a[54:55], v241 offset:0\n\t"
"ds_read_b64_tr_b16 a[58:59], v242 offset:0\n\t"
"ds_read_b64_tr_b16 a[62:63], v243 offset:0\n\t"
"s_waitcnt lgkmcnt(0)\n\t"
"buffer_load_dwordx4 v10, s[52:55], s77 offen lds\n\t"
"v_mfma_f32_16x16x32_bf16 v[152:155], a[132:135], v[20:23], v[152:155]\n\t"
"ds_read_b128 v[128:131], v6 offset:0\n\t"
"buffer_load_dwordx4 v11, s[52:55], s77 offen offset:1024 lds\n\t"
"v_mfma_f32_16x16x32_bf16 v[152:155], a[136:139], v[24:27], v[152:155]\n\t"
"ds_read_b128 v[132:135], v6 offset:1024\n\t"
"buffer_load_dwordx4 v12, s[52:55], s77 offen offset:2048 lds\n\t"
"v_mfma_f32_16x16x32_bf16 v[152:155], a[140:143], v[28:31], v[152:155]\n\t"
"ds_read_b128 v[136:139], v6 offset:2048\n\t"
"buffer_load_dwordx4 v13, s[52:55], s77 offen offset:3072 lds\n\t"
"v_mfma_f32_16x16x32_bf16 v[156:159], a[192:195], v[16:19], 0\n\t"
"ds_read_b128 v[140:143], v6 offset:3072\n\t"
"s_add_u32 m0, s72, 24576\n\t"
"v_mul_f32 v160, v160, v230\n\t"
"v_mfma_f32_16x16x32_bf16 v[156:159], a[196:199], v[20:23], v[156:159]\n\t"
"s_waitcnt vmcnt(26)\n\t"
"s_add_u32 s77, s77, 229376\n\t"
"ds_read_b64_tr_b16 a[64:65], v236 offset:24576\n\t"
"v_mul_f32 v161, v161, v230\n\t"
"v_mfma_f32_16x16x32_bf16 v[156:159], a[200:203], v[24:27], v[156:159]\n\t"
"ds_read_b64_tr_b16 a[68:69], v237 offset:24576\n\t"
"ds_read_b64_tr_b16 a[72:73], v238 offset:24576\n\t"
"ds_read_b64_tr_b16 a[76:77], v239 offset:24576\n\t"
"ds_read_b64_tr_b16 a[80:81], v240 offset:24576\n\t"
"ds_read_b64_tr_b16 a[84:85], v241 offset:24576\n\t"
"ds_read_b64_tr_b16 a[88:89], v242 offset:24576\n\t"
"ds_read_b64_tr_b16 a[92:93], v243 offset:24576\n\t"
"s_and_b32 s77, s77, 2097151\n\t"
"s_waitcnt lgkmcnt(0)\n\t"
"buffer_load_dwordx4 v10, s[52:55], s77 offen lds\n\t"
"v_mfma_f32_16x16x32_bf16 v[156:159], a[204:207], v[28:31], v[156:159]\n\t"
"buffer_load_dwordx4 v11, s[52:55], s77 offen offset:1024 lds\n\t"
"v_mfma_f32_16x16x32_bf16 v[152:155], a[144:147], v[128:131], v[152:155]\n\t"
"ds_read_b128 v[144:147], v7 offset:0\n\t"
"buffer_load_dwordx4 v12, s[52:55], s77 offen offset:2048 lds\n\t"
"v_mfma_f32_16x16x32_bf16 v[152:155], a[148:151], v[132:135], v[152:155]\n\t"
"ds_read_b128 v[148:151], v7 offset:1024\n\t"
"buffer_load_dwordx4 v13, s[52:55], s77 offen offset:3072 lds\n\t"
"v_mfma_f32_16x16x32_bf16 v[152:155], a[152:155], v[136:139], v[152:155]\n\t"
"ds_write_b128 v9, v[32:35] offset:4096\n\t"
"v_mfma_f32_16x16x32_bf16 v[152:155], a[156:159], v[140:143], v[152:155]\n\t"
"s_add_u32 m0, s72, 28672\n\t"
"ds_read_b128 v[200:203], v7 offset:2048\n\t"
"v_mul_f32 v162, v162, v230\n\t"
"v_mfma_f32_16x16x32_bf16 v[156:159], a[208:211], v[128:131], v[156:159]\n\t"
"s_waitcnt vmcnt(24)\n\t"
"ds_read_b128 v[204:207], v7 offset:3072\n\t"
"ds_read_b64_tr_b16 a[66:67], v236 offset:28672\n\t"
"ds_read_b64_tr_b16 a[70:71], v237 offset:28672\n\t"
"ds_read_b64_tr_b16 a[74:75], v238 offset:28672\n\t"
"ds_read_b64_tr_b16 a[78:79], v239 offset:28672\n\t"
"ds_read_b64_tr_b16 a[82:83], v240 offset:28672\n\t"
"ds_read_b64_tr_b16 a[86:87], v241 offset:28672\n\t"
"ds_read_b64_tr_b16 a[90:91], v242 offset:28672\n\t"
"ds_read_b64_tr_b16 a[94:95], v243 offset:28672\n\t"
"s_add_u32 s77, s77, 32768\n\t"
"s_waitcnt lgkmcnt(0)\n\t"
"buffer_load_dwordx4 v10, s[52:55], s77 offen lds\n\t"
"v_mfma_f32_16x16x32_bf16 v[156:159], a[212:215], v[132:135], v[156:159]\n\t"
"buffer_load_dwordx4 v11, s[52:55], s77 offen offset:1024 lds\n\t"
"v_mfma_f32_16x16x32_bf16 v[156:159], a[216:219], v[136:139], v[156:159]\n\t"
"ds_read_b128 v[128:131], v8 offset:0\n\t"
"v_mul_f32 v163, v163, v230\n\t"
"v_mul_f32 v164, v164, v230\n\t"
"v_mul_f32 v165, v165, v230\n\t"
"v_mul_f32 v166, v166, v230\n\t"
"v_mul_f32 v167, v167, v230\n\t"
"v_cvt_pk_bf16_f32 v224, v160, v161\n\t"
"v_cvt_pk_bf16_f32 v225, v162, v163\n\t"
"v_cvt_pk_bf16_f32 v226, v164, v165\n\t"
"v_cvt_pk_bf16_f32 v227, v166, v167\n\t"
"s_cmp_gt_u32 s10, 7\n\t"
"s_cselect_b32 s78, 1073741824, 0\n\t"
"v_add_u32 v94, s78, v250\n\t"
"v_mfma_f32_16x16x32_bf16 v[156:159], a[220:223], v[140:143], v[156:159]\n\t"
"ds_write_b128 v9, v[36:39] offset:5120\n\t"
"ds_read_b128 v[132:135], v8 offset:1024\n\t"
"v_mfma_f32_16x16x32_bf16 v[152:155], a[160:163], v[144:147], v[152:155]\n\t"
"s_waitcnt vmcnt(20)\n\t"
"buffer_store_dwordx4 v[224:227], v94, s[56:59], s98 offen sc1\n\t"
"buffer_load_dwordx4 v12, s[52:55], s77 offen offset:2048 lds\n\t"
"ds_read_b128 v[136:139], v8 offset:2048\n\t"
"s_or_b32 s90, s86, 2\n\t"
"v_cmp_le_u64 vcc, s[90:91], v[2:3]\n\t"
"v_cmp_ge_u64 s[92:93], s[88:89], v[2:3]\n\t"
"s_nop 0\n\t"
"s_and_b64 vcc, vcc, s[92:93]\n\t"
"s_cmp_eq_u64 vcc, -1\n\t"
"s_cbranch_scc0 LPS18_%=\n\t"
"LPR18_%=:\n\t"
"v_mfma_f32_16x16x32_bf16 v[152:155], a[164:167], v[148:151], v[152:155]\n\t"
"buffer_load_dwordx4 v13, s[52:55], s77 offen offset:3072 lds\n\t"
"s_add_u32 m0, s72, 4096\n\t"
"ds_read_b128 v[140:143], v8 offset:3072\n\t"
"v_mfma_f32_16x16x32_bf16 v[152:155], a[168:171], v[200:203], v[152:155]\n\t"
"ds_read_b64_tr_b16 a[96:97], v236 offset:4096\n\t"
"ds_read_b64_tr_b16 a[100:101], v237 offset:4096\n\t"
"ds_read_b64_tr_b16 a[104:105], v238 offset:4096\n\t"
"ds_read_b64_tr_b16 a[108:109], v239 offset:4096\n\t"
"ds_read_b64_tr_b16 a[112:113], v240 offset:4096\n\t"
"ds_read_b64_tr_b16 a[116:117], v241 offset:4096\n\t"
"ds_read_b64_tr_b16 a[120:121], v242 offset:4096\n\t"
"ds_read_b64_tr_b16 a[124:125], v243 offset:4096\n\t"
"s_add_u32 s77, s77, 229376\n\t"
"s_and_b32 s77, s77, 2097151\n\t"
"s_waitcnt lgkmcnt(0)\n\t"
"buffer_load_dwordx4 v10, s[52:55], s77 offen lds\n\t"
"v_mfma_f32_16x16x32_bf16 v[152:155], a[172:175], v[204:207], v[152:155]\n\t"
"s_add_u32 s22, s94, 0\n\t"
"ds_write_b128 v9, v[40:43] offset:6144\n\t"
"v_mfma_f32_16x16x32_bf16 v[156:159], a[224:227], v[144:147], v[156:159]\n\t"
"buffer_load_dwordx4 v11, s[52:55], s77 offen offset:1024 lds\n\t"
"buffer_load_dwordx2 v[2:3], v15, s[56:59], s16 offen sc1\n\t"
"v_mfma_f32_16x16x32_bf16 v[156:159], a[228:231], v[148:151], v[156:159]\n\t"
"s_add_u32 s98, s98, 32768\n\t"
"buffer_load_dwordx4 v12, s[52:55], s77 offen offset:2048 lds\n\t"
"v_mfma_f32_16x16x32_bf16 v[156:159], a[232:235], v[200:203], v[156:159]\n\t"
"buffer_load_dwordx4 v13, s[52:55], s77 offen offset:3072 lds\n\t"
"v_mfma_f32_16x16x32_bf16 v[156:159], a[236:239], v[204:207], v[156:159]\n\t"
"s_add_u32 s77, s77, 32768\n\t"
"buffer_load_dwordx4 v[168:171], v232, s[56:59], s22 offen sc1\n\t"
"v_mfma_f32_16x16x32_bf16 v[152:155], a[176:179], v[128:131], v[152:155]\n\t"
"ds_write_b128 v9, v[44:47] offset:7168\n\t"
"v_mfma_f32_16x16x32_bf16 v[152:155], a[180:183], v[132:135], v[152:155]\n\t"
"buffer_load_dwordx4 v[172:175], v232, s[56:59], s22 offen offset:1024 sc1\n\t"
"v_mfma_f32_16x16x32_bf16 v[152:155], a[184:187], v[136:139], v[152:155]\n\t"
"buffer_load_dwordx4 v[176:179], v233, s[56:59], s22 offen sc1\n\t"
"v_mfma_f32_16x16x32_bf16 v[152:155], a[188:191], v[140:143], v[152:155]\n\t"
"ds_read_b128 v[220:223], v249 offset:41728\n\t"
"v_mfma_f32_16x16x32_bf16 v[156:159], a[240:243], v[128:131], v[156:159]\n\t"
"v_mfma_f32_16x16x32_bf16 v[156:159], a[244:247], v[132:135], v[156:159]\n\t"
"s_waitcnt lgkmcnt(0)\n\t"
"buffer_load_dwordx4 v[180:183], v233, s[56:59], s22 offen offset:1024 sc1\n\t"
"v_add_f32 v220, v220, v221\n\t"
"v_mfma_f32_16x16x32_bf16 v[156:159], a[248:251], v[136:139], v[156:159]\n\t"
"s_waitcnt vmcnt(26)\n\t"
"ds_read_b64_tr_b16 a[98:99], v236 offset:32768\n\t"
"v_add_f32 v222, v222, v223\n\t"
"v_mfma_f32_16x16x32_bf16 v[156:159], a[252:255], v[140:143], v[156:159]\n\t"
"s_barrier\n\t"
"ds_read_b128 v[128:131], v6 offset:4096\n\t"
"v_add_f32 v220, v220, v222\n\t"
"v_mfma_f32_16x16x32_bf16 v[160:163], a[128:131], v[32:35], 0\n\t"
"ds_read_b128 v[132:135], v6 offset:5120\n\t"
"buffer_load_dwordx4 v[184:187], v234, s[56:59], s22 offen sc1\n\t"
"v_mfma_f32_16x16x32_bf16 v[160:163], a[132:135], v[36:39], v[160:163]\n\t"
"ds_read_b128 v[136:139], v6 offset:6144\n\t"
"v_rcp_f32 v230, v220\n\t"
"v_mfma_f32_16x16x32_bf16 v[160:163], a[136:139], v[40:43], v[160:163]\n\t"
"ds_read_b128 v[140:143], v6 offset:7168\n\t"
"v_mul_f32 v152, v152, v230\n\t"
"v_mfma_f32_16x16x32_bf16 v[160:163], a[140:143], v[44:47], v[160:163]\n\t"
"ds_read_b128 v[144:147], v7 offset:4096\n\t"
"buffer_load_dwordx4 v[188:191], v234, s[56:59], s22 offen offset:1024 sc1\n\t"
"v_mfma_f32_16x16x32_bf16 v[164:167], a[192:195], v[32:35], 0\n\t"
"s_add_u32 m0, s72, 32768\n\t"
"ds_read_b128 v[148:151], v7 offset:5120\n\t"
"v_mul_f32 v153, v153, v230\n\t"
"v_mfma_f32_16x16x32_bf16 v[164:167], a[196:199], v[36:39], v[164:167]\n\t"
"ds_read_b128 v[200:203], v7 offset:6144\n\t"
"v_mul_f32 v154, v154, v230\n\t"
"v_mfma_f32_16x16x32_bf16 v[164:167], a[200:203], v[40:43], v[164:167]\n\t"
"buffer_load_dwordx4 v[192:195], v235, s[56:59], s22 offen sc1\n\t"
"v_mfma_f32_16x16x32_bf16 v[164:167], a[204:207], v[44:47], v[164:167]\n\t"
"s_waitcnt lgkmcnt(6)\n\t"
"ds_read_b128 v[204:207], v7 offset:7168\n\t"
"v_mul_f32 v155, v155, v230\n\t"
"v_mfma_f32_16x16x32_bf16 v[160:163], a[144:147], v[128:131], v[160:163]\n\t"
"s_waitcnt lgkmcnt(6)\n\t"
"ds_read_b64_tr_b16 a[102:103], v237 offset:32768\n\t"
"v_mul_f32 v156, v156, v230\n\t"
"v_mfma_f32_16x16x32_bf16 v[160:163], a[148:151], v[132:135], v[160:163]\n\t"
"s_waitcnt lgkmcnt(6)\n\t"
"buffer_load_dwordx4 v[196:199], v235, s[56:59], s22 offen offset:1024 sc1\n\t"
"v_mfma_f32_16x16x32_bf16 v[160:163], a[152:155], v[136:139], v[160:163]\n\t"
"s_waitcnt lgkmcnt(5)\n\t"
"s_add_u32 s22, s94, 32768\n\t"
"ds_read_b64_tr_b16 a[106:107], v238 offset:32768\n\t"
"buffer_load_dwordx4 v[48:51], v232, s[56:59], s22 offen sc1\n\t"
"v_mfma_f32_16x16x32_bf16 v[160:163], a[156:159], v[140:143], v[160:163]\n\t"
"ds_read_b64_tr_b16 a[110:111], v239 offset:32768\n\t"
"ds_read_b64_tr_b16 a[114:115], v240 offset:32768\n\t"
"ds_read_b64_tr_b16 a[118:119], v241 offset:32768\n\t"
"ds_read_b64_tr_b16 a[122:123], v242 offset:32768\n\t"
"ds_read_b64_tr_b16 a[126:127], v243 offset:32768\n\t"
"s_waitcnt lgkmcnt(0)\n\t"
"buffer_load_dwordx4 v10, s[52:55], s77 offen lds\n\t"
"v_mfma_f32_16x16x32_bf16 v[164:167], a[208:211], v[128:131], v[164:167]\n\t"
"s_add_u32 s81, s65, 0\n\t"
"s_and_b32 s81, s81, 3\n\t"
"buffer_load_dwordx4 v11, s[52:55], s77 offen offset:1024 lds\n\t"
"v_mfma_f32_16x16x32_bf16 v[164:167], a[212:215], v[132:135], v[164:167]\n\t"
"s_lshl_b32 s81, s81, 11\n\t"
"ds_read_b128 v[128:131], v8 offset:4096\n\t"
"buffer_load_dwordx4 v12, s[52:55], s77 offen offset:2048 lds\n\t"
"v_mfma_f32_16x16x32_bf16 v[164:167], a[216:219], v[136:139], v[164:167]\n\t"
"v_mul_f32 v157, v157, v230\n\t"
"v_mul_f32 v158, v158, v230\n\t"
"v_mul_f32 v159, v159, v230\n\t"
"v_cvt_pk_bf16_f32 v224, v152, v153\n\t"
"v_cvt_pk_bf16_f32 v225, v154, v155\n\t"
"v_cvt_pk_bf16_f32 v226, v156, v157\n\t"
"v_cvt_pk_bf16_f32 v227, v158, v159\n\t"
"s_cmp_gt_u32 s10, 8\n\t"
"s_cselect_b32 s78, 1073741824, 0\n\t"
"v_add_u32 v94, s78, v250\n\t"
"v_mfma_f32_16x16x32_bf16 v[164:167], a[220:223], v[140:143], v[164:167]\n\t"
"ds_read_b128 v[132:135], v8 offset:5120\n\t"
"buffer_load_dwordx4 v13, s[52:55], s77 offen offset:3072 lds\n\t"
"v_mfma_f32_16x16x32_bf16 v[160:163], a[160:163], v[144:147], v[160:163]\n\t"
"buffer_store_dwordx4 v[224:227], v94, s[56:59], s98 offen sc1\n\t"
"ds_read_b128 v[136:139], v8 offset:6144\n\t"
"v_mfma_f32_16x16x32_bf16 v[160:163], a[164:167], v[148:151], v[160:163]\n\t"
"ds_read_b128 v[140:143], v8 offset:7168\n\t"
"s_add_u32 s77, s77, 229376\n\t"
"buffer_load_dwordx4 v[52:55], v232, s[56:59], s22 offen offset:1024 sc1\n\t"
"v_mfma_f32_16x16x32_bf16 v[160:163], a[168:171], v[200:203], v[160:163]\n\t"
"s_and_b32 s77, s77, 2097151\n\t"
"buffer_load_dwordx4 v[56:59], v233, s[56:59], s22 offen sc1\n\t"
"v_mfma_f32_16x16x32_bf16 v[160:163], a[172:175], v[204:207], v[160:163]\n\t"
"s_add_u32 s98, s98, 32768\n\t"
"buffer_load_dwordx4 v[60:63], v233, s[56:59], s22 offen offset:1024 sc1\n\t"
"v_mfma_f32_16x16x32_bf16 v[164:167], a[224:227], v[144:147], v[164:167]\n\t"
"s_waitcnt vmcnt(35)\n\t"
"ds_read_b64_tr_b16 a[160:161], v238 offset:36864\n\t"
"v_add_u32 v80, s81, v250\n\t"
"v_mfma_f32_16x16x32_bf16 v[164:167], a[228:231], v[148:151], v[164:167]\n\t"
"s_add_u32 s81, s97, 341\n\t"
"s_and_b32 s81, s81, 1023\n\t"
"buffer_load_dwordx4 v[64:67], v234, s[56:59], s22 offen sc1\n\t"
"v_mfma_f32_16x16x32_bf16 v[164:167], a[232:235], v[200:203], v[164:167]\n\t"
"s_waitcnt vmcnt(32)\n\t"
"ds_read_b64_tr_b16 a[162:163], v238 offset:0\n\t"
"v_mov_b32 v84, s81\n\t"
"v_mfma_f32_16x16x32_bf16 v[164:167], a[236:239], v[204:207], v[164:167]\n\t"
"s_waitcnt lgkmcnt(5)\n\t"
"s_waitcnt vmcnt(28)\n\t"
"s_add_u32 s81, s65, 1\n\t"
"s_and_b32 s81, s81, 3\n\t"
"ds_read_b64_tr_b16 a[164:165], v238 offset:24576\n\t"
"s_lshl_b32 s81, s81, 11\n\t"
"v_add_u32 v81, s81, v250\n\t"
"v_mfma_f32_16x16x32_bf16 v[160:163], a[176:179], v[128:131], v[160:163]\n\t"
"s_waitcnt lgkmcnt(5)\n\t"
"s_add_u32 s81, s97, 682\n\t"
"s_and_b32 s81, s81, 1023\n\t"
"ds_read_b128 v[220:223], v249 offset:41984\n\t"
"buffer_load_dwordx4 v[68:71], v234, s[56:59], s22 offen offset:1024 sc1\n\t"
"v_mfma_f32_16x16x32_bf16 v[160:163], a[180:183], v[132:135], v[160:163]\n\t"
"s_waitcnt lgkmcnt(5)\n\t"
"s_waitcnt vmcnt(24)\n\t"
"ds_read_b64_tr_b16 a[166:167], v238 offset:28672\n\t"
"v_mov_b32 v85, s81\n\t"
"v_mfma_f32_16x16x32_bf16 v[160:163], a[184:187], v[136:139], v[160:163]\n\t"
"s_waitcnt lgkmcnt(5)\n\t"
"s_waitcnt vmcnt(19)\n\t"
"s_add_u32 s81, s65, 2\n\t"
"s_and_b32 s81, s81, 3\n\t"
"ds_read_b64_tr_b16 a[168:169], v238 offset:4096\n\t"
"v_add_u32 v9, -4096, v9\n\t"
"v_mfma_f32_16x16x32_bf16 v[160:163], a[188:191], v[140:143], v[160:163]\n\t"
"s_waitcnt vmcnt(6)\n\t"
"s_lshl_b32 s81, s81, 11\n\t"
"ds_read_b64_tr_b16 a[170:171], v238 offset:32768\n\t"
"buffer_load_dwordx4 v[72:75], v235, s[56:59], s22 offen sc1\n\t"
"v_mfma_f32_16x16x32_bf16 v[164:167], a[240:243], v[128:131], v[164:167]\n\t"
"ds_read_b64_tr_b16 a[176:177], v239 offset:36864\n\t"
"v_add_u32 v6, -4096, v6\n\t"
"v_mfma_f32_16x16x32_bf16 v[164:167], a[244:247], v[132:135], v[164:167]\n\t"
"s_waitcnt lgkmcnt(4)\n\t"
"ds_read_b64_tr_b16 a[178:179], v239 offset:0\n\t"
"v_add_f32 v220, v220, v221\n\t"
"v_mfma_f32_16x16x32_bf16 v[164:167], a[248:251], v[136:139], v[164:167]\n\t"
"buffer_load_dwordx4 v[76:79], v235, s[56:59], s22 offen offset:1024 sc1\n\t"
"v_mfma_f32_16x16x32_bf16 v[164:167], a[252:255], v[140:143], v[164:167]\n\t"
"ds_read_b64_tr_b16 a[180:181], v239 offset:24576\n\t"
"v_add_f32 v222, v222, v223\n\t"
"v_mfma_f32_16x16x32_bf16 v[128:131], v[104:107], v[168:171], 0\n\t"
"ds_read_b64_tr_b16 a[182:183], v239 offset:28672\n\t"
"v_add_f32 v220, v220, v222\n\t"
"v_mfma_f32_16x16x32_bf16 v[128:131], a[8:11], v[172:175], v[128:131]\n\t"
"ds_read_b64_tr_b16 a[184:185], v239 offset:4096\n\t"
"v_add_u32 v7, -2048, v7\n\t"
"v_mfma_f32_16x16x32_bf16 v[128:131], a[40:43], v[176:179], v[128:131]\n\t"
"ds_read_b64_tr_b16 a[186:187], v239 offset:32768\n\t"
"v_add_u32 v8, 0, v8\n\t"
"v_mfma_f32_16x16x32_bf16 v[128:131], a[72:75], v[180:183], v[128:131]\n\t"
"v_rcp_f32 v230, v220\n\t"
"s_or_b32 s90, s86, 4\n\t"
"v_cmp_le_u64 vcc, s[90:91], v[2:3]\n\t"
"v_cmp_ge_u64 s[92:93], s[88:89], v[2:3]\n\t"
"s_nop 0\n\t"
"s_and_b64 vcc, vcc, s[92:93]\n\t"
"s_cmp_eq_u64 vcc, -1\n\t"
"s_cbranch_scc0 LPS19_%=\n\t"
"LPR19_%=:\n\t"
"v_mfma_f32_16x16x32_bf16 v[128:131], a[104:107], v[184:187], v[128:131]\n\t"
"ds_read_b64_tr_b16 a[192:193], v240 offset:36864\n\t"
"v_mul_f32 v160, v160, v230\n\t"
"v_mfma_f32_16x16x32_bf16 v[128:131], a[160:163], v[188:191], v[128:131]\n\t"
"s_waitcnt lgkmcnt(9)\n\t"
"s_add_u32 s22, s94, 65536\n\t"
"ds_read_b64_tr_b16 a[194:195], v240 offset:0\n\t"
"v_mul_f32 v161, v161, v230\n\t"
"v_mfma_f32_16x16x32_bf16 v[128:131], a[164:167], v[192:195], v[128:131]\n\t"
"s_waitcnt lgkmcnt(8)\n\t"
"ds_read_b64_tr_b16 a[196:197], v240 offset:24576\n\t"
"v_mul_f32 v162, v162, v230\n\t"
"v_mfma_f32_16x16x32_bf16 v[128:131], a[168:171], v[196:199], v[128:131]\n\t"
"ds_read_b64_tr_b16 a[198:199], v240 offset:28672\n\t"
"v_mul_f32 v163, v163, v230\n\t"
"v_mfma_f32_16x16x32_bf16 v[132:135], v[108:111], v[168:171], 0\n\t"
"ds_read_b64_tr_b16 a[200:201], v240 offset:4096\n\t"
"v_mul_f32 v164, v164, v230\n\t"
"v_mfma_f32_16x16x32_bf16 v[132:135], a[12:15], v[172:175], v[132:135]\n\t"
"ds_read_b64_tr_b16 a[202:203], v240 offset:32768\n\t"
"v_mul_f32 v165, v165, v230\n\t"
"v_mfma_f32_16x16x32_bf16 v[132:135], a[44:47], v[176:179], v[132:135]\n\t"
"buffer_load_dwordx4 a[140:143], v232, s[56:59], s22 offen sc1\n\t"
"v_mfma_f32_16x16x32_bf16 v[132:135], a[76:79], v[180:183], v[132:135]\n\t"
"ds_write_b64 v6, v[128:129] offset:0\n\t"
"v_mfma_f32_16x16x32_bf16 v[132:135], a[108:111], v[184:187], v[132:135]\n\t"
"s_waitcnt lgkmcnt(11)\n\t"
"buffer_load_dwordx4 a[156:159], v232, s[56:59], s22 offen offset:1024 sc1\n\t"
"v_mfma_f32_16x16x32_bf16 v[132:135], a[176:179], v[188:191], v[132:135]\n\t"
"s_waitcnt lgkmcnt(9)\n\t"
"buffer_load_dwordx4 a[172:175], v233, s[56:59], s22 offen sc1\n\t"
"v_mfma_f32_16x16x32_bf16 v[132:135], a[180:183], v[192:195], v[132:135]\n\t"
"v_mul_f32 v166, v166, v230\n\t"
"v_mul_f32 v167, v167, v230\n\t"
"v_cvt_pk_bf16_f32 v224, v160, v161\n\t"
"v_cvt_pk_bf16_f32 v225, v162, v163\n\t"
"v_cvt_pk_bf16_f32 v226, v164, v165\n\t"
"v_cvt_pk_bf16_f32 v227, v166, v167\n\t"
"s_cmp_gt_u32 s10, 9\n\t"
"s_cselect_b32 s78, 1073741824, 0\n\t"
"v_add_u32 v94, s78, v250\n\t"
"s_waitcnt lgkmcnt(7)\n\t"
"v_mfma_f32_16x16x32_bf16 v[132:135], a[184:187], v[196:199], v[132:135]\n\t"
"buffer_store_dwordx4 v[224:227], v94, s[56:59], s98 offen sc1\n\t"
"v_mfma_f32_16x16x32_bf16 v[136:139], v[112:115], v[168:171], 0\n\t"
"ds_read_b64_tr_b16 a[208:209], v241 offset:36864\n\t"
"s_add_u32 s98, s98, 32768\n\t"
"v_add_u32 v82, s81, v250\n\t"
"v_mfma_f32_16x16x32_bf16 v[136:139], a[16:19], v[172:175], v[136:139]\n\t"
"ds_read_b64_tr_b16 a[210:211], v241 offset:0\n\t"
"s_add_u32 s81, s97, 243\n\t"
"s_and_b32 s81, s81, 1023\n\t"
"v_mfma_f32_16x16x32_bf16 v[136:139], a[48:51], v[176:179], v[136:139]\n\t"
"ds_write_b64 v6, v[130:131] offset:8\n\t"
"ds_read_b64_tr_b16 a[212:213], v241 offset:24576\n\t"
"v_mfma_f32_16x16x32_bf16 v[136:139], a[80:83], v[180:183], v[136:139]\n\t"
"ds_read_b64_tr_b16 a[214:215], v241 offset:28672\n\t"
"v_mov_b32 v86, s81\n\t"
"v_mfma_f32_16x16x32_bf16 v[136:139], a[112:115], v[184:187], v[136:139]\n\t"
"s_waitcnt lgkmcnt(10)\n\t"
"ds_read_b64_tr_b16 a[216:217], v241 offset:4096\n\t"
"s_add_u32 s81, s65, 3\n\t"
"s_and_b32 s81, s81, 3\n\t"
"v_mfma_f32_16x16x32_bf16 v[136:139], a[192:195], v[188:191], v[136:139]\n\t"
"s_waitcnt lgkmcnt(9)\n\t"
"ds_read_b64_tr_b16 a[218:219], v241 offset:32768\n\t"
"s_lshl_b32 s81, s81, 11\n\t"
"v_add_u32 v83, s81, v250\n\t"
"v_mfma_f32_16x16x32_bf16 v[136:139], a[196:199], v[192:195], v[136:139]\n\t"
"s_waitcnt lgkmcnt(8)\n\t"
"s_add_u32 s81, s97, 781\n\t"
"s_and_b32 s81, s81, 1023\n\t"
"ds_write_b64 v6, v[132:133] offset:1024\n\t"
"v_mfma_f32_16x16x32_bf16 v[136:139], a[200:203], v[196:199], v[136:139]\n\t"
"buffer_load_dwordx4 a[188:191], v233, s[56:59], s22 offen offset:1024 sc1\n\t"
"v_mfma_f32_16x16x32_bf16 v[140:143], v[116:119], v[168:171], 0\n\t"
"ds_read_b64_tr_b16 a[224:225], v242 offset:36864\n\t"
"v_mov_b32 v87, s81\n\t"
"v_mfma_f32_16x16x32_bf16 v[140:143], a[20:23], v[172:175], v[140:143]\n\t"
"ds_read_b64_tr_b16 a[226:227], v242 offset:0\n\t"
"s_mov_b32 s27, 1023\n\t"
"v_and_b32 v88, 15, v1\n\t"
"v_mfma_f32_16x16x32_bf16 v[140:143], a[52:55], v[176:179], v[140:143]\n\t"
"buffer_load_dwordx4 a[204:207], v234, s[56:59], s22 offen sc1\n\t"
"ds_write_b64 v6, v[134:135] offset:1032\n\t"
"ds_read_b64_tr_b16 a[228:229], v242 offset:24576\n\t"
"v_mfma_f32_16x16x32_bf16 v[140:143], a[84:87], v[180:183], v[140:143]\n\t"
"ds_read_b64_tr_b16 a[230:231], v242 offset:28672\n\t"
"v_lshlrev_b32 v88, 11, v88\n\t"
"v_mfma_f32_16x16x32_bf16 v[140:143], a[116:119], v[184:187], v[140:143]\n\t"
"s_waitcnt lgkmcnt(11)\n\t"
"ds_read_b64_tr_b16 a[232:233], v242 offset:4096\n\t"
"v_lshrrev_b32 v89, 4, v1\n\t"
"v_mfma_f32_16x16x32_bf16 v[140:143], a[208:211], v[188:191], v[140:143]\n\t"
"s_waitcnt lgkmcnt(9)\n\t"
"buffer_load_dwordx4 a[220:223], v234, s[56:59], s22 offen offset:1024 sc1\n\t"
"ds_read_b64_tr_b16 a[234:235], v242 offset:32768\n\t"
"v_mfma_f32_16x16x32_bf16 v[140:143], a[212:215], v[192:195], v[140:143]\n\t"
"s_waitcnt lgkmcnt(8)\n\t"
"buffer_load_dwordx4 a[236:239], v235, s[56:59], s22 offen sc1\n\t"
"v_mfma_f32_16x16x32_bf16 v[140:143], a[216:219], v[196:199], v[140:143]\n\t"
"ds_write_b64 v7, v[136:137] offset:0\n\t"
"v_mfma_f32_16x16x32_bf16 v[144:147], v[120:123], v[168:171], 0\n\t"
"ds_read_b64_tr_b16 a[240:241], v243 offset:36864\n\t"
"s_max_u32 s78, s9, 7\n\t"
"v_lshl_add_u32 v88, v89, 3, v88\n\t"
"v_mfma_f32_16x16x32_bf16 v[144:147], a[24:27], v[172:175], v[144:147]\n\t"
"ds_read_b64_tr_b16 a[242:243], v243 offset:0\n\t"
"s_mul_i32 s21, s66, 327680\n\t"
"s_lshl_b32 s81, s85, 8\n\t"
"v_mfma_f32_16x16x32_bf16 v[144:147], a[56:59], v[176:179], v[144:147]\n\t"
"s_add_u32 s21, s21, s81\n\t"
"s_lshl_b32 s81, s69, 6\n\t"
"ds_write_b64 v7, v[138:139] offset:8\n\t"
"ds_read_b64_tr_b16 a[244:245], v243 offset:24576\n\t"
"s_add_u32 s21, s21, s81\n\t"
"s_and_b32 s81, s65, 1\n\t"
"s_lshl_b32 s81, s81, 1\n\t"
"s_add_u32 s81, s81, s70\n\t"
"s_sub_i32 s19, s81, s69\n\t"
"v_mfma_f32_16x16x32_bf16 v[144:147], a[88:91], v[180:183], v[144:147]\n\t"
"buffer_load_dwordx4 a[252:255], v235, s[56:59], s22 offen offset:1024 sc1\n\t"
"ds_read_b64_tr_b16 a[246:247], v243 offset:28672\n\t"
"v_mfma_f32_16x16x32_bf16 v[144:147], a[120:123], v[184:187], v[144:147]\n\t"
"s_waitcnt lgkmcnt(11)\n\t"
"ds_read_b64_tr_b16 a[248:249], v243 offset:4096\n\t"
"v_or_b32 v228, s78, v4\n\t"
"v_mfma_f32_16x16x32_bf16 v[144:147], a[224:227], v[188:191], v[144:147]\n\t"
"s_waitcnt lgkmcnt(9)\n\t"
"ds_read_b64_tr_b16 a[250:251], v243 offset:32768\n\t"
"v_mfma_f32_16x16x32_bf16 v[144:147], a[228:231], v[192:195], v[144:147]\n\t"
"s_waitcnt lgkmcnt(8)\n\t"
"buffer_store_dwordx2 v[228:229], v5, s[56:59], s17 offen sc1\n\t"
"v_mfma_f32_16x16x32_bf16 v[144:147], a[232:235], v[196:199], v[144:147]\n\t"
"ds_write_b64 v7, v[140:141] offset:1024\n\t"
"v_mfma_f32_16x16x32_bf16 v[148:151], v[124:127], v[168:171], 0\n\t"
"ds_read_b64_tr_b16 a[128:129], v236 offset:36864\n\t"
"v_mfma_f32_16x16x32_bf16 v[148:151], a[28:31], v[172:175], v[148:151]\n\t"
"ds_read_b64_tr_b16 a[130:131], v236 offset:0\n\t"
"v_mfma_f32_16x16x32_bf16 v[148:151], a[60:63], v[176:179], v[148:151]\n\t"
"ds_write_b64 v7, v[142:143] offset:1032\n\t"
"ds_read_b64_tr_b16 a[132:133], v236 offset:24576\n\t"
"v_mfma_f32_16x16x32_bf16 v[148:151], a[92:95], v[180:183], v[148:151]\n\t"
"ds_read_b64_tr_b16 a[134:135], v236 offset:28672\n\t"
"v_mfma_f32_16x16x32_bf16 v[148:151], a[124:127], v[184:187], v[148:151]\n\t"
"s_waitcnt lgkmcnt(11)\n\t"
"ds_read_b64_tr_b16 a[136:137], v236 offset:4096\n\t"
"v_mfma_f32_16x16x32_bf16 v[148:151], a[240:243], v[188:191], v[148:151]\n\t"
"s_waitcnt lgkmcnt(9)\n\t"
"ds_read_b64_tr_b16 a[138:139], v236 offset:32768\n\t"
"v_mfma_f32_16x16x32_bf16 v[148:151], a[244:247], v[192:195], v[148:151]\n\t"
"s_waitcnt lgkmcnt(8)\n\t"
"ds_write_b64 v8, v[144:145] offset:0\n\t"
"v_mfma_f32_16x16x32_bf16 v[148:151], a[248:251], v[196:199], v[148:151]\n\t"
"v_mfma_f32_16x16x32_bf16 v[152:155], v[96:99], v[168:171], 0\n\t"
"ds_read_b64_tr_b16 a[144:145], v237 offset:36864\n\t"
"v_mfma_f32_16x16x32_bf16 v[152:155], a[0:3], v[172:175], v[152:155]\n\t"
"ds_read_b64_tr_b16 a[146:147], v237 offset:0\n\t"
"v_mfma_f32_16x16x32_bf16 v[152:155], a[32:35], v[176:179], v[152:155]\n\t"
"ds_write_b64 v8, v[146:147] offset:8\n\t"
"ds_read_b64_tr_b16 a[148:149], v237 offset:24576\n\t"
"v_mfma_f32_16x16x32_bf16 v[152:155], a[64:67], v[180:183], v[152:155]\n\t"
"ds_read_b64_tr_b16 a[150:151], v237 offset:28672\n\t"
"v_mfma_f32_16x16x32_bf16 v[152:155], a[96:99], v[184:187], v[152:155]\n\t"
"s_waitcnt lgkmcnt(11)\n\t"
"ds_read_b64_tr_b16 a[152:153], v237 offset:4096\n\t"
"v_mfma_f32_16x16x32_bf16 v[152:155], a[128:131], v[188:191], v[152:155]\n\t"
"s_waitcnt lgkmcnt(9)\n\t"
"ds_read_b64_tr_b16 a[154:155], v237 offset:32768\n\t"
"v_mfma_f32_16x16x32_bf16 v[152:155], a[132:135], v[192:195], v[152:155]\n\t"
"s_waitcnt lgkmcnt(8)\n\t"
"ds_write_b64 v8, v[148:149] offset:1024\n\t"
"v_mfma_f32_16x16x32_bf16 v[152:155], a[136:139], v[196:199], v[152:155]\n\t"
"v_mfma_f32_16x16x32_bf16 v[156:159], v[100:103], v[168:171], 0\n\t"
"v_mfma_f32_16x16x32_bf16 v[156:159], a[4:7], v[172:175], v[156:159]\n\t"
"ds_write_b64 v8, v[150:151] offset:1032\n\t"
"v_mfma_f32_16x16x32_bf16 v[156:159], a[36:39], v[176:179], v[156:159]\n\t"
"v_mfma_f32_16x16x32_bf16 v[156:159], a[68:71], v[180:183], v[156:159]\n\t"
"v_mfma_f32_16x16x32_bf16 v[156:159], a[100:103], v[184:187], v[156:159]\n\t"
"s_waitcnt lgkmcnt(7)\n\t"
"v_mfma_f32_16x16x32_bf16 v[156:159], a[144:147], v[188:191], v[156:159]\n\t"
"s_waitcnt lgkmcnt(4)\n\t"
"v_mfma_f32_16x16x32_bf16 v[156:159], a[148:151], v[192:195], v[156:159]\n\t"
"s_waitcnt lgkmcnt(2)\n\t"
"v_mfma_f32_16x16x32_bf16 v[156:159], a[152:155], v[196:199], v[156:159]\n\t"
"s_waitcnt lgkmcnt(0)\n\t"
"s_barrier\n\t"
"ds_read_b128 v[208:211], v9 offset:2048\n\t"
"v_mfma_f32_16x16x32_bf16 v[128:131], v[104:107], v[48:51], 0\n\t"
"s_waitcnt vmcnt(16)\n\t"
"s_lshr_b64 s[80:81], s[82:83], 0\n\t"
"s_and_b32 s80, s80, 15\n\t"
"ds_read_b128 v[212:215], v9 offset:3072\n\t"
"v_mfma_f32_16x16x32_bf16 v[128:131], a[8:11], v[52:55], v[128:131]\n\t"
"s_waitcnt vmcnt(15)\n\t"
"s_mul_i32 s80, s80, 32768\n\t"
"s_add_u32 s20, s80, s95\n\t"
"ds_read_b128 v[216:219], v9 offset:4096\n\t"
"v_mfma_f32_16x16x32_bf16 v[128:131], a[40:43], v[56:59], v[128:131]\n\t"
"s_waitcnt vmcnt(14)\n\t"
"s_add_u32 s22, s94, 98304\n\t"
"ds_read_b128 v[220:223], v9 offset:5120\n\t"
"v_mfma_f32_16x16x32_bf16 v[128:131], a[72:75], v[60:63], v[128:131]\n\t"
"s_waitcnt vmcnt(13)\n\t"
"ds_read_b128 v[200:203], v9 offset:0\n\t"
"v_mfma_f32_16x16x32_bf16 v[128:131], a[104:107], v[64:67], v[128:131]\n\t"
"s_waitcnt vmcnt(12)\n\t"
"ds_read_b128 v[204:207], v9 offset:1024\n\t"
"v_mfma_f32_16x16x32_bf16 v[128:131], a[160:163], v[68:71], v[128:131]\n\t"
"s_waitcnt vmcnt(11)\n\t"
"buffer_load_dwordx4 v[168:171], v232, s[56:59], s22 offen sc1\n\t"
"v_mfma_f32_16x16x32_bf16 v[128:131], a[164:167], v[72:75], v[128:131]\n\t"
"s_waitcnt vmcnt(11)\n\t"
"buffer_load_dwordx4 v[172:175], v232, s[56:59], s22 offen offset:1024 sc1\n\t"
"v_mfma_f32_16x16x32_bf16 v[128:131], a[168:171], v[76:79], v[128:131]\n\t"
"s_waitcnt lgkmcnt(3)\n\t"
"v_add_f32 v208, v208, v216\n\t"
"v_add_f32 v209, v209, v217\n\t"
"v_mfma_f32_16x16x32_bf16 v[132:135], v[108:111], v[48:51], 0\n\t"
"v_add_f32 v210, v210, v218\n\t"
"v_add_f32 v211, v211, v219\n\t"
"v_mfma_f32_16x16x32_bf16 v[132:135], a[12:15], v[52:55], v[132:135]\n\t"
"buffer_load_dwordx4 v[176:179], v233, s[56:59], s22 offen sc1\n\t"
"v_mfma_f32_16x16x32_bf16 v[132:135], a[44:47], v[56:59], v[132:135]\n\t"
"ds_write_b64 v6, v[128:129] offset:6144\n\t"
"v_mfma_f32_16x16x32_bf16 v[132:135], a[76:79], v[60:63], v[132:135]\n\t"
"s_waitcnt lgkmcnt(3)\n\t"
"v_add_f32 v212, v212, v220\n\t"
"v_add_f32 v213, v213, v221\n\t"
"v_mfma_f32_16x16x32_bf16 v[132:135], a[108:111], v[64:67], v[132:135]\n\t"
"buffer_load_dwordx4 v[180:183], v233, s[56:59], s22 offen offset:1024 sc1\n\t"
"v_mfma_f32_16x16x32_bf16 v[132:135], a[176:179], v[68:71], v[132:135]\n\t"
"ds_write_b64 v6, v[130:131] offset:6152\n\t"
"v_mfma_f32_16x16x32_bf16 v[132:135], a[180:183], v[72:75], v[132:135]\n\t"
"v_add_f32 v214, v214, v222\n\t"
"v_add_f32 v215, v215, v223\n\t"
"v_mfma_f32_16x16x32_bf16 v[132:135], a[184:187], v[76:79], v[132:135]\n\t"
"buffer_load_dwordx4 v[184:187], v234, s[56:59], s22 offen sc1\n\t"
"v_mfma_f32_16x16x32_bf16 v[136:139], v[112:115], v[48:51], 0\n\t"
"s_waitcnt lgkmcnt(3)\n\t"
"v_add_f32 v208, v208, v200\n\t"
"v_add_f32 v209, v209, v201\n\t"
"v_mfma_f32_16x16x32_bf16 v[136:139], a[16:19], v[52:55], v[136:139]\n\t"
"v_add_f32 v210, v210, v202\n\t"
"v_add_f32 v211, v211, v203\n\t"
"v_mfma_f32_16x16x32_bf16 v[136:139], a[48:51], v[56:59], v[136:139]\n\t"
"ds_write_b64 v6, v[132:133] offset:7168\n\t"
"v_mfma_f32_16x16x32_bf16 v[136:139], a[80:83], v[60:63], v[136:139]\n\t"
"buffer_load_dwordx4 v[188:191], v234, s[56:59], s22 offen offset:1024 sc1\n\t"
"v_mfma_f32_16x16x32_bf16 v[136:139], a[112:115], v[64:67], v[136:139]\n\t"
"s_waitcnt lgkmcnt(3)\n\t"
"v_add_f32 v212, v212, v204\n\t"
"v_add_f32 v213, v213, v205\n\t"
"v_mfma_f32_16x16x32_bf16 v[136:139], a[192:195], v[68:71], v[136:139]\n\t"
"ds_write_b64 v6, v[134:135] offset:7176\n\t"
"v_mfma_f32_16x16x32_bf16 v[136:139], a[196:199], v[72:75], v[136:139]\n\t"
"buffer_load_dwordx4 v[192:195], v235, s[56:59], s22 offen sc1\n\t"
"v_mfma_f32_16x16x32_bf16 v[136:139], a[200:203], v[76:79], v[136:139]\n\t"
"v_add_f32 v214, v214, v206\n\t"
"v_add_f32 v215, v215, v207\n\t"
"v_mfma_f32_16x16x32_bf16 v[140:143], v[116:119], v[48:51], 0\n\t"
"buffer_load_dwordx4 v[196:199], v235, s[56:59], s22 offen offset:1024 sc1\n\t"
"v_mfma_f32_16x16x32_bf16 v[140:143], a[20:23], v[52:55], v[140:143]\n\t"
"v_add_f32 v152, v152, v208\n\t"
"v_add_f32 v153, v153, v209\n\t"
"v_mfma_f32_16x16x32_bf16 v[140:143], a[52:55], v[56:59], v[140:143]\n\t"
"ds_write_b64 v7, v[136:137] offset:6144\n\t"
"v_mfma_f32_16x16x32_bf16 v[140:143], a[84:87], v[60:63], v[140:143]\n\t"
"v_add_f32 v154, v154, v210\n\t"
"v_add_f32 v155, v155, v211\n\t"
"v_mfma_f32_16x16x32_bf16 v[140:143], a[116:119], v[64:67], v[140:143]\n\t"
"buffer_load_dwordx2 v[2:3], v15, s[56:59], s16 offen sc1\n\t"
"v_mfma_f32_16x16x32_bf16 v[140:143], a[208:211], v[68:71], v[140:143]\n\t"
"ds_write_b64 v7, v[138:139] offset:6152\n\t"
"v_mfma_f32_16x16x32_bf16 v[140:143], a[212:215], v[72:75], v[140:143]\n\t"
"v_add_f32 v156, v156, v212\n\t"
"v_add_f32 v157, v157, v213\n\t"
"v_add_f32 v158, v158, v214\n\t"
"v_add_f32 v159, v159, v215\n\t"
"v_mfma_f32_16x16x32_bf16 v[140:143], a[216:219], v[76:79], v[140:143]\n\t"
"v_bfi_b32 v152, s27, v84, v152\n\t"
"v_bfi_b32 v153, s27, v85, v153\n\t"
"v_mfma_f32_16x16x32_bf16 v[144:147], v[120:123], v[48:51], 0\n\t"
"v_bfi_b32 v154, s27, v86, v154\n\t"
"v_bfi_b32 v155, s27, v87, v155\n\t"
"v_mfma_f32_16x16x32_bf16 v[144:147], a[24:27], v[52:55], v[144:147]\n\t"
"v_bfi_b32 v156, s27, v84, v156\n\t"
"v_bfi_b32 v157, s27, v85, v157\n\t"
"v_mfma_f32_16x16x32_bf16 v[144:147], a[56:59], v[56:59], v[144:147]\n\t"
"v_bfi_b32 v158, s27, v86, v158\n\t"
"v_bfi_b32 v159, s27, v87, v159\n\t"
"v_mfma_f32_16x16x32_bf16 v[144:147], a[88:91], v[60:63], v[144:147]\n\t"
"buffer_store_dwordx4 v[152:155], v80, s[56:59], s20 offen sc1\n\t"
"v_mfma_f32_16x16x32_bf16 v[144:147], a[120:123], v[64:67], v[144:147]\n\t"
"buffer_store_dwordx4 v[156:159], v80, s[56:59], s20 offen offset:1024 sc1\n\t"
"v_mfma_f32_16x16x32_bf16 v[144:147], a[224:227], v[68:71], v[144:147]\n\t"
"ds_write_b64 v7, v[140:141] offset:7168\n\t"
"v_mfma_f32_16x16x32_bf16 v[144:147], a[228:231], v[72:75], v[144:147]\n\t"
"v_mfma_f32_16x16x32_bf16 v[144:147], a[232:235], v[76:79], v[144:147]\n\t"
"v_mfma_f32_16x16x32_bf16 v[148:151], v[124:127], v[48:51], 0\n\t"
"ds_write_b64 v7, v[142:143] offset:7176\n\t"
"v_mfma_f32_16x16x32_bf16 v[148:151], a[28:31], v[52:55], v[148:151]\n\t"
"v_mfma_f32_16x16x32_bf16 v[148:151], a[60:63], v[56:59], v[148:151]\n\t"
"v_mfma_f32_16x16x32_bf16 v[148:151], a[92:95], v[60:63], v[148:151]\n\t"
"v_mfma_f32_16x16x32_bf16 v[148:151], a[124:127], v[64:67], v[148:151]\n\t"
"v_mfma_f32_16x16x32_bf16 v[148:151], a[240:243], v[68:71], v[148:151]\n\t"
"v_mfma_f32_16x16x32_bf16 v[148:151], a[244:247], v[72:75], v[148:151]\n\t"
"ds_write_b64 v8, v[144:145] offset:6144\n\t"
"v_mfma_f32_16x16x32_bf16 v[148:151], a[248:251], v[76:79], v[148:151]\n\t"
"v_mfma_f32_16x16x32_bf16 v[160:163], v[96:99], v[48:51], 0\n\t"
"v_mfma_f32_16x16x32_bf16 v[160:163], a[0:3], v[52:55], v[160:163]\n\t"
"ds_write_b64 v8, v[146:147] offset:6152\n\t"
"v_mfma_f32_16x16x32_bf16 v[160:163], a[32:35], v[56:59], v[160:163]\n\t"
"v_mfma_f32_16x16x32_bf16 v[160:163], a[64:67], v[60:63], v[160:163]\n\t"
"v_mfma_f32_16x16x32_bf16 v[160:163], a[96:99], v[64:67], v[160:163]\n\t"
"v_mfma_f32_16x16x32_bf16 v[160:163], a[128:131], v[68:71], v[160:163]\n\t"
"v_mfma_f32_16x16x32_bf16 v[160:163], a[132:135], v[72:75], v[160:163]\n\t"
"ds_write_b64 v8, v[148:149] offset:7168\n\t"
"v_mfma_f32_16x16x32_bf16 v[160:163], a[136:139], v[76:79], v[160:163]\n\t"
"v_mfma_f32_16x16x32_bf16 v[164:167], v[100:103], v[48:51], 0\n\t"
"v_mfma_f32_16x16x32_bf16 v[164:167], a[4:7], v[52:55], v[164:167]\n\t"
"ds_write_b64 v8, v[150:151] offset:7176\n\t"
"v_mfma_f32_16x16x32_bf16 v[164:167], a[36:39], v[56:59], v[164:167]\n\t"
"v_mfma_f32_16x16x32_bf16 v[164:167], a[68:71], v[60:63], v[164:167]\n\t"
"v_mfma_f32_16x16x32_bf16 v[164:167], a[100:103], v[64:67], v[164:167]\n\t"
"v_mfma_f32_16x16x32_bf16 v[164:167], a[144:147], v[68:71], v[164:167]\n\t"
"v_mfma_f32_16x16x32_bf16 v[164:167], a[148:151], v[72:75], v[164:167]\n\t"
"v_mfma_f32_16x16x32_bf16 v[164:167], a[152:155], v[76:79], v[164:167]\n\t"
"s_waitcnt lgkmcnt(0)\n\t"
"s_barrier\n\t"
"ds_read_b128 v[208:211], v9 offset:8192\n\t"
"s_waitcnt vmcnt(20)\n\t"
"v_mfma_f32_16x16x32_bf16 v[128:131], v[104:107], a[140:143], 0\n\t"
"s_waitcnt vmcnt(19)\n\t"
"s_lshr_b64 s[80:81], s[82:83], 4\n\t"
"s_and_b32 s80, s80, 15\n\t"
"ds_read_b128 v[212:215], v9 offset:9216\n\t"
"v_mfma_f32_16x16x32_bf16 v[128:131], a[8:11], a[156:159], v[128:131]\n\t"
"s_waitcnt vmcnt(18)\n\t"
"s_mul_i32 s80, s80, 32768\n\t"
"s_add_u32 s20, s80, s95\n\t"
"ds_read_b128 v[216:219], v9 offset:10240\n\t"
"v_mfma_f32_16x16x32_bf16 v[128:131], a[40:43], a[172:175], v[128:131]\n\t"
"s_waitcnt vmcnt(16)\n\t"
"ds_read_b128 v[220:223], v9 offset:11264\n\t"
"v_mfma_f32_16x16x32_bf16 v[128:131], a[72:75], a[188:191], v[128:131]\n\t"
"s_waitcnt vmcnt(15)\n\t"
"ds_read_b128 v[200:203], v9 offset:6144\n\t"
"v_mfma_f32_16x16x32_bf16 v[128:131], a[104:107], a[204:207], v[128:131]\n\t"
"s_waitcnt vmcnt(14)\n\t"
"ds_read_b128 v[204:207], v9 offset:7168\n\t"
"v_mfma_f32_16x16x32_bf16 v[128:131], a[160:163], a[220:223], v[128:131]\n\t"
"s_waitcnt vmcnt(13)\n\t"
"s_waitcnt lgkmcnt(3)\n\t"
"v_add_f32 v208, v208, v216\n\t"
"v_add_f32 v209, v209, v217\n\t"
"v_mfma_f32_16x16x32_bf16 v[128:131], a[164:167], a[236:239], v[128:131]\n\t"
"s_waitcnt vmcnt(12)\n\t"
"v_add_f32 v210, v210, v218\n\t"
"v_add_f32 v211, v211, v219\n\t"
"v_mfma_f32_16x16x32_bf16 v[128:131], a[168:171], a[252:255], v[128:131]\n\t"
"s_waitcnt lgkmcnt(2)\n\t"
"v_add_f32 v212, v212, v220\n\t"
"v_add_f32 v213, v213, v221\n\t"
"v_mfma_f32_16x16x32_bf16 v[132:135], v[108:111], a[140:143], 0\n\t"
"v_add_f32 v214, v214, v222\n\t"
"v_add_f32 v215, v215, v223\n\t"
"v_mfma_f32_16x16x32_bf16 v[132:135], a[12:15], a[156:159], v[132:135]\n\t"
"s_waitcnt lgkmcnt(1)\n\t"
"v_add_f32 v208, v208, v200\n\t"
"v_add_f32 v209, v209, v201\n\t"
"v_mfma_f32_16x16x32_bf16 v[132:135], a[44:47], a[172:175], v[132:135]\n\t"
"ds_write_b64 v6, v[128:129] offset:0\n\t"
"v_mfma_f32_16x16x32_bf16 v[132:135], a[76:79], a[188:191], v[132:135]\n\t"
"v_add_f32 v210, v210, v202\n\t"
"v_add_f32 v211, v211, v203\n\t"
"v_mfma_f32_16x16x32_bf16 v[132:135], a[108:111], a[204:207], v[132:135]\n\t"
"s_waitcnt lgkmcnt(1)\n\t"
"v_add_f32 v212, v212, v204\n\t"
"v_add_f32 v213, v213, v205\n\t"
"v_mfma_f32_16x16x32_bf16 v[132:135], a[176:179], a[220:223], v[132:135]\n\t"
"ds_write_b64 v6, v[130:131] offset:8\n\t"
"v_mfma_f32_16x16x32_bf16 v[132:135], a[180:183], a[236:239], v[132:135]\n\t"
"v_add_f32 v214, v214, v206\n\t"
"v_add_f32 v215, v215, v207\n\t"
"v_mfma_f32_16x16x32_bf16 v[132:135], a[184:187], a[252:255], v[132:135]\n\t"
"v_add_f32 v160, v160, v208\n\t"
"v_add_f32 v161, v161, v209\n\t"
"v_mfma_f32_16x16x32_bf16 v[136:139], v[112:115], a[140:143], 0\n\t"
"v_add_f32 v162, v162, v210\n\t"
"v_add_f32 v163, v163, v211\n\t"
"v_mfma_f32_16x16x32_bf16 v[136:139], a[16:19], a[156:159], v[136:139]\n\t"
"v_add_f32 v164, v164, v212\n\t"
"v_add_f32 v165, v165, v213\n\t"
"v_mfma_f32_16x16x32_bf16 v[136:139], a[48:51], a[172:175], v[136:139]\n\t"
"ds_write_b64 v6, v[132:133] offset:1024\n\t"
"v_mfma_f32_16x16x32_bf16 v[136:139], a[80:83], a[188:191], v[136:139]\n\t"
"v_add_f32 v166, v166, v214\n\t"
"v_add_f32 v167, v167, v215\n\t"
"v_mfma_f32_16x16x32_bf16 v[136:139], a[112:115], a[204:207], v[136:139]\n\t"
"v_bfi_b32 v160, s27, v84, v160\n\t"
"v_bfi_b32 v161, s27, v85, v161\n\t"
"v_mfma_f32_16x16x32_bf16 v[136:139], a[192:195], a[220:223], v[136:139]\n\t"
"ds_write_b64 v6, v[134:135] offset:1032\n\t"
"v_mfma_f32_16x16x32_bf16 v[136:139], a[196:199], a[236:239], v[136:139]\n\t"
"v_bfi_b32 v162, s27, v86, v162\n\t"
"v_bfi_b32 v163, s27, v87, v163\n\t"
"v_mfma_f32_16x16x32_bf16 v[136:139], a[200:203], a[252:255], v[136:139]\n\t"
"v_bfi_b32 v164, s27, v84, v164\n\t"
"v_bfi_b32 v165, s27, v85, v165\n\t"
"v_mfma_f32_16x16x32_bf16 v[140:143], v[116:119], a[140:143], 0\n\t"
"v_bfi_b32 v166, s27, v86, v166\n\t"
"v_bfi_b32 v167, s27, v87, v167\n\t"
"v_mfma_f32_16x16x32_bf16 v[140:143], a[20:23], a[156:159], v[140:143]\n\t"
"buffer_store_dwordx4 v[160:163], v80, s[56:59], s20 offen sc1\n\t"
"v_mfma_f32_16x16x32_bf16 v[140:143], a[52:55], a[172:175], v[140:143]\n\t"
"ds_write_b64 v7, v[136:137] offset:0\n\t"
"v_mfma_f32_16x16x32_bf16 v[140:143], a[84:87], a[188:191], v[140:143]\n\t"
"buffer_store_dwordx4 v[164:167], v80, s[56:59], s20 offen offset:1024 sc1\n\t"
"v_mfma_f32_16x16x32_bf16 v[140:143], a[116:119], a[204:207], v[140:143]\n\t"
"v_mfma_f32_16x16x32_bf16 v[140:143], a[208:211], a[220:223], v[140:143]\n\t"
"ds_write_b64 v7, v[138:139] offset:8\n\t"
"v_mfma_f32_16x16x32_bf16 v[140:143], a[212:215], a[236:239], v[140:143]\n\t"
"v_mfma_f32_16x16x32_bf16 v[140:143], a[216:219], a[252:255], v[140:143]\n\t"
"v_mfma_f32_16x16x32_bf16 v[144:147], v[120:123], a[140:143], 0\n\t"
"s_max_u32 s78, s9, 10\n\t"
"v_or_b32 v228, s78, v4\n\t"
"v_mfma_f32_16x16x32_bf16 v[144:147], a[24:27], a[156:159], v[144:147]\n\t"
"buffer_store_dwordx2 v[228:229], v5, s[56:59], s17 offen sc1\n\t"
"v_mfma_f32_16x16x32_bf16 v[144:147], a[56:59], a[172:175], v[144:147]\n\t"
"v_mfma_f32_16x16x32_bf16 v[144:147], a[88:91], a[188:191], v[144:147]\n\t"
"v_mfma_f32_16x16x32_bf16 v[144:147], a[120:123], a[204:207], v[144:147]\n\t"
"ds_write_b64 v7, v[140:141] offset:1024\n\t"
"v_mfma_f32_16x16x32_bf16 v[144:147], a[224:227], a[220:223], v[144:147]\n\t"
"v_mfma_f32_16x16x32_bf16 v[144:147], a[228:231], a[236:239], v[144:147]\n\t"
"v_mfma_f32_16x16x32_bf16 v[144:147], a[232:235], a[252:255], v[144:147]\n\t"
"ds_write_b64 v7, v[142:143] offset:1032\n\t"
"v_mfma_f32_16x16x32_bf16 v[148:151], v[124:127], a[140:143], 0\n\t"
"s_waitcnt vmcnt(5)\n\t"
"s_or_b32 s90, s86, 7\n\t"
"v_cmp_le_u64 vcc, s[90:91], v[2:3]\n\t"
"v_cmp_ge_u64 s[92:93], s[88:89], v[2:3]\n\t"
"s_nop 0\n\t"
"s_and_b64 vcc, vcc, s[92:93]\n\t"
"s_cmp_eq_u64 vcc, -1\n\t"
"s_cbranch_scc0 LPS20_%=\n\t"
"LPR20_%=:\n\t"
"v_mfma_f32_16x16x32_bf16 v[148:151], a[28:31], a[156:159], v[148:151]\n\t"
"s_add_u32 s22, s94, 131072\n\t"
"buffer_load_dwordx4 v[48:51], v232, s[56:59], s22 offen sc1\n\t"
"v_mfma_f32_16x16x32_bf16 v[148:151], a[60:63], a[172:175], v[148:151]\n\t"
"ds_write_b64 v8, v[144:145] offset:0\n\t"
"v_mfma_f32_16x16x32_bf16 v[148:151], a[92:95], a[188:191], v[148:151]\n\t"
"v_mfma_f32_16x16x32_bf16 v[148:151], a[124:127], a[204:207], v[148:151]\n\t"
"buffer_load_dwordx4 v[52:55], v232, s[56:59], s22 offen offset:1024 sc1\n\t"
"v_mfma_f32_16x16x32_bf16 v[148:151], a[240:243], a[220:223], v[148:151]\n\t"
"ds_write_b64 v8, v[146:147] offset:8\n\t"
"v_mfma_f32_16x16x32_bf16 v[148:151], a[244:247], a[236:239], v[148:151]\n\t"
"v_mfma_f32_16x16x32_bf16 v[148:151], a[248:251], a[252:255], v[148:151]\n\t"
"buffer_load_dwordx4 v[56:59], v233, s[56:59], s22 offen sc1\n\t"
"v_mfma_f32_16x16x32_bf16 v[152:155], v[96:99], a[140:143], 0\n\t"
"v_mfma_f32_16x16x32_bf16 v[152:155], a[0:3], a[156:159], v[152:155]\n\t"
"v_mfma_f32_16x16x32_bf16 v[152:155], a[32:35], a[172:175], v[152:155]\n\t"
"buffer_load_dwordx4 v[60:63], v233, s[56:59], s22 offen offset:1024 sc1\n\t"
"v_mfma_f32_16x16x32_bf16 v[152:155], a[64:67], a[188:191], v[152:155]\n\t"
"v_mfma_f32_16x16x32_bf16 v[152:155], a[96:99], a[204:207], v[152:155]\n\t"
"v_mfma_f32_16x16x32_bf16 v[152:155], a[128:131], a[220:223], v[152:155]\n\t"
"ds_write_b64 v8, v[148:149] offset:1024\n\t"
"v_mfma_f32_16x16x32_bf16 v[152:155], a[132:135], a[236:239], v[152:155]\n\t"
"buffer_load_dwordx4 v[64:67], v234, s[56:59], s22 offen sc1\n\t"
"v_mfma_f32_16x16x32_bf16 v[152:155], a[136:139], a[252:255], v[152:155]\n\t"
"v_mfma_f32_16x16x32_bf16 v[156:159], v[100:103], a[140:143], 0\n\t"
"ds_write_b64 v8, v[150:151] offset:1032\n\t"
"v_mfma_f32_16x16x32_bf16 v[156:159], a[4:7], a[156:159], v[156:159]\n\t"
"buffer_load_dwordx4 v[68:71], v234, s[56:59], s22 offen offset:1024 sc1\n\t"
"v_mfma_f32_16x16x32_bf16 v[156:159], a[36:39], a[172:175], v[156:159]\n\t"
"v_mfma_f32_16x16x32_bf16 v[156:159], a[68:71], a[188:191], v[156:159]\n\t"
"buffer_load_dwordx4 v[72:75], v235, s[56:59], s22 offen sc1\n\t"
"v_mfma_f32_16x16x32_bf16 v[156:159], a[100:103], a[204:207], v[156:159]\n\t"
"v_mfma_f32_16x16x32_bf16 v[156:159], a[144:147], a[220:223], v[156:159]\n\t"
"v_mfma_f32_16x16x32_bf16 v[156:159], a[148:151], a[236:239], v[156:159]\n\t"
"buffer_load_dwordx4 v[76:79], v235, s[56:59], s22 offen offset:1024 sc1\n\t"
"v_mfma_f32_16x16x32_bf16 v[156:159], a[152:155], a[252:255], v[156:159]\n\t"
"s_waitcnt lgkmcnt(0)\n\t"
"s_barrier\n\t"
"ds_read_b128 v[208:211], v9 offset:2048\n\t"
"v_mfma_f32_16x16x32_bf16 v[128:131], v[104:107], v[168:171], 0\n\t"
"s_lshr_b64 s[80:81], s[82:83], 8\n\t"
"s_and_b32 s80, s80, 15\n\t"
"ds_read_b128 v[212:215], v9 offset:3072\n\t"
"v_mfma_f32_16x16x32_bf16 v[128:131], a[8:11], v[172:175], v[128:131]\n\t"
"s_mul_i32 s80, s80, 32768\n\t"
"s_add_u32 s20, s80, s95\n\t"
"ds_read_b128 v[216:219], v9 offset:4096\n\t"
"v_mfma_f32_16x16x32_bf16 v[128:131], a[40:43], v[176:179], v[128:131]\n\t"
"s_add_u32 s22, s94, 163840\n\t"
"ds_read_b128 v[220:223], v9 offset:5120\n\t"
"v_mfma_f32_16x16x32_bf16 v[128:131], a[72:75], v[180:183], v[128:131]\n\t"
"ds_read_b128 v[200:203], v9 offset:0\n\t"
"v_mfma_f32_16x16x32_bf16 v[128:131], a[104:107], v[184:187], v[128:131]\n\t"
"ds_read_b128 v[204:207], v9 offset:1024\n\t"
"v_mfma_f32_16x16x32_bf16 v[128:131], a[160:163], v[188:191], v[128:131]\n\t"
"buffer_load_dwordx4 a[140:143], v232, s[56:59], s22 offen sc1\n\t"
"v_mfma_f32_16x16x32_bf16 v[128:131], a[164:167], v[192:195], v[128:131]\n\t"
"buffer_load_dwordx4 a[156:159], v232, s[56:59], s22 offen offset:1024 sc1\n\t"
"v_mfma_f32_16x16x32_bf16 v[128:131], a[168:171], v[196:199], v[128:131]\n\t"
"s_waitcnt lgkmcnt(3)\n\t"
"v_add_f32 v208, v208, v216\n\t"
"v_add_f32 v209, v209, v217\n\t"
"v_mfma_f32_16x16x32_bf16 v[132:135], v[108:111], v[168:171], 0\n\t"
"v_add_f32 v210, v210, v218\n\t"
"v_add_f32 v211, v211, v219\n\t"
"v_mfma_f32_16x16x32_bf16 v[132:135], a[12:15], v[172:175], v[132:135]\n\t"
"buffer_load_dwordx4 a[172:175], v233, s[56:59], s22 offen sc1\n\t"
"v_mfma_f32_16x16x32_bf16 v[132:135], a[44:47], v[176:179], v[132:135]\n\t"
"ds_write_b64 v6, v[128:129] offset:6144\n\t"
"v_mfma_f32_16x16x32_bf16 v[132:135], a[76:79], v[180:183], v[132:135]\n\t"
"s_waitcnt lgkmcnt(3)\n\t"
"v_add_f32 v212, v212, v220\n\t"
"v_add_f32 v213, v213, v221\n\t"
"v_mfma_f32_16x16x32_bf16 v[132:135], a[108:111], v[184:187], v[132:135]\n\t"
"buffer_load_dwordx4 a[188:191], v233, s[56:59], s22 offen offset:1024 sc1\n\t"
"v_mfma_f32_16x16x32_bf16 v[132:135], a[176:179], v[188:191], v[132:135]\n\t"
"ds_write_b64 v6, v[130:131] offset:6152\n\t"
"v_mfma_f32_16x16x32_bf16 v[132:135], a[180:183], v[192:195], v[132:135]\n\t"
"v_add_f32 v214, v214, v222\n\t"
"v_add_f32 v215, v215, v223\n\t"
"v_mfma_f32_16x16x32_bf16 v[132:135], a[184:187], v[196:199], v[132:135]\n\t"
"buffer_load_dwordx4 a[204:207], v234, s[56:59], s22 offen sc1\n\t"
"v_mfma_f32_16x16x32_bf16 v[136:139], v[112:115], v[168:171], 0\n\t"
"s_waitcnt lgkmcnt(3)\n\t"
"v_add_f32 v208, v208, v200\n\t"
"v_add_f32 v209, v209, v201\n\t"
"v_mfma_f32_16x16x32_bf16 v[136:139], a[16:19], v[172:175], v[136:139]\n\t"
"v_add_f32 v210, v210, v202\n\t"
"v_add_f32 v211, v211, v203\n\t"
"v_mfma_f32_16x16x32_bf16 v[136:139], a[48:51], v[176:179], v[136:139]\n\t"
"ds_write_b64 v6, v[132:133] offset:7168\n\t"
"v_mfma_f32_16x16x32_bf16 v[136:139], a[80:83], v[180:183], v[136:139]\n\t"
"buffer_load_dwordx4 a[220:223], v234, s[56:59], s22 offen offset:1024 sc1\n\t"
"v_mfma_f32_16x16x32_bf16 v[136:139], a[112:115], v[184:187], v[136:139]\n\t"
"s_waitcnt lgkmcnt(3)\n\t"
"v_add_f32 v212, v212, v204\n\t"
"v_add_f32 v213, v213, v205\n\t"
"v_mfma_f32_16x16x32_bf16 v[136:139], a[192:195], v[188:191], v[136:139]\n\t"
"ds_write_b64 v6, v[134:135] offset:7176\n\t"
"v_mfma_f32_16x16x32_bf16 v[136:139], a[196:199], v[192:195], v[136:139]\n\t"
"buffer_load_dwordx4 a[236:239], v235, s[56:59], s22 offen sc1\n\t"
"v_mfma_f32_16x16x32_bf16 v[136:139], a[200:203], v[196:199], v[136:139]\n\t"
"v_add_f32 v214, v214, v206\n\t"
"v_add_f32 v215, v215, v207\n\t"
"v_mfma_f32_16x16x32_bf16 v[140:143], v[116:119], v[168:171], 0\n\t"
"buffer_load_dwordx4 a[252:255], v235, s[56:59], s22 offen offset:1024 sc1\n\t"
"v_mfma_f32_16x16x32_bf16 v[140:143], a[20:23], v[172:175], v[140:143]\n\t"
"v_add_f32 v152, v152, v208\n\t"
"v_add_f32 v153, v153, v209\n\t"
"v_mfma_f32_16x16x32_bf16 v[140:143], a[52:55], v[176:179], v[140:143]\n\t"
"ds_write_b64 v7, v[136:137] offset:6144\n\t"
"v_mfma_f32_16x16x32_bf16 v[140:143], a[84:87], v[180:183], v[140:143]\n\t"
"v_add_f32 v154, v154, v210\n\t"
"v_add_f32 v155, v155, v211\n\t"
"v_mfma_f32_16x16x32_bf16 v[140:143], a[116:119], v[184:187], v[140:143]\n\t"
"buffer_load_dwordx2 v[2:3], v15, s[56:59], s16 offen sc1\n\t"
"v_mfma_f32_16x16x32_bf16 v[140:143], a[208:211], v[188:191], v[140:143]\n\t"
"ds_write_b64 v7, v[138:139] offset:6152\n\t"
"v_mfma_f32_16x16x32_bf16 v[140:143], a[212:215], v[192:195], v[140:143]\n\t"
"v_add_f32 v156, v156, v212\n\t"
"v_add_f32 v157, v157, v213\n\t"
"v_add_f32 v158, v158, v214\n\t"
"v_add_f32 v159, v159, v215\n\t"
"v_mfma_f32_16x16x32_bf16 v[140:143], a[216:219], v[196:199], v[140:143]\n\t"
"v_bfi_b32 v152, s27, v84, v152\n\t"
"v_bfi_b32 v153, s27, v85, v153\n\t"
"v_mfma_f32_16x16x32_bf16 v[144:147], v[120:123], v[168:171], 0\n\t"
"v_bfi_b32 v154, s27, v86, v154\n\t"
"v_bfi_b32 v155, s27, v87, v155\n\t"
"v_mfma_f32_16x16x32_bf16 v[144:147], a[24:27], v[172:175], v[144:147]\n\t"
"v_bfi_b32 v156, s27, v84, v156\n\t"
"v_bfi_b32 v157, s27, v85, v157\n\t"
"v_mfma_f32_16x16x32_bf16 v[144:147], a[56:59], v[176:179], v[144:147]\n\t"
"v_bfi_b32 v158, s27, v86, v158\n\t"
"v_bfi_b32 v159, s27, v87, v159\n\t"
"v_mfma_f32_16x16x32_bf16 v[144:147], a[88:91], v[180:183], v[144:147]\n\t"
"buffer_store_dwordx4 v[152:155], v80, s[56:59], s20 offen sc1\n\t"
"v_mfma_f32_16x16x32_bf16 v[144:147], a[120:123], v[184:187], v[144:147]\n\t"
"buffer_store_dwordx4 v[156:159], v80, s[56:59], s20 offen offset:1024 sc1\n\t"
"v_mfma_f32_16x16x32_bf16 v[144:147], a[224:227], v[188:191], v[144:147]\n\t"
"ds_write_b64 v7, v[140:141] offset:7168\n\t"
"v_mfma_f32_16x16x32_bf16 v[144:147], a[228:231], v[192:195], v[144:147]\n\t"
"v_mfma_f32_16x16x32_bf16 v[144:147], a[232:235], v[196:199], v[144:147]\n\t"
"v_mfma_f32_16x16x32_bf16 v[148:151], v[124:127], v[168:171], 0\n\t"
"ds_write_b64 v7, v[142:143] offset:7176\n\t"
"v_mfma_f32_16x16x32_bf16 v[148:151], a[28:31], v[172:175], v[148:151]\n\t"
"v_mfma_f32_16x16x32_bf16 v[148:151], a[60:63], v[176:179], v[148:151]\n\t"
"v_mfma_f32_16x16x32_bf16 v[148:151], a[92:95], v[180:183], v[148:151]\n\t"
"v_mfma_f32_16x16x32_bf16 v[148:151], a[124:127], v[184:187], v[148:151]\n\t"
"v_mfma_f32_16x16x32_bf16 v[148:151], a[240:243], v[188:191], v[148:151]\n\t"
"v_mfma_f32_16x16x32_bf16 v[148:151], a[244:247], v[192:195], v[148:151]\n\t"
"ds_write_b64 v8, v[144:145] offset:6144\n\t"
"v_mfma_f32_16x16x32_bf16 v[148:151], a[248:251], v[196:199], v[148:151]\n\t"
"v_mfma_f32_16x16x32_bf16 v[160:163], v[96:99], v[168:171], 0\n\t"
"v_mfma_f32_16x16x32_bf16 v[160:163], a[0:3], v[172:175], v[160:163]\n\t"
"ds_write_b64 v8, v[146:147] offset:6152\n\t"
"v_mfma_f32_16x16x32_bf16 v[160:163], a[32:35], v[176:179], v[160:163]\n\t"
"v_mfma_f32_16x16x32_bf16 v[160:163], a[64:67], v[180:183], v[160:163]\n\t"
"v_mfma_f32_16x16x32_bf16 v[160:163], a[96:99], v[184:187], v[160:163]\n\t"
"v_mfma_f32_16x16x32_bf16 v[160:163], a[128:131], v[188:191], v[160:163]\n\t"
"v_mfma_f32_16x16x32_bf16 v[160:163], a[132:135], v[192:195], v[160:163]\n\t"
"ds_write_b64 v8, v[148:149] offset:7168\n\t"
"v_mfma_f32_16x16x32_bf16 v[160:163], a[136:139], v[196:199], v[160:163]\n\t"
"v_mfma_f32_16x16x32_bf16 v[164:167], v[100:103], v[168:171], 0\n\t"
"v_mfma_f32_16x16x32_bf16 v[164:167], a[4:7], v[172:175], v[164:167]\n\t"
"ds_write_b64 v8, v[150:151] offset:7176\n\t"
"v_mfma_f32_16x16x32_bf16 v[164:167], a[36:39], v[176:179], v[164:167]\n\t"
"v_mfma_f32_16x16x32_bf16 v[164:167], a[68:71], v[180:183], v[164:167]\n\t"
"v_mfma_f32_16x16x32_bf16 v[164:167], a[100:103], v[184:187], v[164:167]\n\t"
"v_mfma_f32_16x16x32_bf16 v[164:167], a[144:147], v[188:191], v[164:167]\n\t"
"v_mfma_f32_16x16x32_bf16 v[164:167], a[148:151], v[192:195], v[164:167]\n\t"
"v_mfma_f32_16x16x32_bf16 v[164:167], a[152:155], v[196:199], v[164:167]\n\t"
"s_waitcnt lgkmcnt(0)\n\t"
"s_barrier\n\t"
"ds_read_b128 v[208:211], v9 offset:8192\n\t"
"s_waitcnt vmcnt(18)\n\t"
"v_mfma_f32_16x16x32_bf16 v[128:131], v[104:107], v[48:51], 0\n\t"
"s_waitcnt vmcnt(17)\n\t"
"s_lshr_b64 s[80:81], s[82:83], 12\n\t"
"s_and_b32 s80, s80, 15\n\t"
"ds_read_b128 v[212:215], v9 offset:9216\n\t"
"v_mfma_f32_16x16x32_bf16 v[128:131], a[8:11], v[52:55], v[128:131]\n\t"
"s_waitcnt vmcnt(16)\n\t"
"s_mul_i32 s80, s80, 32768\n\t"
"s_add_u32 s20, s80, s95\n\t"
"ds_read_b128 v[216:219], v9 offset:10240\n\t"
"v_mfma_f32_16x16x32_bf16 v[128:131], a[40:43], v[56:59], v[128:131]\n\t"
"s_waitcnt vmcnt(15)\n\t"
"s_add_u32 s22, s94, 196608\n\t"
"ds_read_b128 v[220:223], v9 offset:11264\n\t"
"v_mfma_f32_16x16x32_bf16 v[128:131], a[72:75], v[60:63], v[128:131]\n\t"
"s_waitcnt vmcnt(14)\n\t"
"ds_read_b128 v[200:203], v9 offset:6144\n\t"
"v_mfma_f32_16x16x32_bf16 v[128:131], a[104:107], v[64:67], v[128:131]\n\t"
"s_waitcnt vmcnt(13)\n\t"
"ds_read_b128 v[204:207], v9 offset:7168\n\t"
"v_mfma_f32_16x16x32_bf16 v[128:131], a[160:163], v[68:71], v[128:131]\n\t"
"s_waitcnt vmcnt(12)\n\t"
"buffer_load_dwordx4 v[168:171], v232, s[56:59], s22 offen sc1\n\t"
"v_mfma_f32_16x16x32_bf16 v[128:131], a[164:167], v[72:75], v[128:131]\n\t"
"s_waitcnt vmcnt(12)\n\t"
"buffer_load_dwordx4 v[172:175], v232, s[56:59], s22 offen offset:1024 sc1\n\t"
"v_mfma_f32_16x16x32_bf16 v[128:131], a[168:171], v[76:79], v[128:131]\n\t"
"s_waitcnt lgkmcnt(3)\n\t"
"v_add_f32 v208, v208, v216\n\t"
"v_add_f32 v209, v209, v217\n\t"
"v_mfma_f32_16x16x32_bf16 v[132:135], v[108:111], v[48:51], 0\n\t"
"v_add_f32 v210, v210, v218\n\t"
"v_add_f32 v211, v211, v219\n\t"
"v_mfma_f32_16x16x32_bf16 v[132:135], a[12:15], v[52:55], v[132:135]\n\t"
"buffer_load_dwordx4 v[176:179], v233, s[56:59], s22 offen sc1\n\t"
"v_mfma_f32_16x16x32_bf16 v[132:135], a[44:47], v[56:59], v[132:135]\n\t"
"ds_write_b64 v6, v[128:129] offset:0\n\t"
"v_mfma_f32_16x16x32_bf16 v[132:135], a[76:79], v[60:63], v[132:135]\n\t"
"s_waitcnt lgkmcnt(3)\n\t"
"v_add_f32 v212, v212, v220\n\t"
"v_add_f32 v213, v213, v221\n\t"
"v_mfma_f32_16x16x32_bf16 v[132:135], a[108:111], v[64:67], v[132:135]\n\t"
"buffer_load_dwordx4 v[180:183], v233, s[56:59], s22 offen offset:1024 sc1\n\t"
"v_mfma_f32_16x16x32_bf16 v[132:135], a[176:179], v[68:71], v[132:135]\n\t"
"ds_write_b64 v6, v[130:131] offset:8\n\t"
"v_mfma_f32_16x16x32_bf16 v[132:135], a[180:183], v[72:75], v[132:135]\n\t"
"v_add_f32 v214, v214, v222\n\t"
"v_add_f32 v215, v215, v223\n\t"
"v_mfma_f32_16x16x32_bf16 v[132:135], a[184:187], v[76:79], v[132:135]\n\t"
"buffer_load_dwordx4 v[184:187], v234, s[56:59], s22 offen sc1\n\t"
"v_mfma_f32_16x16x32_bf16 v[136:139], v[112:115], v[48:51], 0\n\t"
"s_waitcnt lgkmcnt(3)\n\t"
"v_add_f32 v208, v208, v200\n\t"
"v_add_f32 v209, v209, v201\n\t"
"v_mfma_f32_16x16x32_bf16 v[136:139], a[16:19], v[52:55], v[136:139]\n\t"
"v_add_f32 v210, v210, v202\n\t"
"v_add_f32 v211, v211, v203\n\t"
"v_mfma_f32_16x16x32_bf16 v[136:139], a[48:51], v[56:59], v[136:139]\n\t"
"ds_write_b64 v6, v[132:133] offset:1024\n\t"
"v_mfma_f32_16x16x32_bf16 v[136:139], a[80:83], v[60:63], v[136:139]\n\t"
"buffer_load_dwordx4 v[188:191], v234, s[56:59], s22 offen offset:1024 sc1\n\t"
"v_mfma_f32_16x16x32_bf16 v[136:139], a[112:115], v[64:67], v[136:139]\n\t"
"s_waitcnt lgkmcnt(3)\n\t"
"v_add_f32 v212, v212, v204\n\t"
"v_add_f32 v213, v213, v205\n\t"
"v_mfma_f32_16x16x32_bf16 v[136:139], a[192:195], v[68:71], v[136:139]\n\t"
"ds_write_b64 v6, v[134:135] offset:1032\n\t"
"v_mfma_f32_16x16x32_bf16 v[136:139], a[196:199], v[72:75], v[136:139]\n\t"
"buffer_load_dwordx4 v[192:195], v235, s[56:59], s22 offen sc1\n\t"
"v_mfma_f32_16x16x32_bf16 v[136:139], a[200:203], v[76:79], v[136:139]\n\t"
"v_add_f32 v214, v214, v206\n\t"
"v_add_f32 v215, v215, v207\n\t"
"v_mfma_f32_16x16x32_bf16 v[140:143], v[116:119], v[48:51], 0\n\t"
"buffer_load_dwordx4 v[196:199], v235, s[56:59], s22 offen offset:1024 sc1\n\t"
"v_mfma_f32_16x16x32_bf16 v[140:143], a[20:23], v[52:55], v[140:143]\n\t"
"v_add_f32 v160, v160, v208\n\t"
"v_add_f32 v161, v161, v209\n\t"
"v_mfma_f32_16x16x32_bf16 v[140:143], a[52:55], v[56:59], v[140:143]\n\t"
"ds_write_b64 v7, v[136:137] offset:0\n\t"
"v_mfma_f32_16x16x32_bf16 v[140:143], a[84:87], v[60:63], v[140:143]\n\t"
"v_add_f32 v162, v162, v210\n\t"
"v_add_f32 v163, v163, v211\n\t"
"v_mfma_f32_16x16x32_bf16 v[140:143], a[116:119], v[64:67], v[140:143]\n\t"
"v_add_f32 v164, v164, v212\n\t"
"v_add_f32 v165, v165, v213\n\t"
"v_mfma_f32_16x16x32_bf16 v[140:143], a[208:211], v[68:71], v[140:143]\n\t"
"ds_write_b64 v7, v[138:139] offset:8\n\t"
"v_mfma_f32_16x16x32_bf16 v[140:143], a[212:215], v[72:75], v[140:143]\n\t"
"v_add_f32 v166, v166, v214\n\t"
"v_add_f32 v167, v167, v215\n\t"
"v_mfma_f32_16x16x32_bf16 v[140:143], a[216:219], v[76:79], v[140:143]\n\t"
"v_bfi_b32 v160, s27, v84, v160\n\t"
"v_bfi_b32 v161, s27, v85, v161\n\t"
"v_mfma_f32_16x16x32_bf16 v[144:147], v[120:123], v[48:51], 0\n\t"
"v_bfi_b32 v162, s27, v86, v162\n\t"
"v_bfi_b32 v163, s27, v87, v163\n\t"
"v_mfma_f32_16x16x32_bf16 v[144:147], a[24:27], v[52:55], v[144:147]\n\t"
"v_bfi_b32 v164, s27, v84, v164\n\t"
"v_bfi_b32 v165, s27, v85, v165\n\t"
"v_mfma_f32_16x16x32_bf16 v[144:147], a[56:59], v[56:59], v[144:147]\n\t"
"v_bfi_b32 v166, s27, v86, v166\n\t"
"v_bfi_b32 v167, s27, v87, v167\n\t"
"v_mfma_f32_16x16x32_bf16 v[144:147], a[88:91], v[60:63], v[144:147]\n\t"
"buffer_store_dwordx4 v[160:163], v80, s[56:59], s20 offen sc1\n\t"
"v_mfma_f32_16x16x32_bf16 v[144:147], a[120:123], v[64:67], v[144:147]\n\t"
"buffer_store_dwordx4 v[164:167], v80, s[56:59], s20 offen offset:1024 sc1\n\t"
"v_mfma_f32_16x16x32_bf16 v[144:147], a[224:227], v[68:71], v[144:147]\n\t"
"ds_write_b64 v7, v[140:141] offset:1024\n\t"
"v_mfma_f32_16x16x32_bf16 v[144:147], a[228:231], v[72:75], v[144:147]\n\t"
"v_mfma_f32_16x16x32_bf16 v[144:147], a[232:235], v[76:79], v[144:147]\n\t"
"v_mfma_f32_16x16x32_bf16 v[148:151], v[124:127], v[48:51], 0\n\t"
"ds_write_b64 v7, v[142:143] offset:1032\n\t"
"v_mfma_f32_16x16x32_bf16 v[148:151], a[28:31], v[52:55], v[148:151]\n\t"
"v_mfma_f32_16x16x32_bf16 v[148:151], a[60:63], v[56:59], v[148:151]\n\t"
"v_mfma_f32_16x16x32_bf16 v[148:151], a[92:95], v[60:63], v[148:151]\n\t"
"v_mfma_f32_16x16x32_bf16 v[148:151], a[124:127], v[64:67], v[148:151]\n\t"
"v_mfma_f32_16x16x32_bf16 v[148:151], a[240:243], v[68:71], v[148:151]\n\t"
"v_mfma_f32_16x16x32_bf16 v[148:151], a[244:247], v[72:75], v[148:151]\n\t"
"ds_write_b64 v8, v[144:145] offset:0\n\t"
"v_mfma_f32_16x16x32_bf16 v[148:151], a[248:251], v[76:79], v[148:151]\n\t"
"v_mfma_f32_16x16x32_bf16 v[152:155], v[96:99], v[48:51], 0\n\t"
"v_mfma_f32_16x16x32_bf16 v[152:155], a[0:3], v[52:55], v[152:155]\n\t"
"ds_write_b64 v8, v[146:147] offset:8\n\t"
"v_mfma_f32_16x16x32_bf16 v[152:155], a[32:35], v[56:59], v[152:155]\n\t"
"v_mfma_f32_16x16x32_bf16 v[152:155], a[64:67], v[60:63], v[152:155]\n\t"
"v_mfma_f32_16x16x32_bf16 v[152:155], a[96:99], v[64:67], v[152:155]\n\t"
"v_mfma_f32_16x16x32_bf16 v[152:155], a[128:131], v[68:71], v[152:155]\n\t"
"v_mfma_f32_16x16x32_bf16 v[152:155], a[132:135], v[72:75], v[152:155]\n\t"
"ds_write_b64 v8, v[148:149] offset:1024\n\t"
"v_mfma_f32_16x16x32_bf16 v[152:155], a[136:139], v[76:79], v[152:155]\n\t"
"v_mfma_f32_16x16x32_bf16 v[156:159], v[100:103], v[48:51], 0\n\t"
"v_mfma_f32_16x16x32_bf16 v[156:159], a[4:7], v[52:55], v[156:159]\n\t"
"ds_write_b64 v8, v[150:151] offset:1032\n\t"
"v_mfma_f32_16x16x32_bf16 v[156:159], a[36:39], v[56:59], v[156:159]\n\t"
"v_mfma_f32_16x16x32_bf16 v[156:159], a[68:71], v[60:63], v[156:159]\n\t"
"s_waitcnt vmcnt(12)\n\t"
"s_or_b32 s90, s86, 10\n\t"
"v_cmp_le_u64 vcc, s[90:91], v[2:3]\n\t"
"v_cmp_ge_u64 s[92:93], s[88:89], v[2:3]\n\t"
"s_nop 0\n\t"
"s_and_b64 vcc, vcc, s[92:93]\n\t"
"s_cmp_eq_u64 vcc, -1\n\t"
"s_cbranch_scc0 LPS21_%=\n\t"
"LPR21_%=:\n\t"
"v_mfma_f32_16x16x32_bf16 v[156:159], a[100:103], v[64:67], v[156:159]\n\t"
"v_mfma_f32_16x16x32_bf16 v[156:159], a[144:147], v[68:71], v[156:159]\n\t"
"v_mfma_f32_16x16x32_bf16 v[156:159], a[148:151], v[72:75], v[156:159]\n\t"
"v_mfma_f32_16x16x32_bf16 v[156:159], a[152:155], v[76:79], v[156:159]\n\t"
"s_waitcnt lgkmcnt(0)\n\t"
"s_barrier\n\t"
"ds_read_b128 v[208:211], v9 offset:2048\n\t"
"v_mfma_f32_16x16x32_bf16 v[128:131], v[104:107], a[140:143], 0\n\t"
"s_lshr_b64 s[80:81], s[82:83], 16\n\t"
"s_and_b32 s80, s80, 15\n\t"
"ds_read_b128 v[212:215], v9 offset:3072\n\t"
"v_mfma_f32_16x16x32_bf16 v[128:131], a[8:11], a[156:159], v[128:131]\n\t"
"s_mul_i32 s80, s80, 32768\n\t"
"s_add_u32 s20, s80, s95\n\t"
"ds_read_b128 v[216:219], v9 offset:4096\n\t"
"v_mfma_f32_16x16x32_bf16 v[128:131], a[40:43], a[172:175], v[128:131]\n\t"
"s_add_u32 s22, s94, 229376\n\t"
"ds_read_b128 v[220:223], v9 offset:5120\n\t"
"v_mfma_f32_16x16x32_bf16 v[128:131], a[72:75], a[188:191], v[128:131]\n\t"
"ds_read_b128 v[200:203], v9 offset:0\n\t"
"v_mfma_f32_16x16x32_bf16 v[128:131], a[104:107], a[204:207], v[128:131]\n\t"
"ds_read_b128 v[204:207], v9 offset:1024\n\t"
"v_mfma_f32_16x16x32_bf16 v[128:131], a[160:163], a[220:223], v[128:131]\n\t"
"buffer_load_dwordx4 v[48:51], v232, s[56:59], s22 offen sc1\n\t"
"v_mfma_f32_16x16x32_bf16 v[128:131], a[164:167], a[236:239], v[128:131]\n\t"
"buffer_load_dwordx4 v[52:55], v232, s[56:59], s22 offen offset:1024 sc1\n\t"
"v_mfma_f32_16x16x32_bf16 v[128:131], a[168:171], a[252:255], v[128:131]\n\t"
"s_waitcnt lgkmcnt(3)\n\t"
"v_add_f32 v208, v208, v216\n\t"
"v_add_f32 v209, v209, v217\n\t"
"v_mfma_f32_16x16x32_bf16 v[132:135], v[108:111], a[140:143], 0\n\t"
"v_add_f32 v210, v210, v218\n\t"
"v_add_f32 v211, v211, v219\n\t"
"v_mfma_f32_16x16x32_bf16 v[132:135], a[12:15], a[156:159], v[132:135]\n\t"
"buffer_load_dwordx4 v[56:59], v233, s[56:59], s22 offen sc1\n\t"
"v_mfma_f32_16x16x32_bf16 v[132:135], a[44:47], a[172:175], v[132:135]\n\t"
"ds_write_b64 v6, v[128:129] offset:6144\n\t"
"v_mfma_f32_16x16x32_bf16 v[132:135], a[76:79], a[188:191], v[132:135]\n\t"
"s_waitcnt lgkmcnt(3)\n\t"
"v_add_f32 v212, v212, v220\n\t"
"v_add_f32 v213, v213, v221\n\t"
"v_mfma_f32_16x16x32_bf16 v[132:135], a[108:111], a[204:207], v[132:135]\n\t"
"buffer_load_dwordx4 v[60:63], v233, s[56:59], s22 offen offset:1024 sc1\n\t"
"v_mfma_f32_16x16x32_bf16 v[132:135], a[176:179], a[220:223], v[132:135]\n\t"
"ds_write_b64 v6, v[130:131] offset:6152\n\t"
"v_mfma_f32_16x16x32_bf16 v[132:135], a[180:183], a[236:239], v[132:135]\n\t"
"v_add_f32 v214, v214, v222\n\t"
"v_add_f32 v215, v215, v223\n\t"
"v_mfma_f32_16x16x32_bf16 v[132:135], a[184:187], a[252:255], v[132:135]\n\t"
"buffer_load_dwordx4 v[64:67], v234, s[56:59], s22 offen sc1\n\t"
"v_mfma_f32_16x16x32_bf16 v[136:139], v[112:115], a[140:143], 0\n\t"
"s_waitcnt lgkmcnt(3)\n\t"
"v_add_f32 v208, v208, v200\n\t"
"v_add_f32 v209, v209, v201\n\t"
"v_mfma_f32_16x16x32_bf16 v[136:139], a[16:19], a[156:159], v[136:139]\n\t"
"v_add_f32 v210, v210, v202\n\t"
"v_add_f32 v211, v211, v203\n\t"
"v_mfma_f32_16x16x32_bf16 v[136:139], a[48:51], a[172:175], v[136:139]\n\t"
"ds_write_b64 v6, v[132:133] offset:7168\n\t"
"v_mfma_f32_16x16x32_bf16 v[136:139], a[80:83], a[188:191], v[136:139]\n\t"
"buffer_load_dwordx4 v[68:71], v234, s[56:59], s22 offen offset:1024 sc1\n\t"
"v_mfma_f32_16x16x32_bf16 v[136:139], a[112:115], a[204:207], v[136:139]\n\t"
"s_waitcnt lgkmcnt(3)\n\t"
"v_add_f32 v212, v212, v204\n\t"
"v_add_f32 v213, v213, v205\n\t"
"v_mfma_f32_16x16x32_bf16 v[136:139], a[192:195], a[220:223], v[136:139]\n\t"
"ds_write_b64 v6, v[134:135] offset:7176\n\t"
"v_mfma_f32_16x16x32_bf16 v[136:139], a[196:199], a[236:239], v[136:139]\n\t"
"buffer_load_dwordx4 v[72:75], v235, s[56:59], s22 offen sc1\n\t"
"v_mfma_f32_16x16x32_bf16 v[136:139], a[200:203], a[252:255], v[136:139]\n\t"
"v_add_f32 v214, v214, v206\n\t"
"v_add_f32 v215, v215, v207\n\t"
"v_mfma_f32_16x16x32_bf16 v[140:143], v[116:119], a[140:143], 0\n\t"
"buffer_load_dwordx4 v[76:79], v235, s[56:59], s22 offen offset:1024 sc1\n\t"
"v_mfma_f32_16x16x32_bf16 v[140:143], a[20:23], a[156:159], v[140:143]\n\t"
"v_add_f32 v152, v152, v208\n\t"
"v_add_f32 v153, v153, v209\n\t"
"v_mfma_f32_16x16x32_bf16 v[140:143], a[52:55], a[172:175], v[140:143]\n\t"
"ds_write_b64 v7, v[136:137] offset:6144\n\t"
"v_mfma_f32_16x16x32_bf16 v[140:143], a[84:87], a[188:191], v[140:143]\n\t"
"v_add_f32 v154, v154, v210\n\t"
"v_add_f32 v155, v155, v211\n\t"
"v_mfma_f32_16x16x32_bf16 v[140:143], a[116:119], a[204:207], v[140:143]\n\t"
"v_add_f32 v156, v156, v212\n\t"
"v_add_f32 v157, v157, v213\n\t"
"v_mfma_f32_16x16x32_bf16 v[140:143], a[208:211], a[220:223], v[140:143]\n\t"
"ds_write_b64 v7, v[138:139] offset:6152\n\t"
"v_mfma_f32_16x16x32_bf16 v[140:143], a[212:215], a[236:239], v[140:143]\n\t"
"v_add_f32 v158, v158, v214\n\t"
"v_add_f32 v159, v159, v215\n\t"
"v_mfma_f32_16x16x32_bf16 v[140:143], a[216:219], a[252:255], v[140:143]\n\t"
"v_bfi_b32 v152, s27, v84, v152\n\t"
"v_bfi_b32 v153, s27, v85, v153\n\t"
"v_mfma_f32_16x16x32_bf16 v[144:147], v[120:123], a[140:143], 0\n\t"
"v_bfi_b32 v154, s27, v86, v154\n\t"
"v_bfi_b32 v155, s27, v87, v155\n\t"
"v_mfma_f32_16x16x32_bf16 v[144:147], a[24:27], a[156:159], v[144:147]\n\t"
"v_bfi_b32 v156, s27, v84, v156\n\t"
"v_bfi_b32 v157, s27, v85, v157\n\t"
"v_mfma_f32_16x16x32_bf16 v[144:147], a[56:59], a[172:175], v[144:147]\n\t"
"v_bfi_b32 v158, s27, v86, v158\n\t"
"v_bfi_b32 v159, s27, v87, v159\n\t"
"v_mfma_f32_16x16x32_bf16 v[144:147], a[88:91], a[188:191], v[144:147]\n\t"
"buffer_store_dwordx4 v[152:155], v80, s[56:59], s20 offen sc1\n\t"
"v_mfma_f32_16x16x32_bf16 v[144:147], a[120:123], a[204:207], v[144:147]\n\t"
"buffer_store_dwordx4 v[156:159], v80, s[56:59], s20 offen offset:1024 sc1\n\t"
"v_mfma_f32_16x16x32_bf16 v[144:147], a[224:227], a[220:223], v[144:147]\n\t"
"ds_write_b64 v7, v[140:141] offset:7168\n\t"
"v_mfma_f32_16x16x32_bf16 v[144:147], a[228:231], a[236:239], v[144:147]\n\t"
"v_mfma_f32_16x16x32_bf16 v[144:147], a[232:235], a[252:255], v[144:147]\n\t"
"v_mfma_f32_16x16x32_bf16 v[148:151], v[124:127], a[140:143], 0\n\t"
"ds_write_b64 v7, v[142:143] offset:7176\n\t"
"v_mfma_f32_16x16x32_bf16 v[148:151], a[28:31], a[156:159], v[148:151]\n\t"
"v_mfma_f32_16x16x32_bf16 v[148:151], a[60:63], a[172:175], v[148:151]\n\t"
"v_mfma_f32_16x16x32_bf16 v[148:151], a[92:95], a[188:191], v[148:151]\n\t"
"v_mfma_f32_16x16x32_bf16 v[148:151], a[124:127], a[204:207], v[148:151]\n\t"
"v_mfma_f32_16x16x32_bf16 v[148:151], a[240:243], a[220:223], v[148:151]\n\t"
"v_mfma_f32_16x16x32_bf16 v[148:151], a[244:247], a[236:239], v[148:151]\n\t"
"ds_write_b64 v8, v[144:145] offset:6144\n\t"
"v_mfma_f32_16x16x32_bf16 v[148:151], a[248:251], a[252:255], v[148:151]\n\t"
"v_mfma_f32_16x16x32_bf16 v[160:163], v[96:99], a[140:143], 0\n\t"
"v_mfma_f32_16x16x32_bf16 v[160:163], a[0:3], a[156:159], v[160:163]\n\t"
"ds_write_b64 v8, v[146:147] offset:6152\n\t"
"v_mfma_f32_16x16x32_bf16 v[160:163], a[32:35], a[172:175], v[160:163]\n\t"
"v_mfma_f32_16x16x32_bf16 v[160:163], a[64:67], a[188:191], v[160:163]\n\t"
"v_mfma_f32_16x16x32_bf16 v[160:163], a[96:99], a[204:207], v[160:163]\n\t"
"v_mfma_f32_16x16x32_bf16 v[160:163], a[128:131], a[220:223], v[160:163]\n\t"
"v_mfma_f32_16x16x32_bf16 v[160:163], a[132:135], a[236:239], v[160:163]\n\t"
"ds_write_b64 v8, v[148:149] offset:7168\n\t"
"v_mfma_f32_16x16x32_bf16 v[160:163], a[136:139], a[252:255], v[160:163]\n\t"
"v_mfma_f32_16x16x32_bf16 v[164:167], v[100:103], a[140:143], 0\n\t"
"v_mfma_f32_16x16x32_bf16 v[164:167], a[4:7], a[156:159], v[164:167]\n\t"
"ds_write_b64 v8, v[150:151] offset:7176\n\t"
"v_mfma_f32_16x16x32_bf16 v[164:167], a[36:39], a[172:175], v[164:167]\n\t"
"v_mfma_f32_16x16x32_bf16 v[164:167], a[68:71], a[188:191], v[164:167]\n\t"
"v_mfma_f32_16x16x32_bf16 v[164:167], a[100:103], a[204:207], v[164:167]\n\t"
"v_mfma_f32_16x16x32_bf16 v[164:167], a[144:147], a[220:223], v[164:167]\n\t"
"v_mfma_f32_16x16x32_bf16 v[164:167], a[148:151], a[236:239], v[164:167]\n\t"
"v_mfma_f32_16x16x32_bf16 v[164:167], a[152:155], a[252:255], v[164:167]\n\t"
"s_waitcnt lgkmcnt(0)\n\t"
"s_barrier\n\t"
"ds_read_b128 v[208:211], v9 offset:8192\n\t"
"s_waitcnt vmcnt(19)\n\t"
"v_mfma_f32_16x16x32_bf16 v[128:131], v[104:107], v[168:171], 0\n\t"
"s_waitcnt vmcnt(18)\n\t"
"s_lshr_b64 s[80:81], s[82:83], 20\n\t"
"s_and_b32 s80, s80, 15\n\t"
"ds_read_b128 v[212:215], v9 offset:9216\n\t"
"v_mfma_f32_16x16x32_bf16 v[128:131], a[8:11], v[172:175], v[128:131]\n\t"
"s_waitcnt vmcnt(17)\n\t"
"s_mul_i32 s80, s80, 32768\n\t"
"s_add_u32 s20, s80, s95\n\t"
"ds_read_b128 v[216:219], v9 offset:10240\n\t"
"v_mfma_f32_16x16x32_bf16 v[128:131], a[40:43], v[176:179], v[128:131]\n\t"
"s_waitcnt vmcnt(16)\n\t"
"s_add_u32 s22, s94, 262144\n\t"
"ds_read_b128 v[220:223], v9 offset:11264\n\t"
"v_mfma_f32_16x16x32_bf16 v[128:131], a[72:75], v[180:183], v[128:131]\n\t"
"s_waitcnt vmcnt(15)\n\t"
"ds_read_b128 v[200:203], v9 offset:6144\n\t"
"v_mfma_f32_16x16x32_bf16 v[128:131], a[104:107], v[184:187], v[128:131]\n\t"
"s_waitcnt vmcnt(14)\n\t"
"ds_read_b128 v[204:207], v9 offset:7168\n\t"
"v_mfma_f32_16x16x32_bf16 v[128:131], a[160:163], v[188:191], v[128:131]\n\t"
"s_waitcnt vmcnt(13)\n\t"
"buffer_load_dwordx4 a[140:143], v232, s[56:59], s22 offen sc1\n\t"
"v_mfma_f32_16x16x32_bf16 v[128:131], a[164:167], v[192:195], v[128:131]\n\t"
"s_waitcnt vmcnt(13)\n\t"
"buffer_load_dwordx4 a[156:159], v232, s[56:59], s22 offen offset:1024 sc1\n\t"
"v_mfma_f32_16x16x32_bf16 v[128:131], a[168:171], v[196:199], v[128:131]\n\t"
"s_waitcnt lgkmcnt(3)\n\t"
"v_add_f32 v208, v208, v216\n\t"
"v_add_f32 v209, v209, v217\n\t"
"v_mfma_f32_16x16x32_bf16 v[132:135], v[108:111], v[168:171], 0\n\t"
"v_add_f32 v210, v210, v218\n\t"
"v_add_f32 v211, v211, v219\n\t"
"v_mfma_f32_16x16x32_bf16 v[132:135], a[12:15], v[172:175], v[132:135]\n\t"
"buffer_load_dwordx4 a[172:175], v233, s[56:59], s22 offen sc1\n\t"
"v_mfma_f32_16x16x32_bf16 v[132:135], a[44:47], v[176:179], v[132:135]\n\t"
"ds_write_b64 v6, v[128:129] offset:0\n\t"
"v_mfma_f32_16x16x32_bf16 v[132:135], a[76:79], v[180:183], v[132:135]\n\t"
"s_waitcnt lgkmcnt(3)\n\t"
"v_add_f32 v212, v212, v220\n\t"
"v_add_f32 v213, v213, v221\n\t"
"v_mfma_f32_16x16x32_bf16 v[132:135], a[108:111], v[184:187], v[132:135]\n\t"
"buffer_load_dwordx4 a[188:191], v233, s[56:59], s22 offen offset:1024 sc1\n\t"
"v_mfma_f32_16x16x32_bf16 v[132:135], a[176:179], v[188:191], v[132:135]\n\t"
"ds_write_b64 v6, v[130:131] offset:8\n\t"
"v_mfma_f32_16x16x32_bf16 v[132:135], a[180:183], v[192:195], v[132:135]\n\t"
"v_add_f32 v214, v214, v222\n\t"
"v_add_f32 v215, v215, v223\n\t"
"v_mfma_f32_16x16x32_bf16 v[132:135], a[184:187], v[196:199], v[132:135]\n\t"
"buffer_load_dwordx4 a[204:207], v234, s[56:59], s22 offen sc1\n\t"
"v_mfma_f32_16x16x32_bf16 v[136:139], v[112:115], v[168:171], 0\n\t"
"s_waitcnt lgkmcnt(3)\n\t"
"v_add_f32 v208, v208, v200\n\t"
"v_add_f32 v209, v209, v201\n\t"
"v_mfma_f32_16x16x32_bf16 v[136:139], a[16:19], v[172:175], v[136:139]\n\t"
"v_add_f32 v210, v210, v202\n\t"
"v_add_f32 v211, v211, v203\n\t"
"v_mfma_f32_16x16x32_bf16 v[136:139], a[48:51], v[176:179], v[136:139]\n\t"
"ds_write_b64 v6, v[132:133] offset:1024\n\t"
"v_mfma_f32_16x16x32_bf16 v[136:139], a[80:83], v[180:183], v[136:139]\n\t"
"buffer_load_dwordx4 a[220:223], v234, s[56:59], s22 offen offset:1024 sc1\n\t"
"v_mfma_f32_16x16x32_bf16 v[136:139], a[112:115], v[184:187], v[136:139]\n\t"
"s_waitcnt lgkmcnt(3)\n\t"
"v_add_f32 v212, v212, v204\n\t"
"v_add_f32 v213, v213, v205\n\t"
"v_mfma_f32_16x16x32_bf16 v[136:139], a[192:195], v[188:191], v[136:139]\n\t"
"ds_write_b64 v6, v[134:135] offset:1032\n\t"
"v_mfma_f32_16x16x32_bf16 v[136:139], a[196:199], v[192:195], v[136:139]\n\t"
"buffer_load_dwordx4 a[236:239], v235, s[56:59], s22 offen sc1\n\t"
"v_mfma_f32_16x16x32_bf16 v[136:139], a[200:203], v[196:199], v[136:139]\n\t"
"v_add_f32 v214, v214, v206\n\t"
"v_add_f32 v215, v215, v207\n\t"
"v_mfma_f32_16x16x32_bf16 v[140:143], v[116:119], v[168:171], 0\n\t"
"buffer_load_dwordx4 a[252:255], v235, s[56:59], s22 offen offset:1024 sc1\n\t"
"v_mfma_f32_16x16x32_bf16 v[140:143], a[20:23], v[172:175], v[140:143]\n\t"
"v_add_f32 v160, v160, v208\n\t"
"v_add_f32 v161, v161, v209\n\t"
"v_mfma_f32_16x16x32_bf16 v[140:143], a[52:55], v[176:179], v[140:143]\n\t"
"ds_write_b64 v7, v[136:137] offset:0\n\t"
"v_mfma_f32_16x16x32_bf16 v[140:143], a[84:87], v[180:183], v[140:143]\n\t"
"v_add_f32 v162, v162, v210\n\t"
"v_add_f32 v163, v163, v211\n\t"
"v_mfma_f32_16x16x32_bf16 v[140:143], a[116:119], v[184:187], v[140:143]\n\t"
"v_add_f32 v164, v164, v212\n\t"
"v_add_f32 v165, v165, v213\n\t"
"v_mfma_f32_16x16x32_bf16 v[140:143], a[208:211], v[188:191], v[140:143]\n\t"
"ds_write_b64 v7, v[138:139] offset:8\n\t"
"v_mfma_f32_16x16x32_bf16 v[140:143], a[212:215], v[192:195], v[140:143]\n\t"
"v_add_f32 v166, v166, v214\n\t"
"v_add_f32 v167, v167, v215\n\t"
"v_mfma_f32_16x16x32_bf16 v[140:143], a[216:219], v[196:199], v[140:143]\n\t"
"v_bfi_b32 v160, s27, v84, v160\n\t"
"v_bfi_b32 v161, s27, v85, v161\n\t"
"v_mfma_f32_16x16x32_bf16 v[144:147], v[120:123], v[168:171], 0\n\t"
"v_bfi_b32 v162, s27, v86, v162\n\t"
"v_bfi_b32 v163, s27, v87, v163\n\t"
"v_mfma_f32_16x16x32_bf16 v[144:147], a[24:27], v[172:175], v[144:147]\n\t"
"v_bfi_b32 v164, s27, v84, v164\n\t"
"v_bfi_b32 v165, s27, v85, v165\n\t"
"v_mfma_f32_16x16x32_bf16 v[144:147], a[56:59], v[176:179], v[144:147]\n\t"
"v_bfi_b32 v166, s27, v86, v166\n\t"
"v_bfi_b32 v167, s27, v87, v167\n\t"
"v_mfma_f32_16x16x32_bf16 v[144:147], a[88:91], v[180:183], v[144:147]\n\t"
"buffer_store_dwordx4 v[160:163], v80, s[56:59], s20 offen sc1\n\t"
"v_mfma_f32_16x16x32_bf16 v[144:147], a[120:123], v[184:187], v[144:147]\n\t"
"buffer_store_dwordx4 v[164:167], v80, s[56:59], s20 offen offset:1024 sc1\n\t"
"v_mfma_f32_16x16x32_bf16 v[144:147], a[224:227], v[188:191], v[144:147]\n\t"
"ds_write_b64 v7, v[140:141] offset:1024\n\t"
"v_mfma_f32_16x16x32_bf16 v[144:147], a[228:231], v[192:195], v[144:147]\n\t"
"v_mfma_f32_16x16x32_bf16 v[144:147], a[232:235], v[196:199], v[144:147]\n\t"
"v_mfma_f32_16x16x32_bf16 v[148:151], v[124:127], v[168:171], 0\n\t"
"ds_write_b64 v7, v[142:143] offset:1032\n\t"
"v_mfma_f32_16x16x32_bf16 v[148:151], a[28:31], v[172:175], v[148:151]\n\t"
"v_mfma_f32_16x16x32_bf16 v[148:151], a[60:63], v[176:179], v[148:151]\n\t"
"v_mfma_f32_16x16x32_bf16 v[148:151], a[92:95], v[180:183], v[148:151]\n\t"
"v_mfma_f32_16x16x32_bf16 v[148:151], a[124:127], v[184:187], v[148:151]\n\t"
"v_mfma_f32_16x16x32_bf16 v[148:151], a[240:243], v[188:191], v[148:151]\n\t"
"v_mfma_f32_16x16x32_bf16 v[148:151], a[244:247], v[192:195], v[148:151]\n\t"
"ds_write_b64 v8, v[144:145] offset:0\n\t"
"v_mfma_f32_16x16x32_bf16 v[148:151], a[248:251], v[196:199], v[148:151]\n\t"
"v_mfma_f32_16x16x32_bf16 v[152:155], v[96:99], v[168:171], 0\n\t"
"v_mfma_f32_16x16x32_bf16 v[152:155], a[0:3], v[172:175], v[152:155]\n\t"
"ds_write_b64 v8, v[146:147] offset:8\n\t"
"v_mfma_f32_16x16x32_bf16 v[152:155], a[32:35], v[176:179], v[152:155]\n\t"
"v_mfma_f32_16x16x32_bf16 v[152:155], a[64:67], v[180:183], v[152:155]\n\t"
"v_mfma_f32_16x16x32_bf16 v[152:155], a[96:99], v[184:187], v[152:155]\n\t"
"v_mfma_f32_16x16x32_bf16 v[152:155], a[128:131], v[188:191], v[152:155]\n\t"
"v_mfma_f32_16x16x32_bf16 v[152:155], a[132:135], v[192:195], v[152:155]\n\t"
"ds_write_b64 v8, v[148:149] offset:1024\n\t"
"v_mfma_f32_16x16x32_bf16 v[152:155], a[136:139], v[196:199], v[152:155]\n\t"
"v_mfma_f32_16x16x32_bf16 v[156:159], v[100:103], v[168:171], 0\n\t"
"v_mfma_f32_16x16x32_bf16 v[156:159], a[4:7], v[172:175], v[156:159]\n\t"
"ds_write_b64 v8, v[150:151] offset:1032\n\t"
"v_mfma_f32_16x16x32_bf16 v[156:159], a[36:39], v[176:179], v[156:159]\n\t"
"v_mfma_f32_16x16x32_bf16 v[156:159], a[68:71], v[180:183], v[156:159]\n\t"
"v_mfma_f32_16x16x32_bf16 v[156:159], a[100:103], v[184:187], v[156:159]\n\t"
"v_mfma_f32_16x16x32_bf16 v[156:159], a[144:147], v[188:191], v[156:159]\n\t"
"v_mfma_f32_16x16x32_bf16 v[156:159], a[148:151], v[192:195], v[156:159]\n\t"
"v_mfma_f32_16x16x32_bf16 v[156:159], a[152:155], v[196:199], v[156:159]\n\t"
"s_waitcnt lgkmcnt(0)\n\t"
"s_barrier\n\t"
"ds_read_b128 v[208:211], v9 offset:2048\n\t"
"s_waitcnt vmcnt(19)\n\t"
"v_mfma_f32_16x16x32_bf16 v[128:131], v[104:107], v[48:51], 0\n\t"
"s_waitcnt vmcnt(18)\n\t"
"s_lshr_b64 s[80:81], s[82:83], 24\n\t"
"s_and_b32 s80, s80, 15\n\t"
"ds_read_b128 v[212:215], v9 offset:3072\n\t"
"v_mfma_f32_16x16x32_bf16 v[128:131], a[8:11], v[52:55], v[128:131]\n\t"
"s_waitcnt vmcnt(17)\n\t"
"s_mul_i32 s80, s80, 32768\n\t"
"s_add_u32 s20, s80, s95\n\t"
"ds_read_b128 v[216:219], v9 offset:4096\n\t"
"v_mfma_f32_16x16x32_bf16 v[128:131], a[40:43], v[56:59], v[128:131]\n\t"
"s_waitcnt vmcnt(16)\n\t"
"s_add_u32 s22, s94, 294912\n\t"
"ds_read_b128 v[220:223], v9 offset:5120\n\t"
"v_mfma_f32_16x16x32_bf16 v[128:131], a[72:75], v[60:63], v[128:131]\n\t"
"s_waitcnt vmcnt(15)\n\t"
"ds_read_b128 v[200:203], v9 offset:0\n\t"
"v_mfma_f32_16x16x32_bf16 v[128:131], a[104:107], v[64:67], v[128:131]\n\t"
"s_waitcnt vmcnt(14)\n\t"
"ds_read_b128 v[204:207], v9 offset:1024\n\t"
"v_mfma_f32_16x16x32_bf16 v[128:131], a[160:163], v[68:71], v[128:131]\n\t"
"s_waitcnt vmcnt(13)\n\t"
"buffer_load_dwordx4 v[168:171], v232, s[56:59], s22 offen sc1\n\t"
"v_mfma_f32_16x16x32_bf16 v[128:131], a[164:167], v[72:75], v[128:131]\n\t"
"s_waitcnt vmcnt(13)\n\t"
"buffer_load_dwordx4 v[172:175], v232, s[56:59], s22 offen offset:1024 sc1\n\t"
"v_mfma_f32_16x16x32_bf16 v[128:131], a[168:171], v[76:79], v[128:131]\n\t"
"s_waitcnt lgkmcnt(3)\n\t"
"v_add_f32 v208, v208, v216\n\t"
"v_add_f32 v209, v209, v217\n\t"
"v_mfma_f32_16x16x32_bf16 v[132:135], v[108:111], v[48:51], 0\n\t"
"v_add_f32 v210, v210, v218\n\t"
"v_add_f32 v211, v211, v219\n\t"
"v_mfma_f32_16x16x32_bf16 v[132:135], a[12:15], v[52:55], v[132:135]\n\t"
"buffer_load_dwordx4 v[176:179], v233, s[56:59], s22 offen sc1\n\t"
"v_mfma_f32_16x16x32_bf16 v[132:135], a[44:47], v[56:59], v[132:135]\n\t"
"ds_write_b64 v6, v[128:129] offset:6144\n\t"
"v_mfma_f32_16x16x32_bf16 v[132:135], a[76:79], v[60:63], v[132:135]\n\t"
"s_waitcnt lgkmcnt(3)\n\t"
"v_add_f32 v212, v212, v220\n\t"
"v_add_f32 v213, v213, v221\n\t"
"v_mfma_f32_16x16x32_bf16 v[132:135], a[108:111], v[64:67], v[132:135]\n\t"
"buffer_load_dwordx4 v[180:183], v233, s[56:59], s22 offen offset:1024 sc1\n\t"
"v_mfma_f32_16x16x32_bf16 v[132:135], a[176:179], v[68:71], v[132:135]\n\t"
"ds_write_b64 v6, v[130:131] offset:6152\n\t"
"v_mfma_f32_16x16x32_bf16 v[132:135], a[180:183], v[72:75], v[132:135]\n\t"
"v_add_f32 v214, v214, v222\n\t"
"v_add_f32 v215, v215, v223\n\t"
"v_mfma_f32_16x16x32_bf16 v[132:135], a[184:187], v[76:79], v[132:135]\n\t"
"buffer_load_dwordx4 v[184:187], v234, s[56:59], s22 offen sc1\n\t"
"v_mfma_f32_16x16x32_bf16 v[136:139], v[112:115], v[48:51], 0\n\t"
"s_waitcnt lgkmcnt(3)\n\t"
"v_add_f32 v208, v208, v200\n\t"
"v_add_f32 v209, v209, v201\n\t"
"v_mfma_f32_16x16x32_bf16 v[136:139], a[16:19], v[52:55], v[136:139]\n\t"
"v_add_f32 v210, v210, v202\n\t"
"v_add_f32 v211, v211, v203\n\t"
"v_mfma_f32_16x16x32_bf16 v[136:139], a[48:51], v[56:59], v[136:139]\n\t"
"ds_write_b64 v6, v[132:133] offset:7168\n\t"
"v_mfma_f32_16x16x32_bf16 v[136:139], a[80:83], v[60:63], v[136:139]\n\t"
"buffer_load_dwordx4 v[188:191], v234, s[56:59], s22 offen offset:1024 sc1\n\t"
"v_mfma_f32_16x16x32_bf16 v[136:139], a[112:115], v[64:67], v[136:139]\n\t"
"s_waitcnt lgkmcnt(3)\n\t"
"s_lshr_b32 s80, s65, 1\n\t"
"s_add_u32 s80, s80, 8\n\t"
"v_add_f32 v212, v212, v204\n\t"
"v_mfma_f32_16x16x32_bf16 v[136:139], a[192:195], v[68:71], v[136:139]\n\t"
"s_lshl_b32 s80, s80, 15\n\t"
"s_add_u32 s14, s80, s95\n\t"
"ds_write_b64 v6, v[134:135] offset:7176\n\t"
"v_mfma_f32_16x16x32_bf16 v[136:139], a[196:199], v[72:75], v[136:139]\n\t"
"s_mul_i32 s80, s19, 8192\n\t"
"s_add_u32 s14, s14, s80\n\t"
"v_add_f32 v213, v213, v205\n\t"
"v_mfma_f32_16x16x32_bf16 v[136:139], a[200:203], v[76:79], v[136:139]\n\t"
"s_lshl_b32 s80, s71, 10\n\t"
"s_add_u32 s14, s14, s80\n\t"
"v_add_f32 v214, v214, v206\n\t"
"v_mfma_f32_16x16x32_bf16 v[140:143], v[116:119], v[48:51], 0\n\t"
"buffer_load_dwordx4 v[192:195], v235, s[56:59], s22 offen sc1\n\t"
"v_mfma_f32_16x16x32_bf16 v[140:143], a[20:23], v[52:55], v[140:143]\n\t"
"buffer_load_dwordx4 v[28:31], v83, s[56:59], s14 offen sc1\n\t"
"v_mfma_f32_16x16x32_bf16 v[140:143], a[52:55], v[56:59], v[140:143]\n\t"
"buffer_load_dwordx4 v[196:199], v235, s[56:59], s22 offen offset:1024 sc1\n\t"
"v_mfma_f32_16x16x32_bf16 v[140:143], a[84:87], v[60:63], v[140:143]\n\t"
"ds_write_b64 v7, v[136:137] offset:6144\n\t"
"v_mfma_f32_16x16x32_bf16 v[140:143], a[116:119], v[64:67], v[140:143]\n\t"
"buffer_load_dwordx4 v[24:27], v82, s[56:59], s14 offen sc1\n\t"
"v_mfma_f32_16x16x32_bf16 v[140:143], a[208:211], v[68:71], v[140:143]\n\t"
"buffer_load_dwordx4 v[20:23], v81, s[56:59], s14 offen sc1\n\t"
"v_mfma_f32_16x16x32_bf16 v[140:143], a[212:215], v[72:75], v[140:143]\n\t"
"v_add_f32 v215, v215, v207\n\t"
"v_add_f32 v152, v152, v208\n\t"
"v_add_f32 v153, v153, v209\n\t"
"v_add_f32 v154, v154, v210\n\t"
"v_add_f32 v155, v155, v211\n\t"
"v_add_f32 v156, v156, v212\n\t"
"v_add_f32 v157, v157, v213\n\t"
"v_add_f32 v158, v158, v214\n\t"
"v_add_f32 v159, v159, v215\n\t"
"v_mfma_f32_16x16x32_bf16 v[140:143], a[216:219], v[76:79], v[140:143]\n\t"
"ds_write_b64 v7, v[138:139] offset:6152\n\t"
"v_mfma_f32_16x16x32_bf16 v[144:147], v[120:123], v[48:51], 0\n\t"
"buffer_load_dwordx4 v[16:19], v80, s[56:59], s14 offen sc1\n\t"
"v_mfma_f32_16x16x32_bf16 v[144:147], a[24:27], v[52:55], v[144:147]\n\t"
"v_bfi_b32 v152, s27, v84, v152\n\t"
"v_bfi_b32 v153, s27, v85, v153\n\t"
"v_mfma_f32_16x16x32_bf16 v[144:147], a[56:59], v[56:59], v[144:147]\n\t"
"v_bfi_b32 v154, s27, v86, v154\n\t"
"v_bfi_b32 v155, s27, v87, v155\n\t"
"v_bfi_b32 v156, s27, v84, v156\n\t"
"v_bfi_b32 v157, s27, v85, v157\n\t"
"v_bfi_b32 v158, s27, v86, v158\n\t"
"v_bfi_b32 v159, s27, v87, v159\n\t"
"v_mfma_f32_16x16x32_bf16 v[144:147], a[88:91], v[60:63], v[144:147]\n\t"
"buffer_store_dwordx4 v[152:155], v80, s[56:59], s20 offen sc1\n\t"
"v_mfma_f32_16x16x32_bf16 v[144:147], a[120:123], v[64:67], v[144:147]\n\t"
"buffer_store_dwordx4 v[156:159], v80, s[56:59], s20 offen offset:1024 sc1\n\t"
"v_mfma_f32_16x16x32_bf16 v[144:147], a[224:227], v[68:71], v[144:147]\n\t"
"ds_write_b64 v7, v[140:141] offset:7168\n\t"
"v_mfma_f32_16x16x32_bf16 v[144:147], a[228:231], v[72:75], v[144:147]\n\t"
"v_mfma_f32_16x16x32_bf16 v[144:147], a[232:235], v[76:79], v[144:147]\n\t"
"v_mfma_f32_16x16x32_bf16 v[148:151], v[124:127], v[48:51], 0\n\t"
"ds_write_b64 v7, v[142:143] offset:7176\n\t"
"v_mfma_f32_16x16x32_bf16 v[148:151], a[28:31], v[52:55], v[148:151]\n\t"
"v_mfma_f32_16x16x32_bf16 v[148:151], a[60:63], v[56:59], v[148:151]\n\t"
"v_mfma_f32_16x16x32_bf16 v[148:151], a[92:95], v[60:63], v[148:151]\n\t"
"v_mfma_f32_16x16x32_bf16 v[148:151], a[124:127], v[64:67], v[148:151]\n\t"
"v_mfma_f32_16x16x32_bf16 v[148:151], a[240:243], v[68:71], v[148:151]\n\t"
"v_mfma_f32_16x16x32_bf16 v[148:151], a[244:247], v[72:75], v[148:151]\n\t"
"ds_write_b64 v8, v[144:145] offset:6144\n\t"
"v_mfma_f32_16x16x32_bf16 v[148:151], a[248:251], v[76:79], v[148:151]\n\t"
"v_mfma_f32_16x16x32_bf16 v[160:163], v[96:99], v[48:51], 0\n\t"
"v_mfma_f32_16x16x32_bf16 v[160:163], a[0:3], v[52:55], v[160:163]\n\t"
"ds_write_b64 v8, v[146:147] offset:6152\n\t"
"v_mfma_f32_16x16x32_bf16 v[160:163], a[32:35], v[56:59], v[160:163]\n\t"
"v_mfma_f32_16x16x32_bf16 v[160:163], a[64:67], v[60:63], v[160:163]\n\t"
"v_mfma_f32_16x16x32_bf16 v[160:163], a[96:99], v[64:67], v[160:163]\n\t"
"v_mfma_f32_16x16x32_bf16 v[160:163], a[128:131], v[68:71], v[160:163]\n\t"
"v_mfma_f32_16x16x32_bf16 v[160:163], a[132:135], v[72:75], v[160:163]\n\t"
"ds_write_b64 v8, v[148:149] offset:7168\n\t"
"v_mfma_f32_16x16x32_bf16 v[160:163], a[136:139], v[76:79], v[160:163]\n\t"
"v_mfma_f32_16x16x32_bf16 v[164:167], v[100:103], v[48:51], 0\n\t"
"v_mfma_f32_16x16x32_bf16 v[164:167], a[4:7], v[52:55], v[164:167]\n\t"
"ds_write_b64 v8, v[150:151] offset:7176\n\t"
"v_mfma_f32_16x16x32_bf16 v[164:167], a[36:39], v[56:59], v[164:167]\n\t"
"v_mfma_f32_16x16x32_bf16 v[164:167], a[68:71], v[60:63], v[164:167]\n\t"
"v_mfma_f32_16x16x32_bf16 v[164:167], a[100:103], v[64:67], v[164:167]\n\t"
"v_mfma_f32_16x16x32_bf16 v[164:167], a[144:147], v[68:71], v[164:167]\n\t"
"v_mfma_f32_16x16x32_bf16 v[164:167], a[148:151], v[72:75], v[164:167]\n\t"
"v_mfma_f32_16x16x32_bf16 v[164:167], a[152:155], v[76:79], v[164:167]\n\t"
"s_waitcnt lgkmcnt(0)\n\t"
"s_barrier\n\t"
"ds_read_b128 v[208:211], v9 offset:8192\n\t"
"s_waitcnt vmcnt(23)\n\t"
"v_mfma_f32_16x16x32_bf16 v[128:131], v[104:107], a[140:143], 0\n\t"
"s_waitcnt vmcnt(22)\n\t"
"s_lshr_b64 s[80:81], s[82:83], 28\n\t"
"s_and_b32 s80, s80, 15\n\t"
"ds_read_b128 v[212:215], v9 offset:9216\n\t"
"v_mfma_f32_16x16x32_bf16 v[128:131], a[8:11], a[156:159], v[128:131]\n\t"
"s_waitcnt vmcnt(21)\n\t"
"s_mul_i32 s80, s80, 32768\n\t"
"s_add_u32 s20, s80, s95\n\t"
"ds_read_b128 v[216:219], v9 offset:10240\n\t"
"v_mfma_f32_16x16x32_bf16 v[128:131], a[40:43], a[172:175], v[128:131]\n\t"
"s_waitcnt vmcnt(20)\n\t"
"s_lshr_b64 s[80:81], s[82:83], 32\n\t"
"s_and_b32 s80, s80, 15\n\t"
"ds_read_b128 v[220:223], v9 offset:11264\n\t"
"v_mfma_f32_16x16x32_bf16 v[128:131], a[72:75], a[188:191], v[128:131]\n\t"
"s_waitcnt vmcnt(19)\n\t"
"s_mul_i32 s80, s80, 32768\n\t"
"s_add_u32 s15, s80, s95\n\t"
"ds_read_b128 v[200:203], v9 offset:6144\n\t"
"v_mfma_f32_16x16x32_bf16 v[128:131], a[104:107], a[204:207], v[128:131]\n\t"
"s_waitcnt vmcnt(18)\n\t"
"ds_read_b128 v[204:207], v9 offset:7168\n\t"
"v_mfma_f32_16x16x32_bf16 v[128:131], a[160:163], a[220:223], v[128:131]\n\t"
"s_waitcnt vmcnt(17)\n\t"
"buffer_load_dwordx4 v[40:43], v83, s[56:59], s15 offen sc1\n\t"
"v_mfma_f32_16x16x32_bf16 v[128:131], a[164:167], a[236:239], v[128:131]\n\t"
"s_waitcnt vmcnt(17)\n\t"
"buffer_load_dwordx4 v[44:47], v83, s[56:59], s15 offen offset:1024 sc1\n\t"
"v_mfma_f32_16x16x32_bf16 v[128:131], a[168:171], a[252:255], v[128:131]\n\t"
"buffer_load_dwordx4 v[32:35], v82, s[56:59], s15 offen sc1\n\t"
"v_mfma_f32_16x16x32_bf16 v[132:135], v[108:111], a[140:143], 0\n\t"
"buffer_load_dwordx4 v[36:39], v82, s[56:59], s15 offen offset:1024 sc1\n\t"
"v_mfma_f32_16x16x32_bf16 v[132:135], a[12:15], a[156:159], v[132:135]\n\t"
"buffer_load_dwordx4 v[48:51], v81, s[56:59], s15 offen sc1\n\t"
"v_mfma_f32_16x16x32_bf16 v[132:135], a[44:47], a[172:175], v[132:135]\n\t"
"s_waitcnt lgkmcnt(3)\n\t"
"v_add_f32 v208, v208, v216\n\t"
"v_add_f32 v209, v209, v217\n\t"
"v_mfma_f32_16x16x32_bf16 v[132:135], a[76:79], a[188:191], v[132:135]\n\t"
"buffer_load_dwordx4 v[52:55], v81, s[56:59], s15 offen offset:1024 sc1\n\t"
"v_mfma_f32_16x16x32_bf16 v[132:135], a[108:111], a[204:207], v[132:135]\n\t"
"ds_write_b64 v6, v[128:129] offset:0\n\t"
"v_mfma_f32_16x16x32_bf16 v[132:135], a[176:179], a[220:223], v[132:135]\n\t"
"v_add_f32 v210, v210, v218\n\t"
"v_add_f32 v211, v211, v219\n\t"
"v_mfma_f32_16x16x32_bf16 v[132:135], a[180:183], a[236:239], v[132:135]\n\t"
"s_waitcnt lgkmcnt(3)\n\t"
"v_add_f32 v212, v212, v220\n\t"
"v_add_f32 v213, v213, v221\n\t"
"v_mfma_f32_16x16x32_bf16 v[132:135], a[184:187], a[252:255], v[132:135]\n\t"
"ds_write_b64 v6, v[130:131] offset:8\n\t"
"v_mfma_f32_16x16x32_bf16 v[136:139], v[112:115], a[140:143], 0\n\t"
"s_waitcnt lgkmcnt(2)\n\t"
"s_waitcnt lgkmcnt(3)\n\t"
"v_add_f32 v214, v214, v222\n\t"
"v_add_f32 v215, v215, v223\n\t"
"v_add_f32 v208, v208, v200\n\t"
"v_add_f32 v209, v209, v201\n\t"
"v_add_f32 v210, v210, v202\n\t"
"v_add_f32 v211, v211, v203\n\t"
"v_add_f32 v212, v212, v204\n\t"
"v_add_f32 v213, v213, v205\n\t"
"v_add_f32 v214, v214, v206\n\t"
"v_add_f32 v215, v215, v207\n\t"
"v_add_f32 v160, v160, v208\n\t"
"v_add_f32 v161, v161, v209\n\t"
"v_add_f32 v162, v162, v210\n\t"
"v_add_f32 v163, v163, v211\n\t"
"v_add_f32 v164, v164, v212\n\t"
"v_add_f32 v165, v165, v213\n\t"
"v_add_f32 v166, v166, v214\n\t"
"v_add_f32 v167, v167, v215\n\t"
"v_mfma_f32_16x16x32_bf16 v[136:139], a[16:19], a[156:159], v[136:139]\n\t"
"v_bfi_b32 v160, s27, v84, v160\n\t"
"v_bfi_b32 v161, s27, v85, v161\n\t"
"v_mfma_f32_16x16x32_bf16 v[136:139], a[48:51], a[172:175], v[136:139]\n\t"
"ds_write_b64 v6, v[132:133] offset:1024\n\t"
"v_mfma_f32_16x16x32_bf16 v[136:139], a[80:83], a[188:191], v[136:139]\n\t"
"v_bfi_b32 v162, s27, v86, v162\n\t"
"v_bfi_b32 v163, s27, v87, v163\n\t"
"v_mfma_f32_16x16x32_bf16 v[136:139], a[112:115], a[204:207], v[136:139]\n\t"
"v_bfi_b32 v164, s27, v84, v164\n\t"
"v_bfi_b32 v165, s27, v85, v165\n\t"
"v_bfi_b32 v166, s27, v86, v166\n\t"
"v_bfi_b32 v167, s27, v87, v167\n\t"
"v_mfma_f32_16x16x32_bf16 v[136:139], a[192:195], a[220:223], v[136:139]\n\t"
"buffer_store_dwordx4 v[160:163], v80, s[56:59], s20 offen sc1\n\t"
"v_mfma_f32_16x16x32_bf16 v[136:139], a[196:199], a[236:239], v[136:139]\n\t"
"buffer_store_dwordx4 v[164:167], v80, s[56:59], s20 offen offset:1024 sc1\n\t"
"v_mfma_f32_16x16x32_bf16 v[136:139], a[200:203], a[252:255], v[136:139]\n\t"
"ds_write_b64 v6, v[134:135] offset:1032\n\t"
"v_mfma_f32_16x16x32_bf16 v[140:143], v[116:119], a[140:143], 0\n\t"
"v_mfma_f32_16x16x32_bf16 v[140:143], a[20:23], a[156:159], v[140:143]\n\t"
"v_mfma_f32_16x16x32_bf16 v[140:143], a[52:55], a[172:175], v[140:143]\n\t"
"v_mfma_f32_16x16x32_bf16 v[140:143], a[84:87], a[188:191], v[140:143]\n\t"
"v_mfma_f32_16x16x32_bf16 v[140:143], a[116:119], a[204:207], v[140:143]\n\t"
"v_mfma_f32_16x16x32_bf16 v[140:143], a[208:211], a[220:223], v[140:143]\n\t"
"v_mfma_f32_16x16x32_bf16 v[140:143], a[212:215], a[236:239], v[140:143]\n\t"
"ds_write_b64 v7, v[136:137] offset:0\n\t"
"v_mfma_f32_16x16x32_bf16 v[140:143], a[216:219], a[252:255], v[140:143]\n\t"
"v_mfma_f32_16x16x32_bf16 v[144:147], v[120:123], a[140:143], 0\n\t"
"v_mfma_f32_16x16x32_bf16 v[144:147], a[24:27], a[156:159], v[144:147]\n\t"
"ds_write_b64 v7, v[138:139] offset:8\n\t"
"v_mfma_f32_16x16x32_bf16 v[144:147], a[56:59], a[172:175], v[144:147]\n\t"
"v_mfma_f32_16x16x32_bf16 v[144:147], a[88:91], a[188:191], v[144:147]\n\t"
"v_mfma_f32_16x16x32_bf16 v[144:147], a[120:123], a[204:207], v[144:147]\n\t"
"v_mfma_f32_16x16x32_bf16 v[144:147], a[224:227], a[220:223], v[144:147]\n\t"
"v_mfma_f32_16x16x32_bf16 v[144:147], a[228:231], a[236:239], v[144:147]\n\t"
"ds_write_b64 v7, v[140:141] offset:1024\n\t"
"v_mfma_f32_16x16x32_bf16 v[144:147], a[232:235], a[252:255], v[144:147]\n\t"
"v_mfma_f32_16x16x32_bf16 v[148:151], v[124:127], a[140:143], 0\n\t"
"v_mfma_f32_16x16x32_bf16 v[148:151], a[28:31], a[156:159], v[148:151]\n\t"
"ds_write_b64 v7, v[142:143] offset:1032\n\t"
"v_mfma_f32_16x16x32_bf16 v[148:151], a[60:63], a[172:175], v[148:151]\n\t"
"v_mfma_f32_16x16x32_bf16 v[148:151], a[92:95], a[188:191], v[148:151]\n\t"
"v_mfma_f32_16x16x32_bf16 v[148:151], a[124:127], a[204:207], v[148:151]\n\t"
"v_mfma_f32_16x16x32_bf16 v[148:151], a[240:243], a[220:223], v[148:151]\n\t"
"v_mfma_f32_16x16x32_bf16 v[148:151], a[244:247], a[236:239], v[148:151]\n\t"
"ds_write_b64 v8, v[144:145] offset:0\n\t"
"v_mfma_f32_16x16x32_bf16 v[148:151], a[248:251], a[252:255], v[148:151]\n\t"
"v_mfma_f32_16x16x32_bf16 v[152:155], v[96:99], a[140:143], 0\n\t"
"v_mfma_f32_16x16x32_bf16 v[152:155], a[0:3], a[156:159], v[152:155]\n\t"
"ds_write_b64 v8, v[146:147] offset:8\n\t"
"v_mfma_f32_16x16x32_bf16 v[152:155], a[32:35], a[172:175], v[152:155]\n\t"
"v_mfma_f32_16x16x32_bf16 v[152:155], a[64:67], a[188:191], v[152:155]\n\t"
"v_mfma_f32_16x16x32_bf16 v[152:155], a[96:99], a[204:207], v[152:155]\n\t"
"v_mfma_f32_16x16x32_bf16 v[152:155], a[128:131], a[220:223], v[152:155]\n\t"
"v_mfma_f32_16x16x32_bf16 v[152:155], a[132:135], a[236:239], v[152:155]\n\t"
"ds_write_b64 v8, v[148:149] offset:1024\n\t"
"v_mfma_f32_16x16x32_bf16 v[152:155], a[136:139], a[252:255], v[152:155]\n\t"
"v_mfma_f32_16x16x32_bf16 v[156:159], v[100:103], a[140:143], 0\n\t"
"v_mfma_f32_16x16x32_bf16 v[156:159], a[4:7], a[156:159], v[156:159]\n\t"
"s_lshr_b32 s80, s65, 1\n\t"
"s_add_u32 s80, s80, 8\n\t"
"ds_write_b64 v8, v[150:151] offset:1032\n\t"
"v_mfma_f32_16x16x32_bf16 v[156:159], a[36:39], a[172:175], v[156:159]\n\t"
"s_waitcnt vmcnt(14)\n\t"
"s_lshl_b32 s80, s80, 15\n\t"
"s_add_u32 s79, s80, s21\n\t"
"v_xor_b32 v28, v28, v84\n\t"
"v_mfma_f32_16x16x32_bf16 v[156:159], a[68:71], a[188:191], v[156:159]\n\t"
"s_mul_i32 s80, s19, 64\n\t"
"s_add_u32 s79, s79, s80\n\t"
"v_xor_b32 v29, v29, v85\n\t"
"v_mfma_f32_16x16x32_bf16 v[156:159], a[100:103], a[204:207], v[156:159]\n\t"
"s_lshl_b32 s80, s71, 5\n\t"
"s_add_u32 s79, s79, s80\n\t"
"v_xor_b32 v30, v30, v86\n\t"
"v_mfma_f32_16x16x32_bf16 v[156:159], a[144:147], a[220:223], v[156:159]\n\t"
"v_xor_b32 v31, v31, v87\n\t"
"v_or3_b32 v90, v28, v29, v30\n\t"
"v_mfma_f32_16x16x32_bf16 v[156:159], a[148:151], a[236:239], v[156:159]\n\t"
"s_waitcnt vmcnt(12)\n\t"
"v_or_b32 v90, v31, v90\n\t"
"v_xor_b32 v24, v24, v84\n\t"
"v_mfma_f32_16x16x32_bf16 v[156:159], a[152:155], a[252:255], v[156:159]\n\t"
"s_waitcnt lgkmcnt(0)\n\t"
"s_barrier\n\t"
"ds_read_b128 v[208:211], v9 offset:2048\n\t"
"v_xor_b32 v25, v25, v85\n\t"
"v_mfma_f32_16x16x32_bf16 v[128:131], v[104:107], v[168:171], 0\n\t"
"ds_read_b128 v[212:215], v9 offset:3072\n\t"
"v_xor_b32 v26, v26, v86\n\t"
"v_mfma_f32_16x16x32_bf16 v[128:131], a[8:11], v[172:175], v[128:131]\n\t"
"ds_read_b128 v[216:219], v9 offset:4096\n\t"
"v_xor_b32 v27, v27, v87\n\t"
"v_mfma_f32_16x16x32_bf16 v[128:131], a[40:43], v[176:179], v[128:131]\n\t"
"ds_read_b128 v[220:223], v9 offset:5120\n\t"
"v_or3_b32 v90, v24, v25, v90\n\t"
"v_mfma_f32_16x16x32_bf16 v[128:131], a[72:75], v[180:183], v[128:131]\n\t"
"ds_read_b128 v[200:203], v9 offset:0\n\t"
"v_or3_b32 v90, v26, v27, v90\n\t"
"v_mfma_f32_16x16x32_bf16 v[128:131], a[104:107], v[184:187], v[128:131]\n\t"
"s_waitcnt vmcnt(11)\n\t"
"ds_read_b128 v[204:207], v9 offset:1024\n\t"
"v_xor_b32 v20, v20, v84\n\t"
"v_mfma_f32_16x16x32_bf16 v[128:131], a[160:163], v[188:191], v[128:131]\n\t"
"v_xor_b32 v21, v21, v85\n\t"
"v_xor_b32 v22, v22, v86\n\t"
"v_mfma_f32_16x16x32_bf16 v[128:131], a[164:167], v[192:195], v[128:131]\n\t"
"v_xor_b32 v23, v23, v87\n\t"
"v_or3_b32 v90, v20, v21, v90\n\t"
"v_mfma_f32_16x16x32_bf16 v[128:131], a[168:171], v[196:199], v[128:131]\n\t"
"s_waitcnt vmcnt(10)\n\t"
"v_or3_b32 v90, v22, v23, v90\n\t"
"v_xor_b32 v16, v16, v84\n\t"
"v_mfma_f32_16x16x32_bf16 v[132:135], v[108:111], v[168:171], 0\n\t"
"v_xor_b32 v17, v17, v85\n\t"
"v_xor_b32 v18, v18, v86\n\t"
"v_mfma_f32_16x16x32_bf16 v[132:135], a[12:15], v[172:175], v[132:135]\n\t"
"v_xor_b32 v19, v19, v87\n\t"
"v_or3_b32 v90, v16, v17, v90\n\t"
"v_mfma_f32_16x16x32_bf16 v[132:135], a[44:47], v[176:179], v[132:135]\n\t"
"ds_write_b64 v6, v[128:129] offset:6144\n\t"
"v_mfma_f32_16x16x32_bf16 v[132:135], a[76:79], v[180:183], v[132:135]\n\t"
"s_waitcnt lgkmcnt(4)\n\t"
"v_or3_b32 v90, v18, v19, v90\n\t"
"v_add_f32 v208, v208, v216\n\t"
"v_mfma_f32_16x16x32_bf16 v[132:135], a[108:111], v[184:187], v[132:135]\n\t"
"v_and_b32 v90, s27, v90\n\t"
"v_cmp_ne_u32 vcc, 0, v90\n\t"
"s_nop 0\n\t"
"s_cmp_eq_u64 vcc, 0\n\t"
"s_cbranch_scc0 LTS22_%=\n\t"
"LTR22_%=:\n\t"
"v_mfma_f32_16x16x32_bf16 v[132:135], a[176:179], v[188:191], v[132:135]\n\t"
"ds_write_b64 v6, v[130:131] offset:6152\n\t"
"v_mfma_f32_16x16x32_bf16 v[132:135], a[180:183], v[192:195], v[132:135]\n\t"
"v_add_f32 v24, v24, v28\n\t"
"v_add_f32 v25, v25, v29\n\t"
"v_mfma_f32_16x16x32_bf16 v[132:135], a[184:187], v[196:199], v[132:135]\n\t"
"s_lshr_b64 s[80:81], s[82:83], 36\n\t"
"s_and_b32 s80, s80, 15\n\t"
"v_add_f32 v26, v26, v30\n\t"
"v_mfma_f32_16x16x32_bf16 v[136:139], v[112:115], v[168:171], 0\n\t"
"s_mul_i32 s80, s80, 32768\n\t"
"s_add_u32 s23, s80, s95\n\t"
"v_add_f32 v27, v27, v31\n\t"
"v_mfma_f32_16x16x32_bf16 v[136:139], a[16:19], v[172:175], v[136:139]\n\t"
"ds_write_b64 v6, v[132:133] offset:7168\n\t"
"v_mfma_f32_16x16x32_bf16 v[136:139], a[48:51], v[176:179], v[136:139]\n\t"
"v_add_f32 v24, v24, v20\n\t"
"v_add_f32 v25, v25, v21\n\t"
"v_mfma_f32_16x16x32_bf16 v[136:139], a[80:83], v[180:183], v[136:139]\n\t"
"v_add_f32 v26, v26, v22\n\t"
"v_add_f32 v27, v27, v23\n\t"
"v_add_f32 v16, v16, v24\n\t"
"v_add_f32 v17, v17, v25\n\t"
"v_add_f32 v18, v18, v26\n\t"
"v_add_f32 v19, v19, v27\n\t"
"v_cvt_pk_bf16_f32 v224, v16, v17\n\t"
"v_cvt_pk_bf16_f32 v225, v18, v19\n\t"
"buffer_store_dwordx2 v[224:225], v88, s[60:63], s79 offen\n\t"
"v_mfma_f32_16x16x32_bf16 v[136:139], a[112:115], v[184:187], v[136:139]\n\t"
"buffer_load_dwordx4 v[72:75], v83, s[56:59], s23 offen sc1\n\t"
"v_mfma_f32_16x16x32_bf16 v[136:139], a[192:195], v[188:191], v[136:139]\n\t"
"ds_write_b64 v6, v[134:135] offset:7176\n\t"
"v_mfma_f32_16x16x32_bf16 v[136:139], a[196:199], v[192:195], v[136:139]\n\t"
"buffer_load_dwordx4 v[76:79], v83, s[56:59], s23 offen offset:1024 sc1\n\t"
"v_mfma_f32_16x16x32_bf16 v[136:139], a[200:203], v[196:199], v[136:139]\n\t"
"buffer_load_dwordx4 v[64:67], v82, s[56:59], s23 offen sc1\n\t"
"v_mfma_f32_16x16x32_bf16 v[140:143], v[116:119], v[168:171], 0\n\t"
"v_add_f32 v209, v209, v217\n\t"
"v_add_f32 v210, v210, v218\n\t"
"v_mfma_f32_16x16x32_bf16 v[140:143], a[20:23], v[172:175], v[140:143]\n\t"
"s_lshr_b64 s[80:81], s[82:83], 32\n\t"
"s_and_b32 s80, s80, 15\n\t"
"v_add_f32 v211, v211, v219\n\t"
"v_mfma_f32_16x16x32_bf16 v[140:143], a[52:55], v[176:179], v[140:143]\n\t"
"s_mul_i32 s80, s80, 32768\n\t"
"s_add_u32 s79, s80, s21\n\t"
"ds_write_b64 v7, v[136:137] offset:6144\n\t"
"v_mfma_f32_16x16x32_bf16 v[140:143], a[84:87], v[180:183], v[140:143]\n\t"
"buffer_load_dwordx4 v[68:71], v82, s[56:59], s23 offen offset:1024 sc1\n\t"
"v_mfma_f32_16x16x32_bf16 v[140:143], a[116:119], v[184:187], v[140:143]\n\t"
"s_waitcnt lgkmcnt(7)\n\t"
"v_add_f32 v212, v212, v220\n\t"
"v_add_f32 v213, v213, v221\n\t"
"v_mfma_f32_16x16x32_bf16 v[140:143], a[208:211], v[188:191], v[140:143]\n\t"
"ds_write_b64 v7, v[138:139] offset:6152\n\t"
"v_mfma_f32_16x16x32_bf16 v[140:143], a[212:215], v[192:195], v[140:143]\n\t"
"s_waitcnt lgkmcnt(6)\n\t"
"s_waitcnt lgkmcnt(7)\n\t"
"v_add_f32 v214, v214, v222\n\t"
"v_add_f32 v215, v215, v223\n\t"
"v_add_f32 v208, v208, v200\n\t"
"v_add_f32 v209, v209, v201\n\t"
"v_add_f32 v210, v210, v202\n\t"
"v_add_f32 v211, v211, v203\n\t"
"v_add_f32 v212, v212, v204\n\t"
"v_add_f32 v213, v213, v205\n\t"
"v_add_f32 v214, v214, v206\n\t"
"v_add_f32 v215, v215, v207\n\t"
"v_add_f32 v152, v152, v208\n\t"
"v_add_f32 v153, v153, v209\n\t"
"v_add_f32 v154, v154, v210\n\t"
"v_add_f32 v155, v155, v211\n\t"
"v_add_f32 v156, v156, v212\n\t"
"v_add_f32 v157, v157, v213\n\t"
"v_add_f32 v158, v158, v214\n\t"
"v_add_f32 v159, v159, v215\n\t"
"v_mfma_f32_16x16x32_bf16 v[140:143], a[216:219], v[196:199], v[140:143]\n\t"
"s_waitcnt vmcnt(12)\n\t"
"v_xor_b32 v40, v40, v84\n\t"
"v_xor_b32 v41, v41, v85\n\t"
"v_mfma_f32_16x16x32_bf16 v[144:147], v[120:123], v[168:171], 0\n\t"
"v_xor_b32 v42, v42, v86\n\t"
"v_xor_b32 v43, v43, v87\n\t"
"v_mfma_f32_16x16x32_bf16 v[144:147], a[24:27], v[172:175], v[144:147]\n\t"
"v_or3_b32 v90, v40, v41, v42\n\t"
"v_or_b32 v90, v43, v90\n\t"
"v_mfma_f32_16x16x32_bf16 v[144:147], a[56:59], v[176:179], v[144:147]\n\t"
"ds_write_b64 v7, v[140:141] offset:7168\n\t"
"v_mfma_f32_16x16x32_bf16 v[144:147], a[88:91], v[180:183], v[144:147]\n\t"
"s_waitcnt vmcnt(11)\n\t"
"v_xor_b32 v44, v44, v84\n\t"
"v_xor_b32 v45, v45, v85\n\t"
"v_mfma_f32_16x16x32_bf16 v[144:147], a[120:123], v[184:187], v[144:147]\n\t"
"v_xor_b32 v46, v46, v86\n\t"
"v_xor_b32 v47, v47, v87\n\t"
"v_mfma_f32_16x16x32_bf16 v[144:147], a[224:227], v[188:191], v[144:147]\n\t"
"ds_write_b64 v7, v[142:143] offset:7176\n\t"
"v_mfma_f32_16x16x32_bf16 v[144:147], a[228:231], v[192:195], v[144:147]\n\t"
"v_or3_b32 v90, v44, v45, v90\n\t"
"v_or3_b32 v90, v46, v47, v90\n\t"
"v_mfma_f32_16x16x32_bf16 v[144:147], a[232:235], v[196:199], v[144:147]\n\t"
"s_waitcnt vmcnt(10)\n\t"
"v_xor_b32 v32, v32, v84\n\t"
"v_xor_b32 v33, v33, v85\n\t"
"v_mfma_f32_16x16x32_bf16 v[148:151], v[124:127], v[168:171], 0\n\t"
"v_xor_b32 v34, v34, v86\n\t"
"v_xor_b32 v35, v35, v87\n\t"
"v_mfma_f32_16x16x32_bf16 v[148:151], a[28:31], v[172:175], v[148:151]\n\t"
"v_or3_b32 v90, v32, v33, v90\n\t"
"v_or3_b32 v90, v34, v35, v90\n\t"
"v_mfma_f32_16x16x32_bf16 v[148:151], a[60:63], v[176:179], v[148:151]\n\t"
"ds_write_b64 v8, v[144:145] offset:6144\n\t"
"v_mfma_f32_16x16x32_bf16 v[148:151], a[92:95], v[180:183], v[148:151]\n\t"
"s_waitcnt vmcnt(9)\n\t"
"v_xor_b32 v36, v36, v84\n\t"
"v_xor_b32 v37, v37, v85\n\t"
"v_mfma_f32_16x16x32_bf16 v[148:151], a[124:127], v[184:187], v[148:151]\n\t"
"v_xor_b32 v38, v38, v86\n\t"
"v_xor_b32 v39, v39, v87\n\t"
"v_mfma_f32_16x16x32_bf16 v[148:151], a[240:243], v[188:191], v[148:151]\n\t"
"ds_write_b64 v8, v[146:147] offset:6152\n\t"
"v_mfma_f32_16x16x32_bf16 v[148:151], a[244:247], v[192:195], v[148:151]\n\t"
"v_or3_b32 v90, v36, v37, v90\n\t"
"v_or3_b32 v90, v38, v39, v90\n\t"
"v_mfma_f32_16x16x32_bf16 v[148:151], a[248:251], v[196:199], v[148:151]\n\t"
"s_waitcnt vmcnt(8)\n\t"
"v_xor_b32 v48, v48, v84\n\t"
"v_xor_b32 v49, v49, v85\n\t"
"v_mfma_f32_16x16x32_bf16 v[160:163], v[96:99], v[168:171], 0\n\t"
"v_xor_b32 v50, v50, v86\n\t"
"v_xor_b32 v51, v51, v87\n\t"
"v_mfma_f32_16x16x32_bf16 v[160:163], a[0:3], v[172:175], v[160:163]\n\t"
"v_or3_b32 v90, v48, v49, v90\n\t"
"v_or3_b32 v90, v50, v51, v90\n\t"
"v_mfma_f32_16x16x32_bf16 v[160:163], a[32:35], v[176:179], v[160:163]\n\t"
"ds_write_b64 v8, v[148:149] offset:7168\n\t"
"v_mfma_f32_16x16x32_bf16 v[160:163], a[64:67], v[180:183], v[160:163]\n\t"
"s_waitcnt vmcnt(7)\n\t"
"v_xor_b32 v52, v52, v84\n\t"
"v_xor_b32 v53, v53, v85\n\t"
"v_mfma_f32_16x16x32_bf16 v[160:163], a[96:99], v[184:187], v[160:163]\n\t"
"v_xor_b32 v54, v54, v86\n\t"
"v_xor_b32 v55, v55, v87\n\t"
"v_mfma_f32_16x16x32_bf16 v[160:163], a[128:131], v[188:191], v[160:163]\n\t"
"ds_write_b64 v8, v[150:151] offset:7176\n\t"
"v_mfma_f32_16x16x32_bf16 v[160:163], a[132:135], v[192:195], v[160:163]\n\t"
"v_or3_b32 v90, v52, v53, v90\n\t"
"v_or3_b32 v90, v54, v55, v90\n\t"
"v_mfma_f32_16x16x32_bf16 v[160:163], a[136:139], v[196:199], v[160:163]\n\t"
"v_and_b32 v90, s27, v90\n\t"
"v_cmp_ne_u32 vcc, 0, v90\n\t"
"s_nop 0\n\t"
"s_cmp_eq_u64 vcc, 0\n\t"
"s_cbranch_scc0 LTS23_%=\n\t"
"LTR23_%=:\n\t"
"v_add_f32 v32, v32, v40\n\t"
"v_add_f32 v33, v33, v41\n\t"
"v_add_f32 v34, v34, v42\n\t"
"v_add_f32 v35, v35, v43\n\t"
"v_add_f32 v36, v36, v44\n\t"
"v_add_f32 v37, v37, v45\n\t"
"v_add_f32 v38, v38, v46\n\t"
"v_add_f32 v39, v39, v47\n\t"
"v_add_f32 v32, v32, v48\n\t"
"v_add_f32 v33, v33, v49\n\t"
"v_add_f32 v34, v34, v50\n\t"
"v_add_f32 v35, v35, v51\n\t"
"v_add_f32 v36, v36, v52\n\t"
"v_add_f32 v37, v37, v53\n\t"
"v_add_f32 v38, v38, v54\n\t"
"v_add_f32 v39, v39, v55\n\t"
"v_mfma_f32_16x16x32_bf16 v[164:167], v[100:103], v[168:171], 0\n\t"
"v_add_f32 v152, v152, v32\n\t"
"v_add_f32 v153, v153, v33\n\t"
"v_mfma_f32_16x16x32_bf16 v[164:167], a[4:7], v[172:175], v[164:167]\n\t"
"buffer_load_dwordx4 v[56:59], v81, s[56:59], s23 offen sc1\n\t"
"v_mfma_f32_16x16x32_bf16 v[164:167], a[36:39], v[176:179], v[164:167]\n\t"
"v_add_f32 v154, v154, v34\n\t"
"v_add_f32 v155, v155, v35\n\t"
"v_mfma_f32_16x16x32_bf16 v[164:167], a[68:71], v[180:183], v[164:167]\n\t"
"buffer_load_dwordx4 v[60:63], v81, s[56:59], s23 offen offset:1024 sc1\n\t"
"v_mfma_f32_16x16x32_bf16 v[164:167], a[100:103], v[184:187], v[164:167]\n\t"
"v_add_f32 v156, v156, v36\n\t"
"v_add_f32 v157, v157, v37\n\t"
"v_mfma_f32_16x16x32_bf16 v[164:167], a[144:147], v[188:191], v[164:167]\n\t"
"v_add_f32 v158, v158, v38\n\t"
"v_add_f32 v159, v159, v39\n\t"
"v_mfma_f32_16x16x32_bf16 v[164:167], a[148:151], v[192:195], v[164:167]\n\t"
"v_cvt_pk_bf16_f32 v224, v152, v153\n\t"
"v_cvt_pk_bf16_f32 v225, v154, v155\n\t"
"v_mfma_f32_16x16x32_bf16 v[164:167], a[152:155], v[196:199], v[164:167]\n\t"
"s_waitcnt lgkmcnt(0)\n\t"
"s_barrier\n\t"
"ds_read_b128 v[208:211], v9 offset:8192\n\t"
"ds_read_b128 v[212:215], v9 offset:9216\n\t"
"ds_read_b128 v[216:219], v9 offset:10240\n\t"
"ds_read_b128 v[220:223], v9 offset:11264\n\t"
"ds_read_b128 v[200:203], v9 offset:6144\n\t"
"ds_read_b128 v[204:207], v9 offset:7168\n\t"
"v_cvt_pk_bf16_f32 v226, v156, v157\n\t"
"v_cvt_pk_bf16_f32 v227, v158, v159\n\t"
"buffer_store_dwordx2 v[224:225], v88, s[60:63], s79 offen\n\t"
"buffer_store_dwordx2 v[226:227], v88, s[60:63], s79 offen offset:32\n\t"
"s_waitcnt lgkmcnt(3)\n\t"
"v_add_f32 v208, v208, v216\n\t"
"v_add_f32 v209, v209, v217\n\t"
"v_add_f32 v210, v210, v218\n\t"
"v_add_f32 v211, v211, v219\n\t"
"s_waitcnt lgkmcnt(2)\n\t"
"v_add_f32 v212, v212, v220\n\t"
"v_add_f32 v213, v213, v221\n\t"
"v_add_f32 v214, v214, v222\n\t"
"v_add_f32 v215, v215, v223\n\t"
"s_waitcnt lgkmcnt(1)\n\t"
"v_add_f32 v208, v208, v200\n\t"
"v_add_f32 v209, v209, v201\n\t"
"v_add_f32 v210, v210, v202\n\t"
"v_add_f32 v211, v211, v203\n\t"
"s_waitcnt lgkmcnt(0)\n\t"
"v_add_f32 v212, v212, v204\n\t"
"v_add_f32 v213, v213, v205\n\t"
"v_add_f32 v214, v214, v206\n\t"
"v_add_f32 v215, v215, v207\n\t"
"v_add_f32 v160, v160, v208\n\t"
"v_add_f32 v161, v161, v209\n\t"
"v_add_f32 v162, v162, v210\n\t"
"v_add_f32 v163, v163, v211\n\t"
"v_add_f32 v164, v164, v212\n\t"
"v_add_f32 v165, v165, v213\n\t"
"v_add_f32 v166, v166, v214\n\t"
"v_add_f32 v167, v167, v215\n\t"
"s_or_b32 s78, s67, s65\n\t"
"s_or_b32 s78, s78, s69\n\t"
"s_cmp_eq_u32 s78, 0\n\t"
"s_cselect_b32 s78, 0, 1073741824\n\t"
"s_add_u32 s81, s97, 1\n\t"
"v_mov_b32 v254, s81\n\t"
"v_add_u32 v255, s78, v5\n\t"
"buffer_store_dword v254, v255, s[56:59], s99 offen sc1\n\t"
"s_waitcnt vmcnt(8)\n\t"
"v_xor_b32 v72, v72, v84\n\t"
"v_xor_b32 v73, v73, v85\n\t"
"v_xor_b32 v74, v74, v86\n\t"
"v_xor_b32 v75, v75, v87\n\t"
"v_or3_b32 v90, v72, v73, v74\n\t"
"v_or_b32 v90, v75, v90\n\t"
"s_waitcnt vmcnt(7)\n\t"
"v_xor_b32 v76, v76, v84\n\t"
"v_xor_b32 v77, v77, v85\n\t"
"v_xor_b32 v78, v78, v86\n\t"
"v_xor_b32 v79, v79, v87\n\t"
"v_or3_b32 v90, v76, v77, v90\n\t"
"v_or3_b32 v90, v78, v79, v90\n\t"
"s_waitcnt vmcnt(6)\n\t"
"v_xor_b32 v64, v64, v84\n\t"
"v_xor_b32 v65, v65, v85\n\t"
"v_xor_b32 v66, v66, v86\n\t"
"v_xor_b32 v67, v67, v87\n\t"
"v_or3_b32 v90, v64, v65, v90\n\t"
"v_or3_b32 v90, v66, v67, v90\n\t"
"s_waitcnt vmcnt(5)\n\t"
"v_xor_b32 v68, v68, v84\n\t"
"v_xor_b32 v69, v69, v85\n\t"
"v_xor_b32 v70, v70, v86\n\t"
"v_xor_b32 v71, v71, v87\n\t"
"v_or3_b32 v90, v68, v69, v90\n\t"
"v_or3_b32 v90, v70, v71, v90\n\t"
"v_and_b32 v90, s27, v90\n\t"
"v_cmp_ne_u32 vcc, 0, v90\n\t"
"s_nop 0\n\t"
"s_cmp_eq_u64 vcc, 0\n\t"
"s_cbranch_scc0 LTS24_%=\n\t"
"LTR24_%=:\n\t"
"v_add_f32 v64, v64, v72\n\t"
"v_add_f32 v65, v65, v73\n\t"
"v_add_f32 v66, v66, v74\n\t"
"v_add_f32 v67, v67, v75\n\t"
"v_add_f32 v68, v68, v76\n\t"
"v_add_f32 v69, v69, v77\n\t"
"v_add_f32 v70, v70, v78\n\t"
"v_add_f32 v71, v71, v79\n\t"
"s_waitcnt vmcnt(4)\n\t"
"v_xor_b32 v56, v56, v84\n\t"
"v_xor_b32 v57, v57, v85\n\t"
"v_xor_b32 v58, v58, v86\n\t"
"v_xor_b32 v59, v59, v87\n\t"
"v_or3_b32 v90, v56, v57, v58\n\t"
"v_or_b32 v90, v59, v90\n\t"
"s_waitcnt vmcnt(3)\n\t"
"v_xor_b32 v60, v60, v84\n\t"
"v_xor_b32 v61, v61, v85\n\t"
"v_xor_b32 v62, v62, v86\n\t"
"v_xor_b32 v63, v63, v87\n\t"
"v_or3_b32 v90, v60, v61, v90\n\t"
"v_or3_b32 v90, v62, v63, v90\n\t"
"v_and_b32 v90, s27, v90\n\t"
"v_cmp_ne_u32 vcc, 0, v90\n\t"
"s_nop 0\n\t"
"s_cmp_eq_u64 vcc, 0\n\t"
"s_cbranch_scc0 LTS25_%=\n\t"
"LTR25_%=:\n\t"
"v_add_f32 v64, v64, v56\n\t"
"v_add_f32 v65, v65, v57\n\t"
"v_add_f32 v66, v66, v58\n\t"
"v_add_f32 v67, v67, v59\n\t"
"v_add_f32 v68, v68, v60\n\t"
"v_add_f32 v69, v69, v61\n\t"
"v_add_f32 v70, v70, v62\n\t"
"v_add_f32 v71, v71, v63\n\t"
"v_add_f32 v160, v160, v64\n\t"
"v_add_f32 v161, v161, v65\n\t"
"v_add_f32 v162, v162, v66\n\t"
"v_add_f32 v163, v163, v67\n\t"
"v_add_f32 v164, v164, v68\n\t"
"v_add_f32 v165, v165, v69\n\t"
"v_add_f32 v166, v166, v70\n\t"
"v_add_f32 v167, v167, v71\n\t"
"v_cvt_pk_bf16_f32 v224, v160, v161\n\t"
"v_cvt_pk_bf16_f32 v225, v162, v163\n\t"
"v_cvt_pk_bf16_f32 v226, v164, v165\n\t"
"v_cvt_pk_bf16_f32 v227, v166, v167\n\t"
"s_lshr_b64 s[80:81], s[82:83], 36\n\t"
"s_and_b32 s80, s80, 15\n\t"
"s_mul_i32 s80, s80, 32768\n\t"
"s_add_u32 s79, s80, s21\n\t"
"buffer_store_dwordx2 v[224:225], v88, s[60:63], s79 offen\n\t"
"buffer_store_dwordx2 v[226:227], v88, s[60:63], s79 offen offset:32\n\t"
"s_waitcnt vmcnt(0) lgkmcnt(0)\n\t"
"s_branch LEND_%=\n\t"
"LPS18_%=:\n\t"
"s_mov_b32 s96, 100000\n\t"
"LPS18_%=x:\n\t"
"s_sleep 1\n\t"
"buffer_load_dwordx2 v[2:3], v15, s[56:59], s16 offen sc1\n\t"
"s_waitcnt vmcnt(0) lgkmcnt(0)\n\t"
"v_cmp_le_u64 vcc, s[90:91], v[2:3]\n\t"
"v_cmp_ge_u64 s[92:93], s[88:89], v[2:3]\n\t"
"s_nop 0\n\t"
"s_and_b64 vcc, vcc, s[92:93]\n\t"
"s_cmp_eq_u64 vcc, -1\n\t"
"s_cbranch_scc1 LPR18_%=\n\t"
"s_sub_u32 s96, s96, 1\n\t"
"s_cmp_lg_u32 s96, 0\n\t"
"s_cbranch_scc1 LPS18_%=x\n\t"
"s_branch LPR18_%=\n\t"
"LPS19_%=:\n\t"
"s_mov_b32 s96, 100000\n\t"
"LPS19_%=x:\n\t"
"s_sleep 1\n\t"
"buffer_load_dwordx2 v[2:3], v15, s[56:59], s16 offen sc1\n\t"
"s_waitcnt vmcnt(0) lgkmcnt(0)\n\t"
"v_cmp_le_u64 vcc, s[90:91], v[2:3]\n\t"
"v_cmp_ge_u64 s[92:93], s[88:89], v[2:3]\n\t"
"s_nop 0\n\t"
"s_and_b64 vcc, vcc, s[92:93]\n\t"
"s_cmp_eq_u64 vcc, -1\n\t"
"s_cbranch_scc1 LPR19_%=\n\t"
"s_sub_u32 s96, s96, 1\n\t"
"s_cmp_lg_u32 s96, 0\n\t"
"s_cbranch_scc1 LPS19_%=x\n\t"
"s_branch LPR19_%=\n\t"
"LPS20_%=:\n\t"
"s_mov_b32 s96, 100000\n\t"
"LPS20_%=x:\n\t"
"s_sleep 1\n\t"
"buffer_load_dwordx2 v[2:3], v15, s[56:59], s16 offen sc1\n\t"
"s_waitcnt vmcnt(0) lgkmcnt(0)\n\t"
"v_cmp_le_u64 vcc, s[90:91], v[2:3]\n\t"
"v_cmp_ge_u64 s[92:93], s[88:89], v[2:3]\n\t"
"s_nop 0\n\t"
"s_and_b64 vcc, vcc, s[92:93]\n\t"
"s_cmp_eq_u64 vcc, -1\n\t"
"s_cbranch_scc1 LPR20_%=\n\t"
"s_sub_u32 s96, s96, 1\n\t"
"s_cmp_lg_u32 s96, 0\n\t"
"s_cbranch_scc1 LPS20_%=x\n\t"
"s_branch LPR20_%=\n\t"
"LPS21_%=:\n\t"
"s_mov_b32 s96, 100000\n\t"
"LPS21_%=x:\n\t"
"s_sleep 1\n\t"
"buffer_load_dwordx2 v[2:3], v15, s[56:59], s16 offen sc1\n\t"
"s_waitcnt vmcnt(0) lgkmcnt(0)\n\t"
"v_cmp_le_u64 vcc, s[90:91], v[2:3]\n\t"
"v_cmp_ge_u64 s[92:93], s[88:89], v[2:3]\n\t"
"s_nop 0\n\t"
"s_and_b64 vcc, vcc, s[92:93]\n\t"
"s_cmp_eq_u64 vcc, -1\n\t"
"s_cbranch_scc1 LPR21_%=\n\t"
"s_sub_u32 s96, s96, 1\n\t"
"s_cmp_lg_u32 s96, 0\n\t"
"s_cbranch_scc1 LPS21_%=x\n\t"
"s_branch LPR21_%=\n\t"
"LTS22_%=:\n\t"
"s_mov_b32 s96, 100000\n\t"
"LTS22_%=x:\n\t"
"s_waitcnt vmcnt(0) lgkmcnt(0)\n\t"
"buffer_load_dwordx4 v[28:31], v83, s[56:59], s14 offen sc1\n\t"
"buffer_load_dwordx4 v[24:27], v82, s[56:59], s14 offen sc1\n\t"
"buffer_load_dwordx4 v[20:23], v81, s[56:59], s14 offen sc1\n\t"
"buffer_load_dwordx4 v[16:19], v80, s[56:59], s14 offen sc1\n\t"
"s_waitcnt vmcnt(3)\n\t"
"v_xor_b32 v28, v28, v84\n\t"
"v_xor_b32 v29, v29, v85\n\t"
"v_xor_b32 v30, v30, v86\n\t"
"v_xor_b32 v31, v31, v87\n\t"
"v_or3_b32 v90, v28, v29, v30\n\t"
"v_or_b32 v90, v31, v90\n\t"
"s_waitcnt vmcnt(2)\n\t"
"v_xor_b32 v24, v24, v84\n\t"
"v_xor_b32 v25, v25, v85\n\t"
"v_xor_b32 v26, v26, v86\n\t"
"v_xor_b32 v27, v27, v87\n\t"
"v_or3_b32 v90, v24, v25, v90\n\t"
"v_or3_b32 v90, v26, v27, v90\n\t"
"s_waitcnt vmcnt(1)\n\t"
"v_xor_b32 v20, v20, v84\n\t"
"v_xor_b32 v21, v21, v85\n\t"
"v_xor_b32 v22, v22, v86\n\t"
"v_xor_b32 v23, v23, v87\n\t"
"v_or3_b32 v90, v20, v21, v90\n\t"
"v_or3_b32 v90, v22, v23, v90\n\t"
"s_waitcnt vmcnt(0)\n\t"
"v_xor_b32 v16, v16, v84\n\t"
"v_xor_b32 v17, v17, v85\n\t"
"v_xor_b32 v18, v18, v86\n\t"
"v_xor_b32 v19, v19, v87\n\t"
"v_or3_b32 v90, v16, v17, v90\n\t"
"v_or3_b32 v90, v18, v19, v90\n\t"
"v_and_b32 v90, s27, v90\n\t"
"v_cmp_ne_u32 vcc, 0, v90\n\t"
"s_nop 0\n\t"
"s_cmp_eq_u64 vcc, 0\n\t"
"s_cbranch_scc1 LTR22_%=\n\t"
"s_sub_u32 s96, s96, 1\n\t"
"s_cmp_lg_u32 s96, 0\n\t"
"s_cbranch_scc1 LTS22_%=x\n\t"
"s_branch LTR22_%=\n\t"
"LTS23_%=:\n\t"
"s_mov_b32 s96, 100000\n\t"
"LTS23_%=x:\n\t"
"s_waitcnt vmcnt(0) lgkmcnt(0)\n\t"
"buffer_load_dwordx4 v[40:43], v83, s[56:59], s15 offen sc1\n\t"
"buffer_load_dwordx4 v[44:47], v83, s[56:59], s15 offen offset:1024 sc1\n\t"
"buffer_load_dwordx4 v[32:35], v82, s[56:59], s15 offen sc1\n\t"
"buffer_load_dwordx4 v[36:39], v82, s[56:59], s15 offen offset:1024 sc1\n\t"
"buffer_load_dwordx4 v[48:51], v81, s[56:59], s15 offen sc1\n\t"
"buffer_load_dwordx4 v[52:55], v81, s[56:59], s15 offen offset:1024 sc1\n\t"
"s_waitcnt vmcnt(5)\n\t"
"v_xor_b32 v40, v40, v84\n\t"
"v_xor_b32 v41, v41, v85\n\t"
"v_xor_b32 v42, v42, v86\n\t"
"v_xor_b32 v43, v43, v87\n\t"
"v_or3_b32 v90, v40, v41, v42\n\t"
"v_or_b32 v90, v43, v90\n\t"
"s_waitcnt vmcnt(4)\n\t"
"v_xor_b32 v44, v44, v84\n\t"
"v_xor_b32 v45, v45, v85\n\t"
"v_xor_b32 v46, v46, v86\n\t"
"v_xor_b32 v47, v47, v87\n\t"
"v_or3_b32 v90, v44, v45, v90\n\t"
"v_or3_b32 v90, v46, v47, v90\n\t"
"s_waitcnt vmcnt(3)\n\t"
"v_xor_b32 v32, v32, v84\n\t"
"v_xor_b32 v33, v33, v85\n\t"
"v_xor_b32 v34, v34, v86\n\t"
"v_xor_b32 v35, v35, v87\n\t"
"v_or3_b32 v90, v32, v33, v90\n\t"
"v_or3_b32 v90, v34, v35, v90\n\t"
"s_waitcnt vmcnt(2)\n\t"
"v_xor_b32 v36, v36, v84\n\t"
"v_xor_b32 v37, v37, v85\n\t"
"v_xor_b32 v38, v38, v86\n\t"
"v_xor_b32 v39, v39, v87\n\t"
"v_or3_b32 v90, v36, v37, v90\n\t"
"v_or3_b32 v90, v38, v39, v90\n\t"
"s_waitcnt vmcnt(1)\n\t"
"v_xor_b32 v48, v48, v84\n\t"
"v_xor_b32 v49, v49, v85\n\t"
"v_xor_b32 v50, v50, v86\n\t"
"v_xor_b32 v51, v51, v87\n\t"
"v_or3_b32 v90, v48, v49, v90\n\t"
"v_or3_b32 v90, v50, v51, v90\n\t"
"s_waitcnt vmcnt(0)\n\t"
"v_xor_b32 v52, v52, v84\n\t"
"v_xor_b32 v53, v53, v85\n\t"
"v_xor_b32 v54, v54, v86\n\t"
"v_xor_b32 v55, v55, v87\n\t"
"v_or3_b32 v90, v52, v53, v90\n\t"
"v_or3_b32 v90, v54, v55, v90\n\t"
"v_and_b32 v90, s27, v90\n\t"
"v_cmp_ne_u32 vcc, 0, v90\n\t"
"s_nop 0\n\t"
"s_cmp_eq_u64 vcc, 0\n\t"
"s_cbranch_scc1 LTR23_%=\n\t"
"s_sub_u32 s96, s96, 1\n\t"
"s_cmp_lg_u32 s96, 0\n\t"
"s_cbranch_scc1 LTS23_%=x\n\t"
"s_branch LTR23_%=\n\t"
"LTS24_%=:\n\t"
"s_mov_b32 s96, 100000\n\t"
"LTS24_%=x:\n\t"
"s_waitcnt vmcnt(0) lgkmcnt(0)\n\t"
"buffer_load_dwordx4 v[72:75], v83, s[56:59], s23 offen sc1\n\t"
"buffer_load_dwordx4 v[76:79], v83, s[56:59], s23 offen offset:1024 sc1\n\t"
"buffer_load_dwordx4 v[64:67], v82, s[56:59], s23 offen sc1\n\t"
"buffer_load_dwordx4 v[68:71], v82, s[56:59], s23 offen offset:1024 sc1\n\t"
"s_waitcnt vmcnt(3)\n\t"
"v_xor_b32 v72, v72, v84\n\t"
"v_xor_b32 v73, v73, v85\n\t"
"v_xor_b32 v74, v74, v86\n\t"
"v_xor_b32 v75, v75, v87\n\t"
"v_or3_b32 v90, v72, v73, v74\n\t"
"v_or_b32 v90, v75, v90\n\t"
"s_waitcnt vmcnt(2)\n\t"
"v_xor_b32 v76, v76, v84\n\t"
"v_xor_b32 v77, v77, v85\n\t"
"v_xor_b32 v78, v78, v86\n\t"
"v_xor_b32 v79, v79, v87\n\t"
"v_or3_b32 v90, v76, v77, v90\n\t"
"v_or3_b32 v90, v78, v79, v90\n\t"
"s_waitcnt vmcnt(1)\n\t"
"v_xor_b32 v64, v64, v84\n\t"
"v_xor_b32 v65, v65, v85\n\t"
"v_xor_b32 v66, v66, v86\n\t"
"v_xor_b32 v67, v67, v87\n\t"
"v_or3_b32 v90, v64, v65, v90\n\t"
"v_or3_b32 v90, v66, v67, v90\n\t"
"s_waitcnt vmcnt(0)\n\t"
"v_xor_b32 v68, v68, v84\n\t"
"v_xor_b32 v69, v69, v85\n\t"
"v_xor_b32 v70, v70, v86\n\t"
"v_xor_b32 v71, v71, v87\n\t"
"v_or3_b32 v90, v68, v69, v90\n\t"
"v_or3_b32 v90, v70, v71, v90\n\t"
"v_and_b32 v90, s27, v90\n\t"
"v_cmp_ne_u32 vcc, 0, v90\n\t"
"s_nop 0\n\t"
"s_cmp_eq_u64 vcc, 0\n\t"
"s_cbranch_scc1 LTR24_%=\n\t"
"s_sub_u32 s96, s96, 1\n\t"
"s_cmp_lg_u32 s96, 0\n\t"
"s_cbranch_scc1 LTS24_%=x\n\t"
"s_branch LTR24_%=\n\t"
"LTS25_%=:\n\t"
"s_mov_b32 s96, 100000\n\t"
"LTS25_%=x:\n\t"
"s_waitcnt vmcnt(0) lgkmcnt(0)\n\t"
"buffer_load_dwordx4 v[56:59], v81, s[56:59], s23 offen sc1\n\t"
"buffer_load_dwordx4 v[60:63], v81, s[56:59], s23 offen offset:1024 sc1\n\t"
"s_waitcnt vmcnt(1)\n\t"
"v_xor_b32 v56, v56, v84\n\t"
"v_xor_b32 v57, v57, v85\n\t"
"v_xor_b32 v58, v58, v86\n\t"
"v_xor_b32 v59, v59, v87\n\t"
"v_or3_b32 v90, v56, v57, v58\n\t"
"v_or_b32 v90, v59, v90\n\t"
"s_waitcnt vmcnt(0)\n\t"
"v_xor_b32 v60, v60, v84\n\t"
"v_xor_b32 v61, v61, v85\n\t"
"v_xor_b32 v62, v62, v86\n\t"
"v_xor_b32 v63, v63, v87\n\t"
"v_or3_b32 v90, v60, v61, v90\n\t"
"v_or3_b32 v90, v62, v63, v90\n\t"
"v_and_b32 v90, s27, v90\n\t"
"v_cmp_ne_u32 vcc, 0, v90\n\t"
"s_nop 0\n\t"
"s_cmp_eq_u64 vcc, 0\n\t"
"s_cbranch_scc1 LTR25_%=\n\t"
"s_sub_u32 s96, s96, 1\n\t"
"s_cmp_lg_u32 s96, 0\n\t"
"s_cbranch_scc1 LTS25_%=x\n\t"
"s_branch LTR25_%=\n\t"
"LRESTART_%=:\n\t"
"s_waitcnt vmcnt(0) lgkmcnt(0)\n\t"
"s_barrier\n\t"
"s_mov_b32 s8, 1\n\t"
"s_branch LSTART_%=\n\t"
"LEND_%=:\n\t"

  :: "s"(__builtin_amdgcn_kernarg_segment_ptr()), "s"(blockIdx.x), "v"(threadIdx.x)
  : "v1","v2","v3","v4","v5","v6","v7","v8","v9","v10","v11","v12","v13","v14","v15","v16","v17","v18","v19","v20","v21","v22","v23","v24","v25","v26","v27","v28","v29","v30","v31","v32","v33","v34","v35","v36","v37","v38","v39","v40","v41","v42","v43","v44","v45","v46","v47","v48","v49","v50","v51","v52","v53","v54","v55","v56","v57","v58","v59","v60","v61","v62","v63","v64","v65","v66","v67","v68","v69","v70","v71","v72","v73","v74","v75","v76","v77","v78","v79","v80","v81","v82","v83","v84","v85","v86","v87","v88","v89","v90","v91","v92","v93","v94","v95","v96","v97","v98","v99","v100","v101","v102","v103","v104","v105","v106","v107","v108","v109","v110","v111","v112","v113","v114","v115","v116","v117","v118","v119","v120","v121","v122","v123","v124","v125","v126","v127","v128","v129","v130","v131","v132","v133","v134","v135","v136","v137","v138","v139","v140","v141","v142","v143","v144","v145","v146","v147","v148","v149","v150","v151","v152","v153","v154","v155","v156","v157","v158","v159","v160","v161","v162","v163","v164","v165","v166","v167","v168","v169","v170","v171","v172","v173","v174","v175","v176","v177","v178","v179","v180","v181","v182","v183","v184","v185","v186","v187","v188","v189","v190","v191","v192","v193","v194","v195","v196","v197","v198","v199","v200","v201","v202","v203","v204","v205","v206","v207","v208","v209","v210","v211","v212","v213","v214","v215","v216","v217","v218","v219","v220","v221","v222","v223","v224","v225","v226","v227","v228","v229","v230","v231","v232","v233","v234","v235","v236","v237","v238","v239","v240","v241","v242","v243","v244","v245","v246","v247","v248","v249","v250","v251","v252","v253","v254","v255","a0","a1","a2","a3","a4","a5","a6","a7","a8","a9","a10","a11","a12","a13","a14","a15","a16","a17","a18","a19","a20","a21","a22","a23","a24","a25","a26","a27","a28","a29","a30","a31","a32","a33","a34","a35","a36","a37","a38","a39","a40","a41","a42","a43","a44","a45","a46","a47","a48","a49","a50","a51","a52","a53","a54","a55","a56","a57","a58","a59","a60","a61","a62","a63","a64","a65","a66","a67","a68","a69","a70","a71","a72","a73","a74","a75","a76","a77","a78","a79","a80","a81","a82","a83","a84","a85","a86","a87","a88","a89","a90","a91","a92","a93","a94","a95","a96","a97","a98","a99","a100","a101","a102","a103","a104","a105","a106","a107","a108","a109","a110","a111","a112","a113","a114","a115","a116","a117","a118","a119","a120","a121","a122","a123","a124","a125","a126","a127","a128","a129","a130","a131","a132","a133","a134","a135","a136","a137","a138","a139","a140","a141","a142","a143","a144","a145","a146","a147","a148","a149","a150","a151","a152","a153","a154","a155","a156","a157","a158","a159","a160","a161","a162","a163","a164","a165","a166","a167","a168","a169","a170","a171","a172","a173","a174","a175","a176","a177","a178","a179","a180","a181","a182","a183","a184","a185","a186","a187","a188","a189","a190","a191","a192","a193","a194","a195","a196","a197","a198","a199","a200","a201","a202","a203","a204","a205","a206","a207","a208","a209","a210","a211","a212","a213","a214","a215","a216","a217","a218","a219","a220","a221","a222","a223","a224","a225","a226","a227","a228","a229","a230","a231","a232","a233","a234","a235","a236","a237","a238","a239","a240","a241","a242","a243","a244","a245","a246","a247","a248","a249","a250","a251","a252","a253","a254","a255","s8","s9","s10","s11","s12","s13","s14","s15","s16","s17","s18","s19","s20","s21","s22","s23","s24","s25","s26","s27","s28","s29","s30","s31","s34","s35","s36","s37","s38","s39","s40","s41","s42","s43","s44","s45","s46","s47","s48","s49","s50","s51","s52","s53","s54","s55","s56","s57","s58","s59","s60","s61","s62","s63","s64","s65","s66","s67","s68","s69","s70","s71","s72","s73","s74","s75","s76","s77","s78","s79","s80","s81","s82","s83","s84","s85","s86","s87","s88","s89","s90","s91","s92","s93","s94","s95","s96","s97","s98","s99","vcc","memory");
}

extern "C" void kernel_launch(void* const* d_in, const int* in_sizes, int n_in, void* d_out, int out_size, void* d_ws, size_t ws_size, hipStream_t stream) {
  standalone__mha_block_cached__near_ridge_kernel<<<256, 256, 0, stream>>>(d_in[0], d_in[1], d_in[2], d_in[3], d_in[4], d_out, d_ws, d_ws);
}
```
